```python
import torch

def _softmax(x, axis=-1):
    return torch.softmax(x, dim=axis)

def _gelu(x):
    return torch.nn.functional.gelu(x, approximate='tanh')

def _silu(x):
    return torch.nn.functional.silu(x)

def _sigmoid(x):
    return torch.sigmoid(x)

def _layernorm(x, axis=-1, eps=1e-05):
    m = x.mean(dim=axis, keepdim=True)
    v = x.var(dim=axis, keepdim=True, unbiased=False)
    return (x - m) / torch.sqrt(v + eps)

def _scatter_add(tgt, idx, src, axis=0):
    return tgt.clone().index_add_(axis, idx, src)

def _segment_sum(src, seg, n):
    out = torch.zeros((n,) + src.shape[1:], dtype=src.dtype)
    return out.index_add_(0, seg, src)

def _pad(x, pw):
    flat = tuple((v for pair in reversed(pw) for v in pair))
    return torch.nn.functional.pad(x, flat)
kernel_name = 'gated_mix__memory_aug_recurrent_mem_tr__moe_arch_mod__prompt'

def setup_inputs():

    _w = lambda a: (lambda t: t.to(torch.bfloat16) if t.is_floating_point() else t)(torch.as_tensor(a))

    def _A_setup():
        B, S, Dm = (1, 2048, 1024)
        H, Dh = (32, 128)
        M, seg = (8, 32)
        seg = min(seg, S)
        S = S // seg * seg
        return {'x': _w((lambda *s: torch.randn(*s, dtype=torch.bfloat16) * 0.05)(B, S, Dm)), 'mem0': _w((lambda *s: torch.randn(*s, dtype=torch.bfloat16) * 0.8)(B, M, Dm)), 'Wq': _w((lambda *s: torch.randn(*s, dtype=torch.bfloat16) * 3.2)(Dm, H, Dh) * Dm ** (-0.5)), 'Wk': _w((lambda *s: torch.randn(*s, dtype=torch.bfloat16) * 3.2)(Dm, H, Dh) * Dm ** (-0.5)), 'Wv': _w((lambda *s: torch.randn(*s, dtype=torch.bfloat16) * 0.8)(Dm, H, Dh) * Dm ** (-0.5)), 'Wo': _w((lambda *s: torch.randn(*s, dtype=torch.bfloat16) * 0.8)(H, Dh, Dm) * (H * Dh) ** (-0.5)), 'seg_len': seg}
    _A_in = _A_setup()

    def _B_setup():
        B, S, D, F, C = (1, 2048, 1024, 3584, 8)
        return {'x': _w((lambda *s: torch.randn(*s, dtype=torch.bfloat16) * 0.05)(B, S, D)), 'w_r': _w((lambda *s: torch.randn(*s, dtype=torch.bfloat16) * 1.6)(D)), 'w_gate': _w((lambda *s: torch.randn(*s, dtype=torch.bfloat16) * 0.1)(D, F)), 'w_up': _w((lambda *s: torch.randn(*s, dtype=torch.bfloat16) * 0.1)(D, F)), 'w_down': _w((lambda *s: torch.randn(*s, dtype=torch.bfloat16) * 0.1)(F, D))}
    _B_in = _B_setup()
    return {'A_x': _A_in['x'], 'A_mem0': _A_in['mem0'], 'A_Wq': _A_in['Wq'], 'A_Wk': _A_in['Wk'], 'A_Wv': _A_in['Wv'], 'A_Wo': _A_in['Wo'], 'A_seg_len': _A_in['seg_len'], 'B_x': _B_in['x'], 'B_w_r': _B_in['w_r'], 'B_w_gate': _B_in['w_gate'], 'B_w_up': _B_in['w_up'], 'B_w_down': _B_in['w_down'], 'gate_w': _w((lambda *s: torch.randn(*s, dtype=torch.bfloat16) * 0.05)(1024) * 16.0)}

def reference(A_x, A_mem0, A_Wq, A_Wk, A_Wv, A_Wo, A_seg_len, B_x, B_w_r, B_w_gate, B_w_up, B_w_down, gate_w):

    def _A_ref(x, mem0, Wq, Wk, Wv, Wo, seg_len):
        B, S, Dm = x.shape
        H, Dh = (Wq.shape[1], Wq.shape[2])
        M = mem0.shape[1]
        scale = Dh ** (-0.5)
        n_seg = S // seg_len
        L = M + seg_len
        idx = torch.arange(L)
        mask = (idx[:, None] < M) | (idx[:, None] >= idx[None, :])
        mem = mem0
        outs = []
        for s in range(n_seg):
            xs = x[:, s * seg_len:(s + 1) * seg_len, :].clone()
            h = torch.concatenate([mem, xs], axis=1)
            q = torch.einsum('btd,dhe->bhte', h, Wq)
            k = torch.einsum('btd,dhe->bhte', h, Wk)
            v = torch.einsum('btd,dhe->bhte', h, Wv)
            scores = torch.einsum('bhqe,bhke->bhqk', q, k) * scale
            scores = torch.where(mask[None, None], scores, -1000000000.0)
            p = _softmax(scores, axis=-1)
            o = torch.einsum('bhqk,bhke->bhqe', p, v)
            o = torch.einsum('bhte,hed->btd', o, Wo)
            mem = o[:, :M, :].clone()
            outs.append(o[:, M:, :])
        return torch.concatenate(outs, axis=1)

    def _B_ref(x, w_r, w_gate, w_up, w_down):
        B, S, D = x.shape
        C = 8
        r = x @ w_r
        idx = torch.argsort(-r, axis=-1)[:, :C]
        idx_e = (lambda x, n, axis: torch.repeat_interleave(x, n, dim=axis))(idx.reshape(B, C, 1), D, axis=2)
        x_sel = (lambda x, i, axis: torch.take_along_dim(x, i, dim=axis))(x, idx_e, axis=1)
        g = _silu(x_sel @ w_gate)
        u = x_sel @ w_up
        y = g * u @ w_down
        r_sel = _sigmoid((lambda x, i, axis: torch.take_along_dim(x, i, dim=axis))(r, idx, axis=1))
        y = y * r_sel.reshape(B, C, 1)
        b_off = (torch.arange(B) * S).reshape(B, 1)
        flat_idx = (idx + b_off).reshape(B * C)
        zero = (x * 0.0).reshape(B * S, D)
        upd = _scatter_add(zero, flat_idx, y.reshape(B * C, D), axis=0)
        return x + upd.reshape(B, S, D)
    x = A_x
    g = _sigmoid((x * gate_w).sum(-1, keepdims=True))
    a = _A_ref(x, A_mem0, A_Wq, A_Wk, A_Wv, A_Wo, A_seg_len)
    b = _B_ref(x, B_w_r, B_w_gate, B_w_up, B_w_down)
    return g * a + (1 - g) * b
```

```cpp
#include <hip/hip_runtime.h>
#include <stdint.h>
#include <type_traits>

typedef unsigned short bf16_t;
typedef unsigned long long u64;
typedef float frag_cd __attribute__((ext_vector_type(4)));
typedef unsigned v4u __attribute__((ext_vector_type(4)));
typedef unsigned v8u __attribute__((ext_vector_type(8)));
typedef unsigned v2u __attribute__((ext_vector_type(2)));
typedef float v2f __attribute__((ext_vector_type(2)));
typedef __bf16 bf16x2_t __attribute__((ext_vector_type(2)));
typedef __bf16 v8bf __attribute__((ext_vector_type(8)));
typedef __bf16 v16bf __attribute__((ext_vector_type(16)));
typedef short v4s_t __attribute__((ext_vector_type(4)));
typedef __amdgpu_buffer_rsrc_t rsrc_t;
#define LAS __attribute__((address_space(3)))
#define GAS __attribute__((address_space(1)))

static constexpr int S_TOK = 2048, DM = 1024, NH = 32, DH = 128, MM = 8, SEG = 32, NSEG = 64, HD = NH * DH, FF = 3584, TOPC = 8;
static constexpr int C_ERR = 0, C_READY = 32, C_CAL = 64  , C_CALN = 96, C_G = 128  , C_M = C_G + 1024  , C_A = C_M + 1024  ,
                     C_P = C_A + 128  , C_WORDS = C_P + 256;
static constexpr int RING_BYTES = 4 << 20, SLOT = 1 << 20, CHB = 2 << 20, L_H3 = 192 << 10, L_TS = 256 << 10, SLOT3 = 192 << 10;
static constexpr unsigned SPIN_CAP = 1u << 18;
static constexpr int OOB = 0x7ffffff0, LCAP = 1 << 22, GCAP = 1 << 20;
static constexpr float SCALE = 0.08838834764831845f, LOG2E = 1.4426950408889634f;

struct Args {
    const bf16_t* x; const bf16_t* mem0; const bf16_t* Wq; const bf16_t* Wk; const bf16_t* Wv; const bf16_t* Wo;
    bf16_t* out; unsigned* ctl; unsigned char* ring; unsigned char* qkv; unsigned char* kvm; unsigned char* ox; unsigned char* part;
    const float* gate; const float* ysel; const int* idx; unsigned* dbg;
    int d1, d2, per, dinit;
};

__device__ __forceinline__ unsigned cvt_pk(float lo, float hi) { v2f v = {lo, hi}; return __builtin_bit_cast(unsigned, __builtin_convertvector(v, bf16x2_t)); }
__device__ __forceinline__ float lo16(unsigned p) { return __uint_as_float(p << 16); }
__device__ __forceinline__ float hi16(unsigned p) { return __uint_as_float(p & 0xffff0000u); }
__device__ __forceinline__ float rbf(float f) { return lo16(cvt_pk(f, 0.f)); }
template <int C> __device__ __forceinline__ unsigned dppu(unsigned v) { return (unsigned)__builtin_amdgcn_update_dpp(0, (int)v, C, 0xf, 0xf, true); }
template <int C> __device__ __forceinline__ float dppf(float v) { return __uint_as_float(dppu<C>(__float_as_uint(v))); }
#define ROR8 0x128
#define HMIR 0x141
__device__ __forceinline__ v2u sw16(unsigned a, unsigned b) { auto r = __builtin_amdgcn_permlane16_swap(a, b, false, false); return v2u{r[0], r[1]}; }
__device__ __forceinline__ v2u sw32(unsigned a, unsigned b) { auto r = __builtin_amdgcn_permlane32_swap(a, b, false, false); return v2u{r[0], r[1]}; }
__device__ __forceinline__ float tadd16(float a, float b) { v2u r = sw16(__float_as_uint(a), __float_as_uint(b)); return __uint_as_float(r.x) + __uint_as_float(r.y); }
__device__ __forceinline__ float tadd32(float a, float b) { v2u r = sw32(__float_as_uint(a), __float_as_uint(b)); return __uint_as_float(r.x) + __uint_as_float(r.y); }
__device__ __forceinline__ float tmax16(float a) { v2u r = sw16(__float_as_uint(a), __float_as_uint(a)); return fmaxf(__uint_as_float(r.x), __uint_as_float(r.y)); }
__device__ __forceinline__ float tmax32(float a) { v2u r = sw32(__float_as_uint(a), __float_as_uint(a)); return fmaxf(__uint_as_float(r.x), __uint_as_float(r.y)); }
__device__ __forceinline__ unsigned umax3(unsigned a, unsigned b, unsigned c) { return max(max(a, b), c); }
__device__ __forceinline__ v4u pack_swap(frag_cd a, frag_cd b) { v2u x0 = sw16(cvt_pk(a[0], a[1]), cvt_pk(b[0], b[1])), x1 = sw16(cvt_pk(a[2], a[3]), cvt_pk(b[2], b[3])); return v4u{x0.x, x1.x, x0.y, x1.y}; }
#define MFMA(a, b, c) __builtin_amdgcn_mfma_f32_16x16x32_bf16(__builtin_bit_cast(v8bf, a), __builtin_bit_cast(v8bf, b), c, 0, 0, 0)
#define SMF(a, b, c, ix) __builtin_amdgcn_smfmac_f32_16x16x64_bf16(__builtin_bit_cast(v8bf, a), __builtin_bit_cast(v16bf, b), c, ix, 0, 0)
#define FENCE() asm volatile("" ::: "memory")
#define SB() __builtin_amdgcn_sched_barrier(0)
#define RFL(x) __builtin_amdgcn_readfirstlane(x)
#define BAR() do { FENCE(); __builtin_amdgcn_s_barrier(); FENCE(); } while (0)
#define WAITV(n) do { FENCE(); __builtin_amdgcn_s_waitcnt(0x0F70 | (n)); FENCE(); } while (0)
#define WAITL() asm volatile("s_waitcnt lgkmcnt(0)" ::: "memory")
#define STB(v, rs, off, soff) __builtin_amdgcn_raw_buffer_store_b128(v, rs, off, soff, 16)
template <class T> __device__ __forceinline__ LAS T* lp(unsigned a) { return (LAS T*)(size_t)a; }
__device__ __forceinline__ void glds(const void* g, unsigned voff, unsigned lds) { asm volatile("s_mov_b32 m0, %2\n\ts_nop 0\n\tglobal_load_lds_dwordx4 %0, %1" :: "v"(voff), "s"(g), "s"(lds) : "memory"); }
template <int N> __device__ __forceinline__ void glds1(const void* g, unsigned voff, unsigned lds) {
#define GL(o) "\n\tglobal_load_lds_dwordx4 %0, %1 offset:" #o " sc1"
    if constexpr (N == 1) asm volatile("s_mov_b32 m0, %2\n\ts_nop 0" GL(0) :: "v"(voff), "s"(g), "s"(lds) : "memory");
    if constexpr (N == 3) asm volatile("s_mov_b32 m0, %2\n\ts_nop 0" GL(0) GL(1024) GL(2048) :: "v"(voff), "s"(g), "s"(lds) : "memory");
    if constexpr (N == 4) asm volatile("s_mov_b32 m0, %2\n\ts_nop 0" GL(0) GL(1024) GL(2048) GL(3072) :: "v"(voff), "s"(g), "s"(lds) : "memory");
}
__device__ __forceinline__ rsrc_t mkrs(const void* p, int bytes) { return __builtin_amdgcn_make_buffer_rsrc((void*)p, 0, bytes, 0x00020000); }
__device__ __forceinline__ unsigned gld(const unsigned* p) { return __hip_atomic_load(p, __ATOMIC_RELAXED, __HIP_MEMORY_SCOPE_AGENT); }
__device__ __forceinline__ void gst(unsigned* p, unsigned v) { __hip_atomic_store(p, v, __ATOMIC_RELAXED, __HIP_MEMORY_SCOPE_AGENT); }
__device__ __forceinline__ unsigned rt16() { return (unsigned)__builtin_amdgcn_s_memrealtime() << 4; }

#define STAMP(slot) do {} while (0)
#define STAMPS_FLUSH() do {} while (0)
#define MARK(i) do {} while (0)
#define CNT(i) do {} while (0)
#define CNT_FLUSH() do {} while (0)

#define LATE_THR 100
#define LATE_ADD 480
#define SLIP_THR 320
#define SLIP_ADD 640
#define SLIP_INC 32
__device__ __forceinline__ void wait_t(unsigned T) {
    int rem, cap = 4096;
    asm volatile("1:\n\ts_memrealtime vcc\n\ts_sub_u32 %1, %1, 1\n\ts_cbranch_scc1 3f\n\ts_waitcnt lgkmcnt(0)\n\ts_lshl_b32 %0, vcc_lo, 4\n\ts_sub_i32 %0, %2, %0\n\ts_cmp_ge_i32 %0, 64\n\ts_cbranch_scc1 1b\n\t"
                 "s_cmp_gt_i32 %0, 12\n\ts_cbranch_scc0 3f\n2:\n\ts_sub_i32 %0, %0, 13\n\ts_cmp_gt_i32 %0, 12\n\ts_cbranch_scc1 2b\n3:\n\ts_waitcnt lgkmcnt(0)"
                 : "=&s"(rem), "+s"(cap) : "s"(T) : "scc", "vcc", "memory");
}
__device__ __forceinline__ void sched(unsigned T) { unsigned now = rt16(); if ((int)(T - now) < LATE_THR) T = now + LATE_ADD; wait_t(T); }
template <int N>
__device__ __forceinline__ bool bad(const v4u (&v)[N]) {
    unsigned m = umax3(v[0].x, v[0].y, v[0].z); m = max(m, v[0].w);
#pragma unroll
    for (int i = 1; i < N; ++i) { m = umax3(m, v[i].x, v[i].y); m = umax3(m, v[i].z, v[i].w); }
    return __builtin_amdgcn_ballot_w64(m == 0xFFFFFFFFu) != 0;
}
template <int N>
__device__ __forceinline__ void ldn(rsrc_t rs, const int (&off)[N], int soff, v4u (&v)[N]) {
#pragma unroll
    for (int i = 0; i < N; ++i) v[i] = __builtin_amdgcn_raw_buffer_load_b128(rs, off[i], soff, 16);
}
__device__ __forceinline__ void endpgm() { asm volatile("s_endpgm" ::: "memory"); }
__device__ __forceinline__ void fatal(unsigned* err) { asm volatile("global_store_dword %0, %1, off sc1\n\ts_waitcnt vmcnt(0)\n\ts_endpgm" :: "v"(err), "v"(1u) : "memory"); }
template <int N>
__device__ __forceinline__ void retry(rsrc_t rs, const int (&off)[N], int soff, v4u (&v)[N], unsigned* err, unsigned& budget) {
    if (--budget == 0) fatal(err);
#define RL(d, o) "buffer_load_dwordx4 %" #d ", %" #o ", %[r], %[s] offen sc1\n\t"
#define RH "s_waitcnt vmcnt(0)\n\ts_sleep 2\n\t"
#define RT [r] "s"(rs), [s] "s"(soff) : "memory"
    if constexpr (N == 2) asm volatile(RH RL(0, 2) RL(1, 3) "s_waitcnt vmcnt(0)" : "=&v"(v[0]), "=&v"(v[1]) : "v"(off[0]), "v"(off[1]), RT);
    if constexpr (N == 4) asm volatile(RH RL(0, 4) RL(1, 5) RL(2, 6) RL(3, 7) "s_waitcnt vmcnt(0)" : "=&v"(v[0]), "=&v"(v[1]), "=&v"(v[2]), "=&v"(v[3]) : "v"(off[0]), "v"(off[1]), "v"(off[2]), "v"(off[3]), RT);
    if constexpr (N == 5) asm volatile(RH RL(0, 5) RL(1, 6) RL(2, 7) RL(3, 8) RL(4, 9) "s_waitcnt vmcnt(0)" : "=&v"(v[0]), "=&v"(v[1]), "=&v"(v[2]), "=&v"(v[3]), "=&v"(v[4]) : "v"(off[0]), "v"(off[1]), "v"(off[2]), "v"(off[3]), "v"(off[4]), RT);
}
__device__ __forceinline__ unsigned ldsr(unsigned p) { return __hip_atomic_load(lp<unsigned>(p), __ATOMIC_RELAXED, __HIP_MEMORY_SCOPE_WORKGROUP); }
__device__ __forceinline__ void ldsadd(unsigned p) { asm volatile("s_mov_b64 exec, 1\n\tds_add_u32 %0, %1\n\ts_mov_b64 exec, -1" :: "v"(p), "v"(1u) : "memory"); }
__device__ __forceinline__ v2u trd(unsigned p) { return __builtin_bit_cast(v2u, __builtin_amdgcn_ds_read_tr16_b64_v4i16(lp<v4s_t>(p))); }
__device__ __forceinline__ v8u cat(v2u a, v2u b, v2u c, v2u d) { return v8u{a.x, a.y, b.x, b.y, c.x, c.y, d.x, d.y}; }
__device__ __forceinline__ v4u cat2(v2u a, v2u b) { return v4u{a.x, a.y, b.x, b.y}; }
__device__ __forceinline__ unsigned poll_red(unsigned aC, unsigned aR, unsigned need) {
    unsigned cv; int cap = LCAP; v2f r[4];
    asm volatile("1:\n\tds_read_b32 %0, %6\n\tds_read_b64 %1, %7\n\tds_read_b64 %2, %7 offset:3072\n\tds_read_b64 %3, %7 offset:6144\n\tds_read_b64 %4, %7 offset:9216\n\t"
                 "s_sub_u32 %5, %5, 1\n\ts_cbranch_scc0 2f\n\ts_endpgm\n2:\n\ts_waitcnt lgkmcnt(4)\n\tv_cmp_gt_u32 vcc, %8, %0\n\ts_cbranch_vccnz 1b\n\ts_waitcnt lgkmcnt(0)"
                 : "=&v"(cv), "=&v"(r[0]), "=&v"(r[1]), "=&v"(r[2]), "=&v"(r[3]), "+s"(cap) : "v"(aC), "v"(aR), "s"(need) : "vcc", "scc", "memory");
    return min(cvt_pk((r[0].x + r[1].x) + (r[2].x + r[3].x), (r[0].y + r[1].y) + (r[2].y + r[3].y)), 0xFFFFFFFEu);
}
__device__ __forceinline__ v4u quad(unsigned p0) { return v4u{p0, dppu<0x55>(p0), dppu<0xAA>(p0), dppu<0xFF>(p0)}; }
__device__ __forceinline__ void poll_cnt(unsigned p, unsigned need) { for (int i = 0; ldsr(p) < need; ++i) { if (i > LCAP) endpgm(); } FENCE(); }
__device__ __forceinline__ bool flags_ok(const unsigned* p, int nw, unsigned need, int l) { unsigned v = gld(p + min(l, nw - 1)); bool r = __builtin_amdgcn_ballot_w64(v < need) == 0; FENCE(); return r; }
__device__ __forceinline__ void flags_wait(const unsigned* p, int nw, unsigned need, int l, unsigned* err) {
    for (int i = 0; !flags_ok(p, nw, need, l); ++i) { __builtin_amdgcn_s_sleep(4); if (i > GCAP) fatal(err); }
}

__device__ __forceinline__ void chain(const Args& a, const int c, const unsigned L) {
    const int tid = threadIdx.x, w8 = RFL(tid >> 6), w = w8 & 3, l = tid & 63;
    const int g = c & 3, aa = c >> 2, h = g * 8 + (aa >> 2), jj = aa & 3;
    const int n = l & 15, q = l >> 4, row = n & 7, hf = n >> 3;
    const unsigned L_RED = L + 131072, L_SC = L_RED + 12288, L_SM = L_SC + 2048, L_CNT = L_SM + 512, L_VX = L, L_ST = L + 32768;
    const unsigned cB1 = L_CNT, cB2 = L_CNT + 128, cBv = L_CNT + 256, cV2 = L_CNT + 384;
    const rsrc_t rs = mkrs(a.ring, RING_BYTES);
    unsigned* const err = a.ctl + C_ERR;
    const v4u ones = {~0u, ~0u, ~0u, ~0u};
    if (tid < 128) lp<unsigned>(L_CNT)[tid] = 0;
#pragma unroll
    for (int i = 0; i < 4; ++i) STB(ones, rs, c * 32768 + i * 8192 + tid * 16, 0);
    const unsigned stg = L + w8 * 16384;
    const int prr = 16 * (q >> 1) + 4 * (q & 1) + ((n >> 2) & 1) + 8 * (n >> 3);
    auto stage_w = [&](const bf16_t* W) {
        W += (size_t)256 * w * HD + h * DH + 32 * jj;
#pragma unroll
        for (int ch = 0; ch < 16; ++ch) glds(W + (size_t)16 * ch * HD, (l >> 2) * (HD * 2) + (l & 3) * 16, stg + ch * 1024);
        WAITV(0);
    };
    auto read_w = [&](v8u (&d)[2][4]) {
#pragma unroll
        for (int ct = 0; ct < 2; ++ct)
#pragma unroll
            for (int i = 0; i < 4; ++i) { unsigned p = stg + (64 * i + prr) * 64 + ct * 32 + (l & 3) * 8; d[ct][i] = cat(trd(p), trd(p + 128), trd(p + 2048), trd(p + 2048 + 128)); }
        WAITL();
    };
    const int six = hf ? 0xEEEE : 0x4444;
    const rsrc_t rsm = mkrs(a.mem0, MM * DM * 2);
    const int pos = l >> 3, prr8 = (pos >> 1) + 4 * (pos & 1);
    unsigned cal;
    u64 cnts[4] = {}; (void)cnts;
    auto phA = [&](int lg) { return ((lg >> 13) << 14) + (int)((cal & 1) << 13) + (lg & 8191); };
    auto phB = [&](int lg) { return CHB + ((lg >> 13) << 14) + (int)((cal & 2) << 12) + (lg & 8191); };
    auto h1o = [&](int inner) { return phB(h * 6144 + inner); };
    auto arrive = [&]() {
        if (tid == 192) __hip_atomic_fetch_add(a.ctl + C_READY, 1u, __ATOMIC_RELAXED, __HIP_MEMORY_SCOPE_AGENT);
    };
    auto start = [&]() {
        if (w8 == 3) {
            flags_wait(a.ctl + C_READY, 1, 128 + 32, l, err);
            unsigned v = gld(a.ctl + C_CAL + (l & 3));
            unsigned t = (__builtin_amdgcn_readlane(v, 1) < __builtin_amdgcn_readlane(v, 0) ? 1u : 0u) | (__builtin_amdgcn_readlane(v, 3) < __builtin_amdgcn_readlane(v, 2) ? 2u : 0u);
            lp<unsigned>(L_CNT + 448)[0] = t; WAITL();
        }
        BAR();
        cal = RFL(ldsr(L_CNT + 448));
    };

    if (w8 < 4) {
        v8u wA[2][2][4];
        stage_w(a.Wq); BAR(); arrive(); read_w(wA[0]);
        stage_w(a.Wk); read_w(wA[1]);
        MARK(2);
        BAR();
        const unsigned aRedR = L_RED + (w * 128 + 2 * l) * 4, redW = L_RED + ((w * 48 + q) * 16 + n) * 4;
        const unsigned scW = w < 2 ? L_SC + (hf ? 256 + l * 4 : ((w * 4 + q) * 8 + row) * 4) * 4 : ((q >> 1) == hf ? L_SM + ((hf * 8 + row) * 8 + (q & 1) * 4) * 4 : L_SC + (256 + l * 4) * 4);
        auto run = [&](auto role_) {
        constexpr int ROLE = decltype(role_)::value;
        v4u kx[4]; unsigned fl = 0;
        const unsigned char* xp = a.qkv + h * 24576 + (w & 1) * 4096;
        const unsigned* fG = a.ctl + C_G + h * 32;
        auto xload = [&](int sk, int sv) {
            if (__builtin_amdgcn_ballot_w64(fl <= (unsigned)sv) != 0) flags_wait(fG, 2, sv + 1, l, err);
            glds1<4>(xp + (size_t)sv * (NH * 24576) + 8192, l * 16, L_VX + (sv & 1) * 8192 + w * 4096);
            asm volatile("global_load_dwordx4 %0, %5, %6 sc1\n\tglobal_load_dwordx4 %1, %5, %6 offset:1024 sc1\n\tglobal_load_dwordx4 %2, %5, %6 offset:2048 sc1\n\tglobal_load_dwordx4 %3, %5, %6 offset:3072 sc1\n\t"
                         "global_load_dword %4, %7, %8 sc1\n\ts_waitcnt vmcnt(0)"
                         : "=&v"(kx[0]), "=&v"(kx[1]), "=&v"(kx[2]), "=&v"(kx[3]), "=&v"(fl) : "v"(l * 16), "s"(xp + (size_t)sk * (NH * 24576)), "v"((l & 1) * 4), "s"(fG) : "memory");
        };
        if (ROLE == 0) { xload(0, 0); xload(0, 1); }
        start();
        MARK(0);
        int o1[ROLE == 2 ? 4 : 2], ld2[4], st3, off3[ROLE == 3 ? 5 : 4], o3[ROLE == 3 ? 5 : 4];
        int st1 = h1o((w >> 1) * 2048 + (4 * jj + 2 * (w & 1) + ((l >> 2) & 1)) * 128 + prr8 * 16); if (l & 3) st1 = OOB;
#pragma unroll
        for (int i = 0; i < 2; ++i) { if constexpr (ROLE == 2) { o1[2 * i] = h1o((8 * i + 2 * q + hf) * 128 + row * 16); o1[2 * i + 1] = h1o(2048 + (8 * i + 2 * q + hf) * 128 + row * 16); } else o1[i] = h1o((8 * i + 2 * q + hf) * 128 + row * 16); }
#pragma unroll
        for (int i = 0; i < 4; ++i) { ld2[i] = phA(c * 4096 + i * 1024 + l * 16); off3[i] = phB(L_H3 + g * 16384 + (32 * w + 8 * i + 2 * q + hf) * 128 + row * 16); o3[i] = (row * DM + 256 * w + 64 * i + 16 * q + 8 * hf) * 2; }
        if constexpr (ROLE == 3) { off3[4] = phB(L_TS + g * 128); o3[4] = OOB; }
        st3 = l < 32 ? phB(L_H3 + (l >> 3) * 16384 + c * 128 + row * 16) : (c == 0 && l < 36 ? phB(L_TS + (l & 3) * 128) : OOB);
        const int in1a = h1o((l >> 5) * 2048 + jj * 512 + (l & 31) * 16), in1b = l < 32 ? h1o(4096 + jj * 512 + l * 16) : OOB;
        auto kvcopy = [&](int s) {
            const rsrc_t rk = mkrs(a.kvm, NSEG * NH * 4096);
            const int o = (l & 3) ? OOB : h * 4096 + (4 * jj + ((l >> 2) & 1)) * 128 + prr8 * 16;
#pragma unroll
            for (int t = 0; t < 4; ++t) STB(quad(poll_red(t < 2 ? cB1 : cBv, L_RED + ((2 + t) * 128 + 2 * l) * 4, 4u * (s + 1))), rk, o, s * (NH * 4096) + (t >> 1) * 2048 + (t & 1) * 256);
            WAITV(0);
            if (l == 0) gst(a.ctl + C_M + h * 32 + jj, s + 1);
        };
        v4u sel;
#pragma unroll
        for (int i = 0; i < 4; ++i) sel[i] = (row >> 1) == i ? 0x3F80u << (16 * (row & 1)) : 0u;
        __builtin_amdgcn_s_setprio(3);
        int so = 0, sp = SLOT;
        unsigned budget = SPIN_CAP, tb = rt16() - (1u << 20), add = 0;
        rsrc_t r3 = rsm; int s3 = 0, w3 = 0, x3 = SLOT3, y3 = 2 * SLOT3;
        for (int s = 0;; ++s) {
            u64 stv[11] = {};
            unsigned n4 = 4u * (s + 1);
            asm volatile("" : "+s"(n4));
            STAMP(0);
            constexpr int N3 = ROLE == 3 ? 5 : 4;
            v4u mb[N3];
            if (ROLE == 3 && s) sched(tb);
            BAR();
            STAMP(1);
            ldn<N3>(r3, o3, s3, mb);
            FENCE();
            frag_cd acc[4];
            for (;;) {
#pragma unroll
                for (int m = 0; m < 4; ++m) { acc[m] = frag_cd{}; asm volatile("" : "+v"(acc[m])); }
                SB();
                unsigned mxs = 0;
#pragma unroll
                for (int i = 0; i < 4; ++i) {
                    acc[0] = SMF(mb[i], wA[0][0][i], acc[0], six); mxs = umax3(mxs, mb[i].x, mb[i].y); SB();
                    acc[1] = SMF(mb[i], wA[0][1][i], acc[1], six); mxs = umax3(mxs, mb[i].z, mb[i].w); asm volatile("" : "+v"(mxs)); SB();
                    acc[2] = SMF(mb[i], wA[1][0][i], acc[2], six); if (ROLE == 3 && i == 3) mxs = umax3(mxs, mb[N3 - 1].x, mb[N3 - 1].y); SB();
                    acc[3] = SMF(mb[i], wA[1][1][i], acc[3], six); SB(); }
                if (__builtin_expect(__builtin_amdgcn_ballot_w64(mxs == 0xFFFFFFFFu) == 0, 1)) break;
                CNT(2); retry<N3>(r3, o3, s3, mb, err, budget);
            }
            STAMP(2);
#pragma unroll
            for (int m = 0; m < 4; ++m) {
                float x0 = tadd32(acc[m][0], acc[m][1]), x1 = tadd32(acc[m][2], acc[m][3]);
                lp<float>(redW)[m * 128] = x0; lp<float>(redW)[m * 128 + 64] = x1;
            }
            FENCE();
            ldsadd(cB1);
            if (s == NSEG - 1) { if (ROLE == 2) kvcopy(s); break; }
            STB(quad(poll_red(cB1, aRedR, n4)), rs, st1, so);
            STAMP(3);
            if (ROLE == 3) {
                if (s) { tb = RFL(mb[N3 - 1].x); add = RFL(mb[N3 - 1].y); }
                sched(tb + a.d1 + add);
            }
            BAR();
            STAMP(4);
            if constexpr (ROLE == 0) {
                v4u qv[2];
                ldn<2>(rs, o1, so, qv);
                frag_cd sl, sh;
                for (;;) {
                    sl = frag_cd{}; sh = frag_cd{};
#pragma unroll
                    for (int i = 0; i < 2; ++i) { sl = MFMA(kx[2 * i], qv[i], sl); sh = MFMA(kx[2 * i + 1], qv[i], sh); }
                    if (__builtin_expect(!bad<2>(qv), 1)) break;
                    CNT(0); retry<2>(rs, o1, so, qv, err, budget);
                }
                frag_cd r;
#pragma unroll
                for (int e = 0; e < 4; ++e) r[e] = sl[e] + dppf<ROR8>(sh[e]);
                *lp<frag_cd>(scW) = r;
                FENCE();
                ldsadd(cB2);
                STAMP(5);
                xload(s + 1, min(s + 2, NSEG - 1));
                STAMP(6); STAMP(7); STAMP(8); STAMP(9); STAMP(10);
            } else if constexpr (ROLE == 2) {
                v4u qk[4];
                ldn<4>(rs, o1, so, qk);
                frag_cd sm;
                for (;;) {
                    sm = frag_cd{}; sm = MFMA(qk[1], qk[0], sm); sm = MFMA(qk[3], qk[2], sm);
                    if (__builtin_expect(!bad<4>(qk), 1)) break;
                    CNT(0); retry<4>(rs, o1, so, qk, err, budget);
                }
                *lp<frag_cd>(scW) = sm;
                FENCE();
                ldsadd(cB2);
                STAMP(5);
                kvcopy(s);
                STAMP(6); STAMP(7); STAMP(8); STAMP(9); STAMP(10);
            } else {
                wait_t(tb + a.d1 + add + (a.dinit & 0xffff));
#pragma unroll
                for (int i = 0; i < 4; ++i) STB(ones, rs, ld2[i], sp);
                STB(ones, rs, in1a, sp); STB(ones, rs, in1b, sp);
                STB(ones, rs, st3, x3);
                STAMP(5);
                poll_cnt(cV2, n4);
                STAMP(6); STAMP(7);
                const unsigned t2 = tb + a.d2 + 2 * add;
                sched(t2);
                STAMP(8);
                v4u v[4];
                ldn<4>(rs, ld2, so, v);
                frag_cd y; unsigned tn, an;
                for (;;) {
                    const int late = (int)(rt16() - t2);
                    tn = tb + a.per + 3 * add; an = add;
                    if (late > SLIP_THR) { tn += late + SLIP_ADD; if (s) an = min(add + SLIP_INC, 4000u); }
                    asm volatile("" : "+s"(tn), "+s"(an)); SB();
                    y = frag_cd{};
#pragma unroll
                    for (int i = 0; i < 4; ++i) y = MFMA(sel, v[i], y);
                    if (__builtin_expect(!bad<4>(v), 1)) break;
                    CNT(1); retry<4>(rs, ld2, so, v, err, budget);
                }
                STAMP(9);
#pragma unroll
                for (int e = 0; e < 4; ++e) y[e] += dppf<ROR8>(y[e]);
                v4u oo = pack_swap(y, y);
                oo.x = l < 32 ? oo.x : tn & ~1u; oo.y = l < 32 ? oo.y : an;
                STB(oo, rs, st3, w3);
                STAMP(10);
                tb += a.per + 3 * add;
            }
            STAMPS_FLUSH();
            r3 = rs; s3 = w3; w3 = x3; x3 = y3; y3 = s3;
            { int t = sp; sp = so; so = t; }
#pragma unroll
            for (int i = 0; i < N3; ++i) o3[i] = off3[i];
        }
        };
        if (w < 2) run(std::integral_constant<int, 0>{}); else if (w == 2) run(std::integral_constant<int, 2>{}); else run(std::integral_constant<int, 3>{});
    } else {
        v8u wV[2][4], wO[4][2];
        stage_w(a.Wv); BAR(); read_w(wV);
        { const bf16_t* Wo = a.Wo + (size_t)h * DH * DM + 256 * jj + 64 * w;
#pragma unroll
          for (int i = 0; i < 16; ++i) glds(Wo + (size_t)8 * i * DM, (l >> 3) * (DM * 2) + (l & 7) * 16, stg + i * 1024);
          WAITV(0);
#pragma unroll
          for (int t = 0; t < 4; ++t)
#pragma unroll
              for (int i = 0; i < 2; ++i) { unsigned p = stg + (64 * i + 8 * q + (n >> 2)) * 128 + (t >> 1) * 64 + (l & 3) * 16 + (t & 1) * 8; wO[t][i] = cat(trd(p), trd(p + 512), trd(p + 4096), trd(p + 4096 + 512)); }
          WAITL(); }
        BAR();
        start();
        v2u ua[4][2];
        auto ucalc = [&](int s) {
#pragma unroll
            for (int kt = 0; kt < 2; ++kt) {
                v4u vx[4];
#pragma unroll
                for (int ks = 0; ks < 4; ++ks) vx[ks] = *lp<v4u>(L_VX + (s & 1) * 8192 + (kt * 4 + ks) * 1024 + l * 16);
#pragma unroll
                for (int t = 0; t < 4; ++t) { frag_cd u = {};
#pragma unroll
                    for (int ks = 0; ks < 4; ++ks) { v4u b = {wO[t][ks >> 1][4 * (ks & 1)], wO[t][ks >> 1][4 * (ks & 1) + 1], wO[t][ks >> 1][4 * (ks & 1) + 2], wO[t][ks >> 1][4 * (ks & 1) + 3]}; u = MFMA(vx[ks], b, u); }
                    ua[t][kt] = v2u{cvt_pk(u[0], u[1]), cvt_pk(u[2], u[3])}; }
            }
        };
        ucalc(0);
        const int phf = (n >> 2) & 1, prow = (phf ? 3 - (n & 3) : (n & 3)) + 4 * hf;
        const int st1 = (l & 3) ? OOB : h1o(2 * 2048 + (4 * jj + 2 * (w & 1) + ((l >> 2) & 1)) * 128 + prr8 * 16);
        int o1[2], off3[4], o3[4];
#pragma unroll
        for (int i = 0; i < 2; ++i) o1[i] = h1o(2 * 2048 + (8 * i + 2 * q + hf) * 128 + row * 16);
#pragma unroll
        for (int i = 0; i < 4; ++i) { off3[i] = phB(L_H3 + g * 16384 + (32 * w + 8 * i + 2 * q + hf) * 128 + row * 16); o3[i] = (row * DM + 256 * w + 64 * i + 16 * q + 8 * hf) * 2; }
        const int st2 = phA((32 * jj + 8 * w + 4 * phf + q) * 4096 + h * 128 + prow * 16);
        const unsigned aRedR = L_RED + ((4 + (w & 1)) * 128 + pos * 16 + 4 * (l & 3) + 2 * ((l >> 2) & 1)) * 4;
        const unsigned aSc = L_SC + ((phf * 4 + q) * 8 + prow) * 16, aSm = L_SM + (prow * 8 + 4 * (q & 1) + 2 * phf) * 4;
        const unsigned redW = L_RED + ((w * 48 + 32 + q) * 16 + n) * 4;
        int so = 0, sp = SLOT;
        unsigned budget = SPIN_CAP;
        rsrc_t r3 = rsm; int s3 = 0, w3 = 0, x3 = SLOT3, y3 = 2 * SLOT3;
        for (int s = 0;; ++s) {
            u64 stv[11] = {};
            unsigned n4 = 4u * (s + 1), n3 = 3u * (s + 1);
            asm volatile("" : "+s"(n4), "+s"(n3));
            STAMP(0);
            v4u mb[4];
            __builtin_amdgcn_s_setprio(2);
            BAR();
            asm volatile("s_nop 15\n\ts_nop 15\n\ts_nop 15" ::: "memory");
            STAMP(1);
            ldn<4>(r3, o3, s3, mb);
            FENCE();
            frag_cd acc[2];
            for (;;) {
#pragma unroll
                for (int m = 0; m < 2; ++m) { acc[m] = frag_cd{}; asm volatile("" : "+v"(acc[m])); }
                SB();
                unsigned mxs = 0;
#pragma unroll
                for (int i = 0; i < 4; ++i) {
                    acc[0] = SMF(mb[i], wV[0][i], acc[0], six); mxs = umax3(mxs, mb[i].x, mb[i].y); SB();
                    acc[1] = SMF(mb[i], wV[1][i], acc[1], six); mxs = umax3(mxs, mb[i].z, mb[i].w); asm volatile("" : "+v"(mxs)); SB(); }
                if (__builtin_expect(__builtin_amdgcn_ballot_w64(mxs == 0xFFFFFFFFu) == 0, 1)) break;
                CNT(2); retry<4>(r3, o3, s3, mb, err, budget);
            }
            STAMP(2);
#pragma unroll
            for (int m = 0; m < 2; ++m) {
                float x0 = tadd32(acc[m][0], acc[m][1]), x1 = tadd32(acc[m][2], acc[m][3]);
                lp<float>(redW)[m * 128] = x0; lp<float>(redW)[m * 128 + 64] = x1;
            }
            FENCE();
            ldsadd(cBv);
            if (s == NSEG - 1) break;
            if (w < 2) STB(quad(poll_red(cBv, aRedR, n4)), rs, st1, so);
            STAMP(3);
            __builtin_amdgcn_s_setprio(3);
            BAR();
            asm volatile("s_nop 15\n\ts_nop 15\n\ts_nop 15\n\ts_nop 15" ::: "memory");
            STAMP(4);
            v4u vm[2];
            ldn<2>(rs, o1, so, vm);
            frag_cd z[4];
            for (;;) {
#pragma unroll
                for (int t = 0; t < 4; ++t) z[t] = frag_cd{};
#pragma unroll
                for (int i = 0; i < 2; ++i)
#pragma unroll
                    for (int t = 0; t < 4; ++t) z[t] = SMF(vm[i], wO[t][i], z[t], six);
                if (__builtin_expect(!bad<2>(vm), 1)) break;
                CNT(0); retry<2>(rs, o1, so, vm, err, budget);
            }
            STAMP(5);
            frag_cd sx; v2f s4a, s4b;
            { unsigned cv; int cap = LCAP;
              asm volatile("1:\n\tds_read_b32 %0, %5\n\tds_read_b128 %1, %6\n\tds_read_b64 %2, %7\n\tds_read_b64 %3, %7 offset:256\n\t"
                           "s_sub_u32 %4, %4, 1\n\ts_cbranch_scc0 2f\n\ts_endpgm\n2:\n\ts_waitcnt lgkmcnt(3)\n\tv_cmp_gt_u32 vcc, %8, %0\n\ts_cbranch_vccnz 1b\n\ts_waitcnt lgkmcnt(0)"
                           : "=&v"(cv), "=&v"(sx), "=&v"(s4a), "=&v"(s4b), "+s"(cap) : "v"(cB2), "v"(aSc), "v"(aSm), "s"(n3) : "vcc", "scc", "memory"); }
            STAMP(6);
            float sv[6] = {sx[0], sx[1], sx[2], sx[3], s4a.x + s4b.x, s4a.y + s4b.y};
#pragma unroll
            for (int e = 0; e < 6; e += 2) { unsigned p = cvt_pk(sv[e], sv[e + 1]); p = cvt_pk(lo16(p) * SCALE, hi16(p) * SCALE); sv[e] = lo16(p); sv[e + 1] = hi16(p); }
            float mx = fmaxf(fmaxf(fmaxf(sv[0], sv[1]), sv[2]), fmaxf(fmaxf(sv[3], sv[4]), sv[5]));
            mx = fmaxf(mx, dppf<HMIR>(mx)); mx = tmax16(mx); mx = tmax32(mx);
            const float mk = -mx * LOG2E;
#pragma unroll
            for (int e = 0; e < 6; ++e) sv[e] = __builtin_amdgcn_exp2f(__builtin_fmaf(sv[e], LOG2E, mk));
            v4u pb = {cvt_pk(sv[0], sv[1]), cvt_pk(sv[2], sv[3]), cvt_pk(sv[4], sv[5]), 0u};
            frag_cd fa[4], fb[4];
#pragma unroll
            for (int t = 0; t < 4; ++t) {
                v4u Aa = {ua[t][0].x, ua[t][0].y, cvt_pk(z[t][0], z[t][1]), 0u};
                v4u Ab = {ua[t][1].x, ua[t][1].y, cvt_pk(z[t][2], z[t][3]), 0u};
                frag_cd zero = {};
                fa[t] = MFMA(Aa, pb, zero); fb[t] = MFMA(Ab, pb, zero);
            }
            float sum = __builtin_fmaf(sv[4] + sv[5], 0.5f, (sv[0] + sv[1]) + (sv[2] + sv[3]));
            sum += dppf<HMIR>(sum); sum = tadd16(sum, sum); sum = tadd32(sum, sum);
            const float inv = __builtin_amdgcn_rcpf(sum);
            v4u o;
#pragma unroll
            for (int t = 0; t < 2; ++t) {
                float r[4];
#pragma unroll
                for (int e = 0; e < 4; ++e) { float u = phf ? fb[2 + t][e] : fa[t][e], v = phf ? fb[t][e] : fa[2 + t][e]; r[e] = (u + dppf<HMIR>(v)) * inv; }
                o[2 * t] = cvt_pk(r[0], r[1]); o[2 * t + 1] = cvt_pk(r[2], r[3]);
            }
            STB(o, rs, st2, so);
            FENCE();
            ldsadd(cV2);
            STAMP(7);
            __builtin_amdgcn_s_setprio(0);
            ucalc(s + 1);
            FENCE();
            STAMP(8); STAMP(9); STAMP(10);
            STAMPS_FLUSH();
            r3 = rs; s3 = w3; w3 = x3; x3 = y3; y3 = s3;
            { int t = sp; sp = so; so = t; }
#pragma unroll
            for (int i = 0; i < 4; ++i) o3[i] = off3[i];
        }
    }
}

__device__ __forceinline__ void load_tile(const bf16_t* W, int ld, v4u (&wf)[32], unsigned L, int w8, int l) {
    const int n = l & 15, q = l >> 4;
#pragma unroll
    for (int hh = 0; hh < 2; ++hh) {
#pragma unroll
        for (int i = 0; i < 16; ++i) { int gsw = 2 * (q | ((i & 2) << 1)); glds(W + (size_t)(hh * 512 + (w8 * 16 + i) * 4) * ld, q * ld * 2 + (n ^ gsw) * 16, L + (w8 * 16 + i) * 1024); }
        WAITV(0); BAR();
#pragma unroll
        for (int ks = 0; ks < 16; ++ks) { int r = 8 * q + (n >> 2); int gsw = 2 * ((r & 3) | (((r >> 3) & 1) << 2));
            unsigned p = L + (32 * ks + r) * 256 + ((2 * w8 + ((l >> 1) & 1)) ^ gsw) * 16 + (l & 1) * 8; wf[hh * 16 + ks] = cat2(trd(p), trd(p + 1024)); }
        WAITL(); BAR();
    }
}
__device__ __forceinline__ void seg_gemm(const v4u (&wf)[32], unsigned b0, unsigned b1, frag_cd (&acc)[2]) {
    acc[0] = frag_cd{}; acc[1] = frag_cd{};
    v4u f[8];
#define RD(ks) do { _Pragma("unroll") for (int mt = 0; mt < 2; ++mt) asm volatile("ds_read_b128 %0, %1 offset:%2" : "=v"(f[((ks) & 3) * 2 + mt]) : "v"(((ks) & 1) ? b1 : b0), "n"(((ks) >> 1) * 4096 + mt * 2048) : "memory"); } while (0)
    RD(0); RD(1); RD(2);
#pragma unroll
    for (int ks = 0; ks < 32; ++ks) {
        if (ks + 3 < 32) RD(ks + 3);
        constexpr int dummy = 0; (void)dummy;
        if (ks + 3 < 32) asm volatile("s_waitcnt lgkmcnt(6)" : "+v"(f[(ks & 3) * 2]), "+v"(f[(ks & 3) * 2 + 1]));
        else if (ks == 29) asm volatile("s_waitcnt lgkmcnt(4)" : "+v"(f[(ks & 3) * 2]), "+v"(f[(ks & 3) * 2 + 1]));
        else if (ks == 30) asm volatile("s_waitcnt lgkmcnt(2)" : "+v"(f[(ks & 3) * 2]), "+v"(f[(ks & 3) * 2 + 1]));
        else asm volatile("s_waitcnt lgkmcnt(0)" : "+v"(f[(ks & 3) * 2]), "+v"(f[(ks & 3) * 2 + 1]));
        acc[0] = MFMA(wf[ks], f[(ks & 3) * 2], acc[0]); acc[1] = MFMA(wf[ks], f[(ks & 3) * 2 + 1], acc[1]);
        SB();
    }
#undef RD
}

__device__ __forceinline__ void gwork(const Args& a, const int m, const int h, const unsigned L) {
    const int tid = threadIdx.x, w8 = RFL(tid >> 6), l = tid & 63, n = l & 15, q = l >> 4;
    unsigned* const err = a.ctl + C_ERR;
    const unsigned L_AT = L + 131072, L_KM = L_AT + 24576;
    unsigned* const fG = a.ctl + C_G + h * 32;
    if (m == 2) { if (w8 == 0) flags_wait(fG, 1, 1, l, err); BAR(); }
    v4u wf[32];
    load_tile((m == 0 ? a.Wk : (m == 1 ? a.Wv : a.Wq)) + h * DH, HD, wf, L, w8, l);
    MARK(0);
    auto xissue = [&](int s) {
#pragma unroll
        for (int i = 0; i < 8; ++i) glds(a.x + (size_t)(32 * s + 8 * (i & 3)) * DM + (2 * w8 + (i >> 2)) * 64, (l >> 3) * (DM * 2) + ((l & 7) ^ (4 * (i & 1) + q)) * 16, L + (s & 1) * 65536 + (w8 * 8 + i) * 1024);
    };
    const unsigned rb0 = L + (n >> 3) * 1024 + (n & 7) * 128 + (q ^ (n >> 1)) * 16, rb1 = rb0 ^ 64;
    const rsrc_t rq = mkrs(a.qkv, NSEG * NH * 24576), ro_ = mkrs(a.ox, NSEG * 262144);
    int sto; { int ck = 2 * w8 + (q >> 1), mt = q & 1; int i = m == 1 ? ck >> 2 : 2 * (ck >> 3) + (ck & 1), qq = m == 1 ? ck & 3 : (ck >> 1) & 3; sto = h * 24576 + m * 8192 + ((mt * 4 + i) * 64 + qq * 16 + n) * 16; }
    const unsigned* fM = a.ctl + C_M + h * 32; unsigned* const fA = a.ctl + C_A + (h >> 3) * 32 + m * 8 + (h & 7);
    const rsrc_t rc = mkrs(a.ctl, C_WORDS * 4);
    const int fD = w8 ? OOB : 4 * ((l >= 8 && l < 11 && l - 8 != m) ? C_G + h * 32 + (l - 8) : C_M + h * 32 + (l & 3));
    const int rt = w8 & 1, eq = w8 >> 1;
    const int stx = (h >> 3) * 65536 + ((h & 7) * 2 + (eq >> 1)) * 4096 + rt * 2048 + (eq & 1) * 1024 + ((2 * (q & 1) + (q >> 1)) * 16 + n) * 16;
    auto aissue = [&](int s) {
        glds1<3>(a.qkv + ((size_t)s * NH + h) * 24576 + w8 * 3072, l * 16, L_AT + w8 * 3072);
    };
    auto attend = [&](int s) {
            frag_cd sc[3] = {};
#pragma unroll
            for (int i = 0; i < 4; ++i) {
                v4u qf = *lp<v4u>(L_AT + 16384 + (rt * 4 + i) * 1024 + l * 16);
                sc[0] = MFMA(*lp<v4u>(L_AT + i * 1024 + l * 16), qf, sc[0]);
                sc[1] = MFMA(*lp<v4u>(L_AT + (4 + i) * 1024 + l * 16), qf, sc[1]);
                sc[2] = MFMA(*lp<v4u>(L_KM + (8 * (i >> 1) + 2 * q + (i & 1)) * 128 + (n & 7) * 16), qf, sc[2]);
            }
            float mx = -INFINITY;
#pragma unroll
            for (int t = 0; t < 3; ++t)
#pragma unroll
                for (int e = 0; e < 4; ++e) { float v = rbf(rbf(sc[t][e]) * SCALE); if (t < 2 && 16 * t + 4 * q + e > 16 * rt + n) v = -999292928.0f; sc[t][e] = v; mx = fmaxf(mx, v); }
            mx = tmax16(mx); mx = tmax32(mx);
            float sum = 0.f;
#pragma unroll
            for (int t = 0; t < 3; ++t)
#pragma unroll
                for (int e = 0; e < 4; ++e) { float v = __builtin_amdgcn_exp2f((sc[t][e] - mx) * LOG2E); if (t == 2 && q >= 2) v = 0.f; sc[t][e] = v; sum += v; }
            sum = tadd16(sum, sum); sum = tadd32(sum, sum);
            const float inv = 1.0f / sum;
            const v4u px = {cvt_pk(sc[0][0] * inv, sc[0][1] * inv), cvt_pk(sc[0][2] * inv, sc[0][3] * inv), cvt_pk(sc[1][0] * inv, sc[1][1] * inv), cvt_pk(sc[1][2] * inv, sc[1][3] * inv)};
            const v4u pm = {cvt_pk(sc[2][0] * inv, sc[2][1] * inv), cvt_pk(sc[2][2] * inv, sc[2][3] * inv), 0u, 0u};
            frag_cd o[2];
#pragma unroll
            for (int et = 0; et < 2; ++et) { int ck = 2 * et + ((l >> 1) & 1);
                unsigned p = L_AT + 8192 + (eq * 64 + ck * 16 + 4 * q + (n >> 2)) * 16 + (l & 1) * 8;
                v2u vmm = trd(L_KM + 2048 + (4 * eq + ck) * 128 + (4 * (q & 1) + (n >> 2)) * 16 + (l & 1) * 8);
                o[et] = MFMA(cat2(trd(p), trd(p + 4096)), px, (frag_cd{}));
                o[et] = MFMA((v4u{vmm.x, vmm.y, 0u, 0u}), pm, o[et]); }
            STB(pack_swap(o[0], o[1]), ro_, stx, s * 262144);
            MARK(65 + s / 3);
    };
    int sa = m, staged = 0, puba = 0;
    unsigned fv = 0;
    xissue(0);
    for (int sg = 0; sg < NSEG; ++sg) {
        const int done = sg < 4 ? sg : sg - 1;
        if (sg < 4) WAITV(0);
        WAITV(1);
        if (w8 == 0) { bool ok = !staged && sa < done && __builtin_amdgcn_ballot_w64(fv <= (unsigned)sa) == 0; if (l == 0) lp<unsigned>(L_KM + 4096)[sg & 1] = ok; WAITL(); }
        BAR();
        if (tid == 0) { gst(fG + m, done); if (puba) gst(fA, puba); }
        puba = 0;
        if (staged) { attend(sa); puba = sa + 1; sa += 3; staged = 0; }
        else if (RFL(ldsr(L_KM + 4096 + 4 * (sg & 1)))) {
            aissue(sa); staged = 1;
            if (w8 < 4) glds1<1>(a.kvm + ((size_t)sa * NH + h) * 4096 + w8 * 1024, l * 16, L_KM + w8 * 1024);
        }
        if (sg + 1 < NSEG) xissue(sg + 1);
        fv = __builtin_amdgcn_raw_buffer_load_b32(rc, fD, 0, 16);
        FENCE();
        frag_cd acc[2];
        seg_gemm(wf, rb0 + (sg & 1) * 65536, rb1 + (sg & 1) * 65536, acc);
        MARK(sg + 1); FENCE();
        STB(pack_swap(acc[0], acc[1]), rq, sto, sg * (NH * 24576));
    }
    WAITV(0); BAR();
    if (tid == 0) { gst(fG + m, NSEG); if (puba) gst(fA, puba); }
    for (; sa < NSEG; sa += 3) {
        if (!staged) {
            for (int i = 0; i < 2; ++i) flags_wait(fG + (m + 1 + i) % 3, 1, sa + 1, l, err);
            aissue(sa);
            if (w8 == 0) { flags_wait(fM, 4, sa + 1, l, err); glds1<4>(a.kvm + ((size_t)sa * NH + h) * 4096, l * 16, L_KM); }
            WAITV(0); BAR();
        }
        staged = 0;
        attend(sa);
        WAITV(0); BAR();
        if (tid == 0) gst(fA, sa + 1);
    }
}

__device__ __forceinline__ void owork(const Args& a, const int nt, const int kq, const unsigned L) {
    const int tid = threadIdx.x, w8 = RFL(tid >> 6), l = tid & 63, n = l & 15, q = l >> 4;
    unsigned* const err = a.ctl + C_ERR;
    if (w8 == 0) {
        unsigned char* p = a.ring + ((l >> 1) & 1) * CHB + (l & 1) * 8192 + (nt * 4 + kq) * 16384 + ((l >> 2) & 3) * 2048 + 1024;
        unsigned best = 0;
        if (l < 16) gst((unsigned*)p, ~0u);
        WAITV(0);
#pragma unroll 1
        for (int i = 0; i < 16; ++i) { u64 t1, t2; unsigned v;
            const u64 pi = ((u64)__builtin_amdgcn_readlane((unsigned)((u64)p >> 32), i) << 32) | (unsigned)__builtin_amdgcn_readlane((unsigned)(u64)p, i);
            asm volatile("s_memtime %0\n\ts_waitcnt lgkmcnt(0)\n\tglobal_load_dword %2, %3, %4 sc1\n\ts_waitcnt vmcnt(0)\n\ts_memtime %1\n\ts_waitcnt lgkmcnt(0)"
                         : "=&s"(t1), "=&s"(t2), "=&v"(v) : "v"(0u), "s"(pi) : "memory");
            if ((l & 3) == (i & 3)) best = max(best, ~(unsigned)(t2 - t1)); }
        if (l < 16) gst((unsigned*)p, ~0u);
        if (l < 4) __hip_atomic_fetch_max(a.ctl + C_CAL + l, best, __ATOMIC_RELAXED, __HIP_MEMORY_SCOPE_AGENT);
        WAITV(0);
        if (l == 0) __hip_atomic_fetch_add(a.ctl + C_READY, 1u, __ATOMIC_RELAXED, __HIP_MEMORY_SCOPE_AGENT);
        MARK(66);
        flags_wait(a.ctl + C_G + (nt * 4 + kq) * 32, 1, 1, l, err);
    }
    BAR();
    v4u wf[32];
    load_tile(a.Wo + (size_t)kq * 1024 * DM + nt * 128, DM, wf, L, w8, l);
    MARK(0);
    const unsigned* fA = a.ctl + C_A + kq * 32; unsigned* const fP = a.ctl + C_P + nt * 32;
    const rsrc_t rp = mkrs(a.part, NSEG * 524288);
    auto oissue = [&](int s) {
#pragma unroll
        for (int i = 0; i < 2; ++i) glds1<4>(a.ox + (size_t)s * 262144 + kq * 65536 + w8 * 8192 + i * 4096, l * 16, L + (s & 1) * 65536 + w8 * 8192 + i * 4096);
    };
    int idx[TOPC];
#pragma unroll
    for (int i = 0; i < TOPC; ++i) idx[i] = RFL(a.idx[i]);
    const int fcol = nt * 128 + (2 * kq + (w8 >> 1)) * 16 + 4 * q, frow = 16 * (w8 & 1) + n;
    const rsrc_t rx = mkrs(a.x, S_TOK * DM * 2), rg = mkrs(a.gate, S_TOK * 4);
    struct Fin { v4u p[4]; v2u xv; float gt; };
    auto fissue = [&](int s, bool on) { Fin f; int k = on ? 0 : 0x40000000; asm("" : "+s"(k));
#pragma unroll
        for (int i = 0; i < 4; ++i) f.p[i] = __builtin_amdgcn_raw_buffer_load_b128(rp, k + (nt * 4 + kq) * 16384 + w8 * 1024 + l * 16, s * 524288 + i * 4096, 16);
        f.xv = __builtin_amdgcn_raw_buffer_load_b64(rx, k + (frow * DM + fcol) * 2, s * 65536, 0);
        f.gt = __uint_as_float(__builtin_amdgcn_raw_buffer_load_b32(rg, k + frow * 4, s * 128, 0));
        return f;
    };
    auto finish = [&](int s, const Fin& f) {
        const v4u (&p)[4] = f.p; const v2u xv = f.xv; const float gt = f.gt;
        const int tok = 32 * s + frow; const float omg = rbf(1.0f - gt);
        float b[4] = {lo16(xv.x), hi16(xv.x), lo16(xv.y), hi16(xv.y)};
        int slot = -1;
#pragma unroll
        for (int i = 0; i < TOPC; ++i) if (idx[i] == tok) slot = i;
        if (slot >= 0) { frag_cd y = *(const frag_cd*)(a.ysel + slot * DM + fcol);
#pragma unroll
            for (int e = 0; e < 4; ++e) b[e] = rbf(b[e] + y[e]); }
        float o[4];
#pragma unroll
        for (int e = 0; e < 4; ++e) { float av = rbf((__uint_as_float(p[0][e]) + __uint_as_float(p[1][e])) + (__uint_as_float(p[2][e]) + __uint_as_float(p[3][e]))); o[e] = rbf(gt * av) + rbf(omg * b[e]); }
        *(v2u*)(a.out + (size_t)tok * DM + fcol) = v2u{cvt_pk(o[0], o[1]), cvt_pk(o[2], o[3])};
    };
    const int stp = ((nt * 4 + (w8 >> 1)) * 4 + kq) * 4096 + (w8 & 1) * 2048 + l * 16;
    const unsigned* fW = l < 32 ? fA + min(l, 23) : fP + (l & 3) * 8 + 2 * kq + ((w8 >> 1) & 1);
    unsigned* const fO = fP + kq * 8 + w8;
    unsigned fv = 0;
    auto a_ok = [&](int s) { return ((__builtin_amdgcn_ballot_w64(fv <= (unsigned)s) >> ((s % 3) * 8)) & 255) == 0; };
    auto p_ok = [&](int s) { return (__builtin_amdgcn_ballot_w64(fv <= (unsigned)s) >> 32) == 0; };
    int pre = 0, fin = w8 < 4 ? 0 : NSEG;
    for (int s = 0; s <= NSEG; ++s) {
        if (pre == s) {
            WAITV(0);
            if (s && l == 0) gst(fO, s);
            for (int i = 0;; ++i) {
                fv = gld(fW); WAITV(0);
                if (s < NSEG ? a_ok(s) : fin == NSEG) break;
                if (fin < s && p_ok(fin)) { FENCE(); finish(fin, fissue(fin, true)); ++fin; }
                else { __builtin_amdgcn_s_sleep(2); if (i > GCAP) fatal(err); }
            }
            if (s == NSEG) break;
            oissue(s); ++pre;
            WAITV(0);
        }
        BAR();
        if (s + 1 < NSEG && a_ok(s + 1)) { oissue(s + 1); ++pre; }
        const bool fi = fin < s && p_ok(fin);
        const Fin f = fissue(fin & 63, fi);
        fv = gld(fW);
        FENCE();
        frag_cd acc[2];
        seg_gemm(wf, L + (s & 1) * 65536 + l * 16, L + (s & 1) * 65536 + 1024 + l * 16, acc);
        WAITV(0);
        if (fi) { finish(fin, f); ++fin; }
        if (s && l == 0) gst(fO, s);
        STB(__builtin_bit_cast(v4u, acc[0]), rp, stp, s * 524288); STB(__builtin_bit_cast(v4u, acc[1]), rp, stp + 1024, s * 524288);
        MARK(1 + s);
    }
    MARK(65);
}

__global__ void __launch_bounds__(512, 2) k_mega(Args a) {
    __shared__ __attribute__((aligned(16))) unsigned char smem[163840];
    const unsigned L = (unsigned)(size_t)(LAS unsigned char*)smem;
    const int b = blockIdx.x, u = (b & 3) + 4 * (b >> 3);
    MARK(255);
    if (!(b & 4)) chain(a, u, L);
    else if (u < 96) gwork(a, (u >> 2) % 3, (u & 3) * 8 + (u >> 2) / 3, L);
    else owork(a, (u >> 2) - 24, u & 3, L);
}

__device__ __forceinline__ float bf2f(bf16_t v) { return __uint_as_float(((unsigned)v) << 16); }
__global__ void k_prep(const bf16_t* __restrict__ x, const bf16_t* __restrict__ gate_w, const bf16_t* __restrict__ w_r, float* __restrict__ gate, float* __restrict__ r) {
    int t = blockIdx.x; __shared__ float s1[256], s2[256];
    float a = 0, b = 0;
    for (int k = threadIdx.x; k < DM; k += 256) { float xv = bf2f(x[(size_t)t * DM + k]); a += rbf(xv * bf2f(gate_w[k])); b += xv * bf2f(w_r[k]); }
    s1[threadIdx.x] = a; s2[threadIdx.x] = b; __syncthreads();
    for (int o = 128; o > 0; o >>= 1) { if (threadIdx.x < o) { s1[threadIdx.x] += s1[threadIdx.x + o]; s2[threadIdx.x] += s2[threadIdx.x + o]; } __syncthreads(); }
    if (threadIdx.x == 0) { float z = rbf(s1[0]); gate[t] = rbf(1.0f / (1.0f + __expf(-z))); r[t] = rbf(s2[0]); }
}
__global__ void k_topk(const float* __restrict__ r, int* __restrict__ idx) {
    __shared__ float sv[256]; __shared__ int si[256]; __shared__ int chosen[TOPC];
    for (int c = 0; c < TOPC; ++c) {
        float bv = -INFINITY; int bi = 0x7fffffff;
        for (int t = threadIdx.x; t < S_TOK; t += 256) { bool used = false; for (int q = 0; q < c; ++q) used |= chosen[q] == t;
            float v = r[t]; if (!used && (v > bv || (v == bv && t < bi))) { bv = v; bi = t; } }
        sv[threadIdx.x] = bv; si[threadIdx.x] = bi; __syncthreads();
        for (int o = 128; o > 0; o >>= 1) { if (threadIdx.x < o) { float v2 = sv[threadIdx.x + o]; int i2 = si[threadIdx.x + o];
                if (v2 > sv[threadIdx.x] || (v2 == sv[threadIdx.x] && i2 < si[threadIdx.x])) { sv[threadIdx.x] = v2; si[threadIdx.x] = i2; } } __syncthreads(); }
        if (threadIdx.x == 0) { chosen[c] = si[0]; idx[c] = si[0]; }
        __syncthreads();
    }
}
__global__ void __launch_bounds__(256) k_moe1(const bf16_t* __restrict__ x, const int* __restrict__ idx, const bf16_t* __restrict__ wg, const bf16_t* __restrict__ wu, bf16_t* __restrict__ hbuf) {
    __shared__ float xs[TOPC][DM]; __shared__ float red[2][8][TOPC][32];
    for (int i = threadIdx.x; i < TOPC * DM; i += 256) xs[i / DM][i % DM] = bf2f(x[(size_t)idx[i / DM] * DM + i % DM]);
    __syncthreads();
    int col = threadIdx.x & 31, kp = threadIdx.x >> 5, f = blockIdx.x * 32 + col;
    float ag[TOPC] = {}, au[TOPC] = {};
    for (int k = kp * 128; k < kp * 128 + 128; ++k) { float g = bf2f(wg[(size_t)k * FF + f]), u = bf2f(wu[(size_t)k * FF + f]);
#pragma unroll
        for (int c = 0; c < TOPC; ++c) { ag[c] += xs[c][k] * g; au[c] += xs[c][k] * u; } }
#pragma unroll
    for (int c = 0; c < TOPC; ++c) { red[0][kp][c][col] = ag[c]; red[1][kp][c][col] = au[c]; }
    __syncthreads();
    { int c = threadIdx.x >> 5; float g = 0, u = 0;
#pragma unroll
      for (int q = 0; q < 8; ++q) { g += red[0][q][c][col]; u += red[1][q][c][col]; }
      float gv = rbf(g); float sl = rbf(gv / (1.0f + __expf(-gv))); hbuf[c * FF + f] = (bf16_t)cvt_pk(sl * rbf(u), 0.f); }
}
__global__ void __launch_bounds__(256) k_moe2(const bf16_t* __restrict__ hbuf, const int* __restrict__ idx, const float* __restrict__ r, const bf16_t* __restrict__ wd, float* __restrict__ ysel) {
    __shared__ float hs[TOPC][FF]; __shared__ float red[8][TOPC][32];
    for (int i = threadIdx.x; i < TOPC * FF; i += 256) hs[i / FF][i % FF] = bf2f(hbuf[i]);
    __syncthreads();
    int col = threadIdx.x & 31, kp = threadIdx.x >> 5, n = blockIdx.x * 32 + col;
    float acc[TOPC] = {};
    for (int k = kp * 448; k < kp * 448 + 448; ++k) { float w = bf2f(wd[(size_t)k * DM + n]);
#pragma unroll
        for (int c = 0; c < TOPC; ++c) acc[c] += hs[c][k] * w; }
#pragma unroll
    for (int c = 0; c < TOPC; ++c) red[kp][c][col] = acc[c];
    __syncthreads();
    { int c = threadIdx.x >> 5; float y = 0;
#pragma unroll
      for (int q = 0; q < 8; ++q) y += red[q][c][col];
      float rs = rbf(1.0f / (1.0f + __expf(-r[idx[c]]))); ysel[c * DM + n] = rbf(rbf(y) * rs); }
}

static constexpr int g_force = 0; static constexpr int g_d1 = 1000, g_d2 = 2150, g_per = 2750, g_dinit = 250; static constexpr unsigned* g_dbg = nullptr;
extern "C" void kernel_launch(void* const* d_in, const int* in_sizes, int n_in, void* d_out, int out_size, void* d_ws, size_t ws_size, hipStream_t stream) {
    const bf16_t* x = (const bf16_t*)d_in[0];
    const bf16_t* w_r = (const bf16_t*)d_in[8]; const bf16_t* wg = (const bf16_t*)d_in[9]; const bf16_t* wu = (const bf16_t*)d_in[10]; const bf16_t* wd = (const bf16_t*)d_in[11];
    const bf16_t* gate_w = (const bf16_t*)d_in[12];
    unsigned char* ws = (unsigned char*)d_ws; size_t off = 0;
    auto alloc = [&](size_t bytes) { unsigned char* p = ws + off; off += (bytes + 255) & ~(size_t)255; return p; };
    unsigned* ctl = (unsigned*)alloc(C_WORDS * 4);
    float* gate = (float*)alloc(S_TOK * 4); float* r = (float*)alloc(S_TOK * 4); int* idx = (int*)alloc(64 * 4);
    bf16_t* hbuf = (bf16_t*)alloc(TOPC * FF * 2); float* ysel = (float*)alloc(TOPC * DM * 4);
    off += (size_t)(-(uintptr_t)(ws + off)) & ((2u << 20) - 1);
    unsigned char* ring = alloc(RING_BYTES);
    unsigned char* qkv = alloc((size_t)NSEG * NH * 24576); unsigned char* kvm = alloc((size_t)NSEG * NH * 4096);
    unsigned char* ox = alloc((size_t)NSEG * 262144); unsigned char* part = alloc((size_t)NSEG * 524288);
    hipMemsetAsync(ctl, 0, C_WORDS * 4, stream);
    k_prep<<<S_TOK, 256, 0, stream>>>(x, gate_w, w_r, gate, r);
    k_topk<<<1, 256, 0, stream>>>(r, idx);
    k_moe1<<<FF / 32, 256, 0, stream>>>(x, idx, wg, wu, hbuf);
    k_moe2<<<DM / 32, 256, 0, stream>>>(hbuf, idx, r, wd, ysel);
    Args a{x, (const bf16_t*)d_in[1], (const bf16_t*)d_in[2], (const bf16_t*)d_in[3], (const bf16_t*)d_in[4], (const bf16_t*)d_in[5],
           (bf16_t*)d_out, ctl, ring, qkv, kvm, ox, part, gate, ysel, idx, g_dbg, g_d1 * 16 / 10, g_d2 * 16 / 10, g_per * 16 / 10, g_dinit * 16 / 10 + (g_force << 16)};
    k_mega<<<256, 512, 0, stream>>>(a);
}
```

```cpp
#include <hip/hip_runtime.h>
#include <stdint.h>
#include <type_traits>

typedef unsigned short bf16_t;
typedef unsigned long long u64;
typedef float frag_cd __attribute__((ext_vector_type(4)));
typedef unsigned v4u __attribute__((ext_vector_type(4)));
typedef unsigned v8u __attribute__((ext_vector_type(8)));
typedef unsigned v2u __attribute__((ext_vector_type(2)));
typedef float v2f __attribute__((ext_vector_type(2)));
typedef __bf16 bf16x2_t __attribute__((ext_vector_type(2)));
typedef __bf16 v8bf __attribute__((ext_vector_type(8)));
typedef __bf16 v16bf __attribute__((ext_vector_type(16)));
typedef short v4s_t __attribute__((ext_vector_type(4)));
typedef __amdgpu_buffer_rsrc_t rsrc_t;
#define LAS __attribute__((address_space(3)))
#define GAS __attribute__((address_space(1)))

static constexpr int S_TOK = 2048, DM = 1024, NH = 32, DH = 128, MM = 8, SEG = 32, NSEG = 64, HD = NH * DH, FF = 3584, TOPC = 8;
static constexpr int C_ERR = 0, C_READY = 32, C_CAL = 64  , C_G = 128  , C_M = C_G + 1024  , C_A = C_M + 1024  ,
                     C_P = C_A + 128  , C_R = C_P + 256, C_Y = C_R + 32, C_WORDS = C_Y + 32;
static constexpr int RING_BYTES = 4 << 20, SLOT = 1 << 20, CHB = 2 << 20, L_H3 = 192 << 10, L_TS = 256 << 10, SLOT3 = 192 << 10;
static constexpr unsigned SPIN_CAP = 1u << 18;
static constexpr int OOB = 0x7ffffff0, LCAP = 1 << 22, GCAP = 1 << 20;
static constexpr float SCALE = 0.08838834764831845f, LOG2E = 1.4426950408889634f;

struct Args {
    const bf16_t* x; const bf16_t* mem0; const bf16_t* Wq; const bf16_t* Wk; const bf16_t* Wv; const bf16_t* Wo;
    bf16_t* out; unsigned* ctl; unsigned char* ring; unsigned char* qkv; unsigned char* kvm; unsigned char* ox; unsigned char* part;
    const bf16_t* wr; const bf16_t* wg; const bf16_t* wu; const bf16_t* wd; const bf16_t* gw;
    unsigned* gate  ; unsigned char* ypart; unsigned* dbg;
    int d1, d2, per, dinit;
};

__device__ __forceinline__ unsigned cvt_pk(float lo, float hi) { v2f v = {lo, hi}; return __builtin_bit_cast(unsigned, __builtin_convertvector(v, bf16x2_t)); }
__device__ __forceinline__ float lo16(unsigned p) { return __uint_as_float(p << 16); }
__device__ __forceinline__ float hi16(unsigned p) { return __uint_as_float(p & 0xffff0000u); }
__device__ __forceinline__ float rbf(float f) { return lo16(cvt_pk(f, 0.f)); }
template <int C> __device__ __forceinline__ unsigned dppu(unsigned v) { return (unsigned)__builtin_amdgcn_update_dpp(0, (int)v, C, 0xf, 0xf, true); }
template <int C> __device__ __forceinline__ float dppf(float v) { return __uint_as_float(dppu<C>(__float_as_uint(v))); }
#define ROR8 0x128
#define HMIR 0x141
__device__ __forceinline__ v2u sw16(unsigned a, unsigned b) { auto r = __builtin_amdgcn_permlane16_swap(a, b, false, false); return v2u{r[0], r[1]}; }
__device__ __forceinline__ v2u sw32(unsigned a, unsigned b) { auto r = __builtin_amdgcn_permlane32_swap(a, b, false, false); return v2u{r[0], r[1]}; }
__device__ __forceinline__ float tadd16(float a, float b) { v2u r = sw16(__float_as_uint(a), __float_as_uint(b)); return __uint_as_float(r.x) + __uint_as_float(r.y); }
__device__ __forceinline__ float tadd32(float a, float b) { v2u r = sw32(__float_as_uint(a), __float_as_uint(b)); return __uint_as_float(r.x) + __uint_as_float(r.y); }
__device__ __forceinline__ float tmax16(float a) { v2u r = sw16(__float_as_uint(a), __float_as_uint(a)); return fmaxf(__uint_as_float(r.x), __uint_as_float(r.y)); }
__device__ __forceinline__ float tmax32(float a) { v2u r = sw32(__float_as_uint(a), __float_as_uint(a)); return fmaxf(__uint_as_float(r.x), __uint_as_float(r.y)); }
__device__ __forceinline__ unsigned umax3(unsigned a, unsigned b, unsigned c) { return max(max(a, b), c); }
__device__ __forceinline__ v4u pack_swap(frag_cd a, frag_cd b) { v2u x0 = sw16(cvt_pk(a[0], a[1]), cvt_pk(b[0], b[1])), x1 = sw16(cvt_pk(a[2], a[3]), cvt_pk(b[2], b[3])); return v4u{x0.x, x1.x, x0.y, x1.y}; }
#define MFMA(a, b, c) __builtin_amdgcn_mfma_f32_16x16x32_bf16(__builtin_bit_cast(v8bf, a), __builtin_bit_cast(v8bf, b), c, 0, 0, 0)
#define SMF(a, b, c, ix) __builtin_amdgcn_smfmac_f32_16x16x64_bf16(__builtin_bit_cast(v8bf, a), __builtin_bit_cast(v16bf, b), c, ix, 0, 0)
#define FENCE() asm volatile("" ::: "memory")
#define SB() __builtin_amdgcn_sched_barrier(0)
#define RFL(x) __builtin_amdgcn_readfirstlane(x)
#define BAR() do { FENCE(); __builtin_amdgcn_s_barrier(); FENCE(); } while (0)
#define WAITV(n) do { FENCE(); __builtin_amdgcn_s_waitcnt(0x0F70 | (n)); FENCE(); } while (0)
#define WAITL() asm volatile("s_waitcnt lgkmcnt(0)" ::: "memory")
#define STB(v, rs, off, soff) __builtin_amdgcn_raw_buffer_store_b128(v, rs, off, soff, 16)
template <class T> __device__ __forceinline__ LAS T* lp(unsigned a) { return (LAS T*)(size_t)a; }
__device__ __forceinline__ void glds(const void* g, unsigned voff, unsigned lds) { asm volatile("s_mov_b32 m0, %2\n\ts_nop 0\n\tglobal_load_lds_dwordx4 %0, %1" :: "v"(voff), "s"(g), "s"(lds) : "memory"); }
template <int N> __device__ __forceinline__ void glds1(const void* g, unsigned voff, unsigned lds) {
#define GL(o) "\n\tglobal_load_lds_dwordx4 %0, %1 offset:" #o " sc1"
    if constexpr (N == 1) asm volatile("s_mov_b32 m0, %2\n\ts_nop 0" GL(0) :: "v"(voff), "s"(g), "s"(lds) : "memory");
    if constexpr (N == 3) asm volatile("s_mov_b32 m0, %2\n\ts_nop 0" GL(0) GL(1024) GL(2048) :: "v"(voff), "s"(g), "s"(lds) : "memory");
    if constexpr (N == 4) asm volatile("s_mov_b32 m0, %2\n\ts_nop 0" GL(0) GL(1024) GL(2048) GL(3072) :: "v"(voff), "s"(g), "s"(lds) : "memory");
}
__device__ __forceinline__ rsrc_t mkrs(const void* p, int bytes) { return __builtin_amdgcn_make_buffer_rsrc((void*)p, 0, bytes, 0x00020000); }
__device__ __forceinline__ unsigned gld(const unsigned* p) { return __hip_atomic_load(p, __ATOMIC_RELAXED, __HIP_MEMORY_SCOPE_AGENT); }
__device__ __forceinline__ void gst(unsigned* p, unsigned v) { __hip_atomic_store(p, v, __ATOMIC_RELAXED, __HIP_MEMORY_SCOPE_AGENT); }
__device__ __forceinline__ unsigned rt16() { return (unsigned)__builtin_amdgcn_s_memrealtime() << 4; }

#define STAMP(slot) do {} while (0)
#define STAMPS_FLUSH() do {} while (0)
#define MARK(i) do {} while (0)
#define CNT(i) do {} while (0)
#define CNT_FLUSH() do {} while (0)

#define LATE_THR 100
#define LATE_ADD 480
#define SLIP_THR 320
#define SLIP_ADD 640
#define SLIP_INC 32
__device__ __forceinline__ void wait_t(unsigned T) {
    int rem, cap = 4096;
    asm volatile("1:\n\ts_memrealtime vcc\n\ts_sub_u32 %1, %1, 1\n\ts_cbranch_scc1 3f\n\ts_waitcnt lgkmcnt(0)\n\ts_lshl_b32 %0, vcc_lo, 4\n\ts_sub_i32 %0, %2, %0\n\ts_cmp_ge_i32 %0, 64\n\ts_cbranch_scc1 1b\n\t"
                 "s_cmp_gt_i32 %0, 12\n\ts_cbranch_scc0 3f\n2:\n\ts_sub_i32 %0, %0, 13\n\ts_cmp_gt_i32 %0, 12\n\ts_cbranch_scc1 2b\n3:\n\ts_waitcnt lgkmcnt(0)"
                 : "=&s"(rem), "+s"(cap) : "s"(T) : "scc", "vcc", "memory");
}
__device__ __forceinline__ void sched(unsigned T) { unsigned now = rt16(); if ((int)(T - now) < LATE_THR) T = now + LATE_ADD; wait_t(T); }
template <int N>
__device__ __forceinline__ bool bad(const v4u (&v)[N]) {
    unsigned m = umax3(v[0].x, v[0].y, v[0].z); m = max(m, v[0].w);
#pragma unroll
    for (int i = 1; i < N; ++i) { m = umax3(m, v[i].x, v[i].y); m = umax3(m, v[i].z, v[i].w); }
    return __builtin_amdgcn_ballot_w64(m == 0xFFFFFFFFu) != 0;
}
template <int N>
__device__ __forceinline__ void ldn(rsrc_t rs, const int (&off)[N], int soff, v4u (&v)[N]) {
#pragma unroll
    for (int i = 0; i < N; ++i) v[i] = __builtin_amdgcn_raw_buffer_load_b128(rs, off[i], soff, 16);
}
__device__ __forceinline__ void endpgm() { asm volatile("s_endpgm" ::: "memory"); }
__device__ __forceinline__ void fatal(unsigned* err) { asm volatile("global_store_dword %0, %1, off sc1\n\ts_waitcnt vmcnt(0)\n\ts_endpgm" :: "v"(err), "v"(1u) : "memory"); }
template <int N>
__device__ __forceinline__ void retry(rsrc_t rs, const int (&off)[N], int soff, v4u (&v)[N], unsigned* err, unsigned& budget) {
    if (--budget == 0) fatal(err);
#define RL(d, o) "buffer_load_dwordx4 %" #d ", %" #o ", %[r], %[s] offen sc1\n\t"
#define RH "s_waitcnt vmcnt(0)\n\ts_sleep 2\n\t"
#define RT [r] "s"(rs), [s] "s"(soff) : "memory"
    if constexpr (N == 2) asm volatile(RH RL(0, 2) RL(1, 3) "s_waitcnt vmcnt(0)" : "=&v"(v[0]), "=&v"(v[1]) : "v"(off[0]), "v"(off[1]), RT);
    if constexpr (N == 4) asm volatile(RH RL(0, 4) RL(1, 5) RL(2, 6) RL(3, 7) "s_waitcnt vmcnt(0)" : "=&v"(v[0]), "=&v"(v[1]), "=&v"(v[2]), "=&v"(v[3]) : "v"(off[0]), "v"(off[1]), "v"(off[2]), "v"(off[3]), RT);
    if constexpr (N == 5) asm volatile(RH RL(0, 5) RL(1, 6) RL(2, 7) RL(3, 8) RL(4, 9) "s_waitcnt vmcnt(0)" : "=&v"(v[0]), "=&v"(v[1]), "=&v"(v[2]), "=&v"(v[3]), "=&v"(v[4]) : "v"(off[0]), "v"(off[1]), "v"(off[2]), "v"(off[3]), "v"(off[4]), RT);
}
__device__ __forceinline__ unsigned ldsr(unsigned p) { return __hip_atomic_load(lp<unsigned>(p), __ATOMIC_RELAXED, __HIP_MEMORY_SCOPE_WORKGROUP); }
__device__ __forceinline__ void ldsadd(unsigned p) { asm volatile("s_mov_b64 exec, 1\n\tds_add_u32 %0, %1\n\ts_mov_b64 exec, -1" :: "v"(p), "v"(1u) : "memory"); }
__device__ __forceinline__ v2u trd(unsigned p) { return __builtin_bit_cast(v2u, __builtin_amdgcn_ds_read_tr16_b64_v4i16(lp<v4s_t>(p))); }
__device__ __forceinline__ v8u cat(v2u a, v2u b, v2u c, v2u d) { return v8u{a.x, a.y, b.x, b.y, c.x, c.y, d.x, d.y}; }
__device__ __forceinline__ v4u cat2(v2u a, v2u b) { return v4u{a.x, a.y, b.x, b.y}; }
__device__ __forceinline__ unsigned poll_red(unsigned aC, unsigned aR, unsigned need) {
    unsigned cv; int cap = LCAP; v2f r[4];
    asm volatile("1:\n\tds_read_b32 %0, %6\n\tds_read_b64 %1, %7\n\tds_read_b64 %2, %7 offset:3072\n\tds_read_b64 %3, %7 offset:6144\n\tds_read_b64 %4, %7 offset:9216\n\t"
                 "s_sub_u32 %5, %5, 1\n\ts_cbranch_scc0 2f\n\ts_endpgm\n2:\n\ts_waitcnt lgkmcnt(4)\n\tv_cmp_gt_u32 vcc, %8, %0\n\ts_cbranch_vccnz 1b\n\ts_waitcnt lgkmcnt(0)"
                 : "=&v"(cv), "=&v"(r[0]), "=&v"(r[1]), "=&v"(r[2]), "=&v"(r[3]), "+s"(cap) : "v"(aC), "v"(aR), "s"(need) : "vcc", "scc", "memory");
    return min(cvt_pk((r[0].x + r[1].x) + (r[2].x + r[3].x), (r[0].y + r[1].y) + (r[2].y + r[3].y)), 0xFFFFFFFEu);
}
__device__ __forceinline__ v4u quad(unsigned p0) { return v4u{p0, dppu<0x55>(p0), dppu<0xAA>(p0), dppu<0xFF>(p0)}; }
__device__ __forceinline__ void poll_cnt(unsigned p, unsigned need) { for (int i = 0; ldsr(p) < need; ++i) { if (i > LCAP) endpgm(); } FENCE(); }
__device__ __forceinline__ bool flags_ok(const unsigned* p, int nw, unsigned need, int l) { unsigned v = gld(p + min(l, nw - 1)); bool r = __builtin_amdgcn_ballot_w64(v < need) == 0; FENCE(); return r; }
__device__ __forceinline__ void flags_wait(const unsigned* p, int nw, unsigned need, int l, unsigned* err) {
    for (int i = 0; !flags_ok(p, nw, need, l); ++i) { __builtin_amdgcn_s_sleep(4); if (i > GCAP) fatal(err); }
}

__device__ __forceinline__ void chain(const Args& a, const int c, const unsigned L) {
    const int tid = threadIdx.x, w8 = RFL(tid >> 6), w = w8 & 3, l = tid & 63;
    const int g = c & 3, aa = c >> 2, h = g * 8 + (aa >> 2), jj = aa & 3;
    const int n = l & 15, q = l >> 4, row = n & 7, hf = n >> 3;
    const unsigned L_RED = L + 131072, L_SC = L_RED + 12288, L_SM = L_SC + 2048, L_CNT = L_SM + 512, L_VX = L, L_ST = L + 32768;
    const unsigned cB1 = L_CNT, cB2 = L_CNT + 128, cBv = L_CNT + 256, cV2 = L_CNT + 384;
    const rsrc_t rs = mkrs(a.ring, RING_BYTES);
    unsigned* const err = a.ctl + C_ERR;
    const v4u ones = {~0u, ~0u, ~0u, ~0u};
    if (tid < 128) lp<unsigned>(L_CNT)[tid] = 0;
#pragma unroll
    for (int i = 0; i < 4; ++i) STB(ones, rs, c * 32768 + i * 8192 + tid * 16, 0);
    const unsigned stg = L + w8 * 16384;
    const int prr = 16 * (q >> 1) + 4 * (q & 1) + ((n >> 2) & 1) + 8 * (n >> 3);
    auto stage_w = [&](const bf16_t* W) {
        W += (size_t)256 * w * HD + h * DH + 32 * jj;
#pragma unroll
        for (int ch = 0; ch < 16; ++ch) glds(W + (size_t)16 * ch * HD, (l >> 2) * (HD * 2) + (l & 3) * 16, stg + ch * 1024);
        WAITV(0);
    };
    auto read_w = [&](v8u (&d)[2][4]) {
#pragma unroll
        for (int ct = 0; ct < 2; ++ct)
#pragma unroll
            for (int i = 0; i < 4; ++i) { unsigned p = stg + (64 * i + prr) * 64 + ct * 32 + (l & 3) * 8; d[ct][i] = cat(trd(p), trd(p + 128), trd(p + 2048), trd(p + 2048 + 128)); }
        WAITL();
    };
    const int six = hf ? 0xEEEE : 0x4444;
    const rsrc_t rsm = mkrs(a.mem0, MM * DM * 2);
    const int pos = l >> 3, prr8 = (pos >> 1) + 4 * (pos & 1);
    unsigned cal;
    u64 cnts[4] = {}; (void)cnts;
    auto phA = [&](int lg) { return ((lg >> 13) << 14) + (int)((cal & 1) << 13) + (lg & 8191); };
    auto phB = [&](int lg) { return CHB + ((lg >> 13) << 14) + (int)((cal & 2) << 12) + (lg & 8191); };
    auto h1o = [&](int inner) { return phB(h * 6144 + inner); };
    auto arrive = [&]() {
        if (tid == 192) __hip_atomic_fetch_add(a.ctl + C_READY, 1u, __ATOMIC_RELAXED, __HIP_MEMORY_SCOPE_AGENT);
    };
    auto start = [&]() {
        if (w8 == 3) {
            flags_wait(a.ctl + C_READY, 1, 128 + 32, l, err);
            unsigned v = gld(a.ctl + C_CAL + (l & 3));
            unsigned t = (__builtin_amdgcn_readlane(v, 1) < __builtin_amdgcn_readlane(v, 0) ? 1u : 0u) | (__builtin_amdgcn_readlane(v, 3) < __builtin_amdgcn_readlane(v, 2) ? 2u : 0u);
            lp<unsigned>(L_CNT + 448)[0] = t; WAITL();
        }
        BAR();
        cal = RFL(ldsr(L_CNT + 448));
    };

    if (w8 < 4) {
        v8u wA[2][2][4];
        stage_w(a.Wq); BAR(); arrive(); read_w(wA[0]);
        stage_w(a.Wk); read_w(wA[1]);
        MARK(2);
        BAR();
        const unsigned aRedR = L_RED + (w * 128 + 2 * l) * 4, redW = L_RED + ((w * 48 + q) * 16 + n) * 4;
        const unsigned scW = w < 2 ? L_SC + (hf ? 256 + l * 4 : ((w * 4 + q) * 8 + row) * 4) * 4 : ((q >> 1) == hf ? L_SM + ((hf * 8 + row) * 8 + (q & 1) * 4) * 4 : L_SC + (256 + l * 4) * 4);
        auto run = [&](auto role_) {
        constexpr int ROLE = decltype(role_)::value;
        v4u kx[4]; unsigned fl = 0;
        const unsigned char* xp = a.qkv + h * 24576 + (w & 1) * 4096;
        const unsigned* fG = a.ctl + C_G + h * 32;
        auto xload = [&](int sk, int sv) {
            if (__builtin_amdgcn_ballot_w64(fl <= (unsigned)sv) != 0) flags_wait(fG, 2, sv + 1, l, err);
            glds1<4>(xp + (size_t)sv * (NH * 24576) + 8192, l * 16, L_VX + (sv & 1) * 8192 + w * 4096);
            asm volatile("global_load_dwordx4 %0, %5, %6 sc1\n\tglobal_load_dwordx4 %1, %5, %6 offset:1024 sc1\n\tglobal_load_dwordx4 %2, %5, %6 offset:2048 sc1\n\tglobal_load_dwordx4 %3, %5, %6 offset:3072 sc1\n\t"
                         "global_load_dword %4, %7, %8 sc1\n\ts_waitcnt vmcnt(0)"
                         : "=&v"(kx[0]), "=&v"(kx[1]), "=&v"(kx[2]), "=&v"(kx[3]), "=&v"(fl) : "v"(l * 16), "s"(xp + (size_t)sk * (NH * 24576)), "v"((l & 1) * 4), "s"(fG) : "memory");
        };
        if (ROLE == 0) { xload(0, 0); xload(0, 1); }
        start();
        MARK(0);
        int o1[ROLE == 2 ? 4 : 2], ld2[4], st3, off3[ROLE == 3 ? 5 : 4], o3[ROLE == 3 ? 5 : 4];
        int st1 = h1o((w >> 1) * 2048 + (4 * jj + 2 * (w & 1) + ((l >> 2) & 1)) * 128 + prr8 * 16); if (l & 3) st1 = OOB;
#pragma unroll
        for (int i = 0; i < 2; ++i) { if constexpr (ROLE == 2) { o1[2 * i] = h1o((8 * i + 2 * q + hf) * 128 + row * 16); o1[2 * i + 1] = h1o(2048 + (8 * i + 2 * q + hf) * 128 + row * 16); } else o1[i] = h1o((8 * i + 2 * q + hf) * 128 + row * 16); }
#pragma unroll
        for (int i = 0; i < 4; ++i) { ld2[i] = phA(c * 4096 + i * 1024 + l * 16); off3[i] = phB(L_H3 + g * 16384 + (32 * w + 8 * i + 2 * q + hf) * 128 + row * 16); o3[i] = (row * DM + 256 * w + 64 * i + 16 * q + 8 * hf) * 2; }
        if constexpr (ROLE == 3) { off3[4] = phB(L_TS + g * 128); o3[4] = OOB; }
        st3 = l < 32 ? phB(L_H3 + (l >> 3) * 16384 + c * 128 + row * 16) : (c == 0 && l < 36 ? phB(L_TS + (l & 3) * 128) : OOB);
        const int in1a = h1o((l >> 5) * 2048 + jj * 512 + (l & 31) * 16), in1b = l < 32 ? h1o(4096 + jj * 512 + l * 16) : OOB;
        auto kvcopy = [&](int s) {
            const rsrc_t rk = mkrs(a.kvm, NSEG * NH * 4096);
            const int o = (l & 3) ? OOB : h * 4096 + (4 * jj + ((l >> 2) & 1)) * 128 + prr8 * 16;
#pragma unroll
            for (int t = 0; t < 4; ++t) STB(quad(poll_red(t < 2 ? cB1 : cBv, L_RED + ((2 + t) * 128 + 2 * l) * 4, 4u * (s + 1))), rk, o, s * (NH * 4096) + (t >> 1) * 2048 + (t & 1) * 256);
            WAITV(0);
            if (l == 0) gst(a.ctl + C_M + h * 32 + jj, s + 1);
        };
        v4u sel;
#pragma unroll
        for (int i = 0; i < 4; ++i) sel[i] = (row >> 1) == i ? 0x3F80u << (16 * (row & 1)) : 0u;
        __builtin_amdgcn_s_setprio(3);
        int so = 0, sp = SLOT;
        unsigned budget = SPIN_CAP, tb = rt16() - (1u << 20), add = 0;
        rsrc_t r3 = rsm; int s3 = 0, w3 = 0, x3 = SLOT3, y3 = 2 * SLOT3;
        for (int s = 0;; ++s) {
            u64 stv[11] = {};
            unsigned n4 = 4u * (s + 1);
            asm volatile("" : "+s"(n4));
            STAMP(0);
            constexpr int N3 = ROLE == 3 ? 5 : 4;
            v4u mb[N3];
            if (ROLE == 3 && s) sched(tb);
            BAR();
            STAMP(1);
            ldn<N3>(r3, o3, s3, mb);
            FENCE();
            frag_cd acc[4];
            for (;;) {
#pragma unroll
                for (int m = 0; m < 4; ++m) { acc[m] = frag_cd{}; asm volatile("" : "+v"(acc[m])); }
                SB();
                unsigned mxs = 0;
#pragma unroll
                for (int i = 0; i < 4; ++i) {
                    acc[0] = SMF(mb[i], wA[0][0][i], acc[0], six); mxs = umax3(mxs, mb[i].x, mb[i].y); SB();
                    acc[1] = SMF(mb[i], wA[0][1][i], acc[1], six); mxs = umax3(mxs, mb[i].z, mb[i].w); asm volatile("" : "+v"(mxs)); SB();
                    acc[2] = SMF(mb[i], wA[1][0][i], acc[2], six); if (ROLE == 3 && i == 3) mxs = umax3(mxs, mb[N3 - 1].x, mb[N3 - 1].y); SB();
                    acc[3] = SMF(mb[i], wA[1][1][i], acc[3], six); SB(); }
                if (__builtin_expect(__builtin_amdgcn_ballot_w64(mxs == 0xFFFFFFFFu) == 0, 1)) break;
                CNT(2); retry<N3>(r3, o3, s3, mb, err, budget);
            }
            STAMP(2);
#pragma unroll
            for (int m = 0; m < 4; ++m) {
                float x0 = tadd32(acc[m][0], acc[m][1]), x1 = tadd32(acc[m][2], acc[m][3]);
                lp<float>(redW)[m * 128] = x0; lp<float>(redW)[m * 128 + 64] = x1;
            }
            FENCE();
            ldsadd(cB1);
            if (s == NSEG - 1) { if (ROLE == 2) kvcopy(s); break; }
            STB(quad(poll_red(cB1, aRedR, n4)), rs, st1, so);
            STAMP(3);
            if (ROLE == 3) {
                if (s) { tb = RFL(mb[N3 - 1].x); add = RFL(mb[N3 - 1].y); }
                sched(tb + a.d1 + add);
            }
            BAR();
            STAMP(4);
            if constexpr (ROLE == 0) {
                v4u qv[2];
                ldn<2>(rs, o1, so, qv);
                frag_cd sl, sh;
                for (;;) {
                    sl = frag_cd{}; sh = frag_cd{};
#pragma unroll
                    for (int i = 0; i < 2; ++i) { sl = MFMA(kx[2 * i], qv[i], sl); sh = MFMA(kx[2 * i + 1], qv[i], sh); }
                    if (__builtin_expect(!bad<2>(qv), 1)) break;
                    CNT(0); retry<2>(rs, o1, so, qv, err, budget);
                }
                frag_cd r;
#pragma unroll
                for (int e = 0; e < 4; ++e) r[e] = sl[e] + dppf<ROR8>(sh[e]);
                *lp<frag_cd>(scW) = r;
                FENCE();
                ldsadd(cB2);
                STAMP(5);
                xload(s + 1, min(s + 2, NSEG - 1));
                STAMP(6); STAMP(7); STAMP(8); STAMP(9); STAMP(10);
            } else if constexpr (ROLE == 2) {
                v4u qk[4];
                ldn<4>(rs, o1, so, qk);
                frag_cd sm;
                for (;;) {
                    sm = frag_cd{}; sm = MFMA(qk[1], qk[0], sm); sm = MFMA(qk[3], qk[2], sm);
                    if (__builtin_expect(!bad<4>(qk), 1)) break;
                    CNT(0); retry<4>(rs, o1, so, qk, err, budget);
                }
                *lp<frag_cd>(scW) = sm;
                FENCE();
                ldsadd(cB2);
                STAMP(5);
                kvcopy(s);
                STAMP(6); STAMP(7); STAMP(8); STAMP(9); STAMP(10);
            } else {
                wait_t(tb + a.d1 + add + (a.dinit & 0xffff));
#pragma unroll
                for (int i = 0; i < 4; ++i) STB(ones, rs, ld2[i], sp);
                STB(ones, rs, in1a, sp); STB(ones, rs, in1b, sp);
                STB(ones, rs, st3, x3);
                STAMP(5);
                poll_cnt(cV2, n4);
                STAMP(6); STAMP(7);
                const unsigned t2 = tb + a.d2 + 2 * add;
                sched(t2);
                STAMP(8);
                v4u v[4];
                ldn<4>(rs, ld2, so, v);
                frag_cd y; unsigned tn, an;
                for (;;) {
                    const int late = (int)(rt16() - t2);
                    tn = tb + a.per + 3 * add; an = add;
                    if (late > SLIP_THR) { tn += late + SLIP_ADD; if (s) an = min(add + SLIP_INC, 4000u); }
                    asm volatile("" : "+s"(tn), "+s"(an)); SB();
                    y = frag_cd{};
#pragma unroll
                    for (int i = 0; i < 4; ++i) y = MFMA(sel, v[i], y);
                    if (__builtin_expect(!bad<4>(v), 1)) break;
                    CNT(1); retry<4>(rs, ld2, so, v, err, budget);
                }
                STAMP(9);
#pragma unroll
                for (int e = 0; e < 4; ++e) y[e] += dppf<ROR8>(y[e]);
                v4u oo = pack_swap(y, y);
                oo.x = l < 32 ? oo.x : tn & ~1u; oo.y = l < 32 ? oo.y : an;
                STB(oo, rs, st3, w3);
                STAMP(10);
                tb += a.per + 3 * add;
            }
            STAMPS_FLUSH();
            r3 = rs; s3 = w3; w3 = x3; x3 = y3; y3 = s3;
            { int t = sp; sp = so; so = t; }
#pragma unroll
            for (int i = 0; i < N3; ++i) o3[i] = off3[i];
        }
        };
        if (w < 2) run(std::integral_constant<int, 0>{}); else if (w == 2) run(std::integral_constant<int, 2>{}); else run(std::integral_constant<int, 3>{});
    } else {
        v8u wV[2][4], wO[4][2];
        stage_w(a.Wv); BAR(); read_w(wV);
        { const bf16_t* Wo = a.Wo + (size_t)h * DH * DM + 256 * jj + 64 * w;
#pragma unroll
          for (int i = 0; i < 16; ++i) glds(Wo + (size_t)8 * i * DM, (l >> 3) * (DM * 2) + (l & 7) * 16, stg + i * 1024);
          WAITV(0);
#pragma unroll
          for (int t = 0; t < 4; ++t)
#pragma unroll
              for (int i = 0; i < 2; ++i) { unsigned p = stg + (64 * i + 8 * q + (n >> 2)) * 128 + (t >> 1) * 64 + (l & 3) * 16 + (t & 1) * 8; wO[t][i] = cat(trd(p), trd(p + 512), trd(p + 4096), trd(p + 4096 + 512)); }
          WAITL(); }
        BAR();
        start();
        v2u ua[4][2];
        auto ucalc = [&](int s) {
#pragma unroll
            for (int kt = 0; kt < 2; ++kt) {
                v4u vx[4];
#pragma unroll
                for (int ks = 0; ks < 4; ++ks) vx[ks] = *lp<v4u>(L_VX + (s & 1) * 8192 + (kt * 4 + ks) * 1024 + l * 16);
#pragma unroll
                for (int t = 0; t < 4; ++t) { frag_cd u = {};
#pragma unroll
                    for (int ks = 0; ks < 4; ++ks) { v4u b = {wO[t][ks >> 1][4 * (ks & 1)], wO[t][ks >> 1][4 * (ks & 1) + 1], wO[t][ks >> 1][4 * (ks & 1) + 2], wO[t][ks >> 1][4 * (ks & 1) + 3]}; u = MFMA(vx[ks], b, u); }
                    ua[t][kt] = v2u{cvt_pk(u[0], u[1]), cvt_pk(u[2], u[3])}; }
            }
        };
        ucalc(0);
        const int phf = (n >> 2) & 1, prow = (phf ? 3 - (n & 3) : (n & 3)) + 4 * hf;
        const int st1 = (l & 3) ? OOB : h1o(2 * 2048 + (4 * jj + 2 * (w & 1) + ((l >> 2) & 1)) * 128 + prr8 * 16);
        int o1[2], off3[4], o3[4];
#pragma unroll
        for (int i = 0; i < 2; ++i) o1[i] = h1o(2 * 2048 + (8 * i + 2 * q + hf) * 128 + row * 16);
#pragma unroll
        for (int i = 0; i < 4; ++i) { off3[i] = phB(L_H3 + g * 16384 + (32 * w + 8 * i + 2 * q + hf) * 128 + row * 16); o3[i] = (row * DM + 256 * w + 64 * i + 16 * q + 8 * hf) * 2; }
        const int st2 = phA((32 * jj + 8 * w + 4 * phf + q) * 4096 + h * 128 + prow * 16);
        const unsigned aRedR = L_RED + ((4 + (w & 1)) * 128 + pos * 16 + 4 * (l & 3) + 2 * ((l >> 2) & 1)) * 4;
        const unsigned aSc = L_SC + ((phf * 4 + q) * 8 + prow) * 16, aSm = L_SM + (prow * 8 + 4 * (q & 1) + 2 * phf) * 4;
        const unsigned redW = L_RED + ((w * 48 + 32 + q) * 16 + n) * 4;
        int so = 0, sp = SLOT;
        unsigned budget = SPIN_CAP;
        rsrc_t r3 = rsm; int s3 = 0, w3 = 0, x3 = SLOT3, y3 = 2 * SLOT3;
        for (int s = 0;; ++s) {
            u64 stv[11] = {};
            unsigned n4 = 4u * (s + 1), n3 = 3u * (s + 1);
            asm volatile("" : "+s"(n4), "+s"(n3));
            STAMP(0);
            v4u mb[4];
            __builtin_amdgcn_s_setprio(2);
            BAR();
            asm volatile("s_nop 15\n\ts_nop 15\n\ts_nop 15" ::: "memory");
            STAMP(1);
            ldn<4>(r3, o3, s3, mb);
            FENCE();
            frag_cd acc[2];
            for (;;) {
#pragma unroll
                for (int m = 0; m < 2; ++m) { acc[m] = frag_cd{}; asm volatile("" : "+v"(acc[m])); }
                SB();
                unsigned mxs = 0;
#pragma unroll
                for (int i = 0; i < 4; ++i) {
                    acc[0] = SMF(mb[i], wV[0][i], acc[0], six); mxs = umax3(mxs, mb[i].x, mb[i].y); SB();
                    acc[1] = SMF(mb[i], wV[1][i], acc[1], six); mxs = umax3(mxs, mb[i].z, mb[i].w); asm volatile("" : "+v"(mxs)); SB(); }
                if (__builtin_expect(__builtin_amdgcn_ballot_w64(mxs == 0xFFFFFFFFu) == 0, 1)) break;
                CNT(2); retry<4>(r3, o3, s3, mb, err, budget);
            }
            STAMP(2);
#pragma unroll
            for (int m = 0; m < 2; ++m) {
                float x0 = tadd32(acc[m][0], acc[m][1]), x1 = tadd32(acc[m][2], acc[m][3]);
                lp<float>(redW)[m * 128] = x0; lp<float>(redW)[m * 128 + 64] = x1;
            }
            FENCE();
            ldsadd(cBv);
            if (s == NSEG - 1) break;
            if (w < 2) STB(quad(poll_red(cBv, aRedR, n4)), rs, st1, so);
            STAMP(3);
            __builtin_amdgcn_s_setprio(3);
            BAR();
            asm volatile("s_nop 15\n\ts_nop 15\n\ts_nop 15\n\ts_nop 15" ::: "memory");
            STAMP(4);
            v4u vm[2];
            ldn<2>(rs, o1, so, vm);
            frag_cd z[4];
            for (;;) {
#pragma unroll
                for (int t = 0; t < 4; ++t) z[t] = frag_cd{};
#pragma unroll
                for (int i = 0; i < 2; ++i)
#pragma unroll
                    for (int t = 0; t < 4; ++t) z[t] = SMF(vm[i], wO[t][i], z[t], six);
                if (__builtin_expect(!bad<2>(vm), 1)) break;
                CNT(0); retry<2>(rs, o1, so, vm, err, budget);
            }
            STAMP(5);
            frag_cd sx; v2f s4a, s4b;
            { unsigned cv; int cap = LCAP;
              asm volatile("1:\n\tds_read_b32 %0, %5\n\tds_read_b128 %1, %6\n\tds_read_b64 %2, %7\n\tds_read_b64 %3, %7 offset:256\n\t"
                           "s_sub_u32 %4, %4, 1\n\ts_cbranch_scc0 2f\n\ts_endpgm\n2:\n\ts_waitcnt lgkmcnt(3)\n\tv_cmp_gt_u32 vcc, %8, %0\n\ts_cbranch_vccnz 1b\n\ts_waitcnt lgkmcnt(0)"
                           : "=&v"(cv), "=&v"(sx), "=&v"(s4a), "=&v"(s4b), "+s"(cap) : "v"(cB2), "v"(aSc), "v"(aSm), "s"(n3) : "vcc", "scc", "memory"); }
            STAMP(6);
            float sv[6] = {sx[0], sx[1], sx[2], sx[3], s4a.x + s4b.x, s4a.y + s4b.y};
#pragma unroll
            for (int e = 0; e < 6; e += 2) { unsigned p = cvt_pk(sv[e], sv[e + 1]); p = cvt_pk(lo16(p) * SCALE, hi16(p) * SCALE); sv[e] = lo16(p); sv[e + 1] = hi16(p); }
            float mx = fmaxf(fmaxf(fmaxf(sv[0], sv[1]), sv[2]), fmaxf(fmaxf(sv[3], sv[4]), sv[5]));
            mx = fmaxf(mx, dppf<HMIR>(mx)); mx = tmax16(mx); mx = tmax32(mx);
            const float mk = -mx * LOG2E;
#pragma unroll
            for (int e = 0; e < 6; ++e) sv[e] = __builtin_amdgcn_exp2f(__builtin_fmaf(sv[e], LOG2E, mk));
            v4u pb = {cvt_pk(sv[0], sv[1]), cvt_pk(sv[2], sv[3]), cvt_pk(sv[4], sv[5]), 0u};
            frag_cd fa[4], fb[4];
#pragma unroll
            for (int t = 0; t < 4; ++t) {
                v4u Aa = {ua[t][0].x, ua[t][0].y, cvt_pk(z[t][0], z[t][1]), 0u};
                v4u Ab = {ua[t][1].x, ua[t][1].y, cvt_pk(z[t][2], z[t][3]), 0u};
                frag_cd zero = {};
                fa[t] = MFMA(Aa, pb, zero); fb[t] = MFMA(Ab, pb, zero);
            }
            float sum = __builtin_fmaf(sv[4] + sv[5], 0.5f, (sv[0] + sv[1]) + (sv[2] + sv[3]));
            sum += dppf<HMIR>(sum); sum = tadd16(sum, sum); sum = tadd32(sum, sum);
            const float inv = __builtin_amdgcn_rcpf(sum);
            v4u o;
#pragma unroll
            for (int t = 0; t < 2; ++t) {
                float r[4];
#pragma unroll
                for (int e = 0; e < 4; ++e) { float u = phf ? fb[2 + t][e] : fa[t][e], v = phf ? fb[t][e] : fa[2 + t][e]; r[e] = (u + dppf<HMIR>(v)) * inv; }
                o[2 * t] = cvt_pk(r[0], r[1]); o[2 * t + 1] = cvt_pk(r[2], r[3]);
            }
            STB(o, rs, st2, so);
            FENCE();
            ldsadd(cV2);
            STAMP(7);
            __builtin_amdgcn_s_setprio(0);
            ucalc(s + 1);
            FENCE();
            STAMP(8); STAMP(9); STAMP(10);
            STAMPS_FLUSH();
            r3 = rs; s3 = w3; w3 = x3; x3 = y3; y3 = s3;
            { int t = sp; sp = so; so = t; }
#pragma unroll
            for (int i = 0; i < 4; ++i) o3[i] = off3[i];
        }
    }
}

template <bool WIDE = false>
__device__ __forceinline__ void load_tile(const bf16_t* W, int ld, v4u (&wf)[32], unsigned L, int w8, int l) {
    const int n = l & 15, q = l >> 4;
#pragma unroll
    for (int hh = 0; hh < 2; ++hh) {
#pragma unroll
        for (int i = 0; i < 16; ++i) { const int gsw = 2 * (q | ((i & 2) << 1)), r = hh * 512 + (w8 * 16 + i) * 4;
            glds(W + (WIDE ? (r & 127) * ld + (r >> 7) * 128 : r * ld), q * ld * 2 + (n ^ gsw) * 16, L + (w8 * 16 + i) * 1024); }
        WAITV(0); BAR();
#pragma unroll
        for (int ks = 0; ks < 16; ++ks) { int r = 8 * q + (n >> 2); int gsw = 2 * ((r & 3) | (((r >> 3) & 1) << 2));
            unsigned p = L + (32 * ks + r) * 256 + ((2 * w8 + ((l >> 1) & 1)) ^ gsw) * 16 + (l & 1) * 8; wf[hh * 16 + ks] = cat2(trd(p), trd(p + 1024)); }
        WAITL(); BAR();
    }
}
__device__ __forceinline__ void seg_gemm(const v4u (&wf)[32], unsigned b0, unsigned b1, frag_cd (&acc)[2]) {
    acc[0] = frag_cd{}; acc[1] = frag_cd{};
    v4u f[8];
#define RD(ks) do { _Pragma("unroll") for (int mt = 0; mt < 2; ++mt) asm volatile("ds_read_b128 %0, %1 offset:%2" : "=v"(f[((ks) & 3) * 2 + mt]) : "v"(((ks) & 1) ? b1 : b0), "n"(((ks) >> 1) * 4096 + mt * 2048) : "memory"); } while (0)
    RD(0); RD(1); RD(2);
#pragma unroll
    for (int ks = 0; ks < 32; ++ks) {
        if (ks + 3 < 32) RD(ks + 3);
        constexpr int dummy = 0; (void)dummy;
        if (ks + 3 < 32) asm volatile("s_waitcnt lgkmcnt(6)" : "+v"(f[(ks & 3) * 2]), "+v"(f[(ks & 3) * 2 + 1]));
        else if (ks == 29) asm volatile("s_waitcnt lgkmcnt(4)" : "+v"(f[(ks & 3) * 2]), "+v"(f[(ks & 3) * 2 + 1]));
        else if (ks == 30) asm volatile("s_waitcnt lgkmcnt(2)" : "+v"(f[(ks & 3) * 2]), "+v"(f[(ks & 3) * 2 + 1]));
        else asm volatile("s_waitcnt lgkmcnt(0)" : "+v"(f[(ks & 3) * 2]), "+v"(f[(ks & 3) * 2 + 1]));
        acc[0] = MFMA(wf[ks], f[(ks & 3) * 2], acc[0]); acc[1] = MFMA(wf[ks], f[(ks & 3) * 2 + 1], acc[1]);
        SB();
    }
#undef RD
}

__device__ __forceinline__ void gwork(const Args& a, const int m, const int h, const unsigned L) {
    const int tid = threadIdx.x, w8 = RFL(tid >> 6), l = tid & 63, n = l & 15, q = l >> 4;
    unsigned* const err = a.ctl + C_ERR;
    const unsigned L_AT = L + 131072, L_KM = L_AT + 24576;
    unsigned* const fG = a.ctl + C_G + h * 32;
    if (m == 2) {
        v4u gw[2], wr[2], xv[8][2];
#pragma unroll
        for (int i = 0; i < 2; ++i) { gw[i] = *(const v4u*)(a.gw + 512 * i + 8 * l); wr[i] = *(const v4u*)(a.wr + 512 * i + 8 * l);
#pragma unroll
            for (int t = 0; t < 8; ++t) xv[t][i] = *(const v4u*)(a.x + (size_t)(64 * h + 8 * w8 + t) * DM + 512 * i + 8 * l); }
        float res = 0.f;
#pragma unroll
        for (int t = 0; t < 8; ++t) { float sg = 0.f, sr = 0.f;
#pragma unroll
            for (int i = 0; i < 2; ++i)
#pragma unroll
                for (int e = 0; e < 4; ++e) { const float x0 = lo16(xv[t][i][e]), x1 = hi16(xv[t][i][e]); const unsigned p = cvt_pk(x0 * lo16(gw[i][e]), x1 * hi16(gw[i][e]));
                    sg += lo16(p) + hi16(p); sr = __builtin_fmaf(x0, lo16(wr[i][e]), __builtin_fmaf(x1, hi16(wr[i][e]), sr)); }
            float v = tadd32(sg, sr);
            v = tadd16(v, v); v += dppf<ROR8>(v); v += dppf<0x124>(v); v += dppf<0x122>(v); v += dppf<0x121>(v);
            if ((l & 7) == t) res = v; }
        const unsigned rb = cvt_pk(res, 0.f), tok = 64 * h + 8 * w8 + (l & 7);
        const unsigned val = l < 32 ? __float_as_uint(rbf(1.0f / (1.0f + __expf(-lo16(rb))))) : (((rb & 0x8000u) ? ~rb : rb | 0x8000u) << 16) | (4095u - tok);
        if ((l & 31) < 8) gst(a.gate + (l >> 5) * S_TOK + tok, val);
        WAITV(0); BAR();
        if (tid == 0) gst(a.ctl + C_R + h, 1u);
        if (w8 == 0) flags_wait(fG, 1, 1, l, err);
        BAR();
    }
    v4u wf[32];
    load_tile((m == 0 ? a.Wk : (m == 1 ? a.Wv : a.Wq)) + h * DH, HD, wf, L, w8, l);
    MARK(0);
    auto xissue = [&](int s) {
#pragma unroll
        for (int i = 0; i < 8; ++i) glds(a.x + (size_t)(32 * s + 8 * (i & 3)) * DM + (2 * w8 + (i >> 2)) * 64, (l >> 3) * (DM * 2) + ((l & 7) ^ (4 * (i & 1) + q)) * 16, L + (s & 1) * 65536 + (w8 * 8 + i) * 1024);
    };
    const unsigned rb0 = L + (n >> 3) * 1024 + (n & 7) * 128 + (q ^ (n >> 1)) * 16, rb1 = rb0 ^ 64;
    const rsrc_t rq = mkrs(a.qkv, NSEG * NH * 24576), ro_ = mkrs(a.ox, NSEG * 262144);
    int sto; { int ck = 2 * w8 + (q >> 1), mt = q & 1; int i = m == 1 ? ck >> 2 : 2 * (ck >> 3) + (ck & 1), qq = m == 1 ? ck & 3 : (ck >> 1) & 3; sto = h * 24576 + m * 8192 + ((mt * 4 + i) * 64 + qq * 16 + n) * 16; }
    const unsigned* fM = a.ctl + C_M + h * 32; unsigned* const fA = a.ctl + C_A + (h >> 3) * 32 + m * 8 + (h & 7);
    const rsrc_t rc = mkrs(a.ctl, C_WORDS * 4);
    const int fD = w8 ? OOB : 4 * ((l >= 8 && l < 11 && l - 8 != m) ? C_G + h * 32 + (l - 8) : C_M + h * 32 + (l & 3));
    const int rt = w8 & 1, eq = w8 >> 1;
    const int stx = (h >> 3) * 65536 + ((h & 7) * 2 + (eq >> 1)) * 4096 + rt * 2048 + (eq & 1) * 1024 + ((2 * (q & 1) + (q >> 1)) * 16 + n) * 16;
    auto aissue = [&](int s) {
        glds1<3>(a.qkv + ((size_t)s * NH + h) * 24576 + w8 * 3072, l * 16, L_AT + w8 * 3072);
    };
    auto attend = [&](int s) {
            frag_cd sc[3] = {};
#pragma unroll
            for (int i = 0; i < 4; ++i) {
                v4u qf = *lp<v4u>(L_AT + 16384 + (rt * 4 + i) * 1024 + l * 16);
                sc[0] = MFMA(*lp<v4u>(L_AT + i * 1024 + l * 16), qf, sc[0]);
                sc[1] = MFMA(*lp<v4u>(L_AT + (4 + i) * 1024 + l * 16), qf, sc[1]);
                sc[2] = MFMA(*lp<v4u>(L_KM + (8 * (i >> 1) + 2 * q + (i & 1)) * 128 + (n & 7) * 16), qf, sc[2]);
            }
            float mx = -INFINITY;
#pragma unroll
            for (int t = 0; t < 3; ++t)
#pragma unroll
                for (int e = 0; e < 4; ++e) { float v = rbf(rbf(sc[t][e]) * SCALE); if (t < 2 && 16 * t + 4 * q + e > 16 * rt + n) v = -999292928.0f; sc[t][e] = v; mx = fmaxf(mx, v); }
            mx = tmax16(mx); mx = tmax32(mx);
            float sum = 0.f;
#pragma unroll
            for (int t = 0; t < 3; ++t)
#pragma unroll
                for (int e = 0; e < 4; ++e) { float v = __builtin_amdgcn_exp2f((sc[t][e] - mx) * LOG2E); if (t == 2 && q >= 2) v = 0.f; sc[t][e] = v; sum += v; }
            sum = tadd16(sum, sum); sum = tadd32(sum, sum);
            const float inv = 1.0f / sum;
            const v4u px = {cvt_pk(sc[0][0] * inv, sc[0][1] * inv), cvt_pk(sc[0][2] * inv, sc[0][3] * inv), cvt_pk(sc[1][0] * inv, sc[1][1] * inv), cvt_pk(sc[1][2] * inv, sc[1][3] * inv)};
            const v4u pm = {cvt_pk(sc[2][0] * inv, sc[2][1] * inv), cvt_pk(sc[2][2] * inv, sc[2][3] * inv), 0u, 0u};
            frag_cd o[2];
#pragma unroll
            for (int et = 0; et < 2; ++et) { int ck = 2 * et + ((l >> 1) & 1);
                unsigned p = L_AT + 8192 + (eq * 64 + ck * 16 + 4 * q + (n >> 2)) * 16 + (l & 1) * 8;
                v2u vmm = trd(L_KM + 2048 + (4 * eq + ck) * 128 + (4 * (q & 1) + (n >> 2)) * 16 + (l & 1) * 8);
                o[et] = MFMA(cat2(trd(p), trd(p + 4096)), px, (frag_cd{}));
                o[et] = MFMA((v4u{vmm.x, vmm.y, 0u, 0u}), pm, o[et]); }
            STB(pack_swap(o[0], o[1]), ro_, stx, s * 262144);
            MARK(65 + s / 3);
    };
    int sa = m, staged = 0, puba = 0;
    unsigned fv = 0;
    xissue(0);
    for (int sg = 0; sg < NSEG; ++sg) {
        const int done = sg < 4 ? sg : sg - 1;
        if (sg < 4) WAITV(0);
        WAITV(1);
        if (w8 == 0) { bool ok = !staged && sa < done && __builtin_amdgcn_ballot_w64(fv <= (unsigned)sa) == 0; if (l == 0) lp<unsigned>(L_KM + 4096)[sg & 1] = ok; WAITL(); }
        BAR();
        if (tid == 0) { gst(fG + m, done); if (puba) gst(fA, puba); }
        puba = 0;
        if (staged) { attend(sa); puba = sa + 1; sa += 3; staged = 0; }
        else if (RFL(ldsr(L_KM + 4096 + 4 * (sg & 1)))) {
            aissue(sa); staged = 1;
            if (w8 < 4) glds1<1>(a.kvm + ((size_t)sa * NH + h) * 4096 + w8 * 1024, l * 16, L_KM + w8 * 1024);
        }
        if (sg + 1 < NSEG) xissue(sg + 1);
        fv = __builtin_amdgcn_raw_buffer_load_b32(rc, fD, 0, 16);
        FENCE();
        frag_cd acc[2];
        seg_gemm(wf, rb0 + (sg & 1) * 65536, rb1 + (sg & 1) * 65536, acc);
        MARK(sg + 1); FENCE();
        STB(pack_swap(acc[0], acc[1]), rq, sto, sg * (NH * 24576));
    }
    WAITV(0); BAR();
    if (tid == 0) { gst(fG + m, NSEG); if (puba) gst(fA, puba); }
    for (; sa < NSEG; sa += 3) {
        if (!staged) {
            for (int i = 0; i < 2; ++i) flags_wait(fG + (m + 1 + i) % 3, 1, sa + 1, l, err);
            aissue(sa);
            if (w8 == 0) { flags_wait(fM, 4, sa + 1, l, err); glds1<4>(a.kvm + ((size_t)sa * NH + h) * 4096, l * 16, L_KM); }
            WAITV(0); BAR();
        }
        staged = 0;
        attend(sa);
        WAITV(0); BAR();
        if (tid == 0) gst(fA, sa + 1);
    }
}

__device__ __forceinline__ void owork(const Args& a, const int nt, const int kq, const unsigned L) {
    const int tid = threadIdx.x, w8 = RFL(tid >> 6), l = tid & 63, n = l & 15, q = l >> 4;
    unsigned* const err = a.ctl + C_ERR;
    if (w8 == 0) {
        unsigned char* p = a.ring + ((l >> 1) & 1) * CHB + (l & 1) * 8192 + (nt * 4 + kq) * 16384 + ((l >> 2) & 3) * 2048 + 1024;
        unsigned best = 0;
        if (l < 16) gst((unsigned*)p, ~0u);
        WAITV(0);
#pragma unroll 1
        for (int i = 0; i < 16; ++i) { u64 t1, t2; unsigned v;
            const u64 pi = ((u64)__builtin_amdgcn_readlane((unsigned)((u64)p >> 32), i) << 32) | (unsigned)__builtin_amdgcn_readlane((unsigned)(u64)p, i);
            asm volatile("s_memtime %0\n\ts_waitcnt lgkmcnt(0)\n\tglobal_load_dword %2, %3, %4 sc1\n\ts_waitcnt vmcnt(0)\n\ts_memtime %1\n\ts_waitcnt lgkmcnt(0)"
                         : "=&s"(t1), "=&s"(t2), "=&v"(v) : "v"(0u), "s"(pi) : "memory");
            if ((l & 3) == (i & 3)) best = max(best, ~(unsigned)(t2 - t1)); }
        if (l < 16) gst((unsigned*)p, ~0u);
        if (l < 4) __hip_atomic_fetch_max(a.ctl + C_CAL + l, best, __ATOMIC_RELAXED, __HIP_MEMORY_SCOPE_AGENT);
        WAITV(0);
        if (l == 0) __hip_atomic_fetch_add(a.ctl + C_READY, 1u, __ATOMIC_RELAXED, __HIP_MEMORY_SCOPE_AGENT);
        MARK(66);
        flags_wait(a.ctl + C_G + (nt * 4 + kq) * 32, 1, 1, l, err);
    }
    BAR();
    v4u wf[32];
    const int j = nt * 4 + kq;
    const unsigned L_XS = L + 131072, L_HS = L_XS + 16384;
    if (j < FF / 128) load_tile(a.wg + j * 128, FF, wf, L, w8, l);
    flags_wait(a.ctl + C_R, 32, 1, l, err);
    const rsrc_t rg = mkrs(a.gate, 2 * S_TOK * 4), ry = mkrs(a.ypart, 32 * 28 * 1024);
    unsigned key[TOPC];
    { v4u kv[8]; unsigned prev = ~0u;
#pragma unroll
      for (int i = 0; i < 8; ++i) kv[i] = __builtin_amdgcn_raw_buffer_load_b128(rg, S_TOK * 4 + l * 16, i * 1024, 16);
#pragma unroll
      for (int c = 0; c < TOPC; ++c) { unsigned mx = 0;
#pragma unroll
          for (int i = 0; i < 8; ++i)
#pragma unroll
              for (int e = 0; e < 4; ++e) mx = max(mx, kv[i][e] < prev ? kv[i][e] : 0u);
          mx = max(mx, dppu<0x128>(mx)); mx = max(mx, dppu<0x124>(mx)); mx = max(mx, dppu<0x122>(mx)); mx = max(mx, dppu<0x121>(mx));
          { v2u r = sw16(mx, mx); mx = max(r.x, r.y); r = sw32(mx, mx); mx = max(r.x, r.y); }
          key[c] = prev = RFL(mx); } }
    if (j < FF / 128) {
        unsigned mine = key[0];
#pragma unroll
        for (int c = 1; c < TOPC; ++c) if ((l & 7) == c) mine = key[c];
#pragma unroll
        for (int i = 0; i < 2; ++i) glds(a.x + (2 * w8 + i) * 64, (4095u - (mine & 4095u)) * (DM * 2) + (l >> 3) * 16, L_XS + (2 * w8 + i) * 1024);
        WAITV(0); BAR();
        auto proj = [&]() { frag_cd acc = {};
#pragma unroll
            for (int ks = 0; ks < 32; ++ks) acc = MFMA(wf[ks], *lp<v4u>(L_XS + ks * 512 + q * 128 + (n & 7) * 16), acc);
            return acc; };
        const frag_cd gv = proj();
        load_tile(a.wu + j * 128, FF, wf, L, w8, l);
        const frag_cd uv = proj();
        float hv[4];
#pragma unroll
        for (int e = 0; e < 4; ++e) { const float gg = rbf(gv[e]); hv[e] = rbf(gg / (1.0f + __expf(-gg))) * rbf(uv[e]); }
        if (n < 8) *lp<v2u>(L_HS + (w8 >> 1) * 512 + (2 * (w8 & 1) + (q >> 1)) * 128 + n * 16 + (q & 1) * 8) = v2u{cvt_pk(hv[0], hv[1]), cvt_pk(hv[2], hv[3])};
        WAITL();
        load_tile<true>(a.wd + (size_t)j * 128 * DM, DM, wf, L, w8, l);
#pragma unroll
        for (int cb = 0; cb < 8; ++cb) { frag_cd acc = {};
#pragma unroll
            for (int i = 0; i < 4; ++i) acc = MFMA(wf[4 * cb + i], *lp<v4u>(L_HS + i * 512 + q * 128 + (n & 7) * 16), acc);
            STB(__builtin_bit_cast(v4u, acc), ry, n < 8 ? ((w8 >> 1) * 28 + j) * 1024 + ((w8 & 1) * 32 + q * 8 + n) * 16 : OOB, cb * (4 * 28 * 1024)); }
        WAITV(0); BAR();
        if (tid == 0) gst(a.ctl + C_Y + j, 1u);
    }
    MARK(67);
    load_tile(a.Wo + (size_t)kq * 1024 * DM + nt * 128, DM, wf, L, w8, l);
    MARK(0);
    const unsigned* fA = a.ctl + C_A + kq * 32; unsigned* const fP = a.ctl + C_P + nt * 32;
    const rsrc_t rp = mkrs(a.part, NSEG * 524288);
    auto oissue = [&](int s) {
#pragma unroll
        for (int i = 0; i < 2; ++i) glds1<4>(a.ox + (size_t)s * 262144 + kq * 65536 + w8 * 8192 + i * 4096, l * 16, L + (s & 1) * 65536 + w8 * 8192 + i * 4096);
    };
    const int fcol = nt * 128 + (2 * kq + (w8 >> 1)) * 16 + 4 * q, frow = 16 * (w8 & 1) + n;
    const rsrc_t rx = mkrs(a.x, S_TOK * DM * 2);
    struct Fin { v4u p[4]; v2u xv; float gt; };
    auto fissue = [&](int s, bool on) { Fin f; int k = on ? 0 : 0x40000000; asm("" : "+s"(k));
#pragma unroll
        for (int i = 0; i < 4; ++i) f.p[i] = __builtin_amdgcn_raw_buffer_load_b128(rp, k + (nt * 4 + kq) * 16384 + w8 * 1024 + l * 16, s * 524288 + i * 4096, 16);
        f.xv = __builtin_amdgcn_raw_buffer_load_b64(rx, k + (frow * DM + fcol) * 2, s * 65536, 0);
        f.gt = __uint_as_float(__builtin_amdgcn_raw_buffer_load_b32(rg, k + frow * 4, s * 128, 16));
        return f;
    };
    auto finish = [&](int s, const Fin& f) {
        const v4u (&p)[4] = f.p; const v2u xv = f.xv; const float gt = f.gt;
        const int tok = 32 * s + frow; const float omg = rbf(1.0f - gt);
        float b[4] = {lo16(xv.x), hi16(xv.x), lo16(xv.y), hi16(xv.y)};
        unsigned kk = 0; int slot = 0;
#pragma unroll
        for (int i = 0; i < TOPC; ++i) if ((key[i] & 4095u) == 4095u - tok) { kk = key[i]; slot = i; }
        if (__builtin_amdgcn_ballot_w64(kk != 0)) {
            flags_wait(a.ctl + C_Y, FF / 128, 1, l, err);
            frag_cd y = {};
#pragma unroll 1
            for (int i = 0; i < 28; i += 4) { v4u t[4];
#pragma unroll
                for (int u = 0; u < 4; ++u) t[u] = __builtin_amdgcn_raw_buffer_load_b128(ry, kk ? ((w8 >> 1) * 32 + q * 8 + slot) * 16 + u * 1024 : OOB, (j * 28 + i) * 1024, 16);
#pragma unroll
                for (int u = 0; u < 4; ++u) y += __builtin_bit_cast(frag_cd, t[u]); }
            const float r = hi16((kk & 0x80000000u) ? kk & 0x7fffffffu : ~kk), rs = rbf(1.0f / (1.0f + __expf(-r)));
            if (kk) {
#pragma unroll
                for (int e = 0; e < 4; ++e) b[e] = rbf(b[e] + rbf(rbf(y[e]) * rs)); }
        }
        float o[4];
#pragma unroll
        for (int e = 0; e < 4; ++e) { float av = rbf((__uint_as_float(p[0][e]) + __uint_as_float(p[1][e])) + (__uint_as_float(p[2][e]) + __uint_as_float(p[3][e]))); o[e] = rbf(gt * av) + rbf(omg * b[e]); }
        *(v2u*)(a.out + (size_t)tok * DM + fcol) = v2u{cvt_pk(o[0], o[1]), cvt_pk(o[2], o[3])};
    };
    const int stp = ((nt * 4 + (w8 >> 1)) * 4 + kq) * 4096 + (w8 & 1) * 2048 + l * 16;
    const unsigned* fW = l < 32 ? fA + min(l, 23) : fP + (l & 3) * 8 + 2 * kq + ((w8 >> 1) & 1);
    unsigned* const fO = fP + kq * 8 + w8;
    unsigned fv = 0;
    auto a_ok = [&](int s) { return ((__builtin_amdgcn_ballot_w64(fv <= (unsigned)s) >> ((s % 3) * 8)) & 255) == 0; };
    auto p_ok = [&](int s) { return (__builtin_amdgcn_ballot_w64(fv <= (unsigned)s) >> 32) == 0; };
    int pre = 0, fin = w8 < 4 ? 0 : NSEG;
    for (int s = 0; s <= NSEG; ++s) {
        if (pre == s) {
            WAITV(0);
            if (s && l == 0) gst(fO, s);
            for (int i = 0;; ++i) {
                fv = gld(fW); WAITV(0);
                if (s < NSEG ? a_ok(s) : fin == NSEG) break;
                if (fin < s && p_ok(fin)) { FENCE(); finish(fin, fissue(fin, true)); ++fin; }
                else { __builtin_amdgcn_s_sleep(2); if (i > GCAP) fatal(err); }
            }
            if (s == NSEG) break;
            oissue(s); ++pre;
            WAITV(0);
        }
        BAR();
        if (s + 1 < NSEG && a_ok(s + 1)) { oissue(s + 1); ++pre; }
        const bool fi = fin < s && p_ok(fin);
        const Fin f = fissue(fin & 63, fi);
        fv = gld(fW);
        FENCE();
        frag_cd acc[2];
        seg_gemm(wf, L + (s & 1) * 65536 + l * 16, L + (s & 1) * 65536 + 1024 + l * 16, acc);
        WAITV(0);
        if (fi) { finish(fin, f); ++fin; }
        if (s && l == 0) gst(fO, s);
        STB(__builtin_bit_cast(v4u, acc[0]), rp, stp, s * 524288); STB(__builtin_bit_cast(v4u, acc[1]), rp, stp + 1024, s * 524288);
        MARK(1 + s);
    }
    MARK(65);
}

__global__ void __launch_bounds__(512, 2) k_mega(Args a) {
    __shared__ __attribute__((aligned(16))) unsigned char smem[163840];
    const unsigned L = (unsigned)(size_t)(LAS unsigned char*)smem;
    const int b = blockIdx.x, u = (b & 3) + 4 * (b >> 3);
    MARK(255);
    if (!(b & 4)) chain(a, u, L);
    else if (u < 96) gwork(a, (u >> 2) % 3, (u & 3) * 8 + (u >> 2) / 3, L);
    else owork(a, (u >> 2) - 24, u & 3, L);
}

static constexpr int g_force = 0; static constexpr int g_d1 = 1000, g_d2 = 2150, g_per = 2750, g_dinit = 250; static constexpr unsigned* g_dbg = nullptr;
extern "C" void kernel_launch(void* const* d_in, const int* in_sizes, int n_in, void* d_out, int out_size, void* d_ws, size_t ws_size, hipStream_t stream) {
    auto in = [&](int i) { return (const bf16_t*)d_in[i]; };
    unsigned char* ws = (unsigned char*)d_ws; size_t off = 0;
    auto alloc = [&](size_t bytes) { unsigned char* p = ws + off; off += (bytes + 255) & ~(size_t)255; return p; };
    unsigned* ctl = (unsigned*)alloc(C_WORDS * 4);
    unsigned* gate = (unsigned*)alloc(2 * S_TOK * 4); unsigned char* ypart = alloc(32 * 28 * 1024);
    off += (size_t)(-(uintptr_t)(ws + off)) & ((2u << 20) - 1);
    unsigned char* ring = alloc(RING_BYTES);
    unsigned char* qkv = alloc((size_t)NSEG * NH * 24576); unsigned char* kvm = alloc((size_t)NSEG * NH * 4096);
    unsigned char* ox = alloc((size_t)NSEG * 262144); unsigned char* part = alloc((size_t)NSEG * 524288);
    (void)hipMemsetAsync(ctl, 0, C_WORDS * 4, stream);
    Args a{in(0), in(1), in(2), in(3), in(4), in(5), (bf16_t*)d_out, ctl, ring, qkv, kvm, ox, part, in(8), in(9), in(10), in(11), in(12), gate, ypart, g_dbg,
           g_d1 * 16 / 10, g_d2 * 16 / 10, g_per * 16 / 10, g_dinit * 16 / 10 + (g_force << 16)};
    k_mega<<<256, 512, 0, stream>>>(a);
}
```

```cpp
#include <hip/hip_runtime.h>
#include <stdint.h>
#include <type_traits>

typedef unsigned short bf16_t;
typedef unsigned long long u64;
typedef float frag_cd __attribute__((ext_vector_type(4)));
typedef unsigned v4u __attribute__((ext_vector_type(4)));
typedef unsigned v8u __attribute__((ext_vector_type(8)));
typedef unsigned v2u __attribute__((ext_vector_type(2)));
typedef float v2f __attribute__((ext_vector_type(2)));
typedef __bf16 bf16x2_t __attribute__((ext_vector_type(2)));
typedef __bf16 v8bf __attribute__((ext_vector_type(8)));
typedef __bf16 v16bf __attribute__((ext_vector_type(16)));
typedef short v4s_t __attribute__((ext_vector_type(4)));
typedef __amdgpu_buffer_rsrc_t rsrc_t;
#define LAS __attribute__((address_space(3)))
#define GAS __attribute__((address_space(1)))

static constexpr int S_TOK = 2048, DM = 1024, NH = 32, DH = 128, MM = 8, SEG = 32, NSEG = 64, HD = NH * DH, FF = 3584, TOPC = 8;
static constexpr int C_ERR = 0, C_READY = 32, C_CAL = 64  , C_G = 128  , C_M = C_G + 1024  , C_A = C_M + 1024  ,
                     C_P = C_A + 128  , C_R = C_P + 256, C_Y = C_R + 32, C_WORDS = C_Y + 32;
static constexpr int RING_BYTES = 4 << 20, SLOT = 1 << 20, CHB = 2 << 20, L_H3 = 192 << 10, L_TS = 256 << 10, SLOT3 = 192 << 10;
static constexpr unsigned SPIN_CAP = 1u << 18;
static constexpr int OOB = 0x7ffffff0, LCAP = 1 << 22, GCAP = 1 << 20;
static constexpr float SCALE = 0.08838834764831845f, LOG2E = 1.4426950408889634f;

struct Args {
    const bf16_t* x; const bf16_t* mem0; const bf16_t* Wq; const bf16_t* Wk; const bf16_t* Wv; const bf16_t* Wo;
    bf16_t* out; unsigned* ctl; unsigned char* ring; unsigned char* qkv; unsigned char* kvm; unsigned char* ox; unsigned char* part;
    const bf16_t* wr; const bf16_t* wg; const bf16_t* wu; const bf16_t* wd; const bf16_t* gw;
    unsigned* gate  ; unsigned char* ypart; unsigned* dbg;
    int d1, d2, per, dinit;
};

__device__ __forceinline__ unsigned cvt_pk(float lo, float hi) { v2f v = {lo, hi}; return __builtin_bit_cast(unsigned, __builtin_convertvector(v, bf16x2_t)); }
__device__ __forceinline__ float lo16(unsigned p) { return __uint_as_float(p << 16); }
__device__ __forceinline__ float hi16(unsigned p) { return __uint_as_float(p & 0xffff0000u); }
__device__ __forceinline__ float rbf(float f) { return lo16(cvt_pk(f, 0.f)); }
template <int C> __device__ __forceinline__ unsigned dppu(unsigned v) { return (unsigned)__builtin_amdgcn_update_dpp(0, (int)v, C, 0xf, 0xf, true); }
template <int C> __device__ __forceinline__ float dppf(float v) { return __uint_as_float(dppu<C>(__float_as_uint(v))); }
#define ROR8 0x128
#define HMIR 0x141
__device__ __forceinline__ v2u sw16(unsigned a, unsigned b) { auto r = __builtin_amdgcn_permlane16_swap(a, b, false, false); return v2u{r[0], r[1]}; }
__device__ __forceinline__ v2u sw32(unsigned a, unsigned b) { auto r = __builtin_amdgcn_permlane32_swap(a, b, false, false); return v2u{r[0], r[1]}; }
__device__ __forceinline__ float tadd16(float a, float b) { v2u r = sw16(__float_as_uint(a), __float_as_uint(b)); return __uint_as_float(r.x) + __uint_as_float(r.y); }
__device__ __forceinline__ float tadd32(float a, float b) { v2u r = sw32(__float_as_uint(a), __float_as_uint(b)); return __uint_as_float(r.x) + __uint_as_float(r.y); }
__device__ __forceinline__ float tmax16(float a) { v2u r = sw16(__float_as_uint(a), __float_as_uint(a)); return fmaxf(__uint_as_float(r.x), __uint_as_float(r.y)); }
__device__ __forceinline__ float tmax32(float a) { v2u r = sw32(__float_as_uint(a), __float_as_uint(a)); return fmaxf(__uint_as_float(r.x), __uint_as_float(r.y)); }
__device__ __forceinline__ unsigned umax3(unsigned a, unsigned b, unsigned c) { return max(max(a, b), c); }
__device__ __forceinline__ v4u pack_swap(frag_cd a, frag_cd b) { v2u x0 = sw16(cvt_pk(a[0], a[1]), cvt_pk(b[0], b[1])), x1 = sw16(cvt_pk(a[2], a[3]), cvt_pk(b[2], b[3])); return v4u{x0.x, x1.x, x0.y, x1.y}; }
#define MFMA(a, b, c) __builtin_amdgcn_mfma_f32_16x16x32_bf16(__builtin_bit_cast(v8bf, a), __builtin_bit_cast(v8bf, b), c, 0, 0, 0)
#define SMF(a, b, c, ix) __builtin_amdgcn_smfmac_f32_16x16x64_bf16(__builtin_bit_cast(v8bf, a), __builtin_bit_cast(v16bf, b), c, ix, 0, 0)
#define FENCE() asm volatile("" ::: "memory")
#define SB() __builtin_amdgcn_sched_barrier(0)
#define RFL(x) __builtin_amdgcn_readfirstlane(x)
#define BAR() do { FENCE(); __builtin_amdgcn_s_barrier(); FENCE(); } while (0)
#define WAITV(n) do { FENCE(); __builtin_amdgcn_s_waitcnt(0x0F70 | (n)); FENCE(); } while (0)
#define WAITL() asm volatile("s_waitcnt lgkmcnt(0)" ::: "memory")
__device__ __forceinline__ void STB(v4u v, rsrc_t rs, int off, int soff) { __builtin_amdgcn_raw_buffer_store_b128(v, rs, off, soff, 16); asm volatile("s_nop 1" :: "v"(v) : "memory"); }
template <class T> __device__ __forceinline__ LAS T* lp(unsigned a) { return (LAS T*)(size_t)a; }
__device__ __forceinline__ void glds(const void* g, unsigned voff, unsigned lds) { asm volatile("s_mov_b32 m0, %2\n\ts_nop 0\n\tglobal_load_lds_dwordx4 %0, %1" :: "v"(voff), "s"(g), "s"(lds) : "memory"); }
#define NTC " nt"
#define NTW " nt"
__device__ __forceinline__ void gldsc(const void* g, unsigned voff, unsigned lds) { asm volatile("s_mov_b32 m0, %2\n\ts_nop 0\n\tglobal_load_lds_dwordx4 %0, %1" NTC :: "v"(voff), "s"(g), "s"(lds) : "memory"); }
__device__ __forceinline__ void gldsw(const void* g, unsigned voff, unsigned lds) { asm volatile("s_mov_b32 m0, %2\n\ts_nop 0\n\tglobal_load_lds_dwordx4 %0, %1" NTW :: "v"(voff), "s"(g), "s"(lds) : "memory"); }
template <int N> __device__ __forceinline__ void glds1(const void* g, unsigned voff, unsigned lds) {
#define GL(o) "\n\tglobal_load_lds_dwordx4 %0, %1 offset:" #o " sc1"
    if constexpr (N == 1) asm volatile("s_mov_b32 m0, %2\n\ts_nop 0" GL(0) :: "v"(voff), "s"(g), "s"(lds) : "memory");
    if constexpr (N == 2) asm volatile("s_mov_b32 m0, %2\n\ts_nop 0" GL(0) GL(1024) :: "v"(voff), "s"(g), "s"(lds) : "memory");
    if constexpr (N == 4) asm volatile("s_mov_b32 m0, %2\n\ts_nop 0" GL(0) GL(1024) GL(2048) GL(3072) :: "v"(voff), "s"(g), "s"(lds) : "memory");
}
__device__ __forceinline__ rsrc_t mkrs(const void* p, int bytes) { return __builtin_amdgcn_make_buffer_rsrc((void*)p, 0, bytes, 0x00020000); }
__device__ __forceinline__ unsigned gld(const unsigned* p) { return __hip_atomic_load(p, __ATOMIC_RELAXED, __HIP_MEMORY_SCOPE_AGENT); }
__device__ __forceinline__ void gst(unsigned* p, unsigned v) { __hip_atomic_store(p, v, __ATOMIC_RELAXED, __HIP_MEMORY_SCOPE_AGENT); }
__device__ __forceinline__ unsigned rt16() { return (unsigned)__builtin_amdgcn_s_memrealtime() << 4; }

#define STAMP(slot) do {} while (0)
#define STAMPS_FLUSH() do {} while (0)
#define PT(k) do {} while (0)
#define PFLUSH() do {} while (0)
#define MARK(i) do {} while (0)
#define CNT(i) do {} while (0)
#define CNT_FLUSH() do {} while (0)

#define LATE_THR 100
#define LATE_ADD 480
#define SLIP_THR 320
#define SLIP_ADD 640
#define SLIP_INC 32
__device__ __forceinline__ void wait_t(unsigned T) {
    int rem, cap = 4096;
    asm volatile("1:\n\ts_memrealtime vcc\n\ts_sub_u32 %1, %1, 1\n\ts_cbranch_scc1 3f\n\ts_waitcnt lgkmcnt(0)\n\ts_lshl_b32 %0, vcc_lo, 4\n\ts_sub_i32 %0, %2, %0\n\ts_cmp_ge_i32 %0, 64\n\ts_cbranch_scc1 1b\n\t"
                 "s_cmp_gt_i32 %0, 12\n\ts_cbranch_scc0 3f\n2:\n\ts_sub_i32 %0, %0, 13\n\ts_cmp_gt_i32 %0, 12\n\ts_cbranch_scc1 2b\n3:\n\ts_waitcnt lgkmcnt(0)"
                 : "=&s"(rem), "+s"(cap) : "s"(T) : "scc", "vcc", "memory");
}
__device__ __forceinline__ void sched(unsigned T) { unsigned now = rt16(); if ((int)(T - now) < LATE_THR) T = now + LATE_ADD; wait_t(T); }
template <int N>
__device__ __forceinline__ bool bad(const v4u (&v)[N]) {
    unsigned m = umax3(v[0].x, v[0].y, v[0].z); m = max(m, v[0].w);
#pragma unroll
    for (int i = 1; i < N; ++i) { m = umax3(m, v[i].x, v[i].y); m = umax3(m, v[i].z, v[i].w); }
    return __builtin_amdgcn_ballot_w64(m == 0xFFFFFFFFu) != 0;
}
template <int N>
__device__ __forceinline__ void ldn(rsrc_t rs, const int (&off)[N], int soff, v4u (&v)[N]) {
#pragma unroll
    for (int i = 0; i < N; ++i) v[i] = __builtin_amdgcn_raw_buffer_load_b128(rs, off[i], soff, 16);
}
__device__ __forceinline__ void endpgm() { asm volatile("s_endpgm" ::: "memory"); }
__device__ __forceinline__ void fatal(unsigned* err) { asm volatile("global_store_dword %0, %1, off sc1\n\ts_waitcnt vmcnt(0)\n\ts_endpgm" :: "v"(err), "v"(1u) : "memory"); }
template <int N>
__device__ __forceinline__ void retry(rsrc_t rs, const int (&off)[N], int soff, v4u (&v)[N], unsigned* err, unsigned& budget) {
    if (--budget == 0) fatal(err);
#define RL(d, o) "buffer_load_dwordx4 %" #d ", %" #o ", %[r], %[s] offen sc1\n\t"
#define RH "s_waitcnt vmcnt(0)\n\ts_sleep 2\n\t"
#define RT [r] "s"(rs), [s] "s"(soff) : "memory"
    if constexpr (N == 2) asm volatile(RH RL(0, 2) RL(1, 3) "s_waitcnt vmcnt(0)" : "=&v"(v[0]), "=&v"(v[1]) : "v"(off[0]), "v"(off[1]), RT);
    if constexpr (N == 4) asm volatile(RH RL(0, 4) RL(1, 5) RL(2, 6) RL(3, 7) "s_waitcnt vmcnt(0)" : "=&v"(v[0]), "=&v"(v[1]), "=&v"(v[2]), "=&v"(v[3]) : "v"(off[0]), "v"(off[1]), "v"(off[2]), "v"(off[3]), RT);
    if constexpr (N == 5) asm volatile(RH RL(0, 5) RL(1, 6) RL(2, 7) RL(3, 8) RL(4, 9) "s_waitcnt vmcnt(0)" : "=&v"(v[0]), "=&v"(v[1]), "=&v"(v[2]), "=&v"(v[3]), "=&v"(v[4]) : "v"(off[0]), "v"(off[1]), "v"(off[2]), "v"(off[3]), "v"(off[4]), RT);
}
__device__ __forceinline__ unsigned ldsr(unsigned p) { return __hip_atomic_load(lp<unsigned>(p), __ATOMIC_RELAXED, __HIP_MEMORY_SCOPE_WORKGROUP); }
__device__ __forceinline__ void ldsadd(unsigned p) { asm volatile("s_mov_b64 exec, 1\n\tds_add_u32 %0, %1\n\ts_mov_b64 exec, -1" :: "v"(p), "v"(1u) : "memory"); }
__device__ __forceinline__ v2u trd(unsigned p) { return __builtin_bit_cast(v2u, __builtin_amdgcn_ds_read_tr16_b64_v4i16(lp<v4s_t>(p))); }
__device__ __forceinline__ v8u cat(v2u a, v2u b, v2u c, v2u d) { return v8u{a.x, a.y, b.x, b.y, c.x, c.y, d.x, d.y}; }
__device__ __forceinline__ v4u cat2(v2u a, v2u b) { return v4u{a.x, a.y, b.x, b.y}; }
__device__ __forceinline__ unsigned poll_red(unsigned aC, unsigned aR, unsigned need) {
    unsigned cv; int cap = LCAP; v2f r[4];
    asm volatile("1:\n\tds_read_b32 %0, %6\n\tds_read_b64 %1, %7\n\tds_read_b64 %2, %7 offset:3072\n\tds_read_b64 %3, %7 offset:6144\n\tds_read_b64 %4, %7 offset:9216\n\t"
                 "s_sub_u32 %5, %5, 1\n\ts_cbranch_scc0 2f\n\ts_endpgm\n2:\n\ts_waitcnt lgkmcnt(4)\n\tv_cmp_gt_u32 vcc, %8, %0\n\ts_cbranch_vccnz 1b\n\ts_waitcnt lgkmcnt(0)"
                 : "=&v"(cv), "=&v"(r[0]), "=&v"(r[1]), "=&v"(r[2]), "=&v"(r[3]), "+s"(cap) : "v"(aC), "v"(aR), "s"(need) : "vcc", "scc", "memory");
    return min(cvt_pk((r[0].x + r[1].x) + (r[2].x + r[3].x), (r[0].y + r[1].y) + (r[2].y + r[3].y)), 0xFFFFFFFEu);
}
__device__ __forceinline__ v4u quad(unsigned p0) { return v4u{p0, dppu<0x55>(p0), dppu<0xAA>(p0), dppu<0xFF>(p0)}; }
__device__ __forceinline__ void poll_cnt(unsigned p, unsigned need) { for (int i = 0; ldsr(p) < need; ++i) { if (i > LCAP) endpgm(); } FENCE(); }
__device__ __forceinline__ bool flags_ok(const unsigned* p, int nw, unsigned need, int l) { unsigned v = gld(p + min(l, nw - 1)); bool r = __builtin_amdgcn_ballot_w64(v < need) == 0; FENCE(); return r; }
__device__ __forceinline__ void flags_wait(const unsigned* p, int nw, unsigned need, int l, unsigned* err) {
    for (int i = 0; !flags_ok(p, nw, need, l); ++i) { __builtin_amdgcn_s_sleep(4); if (i > GCAP) fatal(err); }
}

__device__ __forceinline__ void chain(const Args& a, const int c, const unsigned L) {
    const int tid = threadIdx.x, w8 = RFL(tid >> 6), w = w8 & 3, l = tid & 63;
    const int g = c & 3, aa = c >> 2, h = g * 8 + (aa >> 2), jj = aa & 3;
    const int n = l & 15, q = l >> 4, row = n & 7, hf = n >> 3;
    const unsigned L_RED = L + 131072, L_SC = L_RED + 12288, L_SM = L_SC + 2048, L_CNT = L_SM + 512, L_VX = L, L_ST = L + 32768;
    const unsigned cB1 = L_CNT, cB2 = L_CNT + 128, cBv = L_CNT + 256, cV2 = L_CNT + 384;
    const rsrc_t rs = mkrs(a.ring, RING_BYTES);
    unsigned* const err = a.ctl + C_ERR;
    const v4u ones = {~0u, ~0u, ~0u, ~0u};
    if (tid < 128) lp<unsigned>(L_CNT)[tid] = 0;
#pragma unroll
    for (int i = 0; i < 4; ++i) STB(ones, rs, c * 32768 + i * 8192 + tid * 16, 0);
    const unsigned stg = L + w8 * 16384;
    const int prr = 16 * (q >> 1) + 4 * (q & 1) + ((n >> 2) & 1) + 8 * (n >> 3);
    auto stage_w = [&](const bf16_t* W) {
        W += (size_t)256 * w * HD + h * DH + 32 * jj;
#pragma unroll 1
        for (int ch = 0; ch < 16; ++ch) gldsc(W + (size_t)16 * ch * HD, (l >> 2) * (HD * 2) + (l & 3) * 16, stg + ch * 1024);
        WAITV(0);
    };
    auto read_w = [&](v8u (&d)[2][4]) {
#pragma unroll
        for (int ct = 0; ct < 2; ++ct)
#pragma unroll
            for (int i = 0; i < 4; ++i) { unsigned p = stg + (64 * i + prr) * 64 + ct * 32 + (l & 3) * 8; d[ct][i] = cat(trd(p), trd(p + 128), trd(p + 2048), trd(p + 2048 + 128)); }
        WAITL();
    };
    const int six = hf ? 0xEEEE : 0x4444;
    const rsrc_t rsm = mkrs(a.mem0, MM * DM * 2);
    const int pos = l >> 3, prr8 = (pos >> 1) + 4 * (pos & 1);
    unsigned cal;
    u64 cnts[4] = {}; (void)cnts;
    auto phA = [&](int lg) { return ((lg >> 13) << 14) + (int)((cal & 1) << 13) + (lg & 8191); };
    auto phB = [&](int lg) { return CHB + ((lg >> 13) << 14) + (int)((cal & 2) << 12) + (lg & 8191); };
    auto h1o = [&](int inner) { return phB(h * 6144 + inner); };
    auto arrive = [&]() {
        if (tid == 192) __hip_atomic_fetch_add(a.ctl + C_READY, 1u, __ATOMIC_RELAXED, __HIP_MEMORY_SCOPE_AGENT);
    };
    auto start = [&]() {
        if (w8 == 3) {
            flags_wait(a.ctl + C_READY, 1, 128 + 32, l, err);
            unsigned v = gld(a.ctl + C_CAL + (l & 3));
            unsigned t = (__builtin_amdgcn_readlane(v, 1) < __builtin_amdgcn_readlane(v, 0) ? 1u : 0u) | (__builtin_amdgcn_readlane(v, 3) < __builtin_amdgcn_readlane(v, 2) ? 2u : 0u);
            lp<unsigned>(L_CNT + 448)[0] = t; WAITL();
        }
        BAR();
        cal = RFL(ldsr(L_CNT + 448));
    };

    if (w8 < 4) {
        v8u wA[2][2][4];
        stage_w(a.Wq); BAR(); arrive(); read_w(wA[0]);
        stage_w(a.Wk); read_w(wA[1]);
        MARK(2);
        BAR();
        const unsigned aRedR = L_RED + (w * 128 + 2 * l) * 4, redW = L_RED + ((w * 48 + q) * 16 + n) * 4;
        const unsigned scW = w < 2 ? L_SC + (hf ? 256 + l * 4 : ((w * 4 + q) * 8 + row) * 4) * 4 : ((q >> 1) == hf ? L_SM + ((hf * 8 + row) * 8 + (q & 1) * 4) * 4 : L_SC + (256 + l * 4) * 4);
        auto run = [&](auto role_) {
        constexpr int ROLE = decltype(role_)::value;
        v4u kx[4]; unsigned fl = 0;
        const unsigned char* xp = a.qkv + h * 24576 + (w & 1) * 4096;
        const unsigned* fG = a.ctl + C_G + h * 32;
        auto xload = [&](int sk, int sv) {
            if (__builtin_amdgcn_ballot_w64(fl <= (unsigned)sv) != 0) flags_wait(fG, 2, sv + 1, l, err);
            glds1<4>(xp + (size_t)sv * (NH * 24576) + 8192, l * 16, L_VX + (sv & 1) * 8192 + w * 4096);
            asm volatile("global_load_dwordx4 %0, %5, %6 sc1\n\tglobal_load_dwordx4 %1, %5, %6 offset:1024 sc1\n\tglobal_load_dwordx4 %2, %5, %6 offset:2048 sc1\n\tglobal_load_dwordx4 %3, %5, %6 offset:3072 sc1\n\t"
                         "global_load_dword %4, %7, %8 sc1\n\ts_waitcnt vmcnt(0)"
                         : "=&v"(kx[0]), "=&v"(kx[1]), "=&v"(kx[2]), "=&v"(kx[3]), "=&v"(fl) : "v"(l * 16), "s"(xp + (size_t)sk * (NH * 24576)), "v"((l & 1) * 4), "s"(fG) : "memory");
        };
        if (ROLE == 0) { xload(0, 0); xload(0, 1); }
        start();
        MARK(0);
        int o1[ROLE == 2 ? 4 : 2], ld2[4], st3, off3[ROLE == 3 ? 5 : 4], o3[ROLE == 3 ? 5 : 4];
        int st1 = h1o((w >> 1) * 2048 + (4 * jj + 2 * (w & 1) + ((l >> 2) & 1)) * 128 + prr8 * 16); if (l & 3) st1 = OOB;
#pragma unroll
        for (int i = 0; i < 2; ++i) { if constexpr (ROLE == 2) { o1[2 * i] = h1o((8 * i + 2 * q + hf) * 128 + row * 16); o1[2 * i + 1] = h1o(2048 + (8 * i + 2 * q + hf) * 128 + row * 16); } else o1[i] = h1o((8 * i + 2 * q + hf) * 128 + row * 16); }
#pragma unroll
        for (int i = 0; i < 4; ++i) { ld2[i] = phA(c * 4096 + i * 1024 + l * 16); off3[i] = phB(L_H3 + g * 16384 + (32 * w + 8 * i + 2 * q + hf) * 128 + row * 16); o3[i] = (row * DM + 256 * w + 64 * i + 16 * q + 8 * hf) * 2; }
        if constexpr (ROLE == 3) { off3[4] = phB(L_TS + g * 128); o3[4] = OOB; }
        st3 = l < 32 ? phB(L_H3 + (l >> 3) * 16384 + c * 128 + row * 16) : (c == 0 && l < 36 ? phB(L_TS + (l & 3) * 128) : OOB);
        const int in1a = h1o((l >> 5) * 2048 + jj * 512 + (l & 31) * 16), in1b = l < 32 ? h1o(4096 + jj * 512 + l * 16) : OOB;
        auto kvcopy = [&](int s) {
            const rsrc_t rk = mkrs(a.kvm, NSEG * NH * 4096);
            const int o = (l & 3) ? OOB : h * 4096 + (4 * jj + ((l >> 2) & 1)) * 128 + prr8 * 16;
#pragma unroll
            for (int t = 0; t < 4; ++t) STB(quad(poll_red(t < 2 ? cB1 : cBv, L_RED + ((2 + t) * 128 + 2 * l) * 4, 4u * (s + 1))), rk, o, s * (NH * 4096) + (t >> 1) * 2048 + (t & 1) * 256);
            WAITV(0);
            if (l == 0) gst(a.ctl + C_M + h * 32 + jj, s + 1);
        };
        v4u sel;
#pragma unroll
        for (int i = 0; i < 4; ++i) sel[i] = (row >> 1) == i ? 0x3F80u << (16 * (row & 1)) : 0u;
        __builtin_amdgcn_s_setprio(3);
        int so = 0, sp = SLOT;
        unsigned budget = SPIN_CAP, tb = rt16() - (1u << 20), add = 0;
        rsrc_t r3 = rsm; int s3 = 0, w3 = 0, x3 = SLOT3, y3 = 2 * SLOT3;
        for (int s = 0;; ++s) {
            u64 stv[11] = {};
            unsigned n4 = 4u * (s + 1);
            asm volatile("" : "+s"(n4));
            STAMP(0);
            constexpr int N3 = ROLE == 3 ? 5 : 4;
            v4u mb[N3];
            if (ROLE == 3 && s) sched(tb);
            BAR();
            STAMP(1);
            ldn<N3>(r3, o3, s3, mb);
            FENCE();
            frag_cd acc[4];
            for (;;) {
#pragma unroll
                for (int m = 0; m < 4; ++m) { acc[m] = frag_cd{}; asm volatile("" : "+v"(acc[m])); }
                SB();
                unsigned mxs = 0;
#pragma unroll
                for (int i = 0; i < 4; ++i) {
                    acc[0] = SMF(mb[i], wA[0][0][i], acc[0], six); mxs = umax3(mxs, mb[i].x, mb[i].y); SB();
                    acc[1] = SMF(mb[i], wA[0][1][i], acc[1], six); mxs = umax3(mxs, mb[i].z, mb[i].w); asm volatile("" : "+v"(mxs)); SB();
                    acc[2] = SMF(mb[i], wA[1][0][i], acc[2], six); if (ROLE == 3 && i == 3) mxs = umax3(mxs, mb[N3 - 1].x, mb[N3 - 1].y); SB();
                    acc[3] = SMF(mb[i], wA[1][1][i], acc[3], six); SB(); }
                if (__builtin_expect(__builtin_amdgcn_ballot_w64(mxs == 0xFFFFFFFFu) == 0, 1)) break;
                CNT(2); retry<N3>(r3, o3, s3, mb, err, budget);
            }
            STAMP(2);
#pragma unroll
            for (int m = 0; m < 4; ++m) {
                float x0 = tadd32(acc[m][0], acc[m][1]), x1 = tadd32(acc[m][2], acc[m][3]);
                lp<float>(redW)[m * 128] = x0; lp<float>(redW)[m * 128 + 64] = x1;
            }
            FENCE();
            ldsadd(cB1);
            if (s == NSEG - 1) { if (ROLE == 2) kvcopy(s); break; }
            STB(quad(poll_red(cB1, aRedR, n4)), rs, st1, so);
            STAMP(3);
            if (ROLE == 3) {
                if (s) { tb = RFL(mb[N3 - 1].x); add = RFL(mb[N3 - 1].y); }
                sched(tb + a.d1 + add);
            }
            BAR();
            STAMP(4);
            if constexpr (ROLE == 0) {
                v4u qv[2];
                ldn<2>(rs, o1, so, qv);
                frag_cd sl, sh;
                for (;;) {
                    sl = frag_cd{}; sh = frag_cd{};
#pragma unroll
                    for (int i = 0; i < 2; ++i) { sl = MFMA(kx[2 * i], qv[i], sl); sh = MFMA(kx[2 * i + 1], qv[i], sh); }
                    if (__builtin_expect(!bad<2>(qv), 1)) break;
                    CNT(0); retry<2>(rs, o1, so, qv, err, budget);
                }
                frag_cd r;
#pragma unroll
                for (int e = 0; e < 4; ++e) r[e] = sl[e] + dppf<ROR8>(sh[e]);
                *lp<frag_cd>(scW) = r;
                FENCE();
                ldsadd(cB2);
                STAMP(5);
                xload(s + 1, min(s + 2, NSEG - 1));
                STAMP(6); STAMP(7); STAMP(8); STAMP(9); STAMP(10);
            } else if constexpr (ROLE == 2) {
                v4u qk[4];
                ldn<4>(rs, o1, so, qk);
                frag_cd sm;
                for (;;) {
                    sm = frag_cd{}; sm = MFMA(qk[1], qk[0], sm); sm = MFMA(qk[3], qk[2], sm);
                    if (__builtin_expect(!bad<4>(qk), 1)) break;
                    CNT(0); retry<4>(rs, o1, so, qk, err, budget);
                }
                *lp<frag_cd>(scW) = sm;
                FENCE();
                ldsadd(cB2);
                STAMP(5);
                kvcopy(s);
                STAMP(6); STAMP(7); STAMP(8); STAMP(9); STAMP(10);
            } else {
                wait_t(tb + a.d1 + add + (a.dinit & 0xffff));
#pragma unroll
                for (int i = 0; i < 4; ++i) STB(ones, rs, ld2[i], sp);
                STB(ones, rs, in1a, sp); STB(ones, rs, in1b, sp);
                STB(ones, rs, st3, x3);
                STAMP(5);
                poll_cnt(cV2, n4);
                STAMP(6); STAMP(7);
                const unsigned t2 = tb + a.d2 + 2 * add;
                sched(t2);
                STAMP(8);
                v4u v[4];
                ldn<4>(rs, ld2, so, v);
                frag_cd y; unsigned tn, an;
                for (;;) {
                    const int late = (int)(rt16() - t2);
                    tn = tb + a.per + 3 * add; an = add;
                    if (late > SLIP_THR) { tn += late + SLIP_ADD; if (s) an = min(add + SLIP_INC, 4000u); }
                    asm volatile("" : "+s"(tn), "+s"(an)); SB();
                    y = frag_cd{};
#pragma unroll
                    for (int i = 0; i < 4; ++i) y = MFMA(sel, v[i], y);
                    if (__builtin_expect(!bad<4>(v), 1)) break;
                    CNT(1); retry<4>(rs, ld2, so, v, err, budget);
                }
                STAMP(9);
#pragma unroll
                for (int e = 0; e < 4; ++e) y[e] += dppf<ROR8>(y[e]);
                v4u oo = pack_swap(y, y);
                oo.x = l < 32 ? oo.x : tn & ~1u; oo.y = l < 32 ? oo.y : an;
                STB(oo, rs, st3, w3);
                STAMP(10);
                tb += a.per + 3 * add;
            }
            STAMPS_FLUSH();
            r3 = rs; s3 = w3; w3 = x3; x3 = y3; y3 = s3;
            { int t = sp; sp = so; so = t; }
#pragma unroll
            for (int i = 0; i < N3; ++i) o3[i] = off3[i];
        }
        };
        if (w < 2) run(std::integral_constant<int, 0>{}); else if (w == 2) run(std::integral_constant<int, 2>{}); else run(std::integral_constant<int, 3>{});
    } else {
        v8u wV[2][4], wO[4][2];
        stage_w(a.Wv); BAR(); read_w(wV);
        { const bf16_t* Wo = a.Wo + (size_t)h * DH * DM + 256 * jj + 64 * w;
#pragma unroll 1
          for (int i = 0; i < 16; ++i) gldsc(Wo + (size_t)8 * i * DM, (l >> 3) * (DM * 2) + (l & 7) * 16, stg + i * 1024);
          WAITV(0);
#pragma unroll
          for (int t = 0; t < 4; ++t)
#pragma unroll
              for (int i = 0; i < 2; ++i) { unsigned p = stg + (64 * i + 8 * q + (n >> 2)) * 128 + (t >> 1) * 64 + (l & 3) * 16 + (t & 1) * 8; wO[t][i] = cat(trd(p), trd(p + 512), trd(p + 4096), trd(p + 4096 + 512)); }
          WAITL(); }
        BAR();
        start();
        v2u ua[4][2];
        auto ucalc = [&](int s) {
#pragma unroll
            for (int kt = 0; kt < 2; ++kt) {
                v4u vx[4];
#pragma unroll
                for (int ks = 0; ks < 4; ++ks) vx[ks] = *lp<v4u>(L_VX + (s & 1) * 8192 + (kt * 4 + ks) * 1024 + l * 16);
#pragma unroll
                for (int t = 0; t < 4; ++t) { frag_cd u = {};
#pragma unroll
                    for (int ks = 0; ks < 4; ++ks) { v4u b = {wO[t][ks >> 1][4 * (ks & 1)], wO[t][ks >> 1][4 * (ks & 1) + 1], wO[t][ks >> 1][4 * (ks & 1) + 2], wO[t][ks >> 1][4 * (ks & 1) + 3]}; u = MFMA(vx[ks], b, u); }
                    ua[t][kt] = v2u{cvt_pk(u[0], u[1]), cvt_pk(u[2], u[3])}; }
            }
        };
        ucalc(0);
        const int phf = (n >> 2) & 1, prow = (phf ? 3 - (n & 3) : (n & 3)) + 4 * hf;
        const int st1 = (l & 3) ? OOB : h1o(2 * 2048 + (4 * jj + 2 * (w & 1) + ((l >> 2) & 1)) * 128 + prr8 * 16);
        int o1[2], off3[4], o3[4];
#pragma unroll
        for (int i = 0; i < 2; ++i) o1[i] = h1o(2 * 2048 + (8 * i + 2 * q + hf) * 128 + row * 16);
#pragma unroll
        for (int i = 0; i < 4; ++i) { off3[i] = phB(L_H3 + g * 16384 + (32 * w + 8 * i + 2 * q + hf) * 128 + row * 16); o3[i] = (row * DM + 256 * w + 64 * i + 16 * q + 8 * hf) * 2; }
        const int st2 = phA((32 * jj + 8 * w + 4 * phf + q) * 4096 + h * 128 + prow * 16);
        const unsigned aRedR = L_RED + ((4 + (w & 1)) * 128 + pos * 16 + 4 * (l & 3) + 2 * ((l >> 2) & 1)) * 4;
        const unsigned aSc = L_SC + ((phf * 4 + q) * 8 + prow) * 16, aSm = L_SM + (prow * 8 + 4 * (q & 1) + 2 * phf) * 4;
        const unsigned redW = L_RED + ((w * 48 + 32 + q) * 16 + n) * 4;
        int so = 0, sp = SLOT;
        unsigned budget = SPIN_CAP;
        rsrc_t r3 = rsm; int s3 = 0, w3 = 0, x3 = SLOT3, y3 = 2 * SLOT3;
        for (int s = 0;; ++s) {
            u64 stv[11] = {};
            unsigned n4 = 4u * (s + 1), n3 = 3u * (s + 1);
            asm volatile("" : "+s"(n4), "+s"(n3));
            STAMP(0);
            v4u mb[4];
            __builtin_amdgcn_s_setprio(2);
            BAR();
            asm volatile("s_nop 15\n\ts_nop 15\n\ts_nop 15" ::: "memory");
            STAMP(1);
            ldn<4>(r3, o3, s3, mb);
            FENCE();
            frag_cd acc[2];
            for (;;) {
#pragma unroll
                for (int m = 0; m < 2; ++m) { acc[m] = frag_cd{}; asm volatile("" : "+v"(acc[m])); }
                SB();
                unsigned mxs = 0;
#pragma unroll
                for (int i = 0; i < 4; ++i) {
                    acc[0] = SMF(mb[i], wV[0][i], acc[0], six); mxs = umax3(mxs, mb[i].x, mb[i].y); SB();
                    acc[1] = SMF(mb[i], wV[1][i], acc[1], six); mxs = umax3(mxs, mb[i].z, mb[i].w); asm volatile("" : "+v"(mxs)); SB(); }
                if (__builtin_expect(__builtin_amdgcn_ballot_w64(mxs == 0xFFFFFFFFu) == 0, 1)) break;
                CNT(2); retry<4>(r3, o3, s3, mb, err, budget);
            }
            STAMP(2);
#pragma unroll
            for (int m = 0; m < 2; ++m) {
                float x0 = tadd32(acc[m][0], acc[m][1]), x1 = tadd32(acc[m][2], acc[m][3]);
                lp<float>(redW)[m * 128] = x0; lp<float>(redW)[m * 128 + 64] = x1;
            }
            FENCE();
            ldsadd(cBv);
            if (s == NSEG - 1) break;
            if (w < 2) STB(quad(poll_red(cBv, aRedR, n4)), rs, st1, so);
            STAMP(3);
            __builtin_amdgcn_s_setprio(3);
            BAR();
            asm volatile("s_nop 15\n\ts_nop 15\n\ts_nop 15\n\ts_nop 15" ::: "memory");
            STAMP(4);
            v4u vm[2];
            ldn<2>(rs, o1, so, vm);
            frag_cd z[4];
            for (;;) {
#pragma unroll
                for (int t = 0; t < 4; ++t) z[t] = frag_cd{};
#pragma unroll
                for (int i = 0; i < 2; ++i)
#pragma unroll
                    for (int t = 0; t < 4; ++t) z[t] = SMF(vm[i], wO[t][i], z[t], six);
                if (__builtin_expect(!bad<2>(vm), 1)) break;
                CNT(0); retry<2>(rs, o1, so, vm, err, budget);
            }
            STAMP(5);
            frag_cd sx; v2f s4a, s4b;
            { unsigned cv; int cap = LCAP;
              asm volatile("1:\n\tds_read_b32 %0, %5\n\tds_read_b128 %1, %6\n\tds_read_b64 %2, %7\n\tds_read_b64 %3, %7 offset:256\n\t"
                           "s_sub_u32 %4, %4, 1\n\ts_cbranch_scc0 2f\n\ts_endpgm\n2:\n\ts_waitcnt lgkmcnt(3)\n\tv_cmp_gt_u32 vcc, %8, %0\n\ts_cbranch_vccnz 1b\n\ts_waitcnt lgkmcnt(0)"
                           : "=&v"(cv), "=&v"(sx), "=&v"(s4a), "=&v"(s4b), "+s"(cap) : "v"(cB2), "v"(aSc), "v"(aSm), "s"(n3) : "vcc", "scc", "memory"); }
            STAMP(6);
            float sv[6] = {sx[0], sx[1], sx[2], sx[3], s4a.x + s4b.x, s4a.y + s4b.y};
#pragma unroll
            for (int e = 0; e < 6; e += 2) { unsigned p = cvt_pk(sv[e], sv[e + 1]); p = cvt_pk(lo16(p) * SCALE, hi16(p) * SCALE); sv[e] = lo16(p); sv[e + 1] = hi16(p); }
            float mx = fmaxf(fmaxf(fmaxf(sv[0], sv[1]), sv[2]), fmaxf(fmaxf(sv[3], sv[4]), sv[5]));
            mx = fmaxf(mx, dppf<HMIR>(mx)); mx = tmax16(mx); mx = tmax32(mx);
            const float mk = -mx * LOG2E;
#pragma unroll
            for (int e = 0; e < 6; ++e) sv[e] = __builtin_amdgcn_exp2f(__builtin_fmaf(sv[e], LOG2E, mk));
            v4u pb = {cvt_pk(sv[0], sv[1]), cvt_pk(sv[2], sv[3]), cvt_pk(sv[4], sv[5]), 0u};
            frag_cd fa[4], fb[4];
#pragma unroll
            for (int t = 0; t < 4; ++t) {
                v4u Aa = {ua[t][0].x, ua[t][0].y, cvt_pk(z[t][0], z[t][1]), 0u};
                v4u Ab = {ua[t][1].x, ua[t][1].y, cvt_pk(z[t][2], z[t][3]), 0u};
                frag_cd zero = {};
                fa[t] = MFMA(Aa, pb, zero); fb[t] = MFMA(Ab, pb, zero);
            }
            float sum = __builtin_fmaf(sv[4] + sv[5], 0.5f, (sv[0] + sv[1]) + (sv[2] + sv[3]));
            sum += dppf<HMIR>(sum); sum = tadd16(sum, sum); sum = tadd32(sum, sum);
            const float inv = __builtin_amdgcn_rcpf(sum);
            v4u o;
#pragma unroll
            for (int t = 0; t < 2; ++t) {
                float r[4];
#pragma unroll
                for (int e = 0; e < 4; ++e) { float u = phf ? fb[2 + t][e] : fa[t][e], v = phf ? fb[t][e] : fa[2 + t][e]; r[e] = (u + dppf<HMIR>(v)) * inv; }
                o[2 * t] = cvt_pk(r[0], r[1]); o[2 * t + 1] = cvt_pk(r[2], r[3]);
            }
            STB(o, rs, st2, so);
            FENCE();
            ldsadd(cV2);
            STAMP(7);
            __builtin_amdgcn_s_setprio(0);
            ucalc(s + 1);
            FENCE();
            STAMP(8); STAMP(9); STAMP(10);
            STAMPS_FLUSH();
            r3 = rs; s3 = w3; w3 = x3; x3 = y3; y3 = s3;
            { int t = sp; sp = so; so = t; }
#pragma unroll
            for (int i = 0; i < 4; ++i) o3[i] = off3[i];
        }
    }
}

struct Tile { const bf16_t* W; int ld, wide; };
__device__ __forceinline__ void tile_issue(const Tile& T, int t, unsigned L, int w8, int l) {
    const bf16_t* p = T.W + (T.wide ? (w8 & 3) * 32 * T.ld + (2 * t + (w8 >> 2)) * 128 : (t * 256 + w8 * 32) * T.ld);
    const unsigned vo = (l >> 4) * T.ld * 2 + ((l & 15) ^ (2 * (l >> 4))) * 16;
#pragma unroll 1
    for (int i = 0; i < 8; ++i) gldsw(p + i * 4 * T.ld, vo ^ ((i & 2) << 6), L + (t & 1) * 65536 + (w8 * 8 + i) * 1024);
}
template <class F>
__device__ __forceinline__ void tile_load(const Tile& T, v4u (&wf)[32], unsigned L, int w8, int l, F next) {
    const int n = l & 15, q = l >> 4;
    const unsigned p = L + (8 * q + (n >> 2)) * 256 + ((4 * (w8 & 3) + ((l >> 1) & 1)) ^ (2 * ((n >> 2) | ((q & 1) << 2)))) * 16 + (l & 1) * 8;
    bool more = true;
#pragma unroll 1
    for (int hh = 0; hh < 2; ++hh)
#pragma unroll
    for (int u = 0; u < 2; ++u) {
        if (!more) WAITV(0);
        WAITV(8); BAR();
        if ((w8 >> 2) == hh) {
#pragma unroll
            for (int i = 0; i < 16; ++i) { const unsigned pp = (p ^ ((i & 1) * 32)) + u * 65536 + (i >> 1) * 8192; wf[u * 16 + i] = cat2(trd(pp), trd(pp + 1024)); }
            WAITL(); }
        BAR();
        if (hh == 0) tile_issue(T, u + 2, L, w8, l); else more = next(u);
    }
}
#define NB 8
#define GD 4
template <class F>
__device__ __forceinline__ void seg_gemm(const v4u (&wf)[32], unsigned b0, unsigned b1, frag_cd (&acc)[4], F side) {
    v4u f[2 * GD];
#define RD(ks, mt) asm volatile("ds_read_b128 %0, %1 offset:%2" : "=v"(f[((ks) % GD) * 2 + mt]) : "v"(((ks) & 1) ? b1 : b0), "n"(((ks) >> 1) * 4096 + mt * 2048) : "memory")
#define MM(ct, mt) do { if (ks == 0) asm volatile("v_mfma_f32_16x16x32_bf16 %0, %1, %2, 0" : "=&v"(acc[2 * ct + mt]) : "v"(wf[ct]), "v"(f[mt])); \
        else asm volatile("v_mfma_f32_16x16x32_bf16 %0, %1, %2, %0" : "+v"(acc[2 * ct + mt]) : "v"(wf[2 * ks + ct]), "v"(f[(ks % GD) * 2 + mt])); } while (0)
#pragma unroll
    for (int ks = 0; ks < GD - 1; ++ks) { RD(ks, 0); RD(ks, 1); }
#pragma unroll
    for (int ks = 0; ks < 16; ++ks) {
        side(2 * ks);
        asm volatile("s_waitcnt lgkmcnt(%0)" :: "n"(2 * (ks + GD - 1 <= 16 ? GD - 2 : 15 - ks)));
        MM(0, 0);
        if (ks + GD - 1 < 16) RD(ks + GD - 1, 0);
        MM(0, 1);
        side(2 * ks + 1);
        MM(1, 0);
        if (ks + GD - 1 < 16) RD(ks + GD - 1, 1);
        MM(1, 1);
    }
    asm volatile("s_nop 15" : "+v"(acc[0]), "+v"(acc[1]), "+v"(acc[2]), "+v"(acc[3]));
#undef RD
#undef MM
}
__device__ __forceinline__ void half_put(unsigned ex, int w8, int l, const frag_cd (&acc)[4], unsigned it) {
#pragma unroll
    for (int i = 0; i < 4; ++i) *lp<frag_cd>(ex + w8 * 4096 + i * 1024 + l * 16) = acc[i];
    WAITL();
    lp<unsigned>(ex + 16384)[w8] = it; FENCE();
}
__device__ __forceinline__ void half_get(unsigned ex, int w8, int l, frag_cd (&acc)[4], unsigned it) {
    for (int i = 0; ldsr(ex + 16384 + (w8 & 3) * 4) != it; ++i) { if (i > LCAP) endpgm(); }
    FENCE();
#pragma unroll
    for (int i = 0; i < 4; ++i) acc[i] += *lp<frag_cd>(ex + (w8 & 3) * 4096 + i * 1024 + l * 16);
}

__device__ __forceinline__ void gwork(const Args& a, const int m, const int h, const unsigned L) {
    const int tid = threadIdx.x, w8 = RFL(tid >> 6), l = tid & 63, n = l & 15, q = l >> 4;
    unsigned* const err = a.ctl + C_ERR;
    const unsigned L_AT = L + 131072, L_KM = L_AT + 8192, L_EX = L_KM + 4096, L_DEC = L_EX + 16384 + 16;
    if (tid < 4) lp<unsigned>(L_EX + 16384)[tid] = 0;
    unsigned* const fG = a.ctl + C_G + h * 32;
    if (m == 2) {
#pragma unroll 1
        for (int i = 0; i < 16; ++i) glds(a.x + (size_t)(64 * h + 8 * w8) * DM + i * 512, l * 16, L + (w8 * 16 + i) * 1024);
        v4u gw[2], wr[2];
#pragma unroll
        for (int i = 0; i < 2; ++i) { gw[i] = *(const v4u*)(a.gw + 512 * i + 8 * l); wr[i] = *(const v4u*)(a.wr + 512 * i + 8 * l); }
        WAITV(0);
        float res = 0.f;
#pragma unroll 1
        for (int t = 0; t < 8; ++t) { float sg = 0.f, sr = 0.f;
#pragma unroll
            for (int i = 0; i < 2; ++i) { const v4u xv = *lp<v4u>(L + (w8 * 16 + 2 * t + i) * 1024 + l * 16);
#pragma unroll
                for (int e = 0; e < 4; ++e) { const float x0 = lo16(xv[e]), x1 = hi16(xv[e]); const unsigned p = cvt_pk(x0 * lo16(gw[i][e]), x1 * hi16(gw[i][e]));
                    sg += lo16(p) + hi16(p); sr = __builtin_fmaf(x0, lo16(wr[i][e]), __builtin_fmaf(x1, hi16(wr[i][e]), sr)); } }
            float v = tadd32(sg, sr);
            v = tadd16(v, v); v += dppf<ROR8>(v); v += dppf<0x124>(v); v += dppf<0x122>(v); v += dppf<0x121>(v);
            if ((l & 7) == t) res = v; }
        const unsigned rb = cvt_pk(res, 0.f), tok = 64 * h + 8 * w8 + (l & 7);
        const unsigned val = l < 32 ? __float_as_uint(rbf(1.0f / (1.0f + __expf(-lo16(rb))))) : (((rb & 0x8000u) ? ~rb : rb | 0x8000u) << 16) | (4095u - tok);
        if ((l & 31) < 8) gst(a.gate + (l >> 5) * S_TOK + tok, val);
        WAITV(0); BAR();
        if (tid == 0) gst(a.ctl + C_R + h, 1u);
        MARK(90);
        if (w8 == 0) flags_wait(fG, 1, 1, l, err);
        BAR();
    }
    const unsigned xo = (l >> 3) * (DM * 2) + ((l & 7) ^ q) * 16;
    auto xpiece = [&](int s, int i) { glds(a.x + (size_t)(32 * s + 8 * (i & 3)) * DM + (i >> 2) * 64, xo ^ ((i & 1) * 64), L + (s & 1) * 65536 + i * 1024); };
    v4u wf[32];
    { const Tile T{(m == 0 ? a.Wk : (m == 1 ? a.Wv : a.Wq)) + h * DH, HD, 0};
      tile_issue(T, 0, L, w8, l); tile_issue(T, 1, L, w8, l);
      tile_load(T, wf, L, w8, l, [&](int b) { if (b == 0) { _Pragma("unroll 2") for (int i = 0; i < 8; ++i) xpiece(0, w8 * 8 + i); } return true; }); }
    MARK(0);
    const unsigned rb0 = L + (w8 >> 2) * 32768 + (n >> 3) * 1024 + (n & 7) * 128 + (q ^ (n >> 1)) * 16, rb1 = rb0 ^ 64;
    const rsrc_t rq = mkrs(a.qkv, NSEG * NH * 24576), ro_ = mkrs(a.ox, NSEG * 262144);
    int sto; { int ck = 4 * (w8 & 3) + (q >> 1), mt = q & 1; int i = m == 1 ? ck >> 2 : 2 * (ck >> 3) + (ck & 1), qq = m == 1 ? ck & 3 : (ck >> 1) & 3; sto = h * 24576 + m * 8192 + ((mt * 4 + i) * 64 + qq * 16 + n) * 16; }
    const unsigned* fM = a.ctl + C_M + h * 32; unsigned* const fA = a.ctl + C_A + (h >> 3) * 32 + m * 8 + (h & 7);
    const unsigned* fD = (l >= 8 && l < 11 && l - 8 != m) ? fG + (l - 8) : fM + (l & 3);
    const int rt = w8 & 1, eh = (w8 >> 1) & 1;
    const int stx = (h >> 3) * 65536 + ((h & 7) * 2 + eh) * 4096 + rt * 2048 + ((2 * (q & 1) + (q >> 1)) * 16 + n) * 16;
    auto vissue = [&](int s) { glds1<2>(a.qkv + ((size_t)s * NH + h) * 24576 + 8192 + (w8 & 3) * 2048, l * 16, L_AT + (w8 & 3) * 2048); };
    struct KQ { v4u k[8], q[4]; };
    auto kqload = [&](int s) { KQ r;
#pragma unroll
        for (int i = 0; i < 4; ++i) { const int so = (s * NH + h) * 24576; r.q[i] = __builtin_amdgcn_raw_buffer_load_b128(rq, l * 16 + i * 1024, so + 16384 + rt * 4096, 16);
            r.k[i] = __builtin_amdgcn_raw_buffer_load_b128(rq, l * 16 + i * 1024, so, 16); r.k[4 + i] = __builtin_amdgcn_raw_buffer_load_b128(rq, l * 16 + i * 1024, so + 4096, 16); }
        FENCE();
        return r; };
    auto attend = [&](int s, const KQ& kq) {
        frag_cd sc[3] = {};
#pragma unroll
        for (int i = 0; i < 4; ++i) {
            sc[0] = MFMA(kq.k[i], kq.q[i], sc[0]);
            sc[1] = MFMA(kq.k[4 + i], kq.q[i], sc[1]);
            sc[2] = MFMA(*lp<v4u>(L_KM + (8 * (i >> 1) + 2 * q + (i & 1)) * 128 + (n & 7) * 16), kq.q[i], sc[2]);
        }
        v4u vf[4]; v2u vm[4];
#pragma unroll
        for (int et = 0; et < 4; ++et) { const int ck = 8 * eh + 2 * et + ((l >> 1) & 1);
            const unsigned p = L_AT + (ck * 16 + 4 * q + (n >> 2)) * 16 + (l & 1) * 8;
            vf[et] = cat2(trd(p), trd(p + 4096)); vm[et] = trd(L_KM + 2048 + ck * 128 + (4 * (q & 1) + (n >> 2)) * 16 + (l & 1) * 8); }
        float mx = -INFINITY;
#pragma unroll
        for (int t = 0; t < 3; ++t)
#pragma unroll
            for (int e = 0; e < 4; e += 2) { unsigned p = cvt_pk(sc[t][e], sc[t][e + 1]); p = cvt_pk(lo16(p) * SCALE, hi16(p) * SCALE); float v0 = lo16(p), v1 = hi16(p);
                if (t < 2 && 16 * t + 4 * q + e > 16 * rt + n) v0 = -999292928.0f;
                if (t < 2 && 16 * t + 4 * q + e + 1 > 16 * rt + n) v1 = -999292928.0f;
                sc[t][e] = v0; sc[t][e + 1] = v1; mx = fmaxf(fmaxf(mx, v0), v1); }
        mx = tmax16(mx); mx = tmax32(mx);
        float sum = 0.f;
#pragma unroll
        for (int t = 0; t < 3; ++t)
#pragma unroll
            for (int e = 0; e < 4; ++e) { float v = __builtin_amdgcn_exp2f((sc[t][e] - mx) * LOG2E); if (t == 2 && q >= 2) v = 0.f; sc[t][e] = v; sum += v; }
        sum = tadd16(sum, sum); sum = tadd32(sum, sum);
        const float inv = __builtin_amdgcn_rcpf(sum);
        const v4u px = {cvt_pk(sc[0][0] * inv, sc[0][1] * inv), cvt_pk(sc[0][2] * inv, sc[0][3] * inv), cvt_pk(sc[1][0] * inv, sc[1][1] * inv), cvt_pk(sc[1][2] * inv, sc[1][3] * inv)};
        const v4u pm = {cvt_pk(sc[2][0] * inv, sc[2][1] * inv), cvt_pk(sc[2][2] * inv, sc[2][3] * inv), 0u, 0u};
        frag_cd o[4];
#pragma unroll
        for (int et = 0; et < 4; ++et) { o[et] = MFMA(vf[et], px, (frag_cd{})); o[et] = MFMA((v4u{vm[et].x, vm[et].y, 0u, 0u}), pm, o[et]); }
        MARK(65 + s / 3); FENCE();
        STB(pack_swap(o[0], o[1]), ro_, stx, s * 262144); STB(pack_swap(o[2], o[3]), ro_, stx + 1024, s * 262144);
    };
    int sa = m, staged = 0;
    unsigned pacc[8] = {}, plast = 0; (void)pacc; (void)plast;
    PT(7);
    if (w8 >= 4) {
        int pend = 0, puba = 0;
        unsigned fv = 0;
        for (int sg = 0; sg < NSEG; ++sg) {
            const int done = sg < 4 ? sg : sg - 1;
            if (sg < 4) WAITV(0);
            if (w8 == 4) { bool ok = !staged && sa < done && __builtin_amdgcn_ballot_w64(fv <= (unsigned)sa) == 0; if (l == 0) lp<unsigned>(L_DEC)[sg & 1] = ok; WAITL(); }
            PT(0);
            BAR();
            PT(1);
            __builtin_amdgcn_s_setprio(2);
            if (tid == 256) { gst(fG + m, done); if (puba) gst(fA, puba); }
            puba = pend; pend = 0;
            if (staged) { pend = sa + 1; sa += 3; }
            staged = RFL(ldsr(L_DEC + 4 * (sg & 1)));
            if (staged) { vissue(sa); glds1<1>(a.kvm + ((size_t)sa * NH + h) * 4096 + (w8 & 3) * 1024, l * 16, L_KM + (w8 & 3) * 1024); }
            if (w8 == 4) fv = gld(fD);
            FENCE();
            const int sn = sg + 1 < NSEG ? sg + 1 : sg - 1;
            frag_cd acc[4];
            seg_gemm(wf, rb0 + (sg & 1) * 65536, rb1 + (sg & 1) * 65536, acc, [&](int i) { if (i < 2 * NB && !(i & 1)) xpiece(sn, (w8 & 3) * 16 + i / 2); if (i == 2 * NB) __builtin_amdgcn_s_setprio(0); });
            PT(2);
            half_get(L_EX, w8, l, acc, sg + 1);
            WAITV(0);
            STB(pack_swap(acc[0], acc[1]), rq, sto, sg * (NH * 24576)); STB(pack_swap(acc[2], acc[3]), rq, sto + (m == 1 ? 512 : 256), sg * (NH * 24576));
            PT(3);
        }
        PFLUSH();
        WAITV(0); BAR();
        if (tid == 256) { gst(fG + m, NSEG); if (pend | puba) gst(fA, pend | puba); }
        for (; sa < NSEG; sa += 3) {
            if (!staged) {
                if (m != 1) flags_wait(fG + 1, 1, sa + 1, l, err);
                vissue(sa);
                if (w8 == 4) { flags_wait(fM, 4, sa + 1, l, err); glds1<4>(a.kvm + ((size_t)sa * NH + h) * 4096, l * 16, L_KM); }
                WAITV(0); BAR();
            }
            staged = 0;
            BAR();
            if (tid == 256) gst(fA, sa + 1);
        }
    } else {
        __builtin_amdgcn_s_setprio(1);
        WAITV(0);
        for (int sg = 0; sg < NSEG; ++sg) {
            PT(0);
            BAR();
            PT(1);
            const int now = staged;
            staged = RFL(ldsr(L_DEC + 4 * (sg & 1)));
            frag_cd acc[4];
            const int sn = sg + 1 < NSEG ? sg + 1 : sg - 1;
            auto side = [&](int i) { if (i < 2 * (16 - NB) && !(i & 1)) xpiece(sn, w8 * 16 + NB + i / 2); };
            if (now) {
                const KQ kq = kqload(sa);
                seg_gemm(wf, rb0 + (sg & 1) * 65536, rb1 + (sg & 1) * 65536, acc, side);
                half_put(L_EX, w8, l, acc, sg + 1);
                attend(sa, kq);
                WAITV(2);
                sa += 3;
                PT(4);
            } else {
                seg_gemm(wf, rb0 + (sg & 1) * 65536, rb1 + (sg & 1) * 65536, acc, side);
                PT(2);
                half_put(L_EX, w8, l, acc, sg + 1);
                WAITV(0);
                PT(3);
            }
            MARK(sg + 1);
        }
        PFLUSH();
        WAITV(0); BAR();
        for (; sa < NSEG; sa += 3) {
            if (!staged) { if (m != 0) flags_wait(fG, 1, sa + 1, l, err); if (m != 2) flags_wait(fG + 2, 1, sa + 1, l, err); }
            const KQ kq = kqload(sa);
            if (!staged) BAR();
            staged = 0;
            attend(sa, kq);
            WAITV(0); BAR();
        }
    }
}

__device__ __forceinline__ void owork(const Args& a, const int nt, const int kq, const unsigned L) {
    const int tid = threadIdx.x, w8 = RFL(tid >> 6), l = tid & 63, n = l & 15, q = l >> 4, j = nt * 4 + kq;
    unsigned* const err = a.ctl + C_ERR;
    const unsigned L_XS = L + 131072, L_EX = L_XS, L_HS = L_XS + 16384 + 64, L_KEY = L_HS + 2048, L_Y = L_KEY + 64, L_GU = L_Y + 1024;
    if (tid < 4) lp<unsigned>(L_EX + 16384)[tid] = 0;
    const rsrc_t rg = mkrs(a.gate, 2 * S_TOK * 4), ry = mkrs(a.ypart, 32 * 28 * 1024);
    if (w8 == 0) {
        unsigned char* p = a.ring + ((l >> 1) & 1) * CHB + (l & 1) * 8192 + j * 16384 + ((l >> 2) & 3) * 2048 + 1024;
        unsigned best = 0;
        if (l < 16) gst((unsigned*)p, ~0u);
        WAITV(0);
#pragma unroll 1
        for (int i = 0; i < 16; ++i) { u64 t1, t2; unsigned v;
            const u64 pi = ((u64)__builtin_amdgcn_readlane((unsigned)((u64)p >> 32), i) << 32) | (unsigned)__builtin_amdgcn_readlane((unsigned)(u64)p, i);
            asm volatile("s_nop 4\n\ts_memtime %0\n\ts_waitcnt lgkmcnt(0)\n\tglobal_load_dword %2, %3, %4 sc1\n\ts_waitcnt vmcnt(0)\n\ts_memtime %1\n\ts_waitcnt lgkmcnt(0)"
                         : "=&s"(t1), "=&s"(t2), "=&v"(v) : "v"(0u), "s"(pi) : "memory");
            if ((l & 3) == (i & 3)) best = max(best, ~(unsigned)(t2 - t1)); }
        if (l < 16) gst((unsigned*)p, ~0u);
        if (l < 4) __hip_atomic_fetch_max(a.ctl + C_CAL + l, best, __ATOMIC_RELAXED, __HIP_MEMORY_SCOPE_AGENT);
        WAITV(0);
        if (l == 0) __hip_atomic_fetch_add(a.ctl + C_READY, 1u, __ATOMIC_RELAXED, __HIP_MEMORY_SCOPE_AGENT);
        MARK(66);
        flags_wait(a.ctl + C_G + j * 32, 1, 1, l, err);
    } else if (w8 == 1) {
        for (int i = 0; !flags_ok(a.ctl + C_R, 32, 1, l); ++i) { __builtin_amdgcn_s_sleep(32); if (i > GCAP) fatal(err); }
        v4u kv[8]; unsigned prev = ~0u, mine = 0;
#pragma unroll
        for (int i = 0; i < 8; ++i) kv[i] = __builtin_amdgcn_raw_buffer_load_b128(rg, S_TOK * 4 + l * 16, i * 1024, 16);
#pragma unroll 1
        for (int c = 0; c < TOPC; ++c) { unsigned mx = 0;
            FENCE();
#pragma unroll
            for (int i = 0; i < 8; ++i)
#pragma unroll
                for (int e = 0; e < 4; ++e) mx = max(mx, kv[i][e] < prev ? kv[i][e] : 0u);
            mx = max(mx, dppu<0x128>(mx)); mx = max(mx, dppu<0x124>(mx)); mx = max(mx, dppu<0x122>(mx)); mx = max(mx, dppu<0x121>(mx));
            { v2u r = sw16(mx, mx); mx = max(r.x, r.y); r = sw32(mx, mx); mx = max(r.x, r.y); }
            prev = RFL(mx); if ((l & 7) == c) mine = prev; }
        if (l < 8) lp<unsigned>(L_KEY)[l] = mine;
#pragma unroll 1
        for (int i = 0; i < 16; ++i) glds(a.x + i * 64, (4095u - (mine & 4095u)) * (DM * 2) + (l >> 3) * 16, L_XS + i * 1024);
        WAITV(0); WAITL();
    }
    BAR();
    v4u wf[32];
    {
      auto tile = [&](int t) { return t < 2 ? Tile{(t ? a.wu : a.wg) + j * 128, FF, 0} : (t == 2 ? Tile{a.wd + (size_t)j * 128 * DM, DM, 1} : Tile{a.Wo + (size_t)kq * 1024 * DM + nt * 128, DM, 0}); };
      int ti = j < FF / 128 ? 0 : 3;
      Tile T = tile(ti);
      tile_issue(T, 0, L, w8, l); tile_issue(T, 1, L, w8, l);
      frag_cd gv[2] = {};
#pragma unroll 1
      for (;; ++ti) {
        const Tile N = tile(ti + 1);
        tile_load(T, wf, L, w8, l, [&](int b) { if (ti < 3) tile_issue(N, b, L, w8, l); return ti < 3; });
        T = N;
        if (ti == 3) break;
        if (ti < 2) {
            frag_cd acc[2] = {};
#pragma unroll
            for (int ks = 0; ks < 16; ++ks) { const v4u xf = *lp<v4u>(L_XS + ((w8 >> 2) * 16 + ks) * 512 + q * 128 + (n & 7) * 16); acc[0] = MFMA(wf[2 * ks], xf, acc[0]); acc[1] = MFMA(wf[2 * ks + 1], xf, acc[1]); }
            if (ti == 1) {
                frag_cd g = n < 8 ? gv[0] : gv[1], u = n < 8 ? acc[0] : acc[1];
                if (w8 >= 4) { *lp<frag_cd>(L_GU + (w8 & 3) * 2048 + l * 16) = g; *lp<frag_cd>(L_GU + (w8 & 3) * 2048 + 1024 + l * 16) = u; WAITL(); }
                BAR();
                if (w8 < 4) { g += *lp<frag_cd>(L_GU + w8 * 2048 + l * 16); u += *lp<frag_cd>(L_GU + w8 * 2048 + 1024 + l * 16);
                    float hv[4];
#pragma unroll
                    for (int e = 0; e < 4; ++e) { const float gg = rbf(g[e]); hv[e] = rbf(gg / (1.0f + __expf(-gg))) * rbf(u[e]); }
                    *lp<v2u>(L_HS + w8 * 512 + (2 * (n >> 3) + (q >> 1)) * 128 + (n & 7) * 16 + (q & 1) * 8) = v2u{cvt_pk(hv[0], hv[1]), cvt_pk(hv[2], hv[3])};
                    WAITL(); } }
            gv[0] = acc[0]; gv[1] = acc[1];
        } else {
#pragma unroll
            for (int cb = 0; cb < 4; ++cb) { frag_cd acc[2] = {};
#pragma unroll
                for (int i = 0; i < 4; ++i) { const v4u hf = *lp<v4u>(L_HS + i * 512 + q * 128 + (n & 7) * 16); acc[0] = MFMA(wf[8 * cb + 2 * i], hf, acc[0]); acc[1] = MFMA(wf[8 * cb + 2 * i + 1], hf, acc[1]); }
                STB(__builtin_bit_cast(v4u, n < 8 ? acc[0] : acc[1]), ry, ((w8 & 3) * 28 + j) * 1024 + ((n >> 3) * 32 + q * 8 + (n & 7)) * 16, ((w8 >> 2) * 4 + cb) * (4 * 28 * 1024)); }
        }
        MARK(67 + ti);
      }
      if (tid == 0) gst(a.ctl + C_Y + j, 1u);
    }
    MARK(0);
    const unsigned mykey = lp<unsigned>(L_KEY)[l & 7];
    if (w8 == 0) {
        flags_wait(a.ctl + C_Y, FF / 128, 1, l, err);
        frag_cd y = {};
#pragma unroll 1
        for (int i = 0; i < 28; i += 7) { v4u t[7];
#pragma unroll
            for (int u = 0; u < 7; ++u) t[u] = __builtin_amdgcn_raw_buffer_load_b128(ry, l * 16, (j * 28 + i + u) * 1024, 16);
#pragma unroll
            for (int u = 0; u < 7; ++u) y += __builtin_bit_cast(frag_cd, t[u]); }
        const float r = hi16((mykey & 0x80000000u) ? mykey & 0x7fffffffu : ~mykey), rs = rbf(1.0f / (1.0f + __expf(-r)));
#pragma unroll
        for (int e = 0; e < 4; ++e) y[e] = rbf(rbf(y[e]) * rs);
        *lp<frag_cd>(L_Y + l * 16) = y; WAITL();
    }
    const unsigned* fA = a.ctl + C_A + kq * 32; unsigned* const fP = a.ctl + C_P + nt * 32;
    const rsrc_t rp = mkrs(a.part, NSEG * 524288), rx = mkrs(a.x, S_TOK * DM * 2);
    const unsigned* fW = l < 32 ? fA + min(l, 23) : fP + (l & 3) * 4 + kq;
    const unsigned rb = L + (w8 >> 2) * 32768 + l * 16;
    unsigned fv = 0;
    auto a_ok = [&](int s) { return ((__builtin_amdgcn_ballot_w64(fv <= (unsigned)s) >> ((s % 3) * 8)) & 255) == 0; };
    auto p_ok = [&](int s) { return (__builtin_amdgcn_ballot_w64(fv <= (unsigned)s) >> 32) == 0; };
    unsigned pacc[8] = {}, plast = 0; (void)pacc; (void)plast;
    PT(7);
    if (w8 >= 4) {
        auto oissue = [&](int s) {
#pragma unroll
            for (int i = 0; i < 4; ++i) glds1<4>(a.ox + (size_t)s * 262144 + kq * 65536 + (w8 & 3) * 16384 + i * 4096, l * 16, L + (s & 1) * 65536 + (w8 & 3) * 16384 + i * 4096);
        };
        unsigned* const fO = fP + kq * 4 + (w8 & 3);
        const int stp = ((nt * 4 + (w8 & 3)) * 4 + kq) * 4096 + l * 16;
        int pre = 0;
        for (int s = 0; s < NSEG; ++s) {
            if (pre == s) {
                WAITV(0);
                if (l == 0) gst(fO, s);
                for (int i = 0;; ++i) { fv = gld(fW); WAITV(0); if (a_ok(s)) break; __builtin_amdgcn_s_sleep(2); if (i > GCAP) fatal(err); }
                PT(0);
                oissue(s); ++pre;
                WAITV(0);
                PT(1);
            }
            BAR();
            PT(2);
            const bool nx = s + 1 < NSEG && a_ok(s + 1);
            fv = gld(fW);
            FENCE();
            PT(3);
            const unsigned char* src = a.ox + (size_t)(s + 1) * 262144 + kq * 65536 + (w8 & 3) * 16384; const unsigned dst = L + ((s + 1) & 1) * 65536 + (w8 & 3) * 16384;
            frag_cd acc[4];
            __builtin_amdgcn_s_setprio(2);
            seg_gemm(wf, rb + (s & 1) * 65536, rb + (s & 1) * 65536 + 1024, acc, [&](int i) { if (i < 16 && nx) glds1<1>(src + i * 1024, l * 16, dst + i * 1024); if (i == 16) __builtin_amdgcn_s_setprio(0); });
            half_get(L_EX, w8, l, acc, s + 1);
            WAITV(0);
            if (l == 0) gst(fO, s);
#pragma unroll
            for (int i = 0; i < 4; ++i) STB(__builtin_bit_cast(v4u, acc[i]), rp, stp + i * 1024, s * 524288);
            pre += nx;
            PT(4);
        }
        WAITV(0);
        if (l == 0) gst(fO, NSEG);
    } else {
        const int fcol = nt * 128 + (2 * kq + (w8 >> 1)) * 16 + 4 * q, frow = 16 * (w8 & 1) + n;
        const unsigned seltok = 4095u - (mykey & 4095u);
        struct Fin { v4u p[4]; v2u xv; float gt; };
        auto fissue = [&](int s, bool on) { Fin f; int k = on ? 0 : 0x40000000; asm("" : "+s"(k));
#pragma unroll
            for (int i = 0; i < 4; ++i) f.p[i] = __builtin_amdgcn_raw_buffer_load_b128(rp, k + j * 16384 + w8 * 1024 + l * 16, s * 524288 + i * 4096, 16);
            f.xv = __builtin_amdgcn_raw_buffer_load_b64(rx, k + (frow * DM + fcol) * 2, s * 65536, 0);
            f.gt = __uint_as_float(__builtin_amdgcn_raw_buffer_load_b32(rg, k + frow * 4, s * 128, 16));
            return f;
        };
        auto finish = [&](int s, const Fin& f) {
            const int tok = 32 * s + frow; const float gt = f.gt, omg = rbf(1.0f - gt);
            float b[4] = {lo16(f.xv.x), hi16(f.xv.x), lo16(f.xv.y), hi16(f.xv.y)};
            for (unsigned mk = (unsigned)__builtin_amdgcn_ballot_w64((seltok >> 4) == (unsigned)(2 * s + (w8 & 1))) & 255u; mk; mk &= mk - 1) {
                const int slot = __builtin_ctz(mk);
                const frag_cd y = *lp<frag_cd>(L_Y + ((w8 >> 1) * 32 + q * 8 + slot) * 16);
                if (__builtin_amdgcn_readlane(seltok, slot) == (unsigned)tok) {
#pragma unroll
                    for (int e = 0; e < 4; ++e) b[e] = rbf(b[e] + y[e]); }
            }
            v2u o;
#pragma unroll
            for (int e = 0; e < 4; e += 2) { float t[2];
#pragma unroll
                for (int u = 0; u < 2; ++u) t[u] = (__uint_as_float(f.p[0][e + u]) + __uint_as_float(f.p[1][e + u])) + (__uint_as_float(f.p[2][e + u]) + __uint_as_float(f.p[3][e + u]));
                unsigned av = cvt_pk(t[0], t[1]); av = cvt_pk(gt * lo16(av), gt * hi16(av)); const unsigned bv = cvt_pk(omg * b[e], omg * b[e + 1]);
                o[e >> 1] = cvt_pk(lo16(av) + lo16(bv), hi16(av) + hi16(bv)); }
            return o;
        };
        auto ostore = [&](int s, v2u o) { *(v2u*)(a.out + (size_t)(32 * s + frow) * DM + fcol) = o; };
        int fin = 0;
        for (int s = 0; s <= NSEG; ++s) {
            for (int i = 0; fin < s && (s == NSEG || !a_ok(s)); ++i) {
                fv = gld(fW); WAITV(0);
                if (p_ok(fin)) { FENCE(); ostore(fin, finish(fin, fissue(fin, true))); ++fin; }
                else { __builtin_amdgcn_s_sleep(2); if (i > GCAP) fatal(err); }
            }
            if (s == NSEG) break;
            PT(0);
            BAR();
            PT(2);
            const bool fi = fin < s && p_ok(fin);
            const Fin f = fissue(fin, fi);
            fv = gld(fW);
            FENCE();
            PT(3);
            __builtin_amdgcn_s_setprio(1);
            frag_cd acc[4];
            seg_gemm(wf, rb + (s & 1) * 65536, rb + (s & 1) * 65536 + 1024, acc, [](int) {});
            half_put(L_EX, w8, l, acc, s + 1);
            WAITV(0);
            PT(4);
            if (fi) { ostore(fin, finish(fin, f)); ++fin; }
            MARK(1 + s);
            PT(5);
        }
    }
    PFLUSH();
    MARK(65);
}

__global__ void __launch_bounds__(512, 2) k_mega(Args a) {
    __shared__ __attribute__((aligned(16))) unsigned char smem[163840];
    const unsigned L = (unsigned)(size_t)(LAS unsigned char*)smem;
    const int b = blockIdx.x, u = (b & 3) + 4 * (b >> 3);
    MARK(255);
    if (!(b & 4)) chain(a, u, L);
    else if (u < 96) gwork(a, (u >> 2) % 3, (u & 3) * 8 + (u >> 2) / 3, L);
    else owork(a, (u >> 2) - 24, u & 3, L);
}

static constexpr int g_force = 0; static constexpr int g_d1 = 950, g_d2 = 2000, g_per = 2500, g_dinit = 250; static constexpr unsigned* g_dbg = nullptr;
extern "C" void kernel_launch(void* const* d_in, const int* in_sizes, int n_in, void* d_out, int out_size, void* d_ws, size_t ws_size, hipStream_t stream) {
    auto in = [&](int i) { return (const bf16_t*)d_in[i]; };
    unsigned char* ws = (unsigned char*)d_ws; size_t off = 0;
    auto alloc = [&](size_t bytes) { unsigned char* p = ws + off; off += (bytes + 255) & ~(size_t)255; return p; };
    unsigned* ctl = (unsigned*)alloc(C_WORDS * 4);
    unsigned* gate = (unsigned*)alloc(2 * S_TOK * 4); unsigned char* ypart = alloc(32 * 28 * 1024);
    off += (size_t)(-(uintptr_t)(ws + off)) & ((2u << 20) - 1);
    unsigned char* ring = alloc(RING_BYTES);
    unsigned char* qkv = alloc((size_t)NSEG * NH * 24576); unsigned char* kvm = alloc((size_t)NSEG * NH * 4096);
    unsigned char* ox = alloc((size_t)NSEG * 262144); unsigned char* part = alloc((size_t)NSEG * 524288);
    (void)hipMemsetAsync(ctl, 0, C_WORDS * 4, stream);
    Args a{in(0), in(1), in(2), in(3), in(4), in(5), (bf16_t*)d_out, ctl, ring, qkv, kvm, ox, part, in(8), in(9), in(10), in(11), in(12), gate, ypart, g_dbg,
           g_d1 * 16 / 10, g_d2 * 16 / 10, g_per * 16 / 10, g_dinit * 16 / 10 + (g_force << 16)};
    k_mega<<<256, 512, 0, stream>>>(a);
}
```

```cpp
#include <hip/hip_runtime.h>
#include <stdint.h>
#include <type_traits>

typedef unsigned short bf16_t;
typedef unsigned long long u64;
typedef float frag_cd __attribute__((ext_vector_type(4)));
typedef unsigned v4u __attribute__((ext_vector_type(4)));
typedef unsigned v8u __attribute__((ext_vector_type(8)));
typedef unsigned v2u __attribute__((ext_vector_type(2)));
typedef float v2f __attribute__((ext_vector_type(2)));
typedef __bf16 bf16x2_t __attribute__((ext_vector_type(2)));
typedef __bf16 v8bf __attribute__((ext_vector_type(8)));
typedef __bf16 v16bf __attribute__((ext_vector_type(16)));
typedef short v4s_t __attribute__((ext_vector_type(4)));
typedef __amdgpu_buffer_rsrc_t rsrc_t;
#define LAS __attribute__((address_space(3)))
#define GAS __attribute__((address_space(1)))

static constexpr int S_TOK = 2048, DM = 1024, NH = 32, DH = 128, MM = 8, SEG = 32, NSEG = 64, HD = NH * DH, FF = 3584, TOPC = 8;
static constexpr int C_ERR = 0, C_READY = 32, C_CAL = 64  , C_G = 128  , C_M = C_G + 1024  , C_A = C_M + 1024  ,
                     C_P = C_A + 128  , C_R = C_P + 256, C_Y = C_R + 32, C_WORDS = C_Y + 32;
static constexpr int RING_BYTES = 4 << 20, SLOT = 1 << 20, CHB = 2 << 20, L_H3 = 192 << 10, L_TS = 256 << 10, SLOT3 = 192 << 10;
static constexpr unsigned SPIN_CAP = 1u << 18;
static constexpr int OOB = 0x7ffffff0, LCAP = 1 << 22, GCAP = 1 << 20;
static constexpr float SCALE = 0.08838834764831845f, LOG2E = 1.4426950408889634f;

struct Args {
    const bf16_t* x; const bf16_t* mem0; const bf16_t* Wq; const bf16_t* Wk; const bf16_t* Wv; const bf16_t* Wo;
    bf16_t* out; unsigned* ctl; unsigned char* ring; unsigned char* qkv; unsigned char* kvm; unsigned char* ox; unsigned char* part;
    const bf16_t* wr; const bf16_t* wg; const bf16_t* wu; const bf16_t* wd; const bf16_t* gw;
    unsigned* gate  ; unsigned char* ypart; unsigned* dbg;
    int d1, d2, per, dinit;
};

__device__ __forceinline__ unsigned cvt_pk(float lo, float hi) { v2f v = {lo, hi}; return __builtin_bit_cast(unsigned, __builtin_convertvector(v, bf16x2_t)); }
__device__ __forceinline__ float lo16(unsigned p) { return __uint_as_float(p << 16); }
__device__ __forceinline__ float hi16(unsigned p) { return __uint_as_float(p & 0xffff0000u); }
__device__ __forceinline__ float rbf(float f) { return lo16(cvt_pk(f, 0.f)); }
template <int C> __device__ __forceinline__ unsigned dppu(unsigned v) { return (unsigned)__builtin_amdgcn_update_dpp(0, (int)v, C, 0xf, 0xf, true); }
template <int C> __device__ __forceinline__ float dppf(float v) { return __uint_as_float(dppu<C>(__float_as_uint(v))); }
#define ROR8 0x128
#define HMIR 0x141
__device__ __forceinline__ v2u sw16(unsigned a, unsigned b) { auto r = __builtin_amdgcn_permlane16_swap(a, b, false, false); return v2u{r[0], r[1]}; }
__device__ __forceinline__ v2u sw32(unsigned a, unsigned b) { auto r = __builtin_amdgcn_permlane32_swap(a, b, false, false); return v2u{r[0], r[1]}; }
__device__ __forceinline__ float tadd16(float a, float b) { v2u r = sw16(__float_as_uint(a), __float_as_uint(b)); return __uint_as_float(r.x) + __uint_as_float(r.y); }
__device__ __forceinline__ float tadd32(float a, float b) { v2u r = sw32(__float_as_uint(a), __float_as_uint(b)); return __uint_as_float(r.x) + __uint_as_float(r.y); }
__device__ __forceinline__ float tmax16(float a) { v2u r = sw16(__float_as_uint(a), __float_as_uint(a)); return fmaxf(__uint_as_float(r.x), __uint_as_float(r.y)); }
__device__ __forceinline__ float tmax32(float a) { v2u r = sw32(__float_as_uint(a), __float_as_uint(a)); return fmaxf(__uint_as_float(r.x), __uint_as_float(r.y)); }
__device__ __forceinline__ unsigned umax3(unsigned a, unsigned b, unsigned c) { return max(max(a, b), c); }
__device__ __forceinline__ v4u pack_swap(frag_cd a, frag_cd b) { v2u x0 = sw16(cvt_pk(a[0], a[1]), cvt_pk(b[0], b[1])), x1 = sw16(cvt_pk(a[2], a[3]), cvt_pk(b[2], b[3])); return v4u{x0.x, x1.x, x0.y, x1.y}; }
#define MFMA(a, b, c) __builtin_amdgcn_mfma_f32_16x16x32_bf16(__builtin_bit_cast(v8bf, a), __builtin_bit_cast(v8bf, b), c, 0, 0, 0)
#define SMF(a, b, c, ix) __builtin_amdgcn_smfmac_f32_16x16x64_bf16(__builtin_bit_cast(v8bf, a), __builtin_bit_cast(v16bf, b), c, ix, 0, 0)
#define FENCE() asm volatile("" ::: "memory")
#define SB() __builtin_amdgcn_sched_barrier(0)
#define RFL(x) __builtin_amdgcn_readfirstlane(x)
#define BAR() do { FENCE(); __builtin_amdgcn_s_barrier(); FENCE(); } while (0)
#define WAITV(n) do { FENCE(); __builtin_amdgcn_s_waitcnt(0x0F70 | (n)); FENCE(); } while (0)
#define WAITL() asm volatile("s_waitcnt lgkmcnt(0)" ::: "memory")
__device__ __forceinline__ void STB(v4u v, rsrc_t rs, int off, int soff) { __builtin_amdgcn_raw_buffer_store_b128(v, rs, off, soff, 16); asm volatile("s_nop 1" :: "v"(v) : "memory"); }
template <class T> __device__ __forceinline__ LAS T* lp(unsigned a) { return (LAS T*)(size_t)a; }
__device__ __forceinline__ void glds(const void* g, unsigned voff, unsigned lds) { asm volatile("s_mov_b32 m0, %2\n\ts_nop 0\n\tglobal_load_lds_dwordx4 %0, %1" :: "v"(voff), "s"(g), "s"(lds) : "memory"); }
#define NTC " nt"
#define NTW " nt"
__device__ __forceinline__ void gldsc(const void* g, unsigned voff, unsigned lds) { asm volatile("s_mov_b32 m0, %2\n\ts_nop 0\n\tglobal_load_lds_dwordx4 %0, %1" NTC :: "v"(voff), "s"(g), "s"(lds) : "memory"); }
__device__ __forceinline__ void gldsw(const void* g, unsigned voff, unsigned lds) { asm volatile("s_mov_b32 m0, %2\n\ts_nop 0\n\tglobal_load_lds_dwordx4 %0, %1" NTW :: "v"(voff), "s"(g), "s"(lds) : "memory"); }
template <int N> __device__ __forceinline__ void glds1(const void* g, unsigned voff, unsigned lds) {
#define GL(o) "\n\tglobal_load_lds_dwordx4 %0, %1 offset:" #o " sc1"
    if constexpr (N == 1) asm volatile("s_mov_b32 m0, %2\n\ts_nop 0" GL(0) :: "v"(voff), "s"(g), "s"(lds) : "memory");
    if constexpr (N == 2) asm volatile("s_mov_b32 m0, %2\n\ts_nop 0" GL(0) GL(1024) :: "v"(voff), "s"(g), "s"(lds) : "memory");
    if constexpr (N == 4) asm volatile("s_mov_b32 m0, %2\n\ts_nop 0" GL(0) GL(1024) GL(2048) GL(3072) :: "v"(voff), "s"(g), "s"(lds) : "memory");
}
__device__ __forceinline__ rsrc_t mkrs(const void* p, int bytes) { return __builtin_amdgcn_make_buffer_rsrc((void*)p, 0, bytes, 0x00020000); }
__device__ __forceinline__ unsigned gld(const unsigned* p) { return __hip_atomic_load(p, __ATOMIC_RELAXED, __HIP_MEMORY_SCOPE_AGENT); }
__device__ __forceinline__ void gst(unsigned* p, unsigned v) { __hip_atomic_store(p, v, __ATOMIC_RELAXED, __HIP_MEMORY_SCOPE_AGENT); }
__device__ __forceinline__ void gst1(unsigned* p, unsigned v) { asm volatile("s_mov_b64 exec, 1\n\tglobal_store_dword %0, %1, %2 sc1\n\ts_mov_b64 exec, -1" :: "v"(0u), "v"(v), "s"(p) : "memory"); }
__device__ __forceinline__ unsigned rt16() { return (unsigned)__builtin_amdgcn_s_memrealtime() << 4; }

#define STAMP(slot) do {} while (0)
#define STAMPS_FLUSH() do {} while (0)
#define PT(k) do {} while (0)
#define PFLUSH() do {} while (0)
#define MARK(i) do {} while (0)
#define MARKC(i) do {} while (0)
#define CNT(i) do {} while (0)
#define CNT_FLUSH() do {} while (0)

#define LATE_THR -100000
#define LATE_ADD 480
#define SLIP_THR 200
#define SLIP_ADD 640
#define SLIP_INC 10
__device__ __forceinline__ void wait_t(unsigned T) {
    int rem, cap = 4096;
    asm volatile("1:\n\ts_memrealtime vcc\n\ts_sub_u32 %1, %1, 1\n\ts_cbranch_scc1 3f\n\ts_waitcnt lgkmcnt(0)\n\ts_lshl_b32 %0, vcc_lo, 4\n\ts_sub_i32 %0, %2, %0\n\ts_cmp_ge_i32 %0, 64\n\ts_cbranch_scc1 1b\n\t"
                 "s_cmp_gt_i32 %0, 12\n\ts_cbranch_scc0 3f\n2:\n\ts_sub_i32 %0, %0, 13\n\ts_cmp_gt_i32 %0, 12\n\ts_cbranch_scc1 2b\n3:\n\ts_waitcnt lgkmcnt(0)"
                 : "=&s"(rem), "+s"(cap) : "s"(T) : "scc", "vcc", "memory");
}
__device__ __forceinline__ void sched(unsigned T) { unsigned now = rt16(); if ((int)(T - now) < LATE_THR) T = now + LATE_ADD; wait_t(T); }
template <int N>
__device__ __forceinline__ bool bad(const v4u (&v)[N]) {
    unsigned m = umax3(v[0].x, v[0].y, v[0].z); m = max(m, v[0].w);
#pragma unroll
    for (int i = 1; i < N; ++i) { m = umax3(m, v[i].x, v[i].y); m = umax3(m, v[i].z, v[i].w); }
    return __builtin_amdgcn_ballot_w64(m == 0xFFFFFFFFu) != 0;
}
template <int N>
__device__ __forceinline__ void ldn(rsrc_t rs, const int (&off)[N], int soff, v4u (&v)[N]) {
#pragma unroll
    for (int i = 0; i < N; ++i) v[i] = __builtin_amdgcn_raw_buffer_load_b128(rs, off[i], soff, 16);
}
__device__ __forceinline__ void endpgm() { asm volatile("s_endpgm" ::: "memory"); }
__device__ __forceinline__ void fatal(unsigned* err) { asm volatile("global_store_dword %0, %1, off sc1\n\ts_waitcnt vmcnt(0)\n\ts_endpgm" :: "v"(err), "v"(1u) : "memory"); }
template <int N>
__device__ __forceinline__ void retry(rsrc_t rs, const int (&off)[N], int soff, v4u (&v)[N], unsigned* err, unsigned& budget) {
    if (--budget == 0) fatal(err);
#define RL(d, o) "buffer_load_dwordx4 %" #d ", %" #o ", %[r], %[s] offen sc1\n\t"
#define RH "s_waitcnt vmcnt(0)\n\ts_sleep 2\n\t"
#define RT [r] "s"(rs), [s] "s"(soff) : "memory"
    if constexpr (N == 2) asm volatile(RH RL(0, 2) RL(1, 3) "s_waitcnt vmcnt(0)" : "=&v"(v[0]), "=&v"(v[1]) : "v"(off[0]), "v"(off[1]), RT);
    if constexpr (N == 4) asm volatile(RH RL(0, 4) RL(1, 5) RL(2, 6) RL(3, 7) "s_waitcnt vmcnt(0)" : "=&v"(v[0]), "=&v"(v[1]), "=&v"(v[2]), "=&v"(v[3]) : "v"(off[0]), "v"(off[1]), "v"(off[2]), "v"(off[3]), RT);
    if constexpr (N == 5) asm volatile(RH RL(0, 5) RL(1, 6) RL(2, 7) RL(3, 8) RL(4, 9) "s_waitcnt vmcnt(0)" : "=&v"(v[0]), "=&v"(v[1]), "=&v"(v[2]), "=&v"(v[3]), "=&v"(v[4]) : "v"(off[0]), "v"(off[1]), "v"(off[2]), "v"(off[3]), "v"(off[4]), RT);
}
__device__ __forceinline__ unsigned ldsr(unsigned p) { return __hip_atomic_load(lp<unsigned>(p), __ATOMIC_RELAXED, __HIP_MEMORY_SCOPE_WORKGROUP); }
__device__ __forceinline__ void ldsadd(unsigned p) { asm volatile("s_mov_b64 exec, 1\n\tds_add_u32 %0, %1\n\ts_mov_b64 exec, -1" :: "v"(p), "v"(1u) : "memory"); }
__device__ __forceinline__ v2u trd(unsigned p) { return __builtin_bit_cast(v2u, __builtin_amdgcn_ds_read_tr16_b64_v4i16(lp<v4s_t>(p))); }
__device__ __forceinline__ v8u cat(v2u a, v2u b, v2u c, v2u d) { return v8u{a.x, a.y, b.x, b.y, c.x, c.y, d.x, d.y}; }
__device__ __forceinline__ v4u cat2(v2u a, v2u b) { return v4u{a.x, a.y, b.x, b.y}; }
__device__ __forceinline__ unsigned poll_red(unsigned aC, unsigned aR, unsigned need) {
    unsigned cv; int cap = LCAP; v2f r[4];
    asm volatile("1:\n\tds_read_b32 %0, %6\n\tds_read_b64 %1, %7\n\tds_read_b64 %2, %7 offset:3072\n\tds_read_b64 %3, %7 offset:6144\n\tds_read_b64 %4, %7 offset:9216\n\t"
                 "s_sub_u32 %5, %5, 1\n\ts_cbranch_scc0 2f\n\ts_endpgm\n2:\n\ts_waitcnt lgkmcnt(4)\n\tv_cmp_gt_u32 vcc, %8, %0\n\ts_cbranch_vccnz 1b\n\ts_waitcnt lgkmcnt(0)"
                 : "=&v"(cv), "=&v"(r[0]), "=&v"(r[1]), "=&v"(r[2]), "=&v"(r[3]), "+s"(cap) : "v"(aC), "v"(aR), "s"(need) : "vcc", "scc", "memory");
    return min(cvt_pk((r[0].x + r[1].x) + (r[2].x + r[3].x), (r[0].y + r[1].y) + (r[2].y + r[3].y)), 0xFFFFFFFEu);
}
__device__ __forceinline__ v4u quad(unsigned p0) { return v4u{p0, dppu<0x55>(p0), dppu<0xAA>(p0), dppu<0xFF>(p0)}; }
__device__ __forceinline__ void poll_cnt(unsigned p, unsigned need) { for (int i = 0; ldsr(p) < need; ++i) { if (i > LCAP) endpgm(); } FENCE(); }
__device__ __forceinline__ bool flags_ok(const unsigned* p, int nw, unsigned need, int l) { unsigned v = gld(p + min(l, nw - 1)); bool r = __builtin_amdgcn_ballot_w64(v < need) == 0; FENCE(); return r; }
__device__ __forceinline__ void flags_wait(const unsigned* p, int nw, unsigned need, int l, unsigned* err) {
    for (int i = 0; !flags_ok(p, nw, need, l); ++i) { __builtin_amdgcn_s_sleep(4); if (i > GCAP) fatal(err); }
}

__device__ __forceinline__ void chain(const Args& a, const int c, const unsigned L) {
    const int tid = threadIdx.x, w8 = RFL(tid >> 6), w = w8 & 3, l = tid & 63;
    const int g = c & 3, aa = c >> 2, h = g * 8 + (aa >> 2), jj = aa & 3;
    const int n = l & 15, q = l >> 4, row = n & 7, hf = n >> 3;
    const unsigned L_RED = L + 131072, L_SC = L_RED + 12288, L_SM = L_SC + 2048, L_CNT = L_SM + 512, L_VX = L, L_ST = L + 32768;
    const unsigned cB1 = L_CNT, cB2 = L_CNT + 128, cBv = L_CNT + 256, cV2 = L_CNT + 384;
    const rsrc_t rs = mkrs(a.ring, RING_BYTES);
    unsigned* const err = a.ctl + C_ERR;
    const v4u ones = {~0u, ~0u, ~0u, ~0u};
    if (tid < 128) lp<unsigned>(L_CNT)[tid] = 0;
#pragma unroll
    for (int i = 0; i < 4; ++i) STB(ones, rs, c * 32768 + i * 8192 + tid * 16, 0);
    const unsigned stg = L + w8 * 16384;
    const int prr = 16 * (q >> 1) + 4 * (q & 1) + ((n >> 2) & 1) + 8 * (n >> 3);
    auto stage_w = [&](const bf16_t* W) {
        W += (size_t)256 * w * HD + h * DH + 32 * jj;
#pragma unroll 1
        for (int ch = 0; ch < 16; ++ch) gldsc(W + (size_t)16 * ch * HD, (l >> 2) * (HD * 2) + (l & 3) * 16, stg + ch * 1024);
        WAITV(0);
    };
    auto read_w = [&](v8u (&d)[2][4]) {
#pragma unroll
        for (int ct = 0; ct < 2; ++ct)
#pragma unroll
            for (int i = 0; i < 4; ++i) { unsigned p = stg + (64 * i + prr) * 64 + ct * 32 + (l & 3) * 8; d[ct][i] = cat(trd(p), trd(p + 128), trd(p + 2048), trd(p + 2048 + 128)); }
        WAITL();
    };
    const int six = hf ? 0xEEEE : 0x4444;
    const rsrc_t rsm = mkrs(a.mem0, MM * DM * 2);
    const int pos = l >> 3, prr8 = (pos >> 1) + 4 * (pos & 1);
    unsigned cal;
    u64 cnts[4] = {}; (void)cnts;
    auto phA = [&](int lg) { return ((lg >> 13) << 14) + (int)((cal & 1) << 13) + (lg & 8191); };
    auto phB = [&](int lg) { return CHB + ((lg >> 13) << 14) + (int)((cal & 2) << 12) + (lg & 8191); };
    auto h1o = [&](int inner) { return phB(h * 6144 + inner); };
    auto arrive = [&]() {
        if (tid == 192) __hip_atomic_fetch_add(a.ctl + C_READY, 1u, __ATOMIC_RELAXED, __HIP_MEMORY_SCOPE_AGENT);
    };
    auto start = [&]() {
        if (w8 == 3) {
            flags_wait(a.ctl + C_READY, 1, 128 + 32, l, err);
            unsigned v = gld(a.ctl + C_CAL + (l & 3));
            unsigned t = (__builtin_amdgcn_readlane(v, 1) < __builtin_amdgcn_readlane(v, 0) ? 1u : 0u) | (__builtin_amdgcn_readlane(v, 3) < __builtin_amdgcn_readlane(v, 2) ? 2u : 0u);
            lp<unsigned>(L_CNT + 448)[0] = t; WAITL();
        }
        BAR();
        cal = RFL(ldsr(L_CNT + 448));
    };

    if (w8 < 4) {
        v8u wA[2][2][4];
        stage_w(a.Wq); BAR(); arrive(); read_w(wA[0]);
        stage_w(a.Wk); read_w(wA[1]);
        MARK(2);
        BAR();
        const unsigned aRedR = L_RED + (w * 128 + 2 * l) * 4, redW = L_RED + ((w * 48 + q) * 16 + n) * 4;
        const unsigned scW = w < 2 ? L_SC + (hf ? 256 + l * 4 : ((w * 4 + q) * 8 + row) * 4) * 4 : ((q >> 1) == hf ? L_SM + ((hf * 8 + row) * 8 + (q & 1) * 4) * 4 : L_SC + (256 + l * 4) * 4);
        auto run = [&](auto role_) {
        constexpr int ROLE = decltype(role_)::value;
        v4u kx[4]; unsigned fl = 0;
        const unsigned char* xp = a.qkv + h * 24576 + (w & 1) * 4096;
        const unsigned* fG = a.ctl + C_G + h * 32;
        auto xload = [&](int sk, int sv) {
            if (__builtin_amdgcn_ballot_w64(fl <= (unsigned)sv) != 0) flags_wait(fG, 2, sv + 1, l, err);
            glds1<4>(xp + (size_t)sv * (NH * 24576) + 8192, l * 16, L_VX + (sv & 1) * 8192 + w * 4096);
            asm volatile("global_load_dwordx4 %0, %5, %6 sc1\n\tglobal_load_dwordx4 %1, %5, %6 offset:1024 sc1\n\tglobal_load_dwordx4 %2, %5, %6 offset:2048 sc1\n\tglobal_load_dwordx4 %3, %5, %6 offset:3072 sc1\n\t"
                         "global_load_dword %4, %7, %8 sc1\n\ts_waitcnt vmcnt(0)"
                         : "=&v"(kx[0]), "=&v"(kx[1]), "=&v"(kx[2]), "=&v"(kx[3]), "=&v"(fl) : "v"(l * 16), "s"(xp + (size_t)sk * (NH * 24576)), "v"((l & 1) * 4), "s"(fG) : "memory");
        };
        if (ROLE == 0) { xload(0, 0); xload(0, 1); }
        start();
        MARK(0); MARKC(4);
        int o1[ROLE == 2 ? 4 : 2], ld2[4], st3, off3[ROLE == 3 ? 5 : 4], o3[ROLE == 3 ? 5 : 4];
        int st1 = h1o((w >> 1) * 2048 + (4 * jj + 2 * (w & 1) + ((l >> 2) & 1)) * 128 + prr8 * 16); if (l & 3) st1 = OOB;
#pragma unroll
        for (int i = 0; i < 2; ++i) { if constexpr (ROLE == 2) { o1[2 * i] = h1o((8 * i + 2 * q + hf) * 128 + row * 16); o1[2 * i + 1] = h1o(2048 + (8 * i + 2 * q + hf) * 128 + row * 16); } else o1[i] = h1o((8 * i + 2 * q + hf) * 128 + row * 16); }
#pragma unroll
        for (int i = 0; i < 4; ++i) { ld2[i] = phA(c * 4096 + i * 1024 + l * 16); off3[i] = phB(L_H3 + g * 16384 + (32 * w + 8 * i + 2 * q + hf) * 128 + row * 16); o3[i] = (row * DM + 256 * w + 64 * i + 16 * q + 8 * hf) * 2; }
        if constexpr (ROLE == 3) { off3[4] = phB(L_TS + g * 128); o3[4] = OOB; }
        st3 = l < 32 ? phB(L_H3 + (l >> 3) * 16384 + c * 128 + row * 16) : (c == 0 && l < 36 ? phB(L_TS + (l & 3) * 128) : OOB);
        const int in1a = h1o((l >> 5) * 2048 + jj * 512 + (l & 31) * 16), in1b = l < 32 ? h1o(4096 + jj * 512 + l * 16) : OOB;
        auto kvcopy = [&](int s) {
            poll_cnt(cB1, 4u * (s + 1)); poll_cnt(cBv, 4u * (s + 1));
            const unsigned p = L_RED + (((2 + q) * 8 + 2 * (row & 3) + (row >> 2)) * 16 + hf * 8) * 4;
            v4u o;
#pragma unroll
            for (int u = 0; u < 2; ++u) { const frag_cd t = (*lp<frag_cd>(p + 16 * u) + *lp<frag_cd>(p + 16 * u + 3072)) + (*lp<frag_cd>(p + 16 * u + 6144) + *lp<frag_cd>(p + 16 * u + 9216));
                o[2 * u] = cvt_pk(t[0], t[1]); o[2 * u + 1] = cvt_pk(t[2], t[3]); }
            gst1(a.ctl + C_M + h * 32 + jj, s);
            STB(o, mkrs(a.kvm, NSEG * NH * 4096), h * 4096 + (q >> 1) * 2048 + (4 * jj + 2 * (q & 1)) * 128 + n * 16, s * (NH * 4096));
            if (s == NSEG - 1) { WAITV(0); gst1(a.ctl + C_M + h * 32 + jj, s + 1); }
        };
        v4u sel;
#pragma unroll
        for (int i = 0; i < 4; ++i) sel[i] = (row >> 1) == i ? 0x3F80u << (16 * (row & 1)) : 0u;
        __builtin_amdgcn_s_setprio(3);
        int so = 0, sp = SLOT;
        unsigned budget = SPIN_CAP, tb = rt16() - (1u << 20), add = 0;
        rsrc_t r3 = rsm; int s3 = 0, w3 = 0, x3 = SLOT3, y3 = 2 * SLOT3;
        for (int s = 0;; ++s) {
            u64 stv[11] = {};
            unsigned n4 = 4u * (s + 1);
            asm volatile("" : "+s"(n4));
            STAMP(0);
            constexpr int N3 = ROLE == 3 ? 5 : 4;
            v4u mb[N3];
            if (ROLE == 3 && s) sched(tb);
            BAR();
            STAMP(1);
            ldn<N3>(r3, o3, s3, mb);
            FENCE();
            frag_cd acc[4];
            for (;;) {
#pragma unroll
                for (int m = 0; m < 4; ++m) { acc[m] = frag_cd{}; asm volatile("" : "+v"(acc[m])); }
                SB();
                unsigned mxs = 0;
#pragma unroll
                for (int i = 0; i < 4; ++i) {
                    acc[0] = SMF(mb[i], wA[0][0][i], acc[0], six); mxs = umax3(mxs, mb[i].x, mb[i].y); SB();
                    acc[1] = SMF(mb[i], wA[0][1][i], acc[1], six); mxs = umax3(mxs, mb[i].z, mb[i].w); asm volatile("" : "+v"(mxs)); SB();
                    acc[2] = SMF(mb[i], wA[1][0][i], acc[2], six); if (ROLE == 3 && i == 3) mxs = umax3(mxs, mb[N3 - 1].x, mb[N3 - 1].y); SB();
                    acc[3] = SMF(mb[i], wA[1][1][i], acc[3], six); SB(); }
                if (__builtin_expect(__builtin_amdgcn_ballot_w64(mxs == 0xFFFFFFFFu) == 0, 1)) break;
                CNT(2); retry<N3>(r3, o3, s3, mb, err, budget);
            }
            STAMP(2);
#pragma unroll
            for (int m = 0; m < 4; ++m) {
                float x0 = tadd32(acc[m][0], acc[m][1]), x1 = tadd32(acc[m][2], acc[m][3]);
                lp<float>(redW)[m * 128] = x0; lp<float>(redW)[m * 128 + 64] = x1;
            }
            FENCE();
            ldsadd(cB1);
            if (s == NSEG - 1) { if (ROLE == 2) kvcopy(s); break; }
            STB(quad(poll_red(cB1, aRedR, n4)), rs, st1, so);
            STAMP(3);
            if (ROLE == 3) {
                if (s) { tb = RFL(mb[N3 - 1].x); add = RFL(mb[N3 - 1].y); }
                sched(tb + a.d1 + add);
            }
            BAR();
            STAMP(4);
            if constexpr (ROLE == 0) {
                v4u qv[2];
                ldn<2>(rs, o1, so, qv);
                frag_cd sl, sh;
                for (;;) {
                    sl = frag_cd{}; sh = frag_cd{};
#pragma unroll
                    for (int i = 0; i < 2; ++i) { sl = MFMA(kx[2 * i], qv[i], sl); sh = MFMA(kx[2 * i + 1], qv[i], sh); }
                    if (__builtin_expect(!bad<2>(qv), 1)) break;
                    CNT(0); retry<2>(rs, o1, so, qv, err, budget);
                }
                frag_cd r;
#pragma unroll
                for (int e = 0; e < 4; ++e) r[e] = sl[e] + dppf<ROR8>(sh[e]);
                *lp<frag_cd>(scW) = r;
                FENCE();
                ldsadd(cB2);
                STAMP(5);
                xload(s + 1, min(s + 2, NSEG - 1));
                STAMP(6); STAMP(7); STAMP(8); STAMP(9); STAMP(10);
            } else if constexpr (ROLE == 2) {
                v4u qk[4];
                ldn<4>(rs, o1, so, qk);
                frag_cd sm;
                for (;;) {
                    sm = frag_cd{}; sm = MFMA(qk[1], qk[0], sm); sm = MFMA(qk[3], qk[2], sm);
                    if (__builtin_expect(!bad<4>(qk), 1)) break;
                    CNT(0); retry<4>(rs, o1, so, qk, err, budget);
                }
                *lp<frag_cd>(scW) = sm;
                FENCE();
                ldsadd(cB2);
                STAMP(5);
                kvcopy(s);
                STAMP(6); STAMP(7); STAMP(8); STAMP(9); STAMP(10);
            } else {
                wait_t(tb + a.d1 + add + (a.dinit & 0xffff));
#pragma unroll
                for (int i = 0; i < 4; ++i) STB(ones, rs, ld2[i], sp);
                STB(ones, rs, in1a, sp); STB(ones, rs, in1b, sp);
                STB(ones, rs, st3, x3);
                STAMP(5);
                poll_cnt(cV2, n4);
                STAMP(6); STAMP(7);
                const unsigned t2 = tb + a.d2 + 2 * add;
                sched(t2);
                STAMP(8);
                v4u v[4];
                ldn<4>(rs, ld2, so, v);
                frag_cd y; unsigned tn, an;
                for (;;) {
                    const int late = (int)(rt16() - t2);
                    tn = tb + a.per + 3 * add; an = add;
                    if (late > SLIP_THR) { tn += late + SLIP_ADD; if (s > 3) an = min(add + SLIP_INC, 4000u); }
                    asm volatile("" : "+s"(tn), "+s"(an)); SB();
                    y = frag_cd{};
#pragma unroll
                    for (int i = 0; i < 4; ++i) y = MFMA(sel, v[i], y);
                    if (__builtin_expect(!bad<4>(v), 1)) break;
                    CNT(1); retry<4>(rs, ld2, so, v, err, budget);
                }
                STAMP(9);
#pragma unroll
                for (int e = 0; e < 4; ++e) y[e] += dppf<ROR8>(y[e]);
                v4u oo = pack_swap(y, y);
                oo.x = l < 32 ? oo.x : tn & ~1u; oo.y = l < 32 ? oo.y : an;
                STB(oo, rs, st3, w3);
                STAMP(10);
                tb += a.per + 3 * add;
            }
            STAMPS_FLUSH();
            r3 = rs; s3 = w3; w3 = x3; x3 = y3; y3 = s3;
            { int t = sp; sp = so; so = t; }
#pragma unroll
            for (int i = 0; i < N3; ++i) o3[i] = off3[i];
        }
        };
        if (w < 2) run(std::integral_constant<int, 0>{}); else if (w == 2) run(std::integral_constant<int, 2>{}); else run(std::integral_constant<int, 3>{});
    } else {
        v8u wV[2][4], wO[4][2];
        stage_w(a.Wv); BAR(); read_w(wV);
        { const bf16_t* Wo = a.Wo + (size_t)h * DH * DM + 256 * jj + 64 * w;
#pragma unroll 1
          for (int i = 0; i < 16; ++i) gldsc(Wo + (size_t)8 * i * DM, (l >> 3) * (DM * 2) + (l & 7) * 16, stg + i * 1024);
          WAITV(0);
#pragma unroll
          for (int t = 0; t < 4; ++t)
#pragma unroll
              for (int i = 0; i < 2; ++i) { unsigned p = stg + (64 * i + 8 * q + (n >> 2)) * 128 + (t >> 1) * 64 + (l & 3) * 16 + (t & 1) * 8; wO[t][i] = cat(trd(p), trd(p + 512), trd(p + 4096), trd(p + 4096 + 512)); }
          WAITL(); }
        BAR();
        start();
        v2u ua[4][2];
        auto ucalc = [&](int s) {
#pragma unroll
            for (int kt = 0; kt < 2; ++kt) {
                v4u vx[4];
#pragma unroll
                for (int ks = 0; ks < 4; ++ks) vx[ks] = *lp<v4u>(L_VX + (s & 1) * 8192 + (kt * 4 + ks) * 1024 + l * 16);
#pragma unroll
                for (int t = 0; t < 4; ++t) { frag_cd u = {};
#pragma unroll
                    for (int ks = 0; ks < 4; ++ks) { v4u b = {wO[t][ks >> 1][4 * (ks & 1)], wO[t][ks >> 1][4 * (ks & 1) + 1], wO[t][ks >> 1][4 * (ks & 1) + 2], wO[t][ks >> 1][4 * (ks & 1) + 3]}; u = MFMA(vx[ks], b, u); }
                    ua[t][kt] = v2u{cvt_pk(u[0], u[1]), cvt_pk(u[2], u[3])}; }
            }
        };
        ucalc(0);
        const int phf = (n >> 2) & 1, prow = (phf ? 3 - (n & 3) : (n & 3)) + 4 * hf;
        const int st1 = (l & 3) ? OOB : h1o(2 * 2048 + (4 * jj + 2 * (w & 1) + ((l >> 2) & 1)) * 128 + prr8 * 16);
        int o1[2], off3[4], o3[4];
#pragma unroll
        for (int i = 0; i < 2; ++i) o1[i] = h1o(2 * 2048 + (8 * i + 2 * q + hf) * 128 + row * 16);
#pragma unroll
        for (int i = 0; i < 4; ++i) { off3[i] = phB(L_H3 + g * 16384 + (32 * w + 8 * i + 2 * q + hf) * 128 + row * 16); o3[i] = (row * DM + 256 * w + 64 * i + 16 * q + 8 * hf) * 2; }
#define ROT2 0
        const int st2 = phA((32 * jj + 8 * w + 4 * phf + q) * 4096 + (h ^ ROT2) * 128 + prow * 16);
        const unsigned aRedR = L_RED + ((4 + (w & 1)) * 128 + pos * 16 + 4 * (l & 3) + 2 * ((l >> 2) & 1)) * 4;
        const unsigned aSc = L_SC + ((phf * 4 + q) * 8 + prow) * 16, aSm = L_SM + (prow * 8 + 4 * (q & 1) + 2 * phf) * 4;
        const unsigned redW = L_RED + ((w * 48 + 32 + q) * 16 + n) * 4;
        int so = 0, sp = SLOT;
        unsigned budget = SPIN_CAP;
        rsrc_t r3 = rsm; int s3 = 0, w3 = 0, x3 = SLOT3, y3 = 2 * SLOT3;
        for (int s = 0;; ++s) {
            u64 stv[11] = {};
            unsigned n4 = 4u * (s + 1), n3 = 3u * (s + 1);
            asm volatile("" : "+s"(n4), "+s"(n3));
            STAMP(0);
            v4u mb[4];
            __builtin_amdgcn_s_setprio(2);
            BAR();
            asm volatile("s_nop 15\n\ts_nop 15\n\ts_nop 15" ::: "memory");
            STAMP(1);
            ldn<4>(r3, o3, s3, mb);
            FENCE();
            frag_cd acc[2];
            for (;;) {
#pragma unroll
                for (int m = 0; m < 2; ++m) { acc[m] = frag_cd{}; asm volatile("" : "+v"(acc[m])); }
                SB();
                unsigned mxs = 0;
#pragma unroll
                for (int i = 0; i < 4; ++i) {
                    acc[0] = SMF(mb[i], wV[0][i], acc[0], six); mxs = umax3(mxs, mb[i].x, mb[i].y); SB();
                    acc[1] = SMF(mb[i], wV[1][i], acc[1], six); mxs = umax3(mxs, mb[i].z, mb[i].w); asm volatile("" : "+v"(mxs)); SB(); }
                if (__builtin_expect(__builtin_amdgcn_ballot_w64(mxs == 0xFFFFFFFFu) == 0, 1)) break;
                CNT(2); retry<4>(r3, o3, s3, mb, err, budget);
            }
            STAMP(2);
#pragma unroll
            for (int m = 0; m < 2; ++m) {
                float x0 = tadd32(acc[m][0], acc[m][1]), x1 = tadd32(acc[m][2], acc[m][3]);
                lp<float>(redW)[m * 128] = x0; lp<float>(redW)[m * 128 + 64] = x1;
            }
            FENCE();
            ldsadd(cBv);
            if (s == NSEG - 1) break;
            if (w < 2) STB(quad(poll_red(cBv, aRedR, n4)), rs, st1, so);
            STAMP(3);
            __builtin_amdgcn_s_setprio(3);
            BAR();
            asm volatile("s_nop 15\n\ts_nop 15\n\ts_nop 15\n\ts_nop 15" ::: "memory");
            STAMP(4);
            v4u vm[2];
            ldn<2>(rs, o1, so, vm);
            frag_cd z[4];
            for (;;) {
#pragma unroll
                for (int t = 0; t < 4; ++t) z[t] = frag_cd{};
#pragma unroll
                for (int i = 0; i < 2; ++i)
#pragma unroll
                    for (int t = 0; t < 4; ++t) z[t] = SMF(vm[i], wO[t][i], z[t], six);
                if (__builtin_expect(!bad<2>(vm), 1)) break;
                CNT(0); retry<2>(rs, o1, so, vm, err, budget);
            }
            STAMP(5);
            frag_cd sx; v2f s4a, s4b;
            { unsigned cv; int cap = LCAP;
              asm volatile("1:\n\tds_read_b32 %0, %5\n\tds_read_b128 %1, %6\n\tds_read_b64 %2, %7\n\tds_read_b64 %3, %7 offset:256\n\t"
                           "s_sub_u32 %4, %4, 1\n\ts_cbranch_scc0 2f\n\ts_endpgm\n2:\n\ts_waitcnt lgkmcnt(3)\n\tv_cmp_gt_u32 vcc, %8, %0\n\ts_cbranch_vccnz 1b\n\ts_waitcnt lgkmcnt(0)"
                           : "=&v"(cv), "=&v"(sx), "=&v"(s4a), "=&v"(s4b), "+s"(cap) : "v"(cB2), "v"(aSc), "v"(aSm), "s"(n3) : "vcc", "scc", "memory"); }
            STAMP(6);
            float sv[6] = {sx[0], sx[1], sx[2], sx[3], s4a.x + s4b.x, s4a.y + s4b.y};
#pragma unroll
            for (int e = 0; e < 6; e += 2) { unsigned p = cvt_pk(sv[e], sv[e + 1]); p = cvt_pk(lo16(p) * SCALE, hi16(p) * SCALE); sv[e] = lo16(p); sv[e + 1] = hi16(p); }
            float mx = fmaxf(fmaxf(fmaxf(sv[0], sv[1]), sv[2]), fmaxf(fmaxf(sv[3], sv[4]), sv[5]));
            mx = fmaxf(mx, dppf<HMIR>(mx)); mx = tmax16(mx); mx = tmax32(mx);
            const float mk = -mx * LOG2E;
#pragma unroll
            for (int e = 0; e < 6; ++e) sv[e] = __builtin_amdgcn_exp2f(__builtin_fmaf(sv[e], LOG2E, mk));
            v4u pb = {cvt_pk(sv[0], sv[1]), cvt_pk(sv[2], sv[3]), cvt_pk(sv[4], sv[5]), 0u};
            frag_cd fa[4], fb[4];
#pragma unroll
            for (int t = 0; t < 4; ++t) {
                v4u Aa = {ua[t][0].x, ua[t][0].y, cvt_pk(z[t][0], z[t][1]), 0u};
                v4u Ab = {ua[t][1].x, ua[t][1].y, cvt_pk(z[t][2], z[t][3]), 0u};
                frag_cd zero = {};
                fa[t] = MFMA(Aa, pb, zero); fb[t] = MFMA(Ab, pb, zero);
            }
            float sum = __builtin_fmaf(sv[4] + sv[5], 0.5f, (sv[0] + sv[1]) + (sv[2] + sv[3]));
            sum += dppf<HMIR>(sum); sum = tadd16(sum, sum); sum = tadd32(sum, sum);
            const float inv = __builtin_amdgcn_rcpf(sum);
            v4u o;
#pragma unroll
            for (int t = 0; t < 2; ++t) {
                float r[4];
#pragma unroll
                for (int e = 0; e < 4; ++e) { float u = phf ? fb[2 + t][e] : fa[t][e], v = phf ? fb[t][e] : fa[2 + t][e]; r[e] = (u + dppf<HMIR>(v)) * inv; }
                o[2 * t] = cvt_pk(r[0], r[1]); o[2 * t + 1] = cvt_pk(r[2], r[3]);
            }
            STB(o, rs, st2, so);
            FENCE();
            ldsadd(cV2);
            STAMP(7);
            __builtin_amdgcn_s_setprio(0);
            ucalc(s + 1);
            FENCE();
            STAMP(8); STAMP(9); STAMP(10);
            STAMPS_FLUSH();
            r3 = rs; s3 = w3; w3 = x3; x3 = y3; y3 = s3;
            { int t = sp; sp = so; so = t; }
#pragma unroll
            for (int i = 0; i < 4; ++i) o3[i] = off3[i];
        }
    }
}

struct Tile { const bf16_t* W; int ld, wide; };
__device__ __forceinline__ void tile_issue(const Tile& T, int t, unsigned L, int w8, int l) {
    const bf16_t* p = T.W + (T.wide ? (w8 & 3) * 32 * T.ld + (2 * t + (w8 >> 2)) * 128 : (t * 256 + w8 * 32) * T.ld);
    const unsigned vo = (l >> 4) * T.ld * 2 + ((l & 15) ^ (2 * (l >> 4))) * 16;
#pragma unroll 1
    for (int i = 0; i < 8; ++i) gldsw(p + i * 4 * T.ld, vo ^ ((i & 2) << 6), L + (t & 1) * 65536 + (w8 * 8 + i) * 1024);
}
template <class F>
__device__ __forceinline__ void tile_load(const Tile& T, v4u (&wf)[32], unsigned L, int w8, int l, F next) {
    const int n = l & 15, q = l >> 4;
    const unsigned p = L + (8 * q + (n >> 2)) * 256 + ((4 * (w8 & 3) + ((l >> 1) & 1)) ^ (2 * ((n >> 2) | ((q & 1) << 2)))) * 16 + (l & 1) * 8;
    bool more = true;
#pragma unroll 1
    for (int hh = 0; hh < 2; ++hh)
#pragma unroll
    for (int u = 0; u < 2; ++u) {
        if (!more) WAITV(0);
        WAITV(8); BAR();
        if ((w8 >> 2) == hh) {
#pragma unroll
            for (int i = 0; i < 16; ++i) { const unsigned pp = (p ^ ((i & 1) * 32)) + u * 65536 + (i >> 1) * 8192; wf[u * 16 + i] = cat2(trd(pp), trd(pp + 1024)); }
            WAITL(); }
        BAR();
        if (hh == 0) tile_issue(T, u + 2, L, w8, l); else more = next(u);
    }
}
#define NB 8
#define GD 4
template <class F>
__device__ __forceinline__ void seg_gemm(const v4u (&wf)[32], unsigned b0, unsigned b1, frag_cd (&acc)[4], F side) {
    v4u f[2 * GD];
#define RD(ks, mt) asm volatile("ds_read_b128 %0, %1 offset:%2" : "=v"(f[((ks) % GD) * 2 + mt]) : "v"(((ks) & 1) ? b1 : b0), "n"(((ks) >> 1) * 4096 + mt * 2048) : "memory")
#define MM(ct, mt) do { if (ks == 0) asm volatile("v_mfma_f32_16x16x32_bf16 %0, %1, %2, 0" : "=&v"(acc[2 * ct + mt]) : "v"(wf[ct]), "v"(f[mt])); \
        else asm volatile("v_mfma_f32_16x16x32_bf16 %0, %1, %2, %0" : "+v"(acc[2 * ct + mt]) : "v"(wf[2 * ks + ct]), "v"(f[(ks % GD) * 2 + mt])); } while (0)
#pragma unroll
    for (int ks = 0; ks < GD - 1; ++ks) { RD(ks, 0); RD(ks, 1); }
#pragma unroll
    for (int ks = 0; ks < 16; ++ks) {
        side(2 * ks);
        asm volatile("s_waitcnt lgkmcnt(%0)" :: "n"(2 * (ks + GD - 1 <= 16 ? GD - 2 : 15 - ks)));
        MM(0, 0);
        if (ks + GD - 1 < 16) RD(ks + GD - 1, 0);
        MM(0, 1);
        side(2 * ks + 1);
        MM(1, 0);
        if (ks + GD - 1 < 16) RD(ks + GD - 1, 1);
        MM(1, 1);
    }
    asm volatile("s_nop 15" : "+v"(acc[0]), "+v"(acc[1]), "+v"(acc[2]), "+v"(acc[3]));
#undef RD
#undef MM
}
__device__ __forceinline__ void half_put(unsigned ex, int w8, int l, const frag_cd (&acc)[4], unsigned it) {
#pragma unroll
    for (int i = 0; i < 4; ++i) *lp<frag_cd>(ex + w8 * 4096 + i * 1024 + l * 16) = acc[i];
    WAITL();
    lp<unsigned>(ex + 16384)[w8] = it; FENCE();
}
__device__ __forceinline__ void half_get(unsigned ex, int w8, int l, frag_cd (&acc)[4], unsigned it) {
    for (int i = 0; ldsr(ex + 16384 + (w8 & 3) * 4) != it; ++i) { if (i > LCAP) endpgm(); }
    FENCE();
#pragma unroll
    for (int i = 0; i < 4; ++i) acc[i] += *lp<frag_cd>(ex + (w8 & 3) * 4096 + i * 1024 + l * 16);
}

__device__ __forceinline__ void gwork(const Args& a, const int m, const int h, const unsigned L) {
    const int tid = threadIdx.x, w8 = RFL(tid >> 6), l = tid & 63, n = l & 15, q = l >> 4;
    unsigned* const err = a.ctl + C_ERR;
    const unsigned L_AT = L + 131072, L_KM = L_AT + 8192, L_EX = L_KM + 4096, L_DEC = L_EX + 16384 + 16;
    if (tid < 4) lp<unsigned>(L_EX + 16384)[tid] = 0;
    unsigned* const fG = a.ctl + C_G + h * 32;
    if (m == 2) {
#pragma unroll 1
        for (int i = 0; i < 16; ++i) glds(a.x + (size_t)(64 * h + 8 * w8) * DM + i * 512, l * 16, L + (w8 * 16 + i) * 1024);
        v4u gw[2], wr[2];
#pragma unroll
        for (int i = 0; i < 2; ++i) { gw[i] = *(const v4u*)(a.gw + 512 * i + 8 * l); wr[i] = *(const v4u*)(a.wr + 512 * i + 8 * l); }
        WAITV(0);
        float res = 0.f;
#pragma unroll 1
        for (int t = 0; t < 8; ++t) { float sg = 0.f, sr = 0.f;
#pragma unroll
            for (int i = 0; i < 2; ++i) { const v4u xv = *lp<v4u>(L + (w8 * 16 + 2 * t + i) * 1024 + l * 16);
#pragma unroll
                for (int e = 0; e < 4; ++e) { const float x0 = lo16(xv[e]), x1 = hi16(xv[e]); const unsigned p = cvt_pk(x0 * lo16(gw[i][e]), x1 * hi16(gw[i][e]));
                    sg += lo16(p) + hi16(p); sr = __builtin_fmaf(x0, lo16(wr[i][e]), __builtin_fmaf(x1, hi16(wr[i][e]), sr)); } }
            float v = tadd32(sg, sr);
            v = tadd16(v, v); v += dppf<ROR8>(v); v += dppf<0x124>(v); v += dppf<0x122>(v); v += dppf<0x121>(v);
            if ((l & 7) == t) res = v; }
        const unsigned rb = cvt_pk(res, 0.f), tok = 64 * h + 8 * w8 + (l & 7);
        const unsigned val = l < 32 ? __float_as_uint(rbf(1.0f / (1.0f + __expf(-lo16(rb))))) : (((rb & 0x8000u) ? ~rb : rb | 0x8000u) << 16) | (4095u - tok);
        if ((l & 31) < 8) gst(a.gate + (l >> 5) * S_TOK + tok, val);
        WAITV(0); BAR();
        if (tid == 0) gst(a.ctl + C_R + h, 1u);
        MARK(90);
        if (w8 == 0) flags_wait(fG, 1, 1, l, err);
        BAR();
    }
    const unsigned xo = (l >> 3) * (DM * 2) + ((l & 7) ^ q) * 16;
    auto xpiece = [&](int s, int i) { glds(a.x + (size_t)(32 * s + 8 * (i & 3)) * DM + (i >> 2) * 64, xo ^ ((i & 1) * 64), L + (s & 1) * 65536 + i * 1024); };
    v4u wf[32];
    { const Tile T{(m == 0 ? a.Wk : (m == 1 ? a.Wv : a.Wq)) + h * DH, HD, 0};
      tile_issue(T, 0, L, w8, l); tile_issue(T, 1, L, w8, l);
      tile_load(T, wf, L, w8, l, [&](int b) { if (b == 0) { _Pragma("unroll 2") for (int i = 0; i < 8; ++i) xpiece(0, w8 * 8 + i); } return true; }); }
    MARK(0);
    const unsigned rb0 = L + (w8 >> 2) * 32768 + (n >> 3) * 1024 + (n & 7) * 128 + (q ^ (n >> 1)) * 16, rb1 = rb0 ^ 64;
    const rsrc_t rq = mkrs(a.qkv, NSEG * NH * 24576), ro_ = mkrs(a.ox, NSEG * 262144);
    int sto; { int ck = 4 * (w8 & 3) + (q >> 1), mt = q & 1; int i = m == 1 ? ck >> 2 : 2 * (ck >> 3) + (ck & 1), qq = m == 1 ? ck & 3 : (ck >> 1) & 3; sto = h * 24576 + m * 8192 + ((mt * 4 + i) * 64 + qq * 16 + n) * 16; }
    const unsigned* fM = a.ctl + C_M + h * 32; unsigned* const fA = a.ctl + C_A + (h >> 3) * 32 + m * 8 + (h & 7);
    const unsigned* fD = (l >= 8 && l < 11 && l - 8 != m) ? fG + (l - 8) : fM + (l & 3);
    const int rt = w8 & 1, eh = (w8 >> 1) & 1;
    const int stx = (h >> 3) * 65536 + ((h & 7) * 2 + eh) * 4096 + rt * 2048 + ((2 * (q & 1) + (q >> 1)) * 16 + n) * 16;
    auto vissue = [&](int s) { glds1<2>(a.qkv + ((size_t)s * NH + h) * 24576 + 8192 + (w8 & 3) * 2048, l * 16, L_AT + (w8 & 3) * 2048); };
    struct KQ { v4u k[8], q[4]; };
    auto kqload = [&](int s) { KQ r;
#pragma unroll
        for (int i = 0; i < 4; ++i) { const int so = (s * NH + h) * 24576; r.q[i] = __builtin_amdgcn_raw_buffer_load_b128(rq, l * 16 + i * 1024, so + 16384 + rt * 4096, 16);
            r.k[i] = __builtin_amdgcn_raw_buffer_load_b128(rq, l * 16 + i * 1024, so, 16); r.k[4 + i] = __builtin_amdgcn_raw_buffer_load_b128(rq, l * 16 + i * 1024, so + 4096, 16); }
        FENCE();
        return r; };
    auto attend = [&](int s, const KQ& kq) {
        frag_cd sc[3] = {};
#pragma unroll
        for (int i = 0; i < 4; ++i) {
            sc[0] = MFMA(kq.k[i], kq.q[i], sc[0]);
            sc[1] = MFMA(kq.k[4 + i], kq.q[i], sc[1]);
            sc[2] = MFMA(*lp<v4u>(L_KM + (8 * (i >> 1) + 2 * q + (i & 1)) * 128 + (n & 7) * 16), kq.q[i], sc[2]);
        }
        v4u vf[4]; v2u vm[4];
#pragma unroll
        for (int et = 0; et < 4; ++et) { const int ck = 8 * eh + 2 * et + ((l >> 1) & 1);
            const unsigned p = L_AT + (ck * 16 + 4 * q + (n >> 2)) * 16 + (l & 1) * 8;
            vf[et] = cat2(trd(p), trd(p + 4096)); vm[et] = trd(L_KM + 2048 + ck * 128 + (4 * (q & 1) + (n >> 2)) * 16 + (l & 1) * 8); }
        float mx = -INFINITY;
#pragma unroll
        for (int t = 0; t < 3; ++t)
#pragma unroll
            for (int e = 0; e < 4; e += 2) { unsigned p = cvt_pk(sc[t][e], sc[t][e + 1]); p = cvt_pk(lo16(p) * SCALE, hi16(p) * SCALE); float v0 = lo16(p), v1 = hi16(p);
                if (t < 2 && 16 * t + 4 * q + e > 16 * rt + n) v0 = -999292928.0f;
                if (t < 2 && 16 * t + 4 * q + e + 1 > 16 * rt + n) v1 = -999292928.0f;
                sc[t][e] = v0; sc[t][e + 1] = v1; mx = fmaxf(fmaxf(mx, v0), v1); }
        mx = tmax16(mx); mx = tmax32(mx);
        float sum = 0.f;
#pragma unroll
        for (int t = 0; t < 3; ++t)
#pragma unroll
            for (int e = 0; e < 4; ++e) { float v = __builtin_amdgcn_exp2f((sc[t][e] - mx) * LOG2E); if (t == 2 && q >= 2) v = 0.f; sc[t][e] = v; sum += v; }
        sum = tadd16(sum, sum); sum = tadd32(sum, sum);
        const float inv = __builtin_amdgcn_rcpf(sum);
        const v4u px = {cvt_pk(sc[0][0] * inv, sc[0][1] * inv), cvt_pk(sc[0][2] * inv, sc[0][3] * inv), cvt_pk(sc[1][0] * inv, sc[1][1] * inv), cvt_pk(sc[1][2] * inv, sc[1][3] * inv)};
        const v4u pm = {cvt_pk(sc[2][0] * inv, sc[2][1] * inv), cvt_pk(sc[2][2] * inv, sc[2][3] * inv), 0u, 0u};
        frag_cd o[4];
#pragma unroll
        for (int et = 0; et < 4; ++et) { o[et] = MFMA(vf[et], px, (frag_cd{})); o[et] = MFMA((v4u{vm[et].x, vm[et].y, 0u, 0u}), pm, o[et]); }
        MARK(65 + s / 3); FENCE();
        STB(pack_swap(o[0], o[1]), ro_, stx, s * 262144); STB(pack_swap(o[2], o[3]), ro_, stx + 1024, s * 262144);
    };
    int sa = m, staged = 0;
    unsigned pacc[8] = {}, plast = 0; (void)pacc; (void)plast;
    PT(7);
    if (w8 >= 4) {
        int pend = 0, puba = 0;
        unsigned fv = 0;
        for (int sg = 0; sg < NSEG; ++sg) {
            const int done = sg < 4 ? sg : sg - 1;
            if (sg < 4) WAITV(0);
            if (w8 == 4) { bool ok = !staged && sa < done && __builtin_amdgcn_ballot_w64(fv <= (unsigned)sa) == 0; if (l == 0) lp<unsigned>(L_DEC)[sg & 1] = ok; WAITL(); }
            PT(0);
            BAR();
            PT(1);
            __builtin_amdgcn_s_setprio(2);
            if (tid == 256) { gst(fG + m, done); if (puba) gst(fA, puba); }
            puba = pend; pend = 0;
            if (staged) { pend = sa + 1; sa += 3; }
            staged = RFL(ldsr(L_DEC + 4 * (sg & 1)));
            if (staged) { vissue(sa); glds1<1>(a.kvm + ((size_t)sa * NH + h) * 4096 + (w8 & 3) * 1024, l * 16, L_KM + (w8 & 3) * 1024); }
            if (w8 == 4) fv = gld(fD);
            FENCE();
            const int sn = sg + 1 < NSEG ? sg + 1 : sg - 1;
            frag_cd acc[4];
            seg_gemm(wf, rb0 + (sg & 1) * 65536, rb1 + (sg & 1) * 65536, acc, [&](int i) { if (i < 2 * NB && !(i & 1)) xpiece(sn, (w8 & 3) * 16 + i / 2); if (i == 2 * NB) __builtin_amdgcn_s_setprio(0); });
            PT(2);
            half_get(L_EX, w8, l, acc, sg + 1);
            WAITV(0);
            STB(pack_swap(acc[0], acc[1]), rq, sto, sg * (NH * 24576)); STB(pack_swap(acc[2], acc[3]), rq, sto + (m == 1 ? 512 : 256), sg * (NH * 24576));
            PT(3);
        }
        PFLUSH();
        WAITV(0); BAR();
        if (tid == 256) { gst(fG + m, NSEG); if (pend | puba) gst(fA, pend | puba); }
        for (; sa < NSEG; sa += 3) {
            if (!staged) {
                if (m != 1) flags_wait(fG + 1, 1, sa + 1, l, err);
                vissue(sa);
                if (w8 == 4) { flags_wait(fM, 4, sa + 1, l, err); glds1<4>(a.kvm + ((size_t)sa * NH + h) * 4096, l * 16, L_KM); }
                WAITV(0); BAR();
            }
            staged = 0;
            BAR();
            if (tid == 256) gst(fA, sa + 1);
        }
    } else {
        __builtin_amdgcn_s_setprio(1);
        WAITV(0);
        for (int sg = 0; sg < NSEG; ++sg) {
            PT(0);
            BAR();
            PT(1);
            const int now = staged;
            staged = RFL(ldsr(L_DEC + 4 * (sg & 1)));
            frag_cd acc[4];
            const int sn = sg + 1 < NSEG ? sg + 1 : sg - 1;
            auto side = [&](int i) { if (i < 2 * (16 - NB) && !(i & 1)) xpiece(sn, w8 * 16 + NB + i / 2); };
            if (now) {
                const KQ kq = kqload(sa);
                seg_gemm(wf, rb0 + (sg & 1) * 65536, rb1 + (sg & 1) * 65536, acc, side);
                half_put(L_EX, w8, l, acc, sg + 1);
                attend(sa, kq);
                WAITV(2);
                sa += 3;
                PT(4);
            } else {
                seg_gemm(wf, rb0 + (sg & 1) * 65536, rb1 + (sg & 1) * 65536, acc, side);
                PT(2);
                half_put(L_EX, w8, l, acc, sg + 1);
                WAITV(0);
                PT(3);
            }
            MARK(sg + 1);
        }
        PFLUSH();
        WAITV(0); BAR();
        for (; sa < NSEG; sa += 3) {
            if (!staged) { if (m != 0) flags_wait(fG, 1, sa + 1, l, err); if (m != 2) flags_wait(fG + 2, 1, sa + 1, l, err); }
            const KQ kq = kqload(sa);
            if (!staged) BAR();
            staged = 0;
            attend(sa, kq);
            WAITV(0); BAR();
        }
    }
}

__device__ __forceinline__ void owork(const Args& a, const int nt, const int kq, const unsigned L) {
    const int tid = threadIdx.x, w8 = RFL(tid >> 6), l = tid & 63, n = l & 15, q = l >> 4, j = nt * 4 + kq;
    unsigned* const err = a.ctl + C_ERR;
    const unsigned L_XS = L + 131072, L_EX = L_XS, L_HS = L_XS + 16384 + 64, L_KEY = L_HS + 2048, L_Y = L_KEY + 64, L_GU = L_Y + 1024;
    if (tid < 4) lp<unsigned>(L_EX + 16384)[tid] = 0;
    const rsrc_t rg = mkrs(a.gate, 2 * S_TOK * 4), ry = mkrs(a.ypart, 32 * 28 * 1024);
    if (w8 == 0) {
        unsigned char* p = a.ring + ((l >> 1) & 1) * CHB + (l & 1) * 8192 + j * 16384 + ((l >> 2) & 3) * 2048 + 1024;
        unsigned best = 0;
        if (l < 16) gst((unsigned*)p, ~0u);
        WAITV(0);
#pragma unroll 1
        for (int i = 0; i < 16; ++i) { u64 t1, t2; unsigned v;
            const u64 pi = ((u64)__builtin_amdgcn_readlane((unsigned)((u64)p >> 32), i) << 32) | (unsigned)__builtin_amdgcn_readlane((unsigned)(u64)p, i);
            asm volatile("s_nop 4\n\ts_memtime %0\n\ts_waitcnt lgkmcnt(0)\n\tglobal_load_dword %2, %3, %4 sc1\n\ts_waitcnt vmcnt(0)\n\ts_memtime %1\n\ts_waitcnt lgkmcnt(0)"
                         : "=&s"(t1), "=&s"(t2), "=&v"(v) : "v"(0u), "s"(pi) : "memory");
            if ((l & 3) == (i & 3)) best = max(best, ~(unsigned)(t2 - t1)); }
        if (l < 16) gst((unsigned*)p, ~0u);
        if (l < 4) __hip_atomic_fetch_max(a.ctl + C_CAL + l, best, __ATOMIC_RELAXED, __HIP_MEMORY_SCOPE_AGENT);
        WAITV(0);
        if (l == 0) __hip_atomic_fetch_add(a.ctl + C_READY, 1u, __ATOMIC_RELAXED, __HIP_MEMORY_SCOPE_AGENT);
        MARK(66);
        flags_wait(a.ctl + C_G + j * 32, 1, 1, l, err);
    } else if (w8 == 1) {
        for (int i = 0; !flags_ok(a.ctl + C_R, 32, 1, l); ++i) { __builtin_amdgcn_s_sleep(32); if (i > GCAP) fatal(err); }
        v4u kv[8]; unsigned prev = ~0u, mine = 0;
#pragma unroll
        for (int i = 0; i < 8; ++i) kv[i] = __builtin_amdgcn_raw_buffer_load_b128(rg, S_TOK * 4 + l * 16, i * 1024, 16);
#pragma unroll 1
        for (int c = 0; c < TOPC; ++c) { unsigned mx = 0;
            FENCE();
#pragma unroll
            for (int i = 0; i < 8; ++i)
#pragma unroll
                for (int e = 0; e < 4; ++e) mx = max(mx, kv[i][e] < prev ? kv[i][e] : 0u);
            mx = max(mx, dppu<0x128>(mx)); mx = max(mx, dppu<0x124>(mx)); mx = max(mx, dppu<0x122>(mx)); mx = max(mx, dppu<0x121>(mx));
            { v2u r = sw16(mx, mx); mx = max(r.x, r.y); r = sw32(mx, mx); mx = max(r.x, r.y); }
            prev = RFL(mx); if ((l & 7) == c) mine = prev; }
        if (l < 8) lp<unsigned>(L_KEY)[l] = mine;
#pragma unroll 1
        for (int i = 0; i < 16; ++i) glds(a.x + i * 64, (4095u - (mine & 4095u)) * (DM * 2) + (l >> 3) * 16, L_XS + i * 1024);
        WAITV(0); WAITL();
    }
    BAR();
    v4u wf[32];
    {
      auto tile = [&](int t) { return t < 2 ? Tile{(t ? a.wu : a.wg) + j * 128, FF, 0} : (t == 2 ? Tile{a.wd + (size_t)j * 128 * DM, DM, 1} : Tile{a.Wo + (size_t)kq * 1024 * DM + nt * 128, DM, 0}); };
      int ti = j < FF / 128 ? 0 : 3;
      Tile T = tile(ti);
      tile_issue(T, 0, L, w8, l); tile_issue(T, 1, L, w8, l);
      frag_cd gv[2] = {};
#pragma unroll 1
      for (;; ++ti) {
        const Tile N = tile(ti + 1);
        tile_load(T, wf, L, w8, l, [&](int b) { if (ti < 3) tile_issue(N, b, L, w8, l); return ti < 3; });
        T = N;
        if (ti == 3) break;
        if (ti < 2) {
            frag_cd acc[2] = {};
#pragma unroll
            for (int ks = 0; ks < 16; ++ks) { const v4u xf = *lp<v4u>(L_XS + ((w8 >> 2) * 16 + ks) * 512 + q * 128 + (n & 7) * 16); acc[0] = MFMA(wf[2 * ks], xf, acc[0]); acc[1] = MFMA(wf[2 * ks + 1], xf, acc[1]); }
            if (ti == 1) {
                frag_cd g = n < 8 ? gv[0] : gv[1], u = n < 8 ? acc[0] : acc[1];
                if (w8 >= 4) { *lp<frag_cd>(L_GU + (w8 & 3) * 2048 + l * 16) = g; *lp<frag_cd>(L_GU + (w8 & 3) * 2048 + 1024 + l * 16) = u; WAITL(); }
                BAR();
                if (w8 < 4) { g += *lp<frag_cd>(L_GU + w8 * 2048 + l * 16); u += *lp<frag_cd>(L_GU + w8 * 2048 + 1024 + l * 16);
                    float hv[4];
#pragma unroll
                    for (int e = 0; e < 4; ++e) { const float gg = rbf(g[e]); hv[e] = rbf(gg / (1.0f + __expf(-gg))) * rbf(u[e]); }
                    *lp<v2u>(L_HS + w8 * 512 + (2 * (n >> 3) + (q >> 1)) * 128 + (n & 7) * 16 + (q & 1) * 8) = v2u{cvt_pk(hv[0], hv[1]), cvt_pk(hv[2], hv[3])};
                    WAITL(); } }
            gv[0] = acc[0]; gv[1] = acc[1];
        } else {
#pragma unroll
            for (int cb = 0; cb < 4; ++cb) { frag_cd acc[2] = {};
#pragma unroll
                for (int i = 0; i < 4; ++i) { const v4u hf = *lp<v4u>(L_HS + i * 512 + q * 128 + (n & 7) * 16); acc[0] = MFMA(wf[8 * cb + 2 * i], hf, acc[0]); acc[1] = MFMA(wf[8 * cb + 2 * i + 1], hf, acc[1]); }
                STB(__builtin_bit_cast(v4u, n < 8 ? acc[0] : acc[1]), ry, ((w8 & 3) * 28 + j) * 1024 + ((n >> 3) * 32 + q * 8 + (n & 7)) * 16, ((w8 >> 2) * 4 + cb) * (4 * 28 * 1024)); }
        }
        MARK(67 + ti);
      }
      if (tid == 0) gst(a.ctl + C_Y + j, 1u);
    }
    MARK(0);
    const unsigned mykey = lp<unsigned>(L_KEY)[l & 7];
    if (w8 == 0) {
        flags_wait(a.ctl + C_Y, FF / 128, 1, l, err);
        frag_cd y = {};
#pragma unroll 1
        for (int i = 0; i < 28; i += 7) { v4u t[7];
#pragma unroll
            for (int u = 0; u < 7; ++u) t[u] = __builtin_amdgcn_raw_buffer_load_b128(ry, l * 16, (j * 28 + i + u) * 1024, 16);
#pragma unroll
            for (int u = 0; u < 7; ++u) y += __builtin_bit_cast(frag_cd, t[u]); }
        const float r = hi16((mykey & 0x80000000u) ? mykey & 0x7fffffffu : ~mykey), rs = rbf(1.0f / (1.0f + __expf(-r)));
#pragma unroll
        for (int e = 0; e < 4; ++e) y[e] = rbf(rbf(y[e]) * rs);
        *lp<frag_cd>(L_Y + l * 16) = y; WAITL();
    }
    const unsigned* fA = a.ctl + C_A + kq * 32; unsigned* const fP = a.ctl + C_P + nt * 32;
    const rsrc_t rp = mkrs(a.part, NSEG * 524288), rx = mkrs(a.x, S_TOK * DM * 2);
    const unsigned* fW = l < 32 ? fA + min(l, 23) : fP + (l & 3) * 4 + kq;
    const unsigned rb = L + (w8 >> 2) * 32768 + l * 16;
    unsigned fv = 0;
    auto a_ok = [&](int s) { return ((__builtin_amdgcn_ballot_w64(fv <= (unsigned)s) >> ((s % 3) * 8)) & 255) == 0; };
    auto p_ok = [&](int s) { return (__builtin_amdgcn_ballot_w64(fv <= (unsigned)s) >> 32) == 0; };
    unsigned pacc[8] = {}, plast = 0; (void)pacc; (void)plast;
    PT(7);
    if (w8 >= 4) {
        auto oissue = [&](int s) {
#pragma unroll
            for (int i = 0; i < 4; ++i) glds1<4>(a.ox + (size_t)s * 262144 + kq * 65536 + (w8 & 3) * 16384 + i * 4096, l * 16, L + (s & 1) * 65536 + (w8 & 3) * 16384 + i * 4096);
        };
        unsigned* const fO = fP + kq * 4 + (w8 & 3);
        const int stp = ((nt * 4 + (w8 & 3)) * 4 + kq) * 4096 + l * 16;
        int pre = 0;
        for (int s = 0; s < NSEG; ++s) {
            if (pre == s) {
                WAITV(0);
                if (l == 0) gst(fO, s);
                for (int i = 0;; ++i) { fv = gld(fW); WAITV(0); if (a_ok(s)) break; __builtin_amdgcn_s_sleep(2); if (i > GCAP) fatal(err); }
                PT(0);
                oissue(s); ++pre;
                WAITV(0);
                PT(1);
            }
            BAR();
            PT(2);
            const bool nx = s + 1 < NSEG && a_ok(s + 1);
            fv = gld(fW);
            FENCE();
            PT(3);
            const unsigned char* src = a.ox + (size_t)(s + 1) * 262144 + kq * 65536 + (w8 & 3) * 16384; const unsigned dst = L + ((s + 1) & 1) * 65536 + (w8 & 3) * 16384;
            frag_cd acc[4];
            __builtin_amdgcn_s_setprio(2);
            seg_gemm(wf, rb + (s & 1) * 65536, rb + (s & 1) * 65536 + 1024, acc, [&](int i) { if (i < 16 && nx) glds1<1>(src + i * 1024, l * 16, dst + i * 1024); if (i == 16) __builtin_amdgcn_s_setprio(0); });
            half_get(L_EX, w8, l, acc, s + 1);
            WAITV(0);
            if (l == 0) gst(fO, s);
#pragma unroll
            for (int i = 0; i < 4; ++i) STB(__builtin_bit_cast(v4u, acc[i]), rp, stp + i * 1024, s * 524288);
            pre += nx;
            PT(4);
        }
        WAITV(0);
        if (l == 0) gst(fO, NSEG);
    } else {
        const int fcol = nt * 128 + (2 * kq + (w8 >> 1)) * 16 + 4 * q, frow = 16 * (w8 & 1) + n;
        const unsigned seltok = 4095u - (mykey & 4095u);
        struct Fin { v4u p[4]; v2u xv; float gt; };
        auto fissue = [&](int s, bool on) { Fin f; int k = on ? 0 : 0x40000000; asm("" : "+s"(k));
#pragma unroll
            for (int i = 0; i < 4; ++i) f.p[i] = __builtin_amdgcn_raw_buffer_load_b128(rp, k + j * 16384 + w8 * 1024 + l * 16, s * 524288 + i * 4096, 16);
            f.xv = __builtin_amdgcn_raw_buffer_load_b64(rx, k + (frow * DM + fcol) * 2, s * 65536, 0);
            f.gt = __uint_as_float(__builtin_amdgcn_raw_buffer_load_b32(rg, k + frow * 4, s * 128, 16));
            return f;
        };
        auto finish = [&](int s, const Fin& f) {
            const int tok = 32 * s + frow; const float gt = f.gt, omg = rbf(1.0f - gt);
            float b[4] = {lo16(f.xv.x), hi16(f.xv.x), lo16(f.xv.y), hi16(f.xv.y)};
            for (unsigned mk = (unsigned)__builtin_amdgcn_ballot_w64((seltok >> 4) == (unsigned)(2 * s + (w8 & 1))) & 255u; mk; mk &= mk - 1) {
                const int slot = __builtin_ctz(mk);
                const frag_cd y = *lp<frag_cd>(L_Y + ((w8 >> 1) * 32 + q * 8 + slot) * 16);
                if (__builtin_amdgcn_readlane(seltok, slot) == (unsigned)tok) {
#pragma unroll
                    for (int e = 0; e < 4; ++e) b[e] = rbf(b[e] + y[e]); }
            }
            v2u o;
#pragma unroll
            for (int e = 0; e < 4; e += 2) { float t[2];
#pragma unroll
                for (int u = 0; u < 2; ++u) t[u] = (__uint_as_float(f.p[0][e + u]) + __uint_as_float(f.p[1][e + u])) + (__uint_as_float(f.p[2][e + u]) + __uint_as_float(f.p[3][e + u]));
                unsigned av = cvt_pk(t[0], t[1]); av = cvt_pk(gt * lo16(av), gt * hi16(av)); const unsigned bv = cvt_pk(omg * b[e], omg * b[e + 1]);
                o[e >> 1] = cvt_pk(lo16(av) + lo16(bv), hi16(av) + hi16(bv)); }
            return o;
        };
        auto ostore = [&](int s, v2u o) { *(v2u*)(a.out + (size_t)(32 * s + frow) * DM + fcol) = o; };
        int fin = 0;
        for (int s = 0; s <= NSEG; ++s) {
            for (int i = 0; fin < s && (s == NSEG || !a_ok(s)); ++i) {
                fv = gld(fW); WAITV(0);
                if (p_ok(fin)) { FENCE(); ostore(fin, finish(fin, fissue(fin, true))); ++fin; }
                else { __builtin_amdgcn_s_sleep(2); if (i > GCAP) fatal(err); }
            }
            if (s == NSEG) break;
            PT(0);
            BAR();
            PT(2);
            const bool fi = fin < s && p_ok(fin);
            const Fin f = fissue(fin, fi);
            fv = gld(fW);
            FENCE();
            PT(3);
            __builtin_amdgcn_s_setprio(1);
            frag_cd acc[4];
            seg_gemm(wf, rb + (s & 1) * 65536, rb + (s & 1) * 65536 + 1024, acc, [](int) {});
            half_put(L_EX, w8, l, acc, s + 1);
            WAITV(0);
            PT(4);
            if (fi) { ostore(fin, finish(fin, f)); ++fin; }
            MARK(1 + s);
            PT(5);
        }
    }
    PFLUSH();
    MARK(65);
}

__global__ void __launch_bounds__(512, 2) k_mega(Args a) {
    __shared__ __attribute__((aligned(16))) unsigned char smem[163840];
    const unsigned L = (unsigned)(size_t)(LAS unsigned char*)smem;
    const int b = blockIdx.x, u = (b & 3) + 4 * (b >> 3);
    MARK(255);
    if (!(b & 4)) chain(a, u, L);
    else if (u < 96) gwork(a, (u >> 2) % 3, (u & 3) * 8 + (u >> 2) / 3, L);
    else owork(a, (u >> 2) - 24, u & 3, L);
}

static constexpr int g_force = 0; static constexpr int g_d1 = 925, g_d2 = 1925, g_per = 2450, g_dinit = 250; static constexpr unsigned* g_dbg = nullptr;
extern "C" void kernel_launch(void* const* d_in, const int* in_sizes, int n_in, void* d_out, int out_size, void* d_ws, size_t ws_size, hipStream_t stream) {
    auto in = [&](int i) { return (const bf16_t*)d_in[i]; };
    unsigned char* ws = (unsigned char*)d_ws; size_t off = 0;
    auto alloc = [&](size_t bytes) { unsigned char* p = ws + off; off += (bytes + 255) & ~(size_t)255; return p; };
    unsigned* ctl = (unsigned*)alloc(C_WORDS * 4);
    unsigned* gate = (unsigned*)alloc(2 * S_TOK * 4); unsigned char* ypart = alloc(32 * 28 * 1024);
    off += (size_t)(-(uintptr_t)(ws + off)) & ((2u << 20) - 1);
    unsigned char* ring = alloc(RING_BYTES);
    unsigned char* qkv = alloc((size_t)NSEG * NH * 24576); unsigned char* kvm = alloc((size_t)NSEG * NH * 4096);
    unsigned char* ox = alloc((size_t)NSEG * 262144); unsigned char* part = alloc((size_t)NSEG * 524288);
    (void)hipMemsetAsync(ctl, 0, C_WORDS * 4, stream);
    Args a{in(0), in(1), in(2), in(3), in(4), in(5), (bf16_t*)d_out, ctl, ring, qkv, kvm, ox, part, in(8), in(9), in(10), in(11), in(12), gate, ypart, g_dbg,
           g_d1 * 16 / 10, g_d2 * 16 / 10, g_per * 16 / 10, g_dinit * 16 / 10 + (g_force << 16)};
    k_mega<<<256, 512, 0, stream>>>(a);
}
```

```cpp
#include <hip/hip_runtime.h>
#include <stdint.h>
#include <type_traits>

typedef unsigned short bf16_t;
typedef unsigned long long u64;
typedef float frag_cd __attribute__((ext_vector_type(4)));
typedef unsigned v4u __attribute__((ext_vector_type(4)));
typedef unsigned v8u __attribute__((ext_vector_type(8)));
typedef unsigned v2u __attribute__((ext_vector_type(2)));
typedef float v2f __attribute__((ext_vector_type(2)));
typedef __bf16 bf16x2_t __attribute__((ext_vector_type(2)));
typedef __bf16 v8bf __attribute__((ext_vector_type(8)));
typedef __bf16 v16bf __attribute__((ext_vector_type(16)));
typedef short v4s_t __attribute__((ext_vector_type(4)));
typedef __amdgpu_buffer_rsrc_t rsrc_t;
#define LAS __attribute__((address_space(3)))
#define GAS __attribute__((address_space(1)))

static constexpr int S_TOK = 2048, DM = 1024, NH = 32, DH = 128, MM = 8, SEG = 32, NSEG = 64, HD = NH * DH, FF = 3584, TOPC = 8;
static constexpr int C_ERR = 0, C_READY = 32, C_CAL = 64  , C_G = 128  , C_M = C_G + 4096  , C_A = C_M + 4096  ,
                     C_P = C_A + 1024  , C_R = C_P + 4096, C_Y = C_R + 1024  , C_WORDS = C_Y + 1024;
static constexpr int RING_BYTES = 4 << 20, SLOT = 1 << 20, CHB = 2 << 20, L_H3 = 192 << 10, L_TS = 256 << 10, SLOT3 = 192 << 10;
static constexpr unsigned SPIN_CAP = 1u << 18;
static constexpr int OOB = 0x7ffffff0, LCAP = 1 << 22, GCAP = 1 << 20;
static constexpr float SCALE = 0.08838834764831845f, LOG2E = 1.4426950408889634f;

struct Args {
    const bf16_t* x; const bf16_t* mem0; const bf16_t* Wq; const bf16_t* Wk; const bf16_t* Wv; const bf16_t* Wo;
    bf16_t* out; unsigned* ctl; unsigned char* ring; unsigned char* qkv; unsigned char* kvm; unsigned char* ox; unsigned char* part;
    const bf16_t* wr; const bf16_t* wg; const bf16_t* wu; const bf16_t* wd; const bf16_t* gw;
    unsigned* gate  ; unsigned char* ypart; unsigned* dbg;
    int d1, d2, per, dinit;
};

__device__ __forceinline__ unsigned cvt_pk(float lo, float hi) { v2f v = {lo, hi}; return __builtin_bit_cast(unsigned, __builtin_convertvector(v, bf16x2_t)); }
__device__ __forceinline__ float lo16(unsigned p) { return __uint_as_float(p << 16); }
__device__ __forceinline__ float hi16(unsigned p) { return __uint_as_float(p & 0xffff0000u); }
__device__ __forceinline__ float rbf(float f) { return lo16(cvt_pk(f, 0.f)); }
template <int C> __device__ __forceinline__ unsigned dppu(unsigned v) { return (unsigned)__builtin_amdgcn_update_dpp(0, (int)v, C, 0xf, 0xf, true); }
template <int C> __device__ __forceinline__ float dppf(float v) { return __uint_as_float(dppu<C>(__float_as_uint(v))); }
#define ROR8 0x128
#define HMIR 0x141
__device__ __forceinline__ v2u sw16(unsigned a, unsigned b) { auto r = __builtin_amdgcn_permlane16_swap(a, b, false, false); return v2u{r[0], r[1]}; }
__device__ __forceinline__ v2u sw32(unsigned a, unsigned b) { auto r = __builtin_amdgcn_permlane32_swap(a, b, false, false); return v2u{r[0], r[1]}; }
__device__ __forceinline__ float tadd16(float a, float b) { v2u r = sw16(__float_as_uint(a), __float_as_uint(b)); return __uint_as_float(r.x) + __uint_as_float(r.y); }
__device__ __forceinline__ float tadd32(float a, float b) { v2u r = sw32(__float_as_uint(a), __float_as_uint(b)); return __uint_as_float(r.x) + __uint_as_float(r.y); }
__device__ __forceinline__ float tmax16(float a) { v2u r = sw16(__float_as_uint(a), __float_as_uint(a)); return fmaxf(__uint_as_float(r.x), __uint_as_float(r.y)); }
__device__ __forceinline__ float tmax32(float a) { v2u r = sw32(__float_as_uint(a), __float_as_uint(a)); return fmaxf(__uint_as_float(r.x), __uint_as_float(r.y)); }
__device__ __forceinline__ unsigned umax3(unsigned a, unsigned b, unsigned c) { return max(max(a, b), c); }
__device__ __forceinline__ v4u pack_swap(frag_cd a, frag_cd b) { v2u x0 = sw16(cvt_pk(a[0], a[1]), cvt_pk(b[0], b[1])), x1 = sw16(cvt_pk(a[2], a[3]), cvt_pk(b[2], b[3])); return v4u{x0.x, x1.x, x0.y, x1.y}; }
#define MFMA(a, b, c) __builtin_amdgcn_mfma_f32_16x16x32_bf16(__builtin_bit_cast(v8bf, a), __builtin_bit_cast(v8bf, b), c, 0, 0, 0)
#define SMF(a, b, c, ix) __builtin_amdgcn_smfmac_f32_16x16x64_bf16(__builtin_bit_cast(v8bf, a), __builtin_bit_cast(v16bf, b), c, ix, 0, 0)
#define FENCE() asm volatile("" ::: "memory")
#define SB() __builtin_amdgcn_sched_barrier(0)
#define RFL(x) __builtin_amdgcn_readfirstlane(x)
#define BAR() do { FENCE(); __builtin_amdgcn_s_barrier(); FENCE(); } while (0)
#define WAITV(n) do { FENCE(); __builtin_amdgcn_s_waitcnt(0x0F70 | (n)); FENCE(); } while (0)
#define WAITL() asm volatile("s_waitcnt lgkmcnt(0)" ::: "memory")
__device__ __forceinline__ void STB(v4u v, rsrc_t rs, int off, int soff) { __builtin_amdgcn_raw_buffer_store_b128(v, rs, off, soff, 16); asm volatile("s_nop 1" :: "v"(v) : "memory"); }
template <class T> __device__ __forceinline__ LAS T* lp(unsigned a) { return (LAS T*)(size_t)a; }
__device__ __forceinline__ void glds(const void* g, unsigned voff, unsigned lds) { asm volatile("s_mov_b32 m0, %2\n\ts_nop 0\n\tglobal_load_lds_dwordx4 %0, %1" :: "v"(voff), "s"(g), "s"(lds) : "memory"); }
#define NTC " nt"
#define NTW " nt"
__device__ __forceinline__ void gldsc(const void* g, unsigned voff, unsigned lds) { asm volatile("s_mov_b32 m0, %2\n\ts_nop 0\n\tglobal_load_lds_dwordx4 %0, %1" NTC :: "v"(voff), "s"(g), "s"(lds) : "memory"); }
__device__ __forceinline__ void gldsw(const void* g, unsigned voff, unsigned lds) { asm volatile("s_mov_b32 m0, %2\n\ts_nop 0\n\tglobal_load_lds_dwordx4 %0, %1" NTW :: "v"(voff), "s"(g), "s"(lds) : "memory"); }
template <int N> __device__ __forceinline__ void glds1(const void* g, unsigned voff, unsigned lds) {
#define GL(o) "\n\tglobal_load_lds_dwordx4 %0, %1 offset:" #o " sc1"
    if constexpr (N == 1) asm volatile("s_mov_b32 m0, %2\n\ts_nop 0" GL(0) :: "v"(voff), "s"(g), "s"(lds) : "memory");
    if constexpr (N == 2) asm volatile("s_mov_b32 m0, %2\n\ts_nop 0" GL(0) GL(1024) :: "v"(voff), "s"(g), "s"(lds) : "memory");
    if constexpr (N == 4) asm volatile("s_mov_b32 m0, %2\n\ts_nop 0" GL(0) GL(1024) GL(2048) GL(3072) :: "v"(voff), "s"(g), "s"(lds) : "memory");
}
__device__ __forceinline__ rsrc_t mkrs(const void* p, int bytes) { return __builtin_amdgcn_make_buffer_rsrc((void*)p, 0, bytes, 0x00020000); }
__device__ __forceinline__ unsigned gld(const unsigned* p) { return __hip_atomic_load(p, __ATOMIC_RELAXED, __HIP_MEMORY_SCOPE_AGENT); }
__device__ __forceinline__ void gst(unsigned* p, unsigned v) { __hip_atomic_store(p, v, __ATOMIC_RELAXED, __HIP_MEMORY_SCOPE_AGENT); }
__device__ __forceinline__ void gst1(unsigned* p, unsigned v) { asm volatile("s_mov_b64 exec, 1\n\tglobal_store_dword %0, %1, %2 sc1\n\ts_mov_b64 exec, -1" :: "v"(0u), "v"(v), "s"(p) : "memory"); }
__device__ __forceinline__ unsigned rt16() { return (unsigned)__builtin_amdgcn_s_memrealtime() << 4; }

#define NSLOT 12
#define PT(k) do {} while (0)
#define PFLUSH() do {} while (0)
#define MARK(i) do {} while (0)
#define MARKC(i) do {} while (0)

#define VDEL1 6
#define VDEL2 8
#define SLIP_THR 200
#define SLIP_ADD 640
#define SLIP_INC 10
__device__ __forceinline__ void endpgm() { asm volatile("s_endpgm" ::: "memory"); }
__device__ __forceinline__ void fatal(unsigned* err) { asm volatile("global_store_dword %0, %1, off sc1\n\ts_waitcnt vmcnt(0)\n\ts_endpgm" :: "v"(err), "v"(1u) : "memory"); }
__device__ __forceinline__ unsigned ldsr(unsigned p) { return __hip_atomic_load(lp<unsigned>(p), __ATOMIC_RELAXED, __HIP_MEMORY_SCOPE_WORKGROUP); }
__device__ __forceinline__ v2u trd(unsigned p) { return __builtin_bit_cast(v2u, __builtin_amdgcn_ds_read_tr16_b64_v4i16(lp<v4s_t>(p))); }
__device__ __forceinline__ v8u cat(v2u a, v2u b, v2u c, v2u d) { return v8u{a.x, a.y, b.x, b.y, c.x, c.y, d.x, d.y}; }
__device__ __forceinline__ v4u cat2(v2u a, v2u b) { return v4u{a.x, a.y, b.x, b.y}; }
template <int SL, int FAR = 0> __device__ __forceinline__ void flags_wait(unsigned& v, const unsigned* p, unsigned need, unsigned* err) {
    for (int i = 0; __builtin_amdgcn_ballot_w64(v < need) != 0; ++i) {
        if (i) { if (FAR && __builtin_amdgcn_ballot_w64(v + 1 < need) != 0) __builtin_amdgcn_s_sleep(FAR); __builtin_amdgcn_s_sleep(SL); }
        if (v < need) v = gld(p);
        if (i > GCAP) fatal(err); }
    FENCE();
}
template <int SL> __device__ __forceinline__ void flags_wait(const unsigned* p, int nw, unsigned need, int l, unsigned* err) { unsigned v = need - 1; flags_wait<SL>(v, p + min(l, nw - 1) * 32, need, err); }

typedef unsigned v16u __attribute__((ext_vector_type(16)));
typedef unsigned v32u __attribute__((ext_vector_type(32)));
#define STR_(x) #x
#define STR(x) STR_(x)
#define A_STAMPS "0"
#define A_BENCH "0"
static_assert(SLOT == 0x100000 && NH * 4096 == 1 << 17 && SLOT3 == 0x30000 && NH * 24576 == 0xC0000, "");
#define A_CHAIN ".set STAMPS, " A_STAMPS "\n.set BENCH, " A_BENCH "\n" R"(
.macro WAITT T
  s_movk_i32 s76, 0x1000
1: s_memrealtime vcc
  s_sub_u32 s76, s76, 1
  s_cbranch_scc1 3f
  s_waitcnt lgkmcnt(0)
  s_lshl_b32 s77, vcc_lo, 4
  s_sub_i32 s77, \T, s77
  s_cmp_ge_i32 s77, 64
  s_cbranch_scc1 1b
  s_cmp_gt_i32 s77, 12
  s_cbranch_scc0 3f
2: s_sub_i32 s77, s77, 13
  s_cmp_gt_i32 s77, 12
  s_cbranch_scc1 2b
3: s_waitcnt lgkmcnt(0)
.endm
.macro STAMP k
.if STAMPS
  s_memtime s[8+2*\k:9+2*\k]
.endif
.endm
.macro NEXT t
.if STAMPS
  s_waitcnt lgkmcnt(0)
.irp k,0,1,2,3,4,5,6,7,8,9,10
  v_writelane_b32 v[\t], s[8+2*\k], \k
.endr
  s_lshl_b32 s76, s64, 3
  s_add_i32 s76, s76, s63
  s_mul_i32 s76, s76, 48
  v_mbcnt_lo_u32_b32 v[\t+1], -1, 0
  v_lshl_add_u32 v[\t+1], v[\t+1], 2, s76
  s_mov_b64 exec, 0x7ff
  ds_write_b32 v[\t+1], v[\t]
  s_mov_b64 exec, -1
.endif
  s_mov_b32 s64, s65
  s_add_i32 s65, s65, 1
  s_xor_b32 s66, s66, 0x100000
  s_xor_b32 s67, s67, 0x100000
  s_mov_b32 s76, s68
  s_mov_b32 s68, s69
  s_mov_b32 s69, s70
  s_mov_b32 s70, s76
  s_lshl_b32 s72, s64, 17
.endm
.macro RETRY c
.if BENCH
  s_bitset1_b64 s[80+2*\c:81+2*\c], s64
.endif
  s_sub_u32 s71, s71, 1
  s_cbranch_scc1 L_fatal
  s_sleep 2
.endm
.macro LD d, a, so, o
  buffer_load_dwordx4 v[\d:\d+3], v[\a], s[40:43], \so offen offset:\o sc1
.endm
.macro SM d, a, b
  v_smfmac_f32_16x16x64_bf16 v[\d:\d+3], v[\a:\a+3], v[\b:\b+7], v[SIX]
.endm
.macro MX0 a
  v_max_u32 v[MX], v[\a], v[\a+1]
  v_max3_u32 v[MX], v[MX], v[\a+2], v[\a+3]
.endm
.macro MX1 a
  v_max3_u32 v[MX], v[MX], v[\a], v[\a+1]
  v_max3_u32 v[MX], v[MX], v[\a+2], v[\a+3]
.endm
.macro HS a
  v_pk_add_f32 v[\a:\a+1], v[\a:\a+1], v[\a+2:\a+3]
.endm
.macro ZR a, n
.set zi, 0
.rept \n
  v_mov_b64 v[\a+zi:\a+zi+1], 0
.set zi, zi+2
.endr
.endm
.macro CAP
  s_sub_u32 s76, s76, 1
  s_cbranch_scc1 L_fatal
.endm
  s_mov_b32 s71, 0x40000
  s_mov_b64 s[80:81], 0
  s_mov_b64 s[82:83], 0
  s_mov_b64 s[84:85], 0
  s_mov_b64 s[86:87], 0
  s_cmp_gt_u32 s63, 3
  s_cbranch_scc1 LV_init
; ======== waves 0..3
.set O3,168
.set ST1,169
.set RW1,170
.set RW2,171
.set RW3,172
.set RR,173
.set FW,174
.set FR,175
.set SIX,176
.set STK,177
.set FL,183
.set MB,128
.set AC,144
.set TAG,160
.set MX,161
.set RES,162
.set FA,163
.set FB,164
.set RA,184
.set RB,190
.set KX,196
.macro PLQ f, r
  ds_read_b32 v[\f], v[FR]
  ds_read_b64 v[\r:\r+1], v[RR]
  ds_read_b64 v[\r+2:\r+3], v[RR] offset:2048
  ds_read_b64 v[\r+4:\r+5], v[RR] offset:4096
.endm
.macro FINQ r
  STAMP 7
  s_waitcnt lgkmcnt(6)
  v_pk_add_f32 v[AC+2:AC+3], v[AC+2:AC+3], v[\r:\r+1]
  s_waitcnt lgkmcnt(4)
  v_pk_add_f32 v[\r+2:\r+3], v[\r+2:\r+3], v[\r+4:\r+5]
  v_pk_add_f32 v[AC+2:AC+3], v[AC+2:AC+3], v[\r+2:\r+3]
  v_cvt_pk_bf16_f32 v[RES], v[AC+2], v[AC+3]
  buffer_store_dword v[RES], v[ST1], s[40:43], s66 offen sc1
.endm
.macro MEMQ
  LD MB, O3, s68, 0
  LD MB+4, O3, s68, 1024
  LD MB+8, O3, s68, 2048
  LD MB+12, O3, s68, 3072
.endm
.macro KVQ
  buffer_store_dword v[RES], v[STK], s[44:47], s72 offen sc1
.endm
.macro FLAGQ val
  v_mov_b32 v[MX], 0
  v_mov_b32 v[FA], \val
  s_mov_b64 exec, 1
  global_store_dword v[MX], v[FA], s[50:51] sc1
  s_mov_b64 exec, -1
.endm
.macro PHASEA R
LQ_loop\R:
  STAMP 0
  ZR AC, 8
  v_mov_b32 v[TAG], s65
.if \R == 3
  WAITT s73
.endif
  s_barrier
  STAMP 1
  MEMQ
  s_mov_b32 s76, 0x400000
LQ_a\R:
  s_waitcnt vmcnt(3)
  SM AC+8, MB, 64
  MX0 MB
  SM AC+12, MB, 96
  SM AC, MB, 0
  SM AC+4, MB, 32
.irp i,1,2
  s_waitcnt vmcnt(3-\i)
  SM AC+8, MB+4*\i, 64+8*\i
  MX1 MB+4*\i
  SM AC+12, MB+4*\i, 96+8*\i
  SM AC, MB+4*\i, 8*\i
  SM AC+4, MB+4*\i, 32+8*\i
.endr
  s_waitcnt vmcnt(0)
  SM AC+8, MB+12, 88
  MX1 MB+12
  SM AC+12, MB+12, 120
  v_cmp_eq_u32 vcc, -1, v[MX]
  SM AC, MB+12, 24
  s_cbranch_vccnz LQ_ra\R
  SM AC+4, MB+12, 56
  STAMP 2
  HS AC+8
  HS AC+12
  ds_write2_b32 v[RW2], v[AC+8], v[AC+12] offset1:1
  ds_write2_b32 v[RW3], v[AC+9], v[AC+13] offset1:1
  HS AC
  s_nop 2
  HS AC+4
  ds_write2_b32 v[RW1], v[AC+1], v[AC+5] offset1:1
  ds_write_b32 v[FW], v[TAG]
  STAMP 6
  PLQ FA, RA
  v_mov_b32 v[AC+2], v[AC]
  v_mov_b32 v[AC+3], v[AC+4]
LQ_p\R:
  PLQ FB, RB
  s_waitcnt lgkmcnt(7)
  v_cmp_gt_u32 vcc, s65, v[FA]
  s_cbranch_vccnz LQ_pb\R
  FINQ RA
LQ_pe\R:
  STAMP 3
  s_cmp_eq_u32 s64, 63
  s_cbranch_scc1 LQ_last\R
  s_waitcnt lgkmcnt(0)
  s_barrier
  STAMP 4
.endm
.macro COLDA R
LQ_pb\R:
  PLQ FA, RA
  CAP
  s_waitcnt lgkmcnt(7)
  v_cmp_gt_u32 vcc, s65, v[FB]
  s_cbranch_vccnz LQ_p\R
  FINQ RB
  s_branch LQ_pe\R
LQ_ra\R:
  RETRY 2
  MEMQ
  ZR AC, 8
  s_branch LQ_a\R
LQ_last\R:
  KVQ
  s_waitcnt vmcnt(0) lgkmcnt(0)
  s_barrier
.if \R == 2
  FLAGQ 64
.endif
  s_branch L_end
.endm
  s_mov_b32 s64, 0
  s_mov_b32 s65, 1
  s_mov_b32 s66, 0
  s_mov_b32 s67, 0x100000
  s_mov_b32 s68, 0x60000
  s_mov_b32 s69, 0
  s_mov_b32 s70, 0x30000
  s_mov_b32 s72, 0
  s_setprio 3
  s_cmp_eq_u32 s60, 2
  s_cbranch_scc1 LQ_loop2
  s_cmp_eq_u32 s60, 3
  s_cbranch_scc1 LQ_init3
; ---- waves 0,1: scores against the x rows
  PHASEA 0
  LD MB, 178, s66, 0
  LD MB+4, 178, s66, 1024
LQ_b0:
  s_waitcnt vmcnt(1)
  v_mfma_f32_16x16x32_bf16 v[AC+4:AC+7], v[KX+4:KX+7], v[MB:MB+3], 0
  MX0 MB
  v_mfma_f32_16x16x32_bf16 v[AC:AC+3], v[KX:KX+3], v[MB:MB+3], 0
  s_waitcnt vmcnt(0)
  v_mfma_f32_16x16x32_bf16 v[AC+4:AC+7], v[KX+12:KX+15], v[MB+4:MB+7], v[AC+4:AC+7]
  MX1 MB+4
  v_mfma_f32_16x16x32_bf16 v[AC:AC+3], v[KX+8:KX+11], v[MB+4:MB+7], v[AC:AC+3]
  v_cmp_eq_u32 vcc, -1, v[MX]
  s_nop 5
  s_cbranch_vccnz LQ_rb0
.irp e,0,1,2,3
  v_add_f32_dpp v[AC+\e], v[AC+4+\e], v[AC+\e] quad_perm:[2,3,0,1] row_mask:0xf bank_mask:0xf
.endr
  ds_write_b128 v[180], v[AC:AC+3]
  ds_write_b32 v[FW], v[TAG] offset:2048
  STAMP 5
  KVQ
  s_cmp_eq_u32 s64, 62
  s_cbranch_scc1 LQ_x2
  s_add_i32 s75, s64, 2
  v_cmp_ge_u32 vcc, s75, v[FL]
  s_cbranch_vccnz LQ_xw
LQ_x1:
  s_lshl_b32 s75, s64, 13
  s_and_b32 s75, s75, 0x2000
  s_add_i32 m0, s75, s61
  s_add_u32 s78, s52, 0xC2000
  s_addc_u32 s79, s53, 0
  global_load_lds_dwordx4 v[179], s[78:79] sc1
  global_load_lds_dwordx4 v[179], s[78:79] offset:1024 sc1
  global_load_lds_dwordx4 v[179], s[78:79] offset:2048 sc1
  global_load_lds_dwordx4 v[179], s[78:79] offset:3072 sc1
LQ_x2:
  global_load_dwordx4 v[KX:KX+3], v[179], s[52:53] sc1
  global_load_dwordx4 v[KX+4:KX+7], v[179], s[52:53] offset:1024 sc1
  global_load_dwordx4 v[KX+8:KX+11], v[179], s[52:53] offset:2048 sc1
  global_load_dwordx4 v[KX+12:KX+15], v[179], s[52:53] offset:3072 sc1
  s_add_i32 s75, s64, 3
  v_cmp_ge_u32 vcc, s75, v[FL]
  s_cbranch_vccz LQ_x3
  global_load_dword v[FL], v[181], s[54:55] sc1
LQ_x3:
  s_add_u32 s52, s52, 0xC0000
  s_addc_u32 s53, s53, 0
  NEXT MB
  s_branch LQ_loop0
  COLDA 0
LQ_rb0:
  RETRY 0
  LD MB, 178, s66, 0
  LD MB+4, 178, s66, 1024
  s_branch LQ_b0
LQ_xw:
  s_mov_b32 s76, 0x100000
LQ_xw1:
  global_load_dword v[FL], v[181], s[54:55] sc1
  s_waitcnt vmcnt(0)
  v_cmp_ge_u32 vcc, s75, v[FL]
  s_cbranch_vccz LQ_x1
  s_sleep 4
  CAP
  s_branch LQ_xw1
; ---- wave 2: scores against the memory rows
.macro QKQ
  LD MB, 178, s66, 0
  LD MB+4, 179, s66, 0
  LD MB+8, 178, s66, 1024
  LD MB+12, 179, s66, 1024
.endm
  PHASEA 2
  QKQ
LQ_b2:
  s_waitcnt vmcnt(3)
  MX0 MB
  s_waitcnt vmcnt(2)
  v_mfma_f32_16x16x32_bf16 v[AC:AC+3], v[MB+4:MB+7], v[MB:MB+3], 0
  MX1 MB+4
  s_waitcnt vmcnt(1)
  MX1 MB+8
  s_waitcnt vmcnt(0)
  v_mfma_f32_16x16x32_bf16 v[AC:AC+3], v[MB+12:MB+15], v[MB+8:MB+11], v[AC:AC+3]
  MX1 MB+12
  v_cmp_eq_u32 vcc, -1, v[MX]
  s_nop 3
  s_cbranch_vccnz LQ_rb2
  v_add_f32_dpp v[AC], v[AC+2], v[AC] quad_perm:[2,3,0,1] row_mask:0xf bank_mask:0xf
  v_add_f32_dpp v[AC+1], v[AC+3], v[AC+1] quad_perm:[2,3,0,1] row_mask:0xf bank_mask:0xf
  ds_write_b64 v[180], v[AC:AC+1]
  ds_write_b32 v[FW], v[TAG] offset:2048
  STAMP 5
  FLAGQ s64
  KVQ
  NEXT MB
  s_branch LQ_loop2
  COLDA 2
LQ_rb2:
  RETRY 0
  QKQ
  s_branch LQ_b2
; ---- wave 3: pacing, clearing, sum over the heads
.macro PARTQ
  LD MB, 178, s66, 0
  LD MB+4, 178, s66, 1024
  LD MB+8, 178, s66, 2048
  LD MB+12, 178, s66, 3072
.endm
LQ_init3:
  s_memrealtime vcc
  v_mov_b64 v[KX+4:KX+5], -1
  v_mov_b64 v[KX+6:KX+7], -1
  v_mov_b32 v[KX+8], s62
  s_waitcnt lgkmcnt(0)
  s_lshl_b32 s73, vcc_lo, 4
  PHASEA 3
  ds_read_b64 v[RA:RA+1], v[KX+8]
  s_waitcnt lgkmcnt(0)
  v_readfirstlane_b32 s73, v[RA]
  v_readfirstlane_b32 s74, v[RA+1]
  s_add_i32 s75, s56, s59
  s_add_i32 s75, s75, s73
  s_add_i32 s75, s75, s74
  WAITT s75
  buffer_store_dwordx4 v[KX+4:KX+7], v[178], s[40:43], s67 offen sc1
  buffer_store_dwordx4 v[KX+4:KX+7], v[178], s[40:43], s67 offen offset:1024 sc1
  buffer_store_dwordx4 v[KX+4:KX+7], v[178], s[40:43], s67 offen offset:2048 sc1
  buffer_store_dwordx4 v[KX+4:KX+7], v[178], s[40:43], s67 offen offset:3072 sc1
  buffer_store_dwordx4 v[KX+4:KX+7], v[179], s[40:43], s67 offen sc1
  buffer_store_dwordx2 v[KX+4:KX+5], v[182], s[40:43], s67 offen sc1
  buffer_store_dwordx2 v[KX+4:KX+5], v[180], s[40:43], s70 offen sc1
  buffer_store_dwordx2 v[KX+4:KX+5], v[181], s[40:43], s70 offen sc1
  KVQ
  STAMP 5
  s_lshl_b32 s75, s74, 1
  s_add_i32 s73, s73, s75
  s_add_i32 s75, s73, s57
  s_add_i32 s73, s73, s58
  s_add_i32 s73, s73, s74
  WAITT s75
  STAMP 8
  PARTQ
LQ_b3:
  s_memrealtime vcc
  s_mov_b32 s78, s73
  s_mov_b32 s79, s74
  s_waitcnt lgkmcnt(0)
  s_lshl_b32 s77, vcc_lo, 4
  s_sub_i32 s77, s77, s75
  s_cmp_gt_i32 s77, )" STR(SLIP_THR) R"(
  s_cbranch_scc1 LQ_slip
LQ_b4:
  s_and_b32 s78, s78, -2
  v_mov_b32 v[RA], s78
  v_mov_b32 v[RA+1], s79
  s_waitcnt vmcnt(3)
  v_mfma_f32_16x16x32_bf16 v[AC:AC+3], v[KX:KX+3], v[MB:MB+3], 0
  MX0 MB
.irp i,1,2,3
  s_waitcnt vmcnt(3-\i)
  v_mfma_f32_16x16x32_bf16 v[AC:AC+3], v[KX:KX+3], v[MB+4*\i:MB+4*\i+3], v[AC:AC+3]
  MX1 MB+4*\i
.endr
  v_cmp_eq_u32 vcc, -1, v[MX]
  STAMP 9
  s_nop 3
  s_cbranch_vccnz LQ_rb3
.irp e,0,1,2,3
  v_add_f32_dpp v[AC+\e], v[AC+\e], v[AC+\e] row_ror:8 row_mask:0xf bank_mask:0xf
.endr
  v_cvt_pk_bf16_f32 v[AC], v[AC], v[AC+1]
  v_cvt_pk_bf16_f32 v[AC+1], v[AC+2], v[AC+3]
  buffer_store_dwordx2 v[AC:AC+1], v[180], s[40:43], s69 offen sc1
  buffer_store_dwordx2 v[RA:RA+1], v[181], s[40:43], s69 offen sc1
  STAMP 10
.if BENCH
  s_memrealtime vcc
  s_waitcnt lgkmcnt(0)
  s_mov_b32 m0, s64
  s_mov_b32 s77, vcc_lo
  v_writelane_b32 v[FL], s77, m0
.endif
  NEXT MB
  s_branch LQ_loop3
  COLDA 3
LQ_rb3:
  RETRY 1
  PARTQ
  s_branch LQ_b3
LQ_slip:
.if BENCH
  s_bitset1_b64 s[86:87], s64
.endif
  s_add_i32 s78, s78, s77
  s_add_i32 s78, s78, )" STR(SLIP_ADD) R"(
  s_cmp_lt_u32 s64, 4
  s_cbranch_scc1 LQ_b4
  s_add_i32 s79, s79, )" STR(SLIP_INC) R"(
  s_min_u32 s79, s79, 4000
  s_branch LQ_b4
; ======== waves 4..7
.set UB,128
.set B1,160
.set O3,168
.set O1,169
.set OT,170
.set ST1,171
.set ST2,172
.set STK,173
.set RW,174
.set RR,175
.set FW,176
.set FR,177
.set FS,178
.set ASC,179
.set ASM,180
.set AVX,181
.set SIX,182
.set RES,184
.set TAG,185
.set MX,186
.set TM,187
.set MB,188
.set AC,204
.set TV,212
.set FA,214
.set RA,216
.set DD,188
.set SX,208
.set SMM,212
.set ZZ,224
.set VM,240
.set PA,248
.set VX,188
.set UU,220
.macro MEMV R
  LD MB, O3, s68, 0
  LD MB+4, O3, s68, 1024
  LD MB+8, O3, s68, 2048
  LD MB+12, O3, s68, 3072
.if \R == 2
  buffer_load_dwordx2 v[TV:TV+1], v[OT], s[40:43], s68 offen sc1
.endif
.endm
.macro SCV
  ds_read_b32 v[FA], v[FS]
  ds_read_b128 v[SX:SX+3], v[ASC]
  ds_read_b64 v[SMM:SMM+1], v[ASM]
.endm
.macro CZV t
  v_cvt_pk_bf16_f32 v[UB+8*\t+2], v[ZZ+4*\t], v[ZZ+4*\t+2]
  v_cvt_pk_bf16_f32 v[UB+8*\t+3], v[ZZ+4*\t+1], v[ZZ+4*\t+3]
.endm
.macro STEPV R
.set XL, (\R == 2) & 1
LV_loop\R:
  STAMP 0
  ZR AC, 4
  ZR ZZ, 8
  v_mov_b32 v[TAG], s65
  s_setprio 2
  s_barrier
.rept )" STR(VDEL1) R"(
  s_nop 7
.endr
  STAMP 1
  MEMV \R
  s_mov_b32 s76, 0x400000
LV_a\R:
  s_waitcnt vmcnt(3+XL)
  SM AC, MB, 64
  MX0 MB
  SM AC+4, MB, 96
.irp i,1,2,3
  s_waitcnt vmcnt(3+XL-\i)
  SM AC, MB+4*\i, 64+8*\i
  MX1 MB+4*\i
  SM AC+4, MB+4*\i, 96+8*\i
.endr
  v_cmp_eq_u32 vcc, -1, v[MX]
  STAMP 2
  s_nop 4
  s_cbranch_vccnz LV_ra\R
  HS AC
  HS AC+4
  ds_write2_b32 v[RW], v[AC], v[AC+4] offset1:1
  ds_write2_b32 v[RW], v[AC+1], v[AC+5] offset0:128 offset1:129
  ds_write_b32 v[FW], v[TAG]
.if \R == 0
LV_p:
  ds_read_b32 v[FA], v[FR]
  ds_read_b64 v[RA:RA+1], v[RR]
  ds_read_b64 v[RA+2:RA+3], v[RR] offset:1024
  ds_read_b64 v[RA+4:RA+5], v[RR] offset:2048
  ds_read_b64 v[RA+6:RA+7], v[RR] offset:3072
  CAP
  s_waitcnt lgkmcnt(4)
  v_cmp_gt_u32 vcc, s65, v[FA]
  s_cbranch_vccnz LV_p
  s_waitcnt lgkmcnt(2)
  HS RA
  s_waitcnt lgkmcnt(0)
  HS RA+4
  v_pk_add_f32 v[RA:RA+1], v[RA:RA+1], v[RA+4:RA+5]
  v_cvt_pk_bf16_f32 v[RES], v[RA], v[RA+1]
  buffer_store_dword v[RES], v[ST1], s[40:43], s66 offen sc1
.endif
  STAMP 3
  s_cmp_eq_u32 s64, 63
  s_cbranch_scc1 LV_last\R
  s_waitcnt lgkmcnt(0)
  ZR DD, 10
.if \R == 2
  s_cmp_eq_u32 s64, 0
  s_cbranch_scc1 LV_t0
LV_t1:
  s_waitcnt vmcnt(0)
  v_readfirstlane_b32 s73, v[TV]
  v_readfirstlane_b32 s74, v[TV+1]
  s_cmp_eq_u32 s73, -1
  s_cbranch_scc1 LV_rt
  ds_write_b64 v[183], v[TV:TV+1]
  s_add_i32 s75, s73, s56
  s_add_i32 s75, s75, s74
  WAITT s75
.endif
  s_setprio 3
  s_barrier
.rept )" STR(VDEL2) R"(
  s_nop 7
.endr
  STAMP 4
  LD VM, O1, s66, 0
  LD VM+4, O1, s66, 1024
  s_mov_b32 s76, 0x400000
LV_b\R:
  s_waitcnt vmcnt(1)
  SM ZZ, VM, 0
  MX0 VM
  SM ZZ+4, VM, 16
  SM ZZ+8, VM, 32
  SM ZZ+12, VM, 48
  s_waitcnt vmcnt(0)
  SM ZZ, VM+4, 8
  MX1 VM+4
  SM ZZ+4, VM+4, 24
  v_cmp_eq_u32 vcc, -1, v[MX]
  ds_read_b32 v[FA], v[FS]
  SM ZZ+8, VM+4, 40
  ds_read_b128 v[SX:SX+3], v[ASC]
  ds_read_b64 v[SMM:SMM+1], v[ASM]
  SM ZZ+12, VM+4, 56
  s_cbranch_vccnz LV_rb\R
  STAMP 5
LV_s\R:
  s_waitcnt lgkmcnt(2)
  v_cmp_gt_u32 vcc, s65, v[FA]
  s_cbranch_vccnz LV_rs\R
  s_waitcnt lgkmcnt(0)
  STAMP 6
  v_max3_f32 v[MX], v[SX], v[SX+1], v[SX+2]
  v_max3_f32 v[TM], v[SX+3], v[SMM], v[SMM+1]
  v_max_f32 v[MX], v[MX], v[TM]
  CZV 0
  v_max_f32_dpp v[MX], v[MX], v[MX] quad_perm:[2,3,0,1] row_mask:0xf bank_mask:0xf
  v_mov_b32 v[TM], v[MX]
  CZV 1
  v_permlane16_swap_b32 v[MX], v[TM]
  v_max_f32 v[MX], v[MX], v[TM]
  v_mov_b32 v[TM], v[MX]
  CZV 2
  v_permlane32_swap_b32 v[MX], v[TM]
  v_max_f32 v[MX], v[MX], v[TM]
  v_mul_f32 v[MX], s79, v[MX]
  v_pk_fma_f32 v[SX:SX+1], v[SX:SX+1], s[78:79], v[MX:MX+1] op_sel_hi:[1,0,0]
  v_pk_fma_f32 v[SMM:SMM+1], v[SMM:SMM+1], s[78:79], v[MX:MX+1] op_sel_hi:[1,0,0]
  v_pk_fma_f32 v[SX+2:SX+3], v[SX+2:SX+3], s[78:79], v[MX:MX+1] op_sel_hi:[1,0,0]
  v_exp_f32 v[SX], v[SX]
  v_exp_f32 v[SX+1], v[SX+1]
  v_exp_f32 v[SMM], v[SMM]
  v_cvt_pk_bf16_f32 v[PA], v[SX], v[SX+1]
  v_exp_f32 v[SMM+1], v[SMM+1]
  v_cvt_pk_bf16_f32 v[PA+1], v[SMM], v[SMM]
  v_exp_f32 v[SX+2], v[SX+2]
  v_exp_f32 v[SX+3], v[SX+3]
  v_cvt_pk_bf16_f32 v[PA+3], v[SMM+1], v[SMM+1]
  v_cvt_pk_bf16_f32 v[PA+2], v[SX+2], v[SX+3]
  CZV 3
  SM DD+16, PA, B1
  SM DD, PA, UB
  SM DD+4, PA, UB+8
  SM DD+8, PA, UB+16
  HS DD+16
  HS DD
  SM DD+12, PA, UB+24
  v_rcp_f32 v[DD+16], v[DD+16]
  v_rcp_f32 v[DD+17], v[DD+17]
  HS DD+4
  HS DD+8
  v_pk_mul_f32 v[DD:DD+1], v[DD:DD+1], v[DD+16:DD+17]
  v_pk_mul_f32 v[DD+4:DD+5], v[DD+4:DD+5], v[DD+16:DD+17]
  v_pk_mul_f32 v[DD+8:DD+9], v[DD+8:DD+9], v[DD+16:DD+17]
  v_cvt_pk_bf16_f32 v[SX], v[DD], v[DD+4]
  HS DD+12
  v_cvt_pk_bf16_f32 v[SX+2], v[DD+1], v[DD+5]
  v_pk_mul_f32 v[DD+12:DD+13], v[DD+12:DD+13], v[DD+16:DD+17]
  v_cvt_pk_bf16_f32 v[SX+1], v[DD+8], v[DD+12]
  v_cvt_pk_bf16_f32 v[SX+3], v[DD+9], v[DD+13]
  buffer_store_dwordx4 v[SX:SX+3], v[ST2], s[40:43], s66 offen sc1
  STAMP 7
  s_setprio 0
.if \R == 0
  buffer_store_dword v[RES], v[STK], s[44:47], s72 offen sc1
.endif
LV_u\R:
  s_lshl_b32 s75, s65, 13
  s_and_b32 s75, s75, 0x2000
  v_add_u32 v[TM], s75, v[AVX]
.irp k,0,1,2,3,4,5,6,7
  ds_read_b128 v[VX+4*\k:VX+4*\k+3], v[TM] offset:1024*\k
.endr
.irp k,0,1,2,3,4,5,6,7
  s_waitcnt lgkmcnt(7-\k)
.irp t,0,1,2,3
.if (\k & 3) == 0
  v_mfma_f32_16x16x32_bf16 v[UU+4*\k+4*\t:UU+4*\k+4*\t+3], v[VX+4*\k:VX+4*\k+3], v[16*\t:16*\t+3], 0
.else
  v_mfma_f32_16x16x32_bf16 v[UU+4*(\k&4)+4*\t:UU+4*(\k&4)+4*\t+3], v[VX+4*\k:VX+4*\k+3], v[16*\t+4*(\k&3):16*\t+4*(\k&3)+3], v[UU+4*(\k&4)+4*\t:UU+4*(\k&4)+4*\t+3]
.endif
.endr
.endr
.irp k,0,4
.irp t,0,1,2,3
  v_cvt_pk_bf16_f32 v[UB+8*\t+\k], v[UU+4*\k+4*\t], v[UU+4*\k+4*\t+1]
  v_cvt_pk_bf16_f32 v[UB+8*\t+\k+1], v[UU+4*\k+4*\t+2], v[UU+4*\k+4*\t+3]
.endr
.endr
  STAMP 8
  NEXT MB
  s_branch LV_loop\R
LV_ra\R:
  RETRY 2
  MEMV \R
  ZR AC, 4
  s_branch LV_a\R
LV_rb\R:
  RETRY 0
  LD VM, O1, s66, 0
  LD VM+4, O1, s66, 1024
  ZR ZZ, 8
  s_branch LV_b\R
LV_rs\R:
  SCV
  CAP
  s_branch LV_s\R
LV_last\R:
.if \R == 0
  buffer_store_dword v[RES], v[STK], s[44:47], s72 offen sc1
.endif
  s_waitcnt vmcnt(0) lgkmcnt(0)
  s_barrier
  s_branch L_end
.if \R == 2
LV_t0:
  s_memrealtime vcc
  s_waitcnt vmcnt(0) lgkmcnt(0)
  s_lshl_b32 s73, vcc_lo, 4
  s_sub_i32 s73, s73, 0x100000
  v_mov_b32 v[TV], s73
  v_mov_b32 v[TV+1], 0
  s_branch LV_t1
LV_rt:
  RETRY 2
  buffer_load_dwordx2 v[TV:TV+1], v[OT], s[40:43], s68 offen sc1
  s_branch LV_t1
.endif
.endm
LV_init:
  s_mov_b32 s64, 63
  s_mov_b32 s65, 0
  s_mov_b32 s66, 0x100000
  s_mov_b32 s67, 0
  s_mov_b32 s68, 0x30000
  s_mov_b32 s69, 0x60000
  s_mov_b32 s70, 0
  s_mov_b32 s78, 0x3e0293ee
  s_mov_b32 s79, 0xbe0293ee
  ZR UB, 16
  v_mov_b32 v[B1], 0x3f803f80
  v_mov_b32 v[B1+1], 0x3f803f80
  v_mov_b32 v[B1+2], 0x3f80
  v_mov_b32 v[B1+3], 0x3f80
  v_mov_b32 v[B1+4], 0x3f803f80
  v_mov_b32 v[B1+5], 0x3f803f80
  v_mov_b64 v[B1+6:B1+7], 0
  s_cmp_eq_u32 s60, 2
  s_cbranch_scc1 LV_u2
  s_cmp_eq_u32 s60, 3
  s_cbranch_scc1 LV_u3
  s_branch LV_u0
  STEPV 0
  STEPV 2
  STEPV 3
L_fatal:
  v_mov_b32 v[MX], 0
  v_mov_b32 v[TAG], 1
  global_store_dword v[MX], v[TAG], s[48:49] sc1
  s_waitcnt vmcnt(0)
  s_endpgm
L_end:
)"
static_assert(__builtin_bit_cast(unsigned, SCALE * LOG2E) == 0x3e0293eeu, "");
#define CL10(p) p "0", p "1", p "2", p "3", p "4", p "5", p "6", p "7", p "8", p "9"
#define CL_ST
#define CL_S CL_ST "s72", "s73", "s74", "s75", "s76", "s77", "s78", "s79", "vcc", "scc", "memory"

__device__ __forceinline__ void chain(const Args& a, const int c, const unsigned L) {
    const int tid = threadIdx.x, w8 = RFL(tid >> 6), w = w8 & 3, l = tid & 63;
    const int g = c & 3, aa = c >> 2, h = g * 8 + (aa >> 2), jj = aa & 3;
    const int n = l & 15, q = l >> 4, hf = (n >> 1) & 1, n2 = (n >> 2) & 1, n3 = n >> 3;
    const unsigned L_RED = L + 131072, L_RV = L_RED + 6144, L_SC = L_RV + 4096, L_SM = L_SC + 2048, L_FL = L_SM + 512, L_TB = L_FL + 3072, L_VX = L_TB + 256;
    const rsrc_t rs = mkrs(a.ring, RING_BYTES);
    unsigned* const err = a.ctl + C_ERR;
    const v4u ones = {~0u, ~0u, ~0u, ~0u};
    if (tid < 384) lp<v2u>(L_FL)[tid] = v2u{0u, 0u};
    if (c < 88 || (c >= 96 && c < 108)) {
#pragma unroll
        for (int i = 0; i < 4; ++i) STB(ones, rs, c * 32768 + i * 8192 + tid * 16, 0); }
    const int lg3 = L_H3 + ((q >> 1) * 2 + n3) * 16384 + c * 128 + n2 * 64 + hf * 32 + (q & 1) * 16 + (n & 1) * 8;
    if (w8 == 3) { const bf16_t* p = a.mem0 + ((c & 1) * 4 + n2 * 2 + (q & 1)) * DM + 16 * (c >> 1) + (n & 3);
        const v2u m = {min(p[0] | (unsigned)p[4] << 16, 0xFFFFFFFEu), min(p[8] | (unsigned)p[12] << 16, 0xFFFFFFFEu)};
#pragma unroll
        for (int i = 0; i < 2; ++i) __builtin_amdgcn_raw_buffer_store_b64(m, rs, CHB + ((lg3 >> 13) << 14) + (lg3 & 8191), 2 * SLOT3 + i * 8192, 16); }
    const unsigned stg = L + w8 * 16384;
    const int prr = 16 * (q >> 1) + (q & 1) + 4 * n2 + 2 * n3;
    v32u W[4];
    auto trd4 = [&](int k, unsigned p, int o1, int o2) { const v2u t[4] = {trd(p), trd(p + o1), trd(p + o2), trd(p + o2 + o1)};
#pragma unroll
        for (int i = 0; i < 8; ++i) W[k >> 2][(k & 3) * 8 + i] = t[i >> 1][i & 1]; };
    unsigned cal;
    auto phA = [&](int lg) { return ((lg >> 13) << 14) + (int)((cal & 1) << 13) + (lg & 8191); };
    auto phB = [&](int lg) { return CHB + ((lg >> 13) << 14) + (int)((cal & 2) << 12) + (lg & 8191); };
    auto h1o = [&](int inner) { return phB(h * 6144 + inner); };
    auto stream = [&](auto issue, auto read) {
        issue(0); issue(1);
        WAITV(8); BAR();
        if (w8 == 3) asm volatile("s_mov_b64 exec, 1\n\tglobal_atomic_add %0, %1, %2 sc1\n\ts_mov_b64 exec, -1" :: "v"(0u), "v"(1u), "s"(a.ctl + C_READY) : "memory");
        read(std::integral_constant<int, 0>{}); WAITL(); issue(2);
        WAITV(8); read(std::integral_constant<int, 1>{}); WAITL(); issue(3);
        WAITV(8); read(std::integral_constant<int, 2>{});
        WAITV(0); read(std::integral_constant<int, 3>{}); WAITL();
    };
    auto start = [&]() {
        if (w8 == 3) {
            flags_wait<4>(a.ctl + C_READY, 1, 128 + 32, l, err);
            unsigned v = gld(a.ctl + C_CAL + (l & 3));
            unsigned t = (__builtin_amdgcn_readlane(v, 1) < __builtin_amdgcn_readlane(v, 0) ? 1u : 0u) | (__builtin_amdgcn_readlane(v, 3) < __builtin_amdgcn_readlane(v, 2) ? 2u : 0u);
            lp<unsigned>(L_TB + 16)[0] = t; WAITL();
        }
        BAR();
        cal = RFL(ldsr(L_TB + 16));
    };
    const unsigned char* xp = a.qkv + h * 24576 + (w & 1) * 4096;
    const unsigned* fG = a.ctl + C_G + h * 128;
    auto lo = [](const void* p) { return (unsigned)(u64)p; };
    auto hi = [](const void* p) { return (unsigned)((u64)p >> 32); };
    v16u S0 = {lo(a.ring), hi(a.ring), RING_BYTES, 0x20000u, lo(a.kvm), hi(a.kvm), NSEG * NH * 4096, 0x20000u, lo(err), hi(err), lo(a.ctl + C_M + h * 128 + jj * 32), hi(a.ctl + C_M + h * 128 + jj * 32),
               lo(xp + NH * 24576), hi(xp + NH * 24576), lo(fG), hi(fG)};
    v16u S1 = {(unsigned)a.d1, (unsigned)a.d2, (unsigned)a.per, a.dinit & 0xffffu, (unsigned)w, L_VX + w * 4096, L_TB, (unsigned)w8};
    v8u cnts;
    v16u A, K;
    const unsigned six = hf ? 0xEEEE : 0x4444;

    if (w8 < 4) {
        { const bf16_t* Wm = (jj >> 1 ? a.Wk : a.Wq) + (size_t)256 * w * HD + h * DH + 64 * (jj & 1);
          const unsigned p = stg + prr * 128 + (((hf ^ (4 * n2 + 2 * n3)) * 16 + (n & 1) * 8) ^ ((w >> 1) * 64));
          stream([&](int i) { _Pragma("unroll 1") for (int j = 0; j < 8; ++j) gldsc(Wm + (size_t)(64 * i + 8 * j) * HD, (l >> 3) * (HD * 2) + ((l & 7) ^ (2 * q)) * 16, stg + (i & 1) * 8192 + j * 1024); },
                 [&](auto i) { _Pragma("unroll") for (int t = 0; t < 4; ++t) trd4(4 * t + i, (p ^ (t * 32)) + (i & 1) * 8192, 1024, 4096); }); }
        MARK(2);
#pragma unroll
        for (int i = 0; i < 4; ++i) K[i] = ((n & 7) >> 1) == i ? 0x3F80u << (16 * (n & 1)) : 0u;
        if (w < 2) {
            flags_wait<4>(fG, 2, 2, l, err);
            glds1<4>(xp + 8192, l * 16, L_VX + w * 4096); glds1<4>(xp + NH * 24576 + 8192, l * 16, L_VX + 8192 + w * 4096);
            const rsrc_t rq = mkrs(xp, 4096);
#pragma unroll
            for (int i = 0; i < 4; ++i) { const v4u t = __builtin_amdgcn_raw_buffer_load_b128(rq, l * 16 + i * 1024, 0, 16);
#pragma unroll
                for (int e = 0; e < 4; ++e) K[4 * i + e] = t[e]; }
            WAITV(0);
        }
        start();
        MARK(0); MARKC(4);
        const int sl = 2 * (jj & 1) + (w >> 1);
        A[0] = phB(L_H3 + g * 16384 + w * 4096 + (l ^ (w & 1)) * 16);
        A[1] = h1o((jj >> 1) * 2048 + ((sl * 2 + n3) * 2 + (w & 1)) * 128 + (q >> 1) * 64 + n2 * 32 + (q & 1) * 16 + (n & 3) * 4);
        A[2] = L_RED + (w ^ 1) * 512 + l * 8; A[3] = L_RED + 2048 + (w ^ 2) * 512 + l * 8; A[4] = L_RED + 4096 + (w ^ 3) * 512 + l * 8; A[5] = L_RED + w * 512 + l * 8;
        A[6] = L_FL + w * 256 + l * 4; A[7] = L_FL + (l & 3) * 256 + l * 4; A[8] = six;
        A[9] = jj >> 1 ? h * 4096 + (4 * sl + (n >> 2)) * 128 + ((w & 1) * 4 + q) * 16 + (n & 3) * 4 : OOB;
        A[10] = w < 3 ? h1o(l * 16) : phA(c * 4096 + l * 16);
        A[11] = w < 2 ? l * 16 : h1o((w == 2 ? 2048 : jj * 1024) + l * 16);
        A[12] = w < 2 ? L_SC + w * 1024 + ((l * 16) ^ ((q & 1) * 32)) : (w == 2 ? L_SM + ((l * 8) ^ ((q & 1) * 16)) : phB(lg3));
        A[13] = w < 2 ? (l & 1) * 128 : (c == 0 && l < 4 ? phB(L_TS + l * 128) : OOB);
        A[14] = h1o(4096 + jj * 512 + l * 8); A[15] = 0;
    } else {
        { const bf16_t* Wo = a.Wo + (size_t)h * DH * DM + 256 * jj + 64 * w; const bf16_t* Wv = a.Wv + (size_t)256 * w * HD + h * DH + 32 * jj;
          const unsigned po = stg + (4 * q + n3 + 16 * n2) * 128 + (((n & 3) * 2) ^ (q & 1)) * 16, pv = stg + prr * 64 + (hf ^ (2 * n2)) * 16 + (n & 1) * 8;
          stream([&](int i) {
                     if (i < 2) { _Pragma("unroll 1") for (int j = 0; j < 8; ++j) gldsc(Wo + (size_t)(64 * i + 8 * j) * DM, (l >> 3) * (DM * 2) + ((l & 7) ^ (l >> 5)) * 16, stg + i * 8192 + j * 1024); }
                     else { _Pragma("unroll 1") for (int j = 0; j < 8; ++j) glds(Wv + (size_t)(128 * (i - 2) + 16 * j) * HD, (l >> 2) * (HD * 2) + ((l & 3) ^ (2 * (q & 1))) * 16, stg + (i & 1) * 8192 + j * 1024); } },
                 [&](auto i) { _Pragma("unroll") for (int t = 0; t < 4; ++t) {
                     if constexpr (i < 2) trd4(2 * t + i, (po ^ ((t >> 1) * 16)) + (t & 1) * 8 + i * 8192, 256, 4096);
                     else trd4(8 + 4 * (t & 1) + 2 * (i - 2) + (t >> 1), (pv ^ ((t & 1) * 32)) + (t >> 1) * 4096 + (i & 1) * 8192, 512, 2048); } }); }
        start();
        A[0] = phB(L_H3 + g * 16384 + w * 4096 + l * 16); A[1] = h1o(4096 + l * 16); A[2] = phB(L_TS + g * 128);
        A[3] = h1o(4096 + ((jj * 2 + n3) * 2 + (w & 1)) * 128 + (q & 1) * 64 + (n & 1) * 32 + (q >> 1) * 16 + ((n >> 1) & 3) * 4);
        A[4] = phA((jj * 32 + w * 8 + (n >> 2) * 2 + (q >> 1)) * 4096 + h * 128 + ((q & 1) * 4 + (n & 3)) * 16);
        A[5] = h * 4096 + 2048 + (4 * jj + (n >> 2)) * 128 + ((w & 1) * 4 + q) * 16 + (n & 3) * 4;
        A[6] = L_RV + w * 1024 + l * 8; A[7] = L_RV + (w & 1) * 512 + l * 8;
        A[8] = L_FL + 1024 + w * 256 + l * 4; A[9] = L_FL + 1024 + (l & 3) * 256 + l * 4; A[10] = L_FL + 2048 + min(l & 3, 2) * 256 + l * 4;
        A[11] = L_SC + q * 512 + hf * 256 + n * 16; A[12] = L_SM + (q & 1) * 256 + hf * 128 + n * 8;
        A[13] = L_VX + q * 256 + (8 * n3 + 4 * hf + 2 * n2 + (n & 1)) * 16; A[14] = six; A[15] = L_TB;
    }
    asm volatile(A_CHAIN : "+{s[40:55]}"(S0), "+{s[56:71]}"(S1), "={s[80:87]}"(cnts), "+{v[168:183]}"(A), "+{v[196:211]}"(K) : "{v[0:31]}"(W[0]), "{v[32:63]}"(W[1]), "{v[64:95]}"(W[2]), "{v[96:127]}"(W[3])
                 : "v128", "v129", CL10("v13"), CL10("v14"), CL10("v15"), "v160", "v161", "v162", "v163", "v164", "v165", "v166", "v167", "v184", "v185", "v186", "v187", "v188", "v189",
                   "v190", "v191", "v192", "v193", "v194", "v195", "v212", "v213", "v214", "v215", "v216", "v217", "v218", "v219", CL10("v22"), CL10("v23"), CL10("v24"), "v250", "v251", CL_S);
}

struct Tile { const bf16_t* W; int ld, wide, perm; };
template <bool SHARED = false> __device__ __forceinline__ void tile_issue(const Tile& T, int t, unsigned L, int w8, int l) {
    const bf16_t* p = T.W + (T.wide ? (w8 & 3) * 32 * T.ld + (2 * t + (w8 >> 2)) * 128 : (t * 256 + w8 * 32) * T.ld);
    const unsigned vo = (T.perm ? (l >> 5) + (l & 16) : l >> 4) * T.ld * 2 + ((l & 15) ^ (2 * (l >> 4))) * 16;
#pragma unroll 1
    for (int i = 0; i < 8; ++i) { const bf16_t* g = p + i * (T.perm ? 2 : 4) * T.ld; const unsigned v = vo ^ ((i & 2) << 6), d = L + (t & 1) * 65536 + (w8 * 8 + i) * 1024;
        if (SHARED) glds(g, v, d); else gldsw(g, v, d); }
}
template <bool SHARED = false, class F>
__device__ __forceinline__ void tile_load(const Tile& T, v4u (&wf)[32], unsigned L, int w8, int l, F next) {
    const int n = l & 15, q = l >> 4;
    const unsigned p = L + (8 * q + (n >> 2)) * 256 + ((4 * (w8 & 3) + ((l >> 1) & 1)) ^ (2 * ((n >> 2) | ((q & 1) << 2)))) * 16 + (l & 1) * 8;
    bool more = true;
#pragma unroll 1
    for (int hh = 0; hh < 2; ++hh)
#pragma unroll
    for (int u = 0; u < 2; ++u) {
        if (!more) WAITV(0);
        WAITV(8); BAR();
        if ((w8 >> 2) == hh) {
#pragma unroll
            for (int i = 0; i < 16; ++i) { const unsigned pp = (p ^ ((i & 1) * 32)) + u * 65536 + (i >> 1) * 8192; wf[u * 16 + i] = cat2(trd(pp), trd(pp + 1024)); }
            WAITL(); }
        BAR();
        if (hh == 0) tile_issue<SHARED>(T, u + 2, L, w8, l); else more = next(u);
    }
}
#define NB 8
#define GD 4
template <int XR, bool ROWS, class F>
__device__ __forceinline__ void seg_gemm(const v4u (&wf)[32], unsigned b0, frag_cd (&acc)[4], F side) {
    v4u f[2 * GD];
    const unsigned bb[4] = {b0, ROWS ? b0 ^ 64 : b0, ROWS ? b0 ^ 128 : b0, ROWS ? b0 ^ 192 : b0};
#define RD(ks, mt) asm volatile("ds_read_b128 %0, %1 offset:%2" : "=v"(f[((ks) % GD) * 2 + mt]) : "v"(bb[(ks) & 3]), "n"(ROWS ? ((ks) >> 2) * 256 + mt * 16384 : ((ks) >> 1) * 4096 + ((ks) & 1) * 1024 + mt * 2048) : "memory")
#define MM(ct, mt) do { if (ks == 0) asm volatile("v_mfma_f32_16x16x32_bf16 %0, %1, %2, 0" : "=&v"(acc[2 * ct + mt]) : "v"(wf[ct]), "v"(f[mt])); \
        else asm volatile("v_mfma_f32_16x16x32_bf16 %0, %1, %2, %0" : "+v"(acc[2 * ct + mt]) : "v"(wf[2 * ks + ct]), "v"(f[(ks % GD) * 2 + mt])); } while (0)
#pragma unroll
    for (int ks = 0; ks < GD - 1; ++ks) { RD(ks, 0); RD(ks, 1); }
#pragma unroll
    for (int ks = 0; ks < 16; ++ks) {
        side(2 * ks);
        asm volatile("s_waitcnt lgkmcnt(%0)" :: "n"(2 * (ks + GD - 1 <= 16 ? GD - 2 : 15 - ks) + (ks >= 13 ? XR : 0)));
        MM(0, 0);
        if (ks + GD - 1 < 16) RD(ks + GD - 1, 0);
        MM(0, 1);
        side(2 * ks + 1);
        MM(1, 0);
        if (ks + GD - 1 < 16) RD(ks + GD - 1, 1);
        MM(1, 1);
    }
    asm volatile("s_nop 7" : "+v"(acc[0]), "+v"(acc[1]), "+v"(acc[2]), "+v"(acc[3]));
#undef RD
#undef MM
}
static_assert(GD == 4, "");
__device__ __forceinline__ float fadd(float a, float b) { float r; asm("v_add_f32 %0, %1, %2" : "=v"(r) : "v"(a), "v"(b)); return r; }
__device__ __forceinline__ float fmul(float a, float b) { float r; asm("v_mul_f32 %0, %1, %2" : "=v"(r) : "v"(a), "v"(b)); return r; }
__device__ __forceinline__ void half_put(unsigned ex, int w8, int l, const frag_cd (&acc)[4]) {
#pragma unroll
    for (int i = 0; i < 4; ++i) *lp<frag_cd>(ex + w8 * 4096 + i * 1024 + l * 16) = acc[i];
    FENCE();
}
struct Half { frag_cd t[4]; unsigned dec; };
__device__ __forceinline__ void half_req(unsigned ex, unsigned dec, Half& x) {
    asm volatile("ds_read_b32 %0, %1" : "=v"(x.dec) : "v"(dec) : "memory");
#pragma unroll
    for (int i = 0; i < 4; ++i) asm volatile("ds_read_b128 %0, %1 offset:%2" : "=v"(x.t[i]) : "v"(ex), "n"(i * 1024) : "memory");
}
template <class F>
__device__ __forceinline__ void half_get(Half& x, frag_cd (&acc)[4], F side) {
    asm volatile("s_waitcnt lgkmcnt(0)" : "+v"(x.t[0]), "+v"(x.t[1]), "+v"(x.t[2]), "+v"(x.t[3]), "+v"(x.dec) :: "memory");
#pragma unroll
    for (int i = 0; i < 4; ++i) { side(i);
#pragma unroll
        for (int e = 0; e < 4; ++e) acc[i][e] = fadd(acc[i][e], x.t[i][e]);
        asm volatile("" : "+v"(acc[i])); }
}
__device__ __forceinline__ void gld_req(unsigned& v, const unsigned* p) { asm volatile("global_load_dword %0, %1, off sc1" : "+v"(v) : "v"(p) : "memory"); }
template <int N> __device__ __forceinline__ void gld_wait(unsigned& v) { asm volatile("s_waitcnt vmcnt(%1)" : "+v"(v) : "n"(N) : "memory"); }
#define NT 0
#define GEARLY 2
#define BARI 25
#define PRIO(n) __builtin_amdgcn_s_setprio(n)

#define SPL 50
struct Finisher {
    rsrc_t rp, rx, rg, ro; int po, frow, xo, w8; unsigned seltok, ly;
    struct Fin { v4u p[4]; v2u xv; float gt; int k; };
    __device__ __forceinline__ Finisher(const Args& a, int j, int w8_, int l, unsigned L_Y) {
        rp = mkrs(a.part, NSEG * 524288); rx = mkrs(a.x, S_TOK * DM * 2); rg = mkrs(a.gate, S_TOK * 4); ro = mkrs(a.out, S_TOK * DM * 2);
        w8 = w8_; po = j * 16384 + w8 * 1024 + l * 16; frow = 16 * (w8 & 1) + (l & 15); xo = (frow * DM + j * 32 + (w8 >> 1) * 16 + 4 * (l >> 4)) * 2;
        seltok = 4095u - (lp<unsigned>(L_Y + 1024)[l & 7] & 4095u); ly = L_Y + ((w8 >> 1) * 32 + (l >> 4) * 8) * 16;
    }
    __device__ __forceinline__ Fin issue(int s, bool on) const { Fin f; int k = on ? 0 : 0x40000000; asm("" : "+s"(k));
#pragma unroll
        for (int i = 0; i < 4; ++i) f.p[i] = __builtin_amdgcn_raw_buffer_load_b128(rp, k + po, s * 524288 + i * 4096, 16);
        f.k = k + xo;
        f.xv = __builtin_amdgcn_raw_buffer_load_b64(rx, f.k, s * 65536, 0);
        f.gt = __uint_as_float(__builtin_amdgcn_raw_buffer_load_b32(rg, k + frow * 4, s * 128, 16));
        return f;
    }
    template <class F> __device__ __forceinline__ void finish(int s, const Fin& f, F side) const {
        const int tok = 32 * s + frow; const float gt = f.gt, omg = rbf(1.0f - gt);
        float b[4] = {lo16(f.xv.x), hi16(f.xv.x), lo16(f.xv.y), hi16(f.xv.y)};
        for (unsigned mk = (unsigned)__builtin_amdgcn_ballot_w64((seltok >> 4) == (unsigned)(2 * s + (w8 & 1))) & 255u; mk; mk &= mk - 1) {
            const int slot = __builtin_ctz(mk);
            const frag_cd y = *lp<frag_cd>(ly + slot * 16);
            if (__builtin_amdgcn_readlane(seltok, slot) == (unsigned)tok) {
#pragma unroll
                for (int e = 0; e < 4; ++e) b[e] = rbf(b[e] + y[e]); }
        }
        v2u o;
#pragma unroll
        for (int e = 0; e < 4; e += 2) { float t[2];
            side(e >> 1);
#pragma unroll
            for (int u = 0; u < 2; ++u) t[u] = fadd(fadd(__uint_as_float(f.p[0][e + u]), __uint_as_float(f.p[1][e + u])), fadd(__uint_as_float(f.p[2][e + u]), __uint_as_float(f.p[3][e + u])));
            unsigned av = cvt_pk(t[0], t[1]); av = cvt_pk(fmul(gt, lo16(av)), fmul(gt, hi16(av))); const unsigned bv = cvt_pk(fmul(omg, b[e]), fmul(omg, b[e + 1]));
            o[e >> 1] = cvt_pk(fadd(lo16(av), lo16(bv)), fadd(hi16(av), hi16(bv))); asm volatile("" : "+v"(o[e >> 1])); }
        __builtin_amdgcn_raw_buffer_store_b64(o, ro, f.k, s * 65536, 0);
    }
};

__device__ __forceinline__ void gwork(const Args& a, const int m, const int h, const unsigned L) {
    const int w8 = RFL(threadIdx.x >> 6); int tid = threadIdx.x, l = tid & 63, n = l & 15, q = l >> 4;
    unsigned* const err = a.ctl + C_ERR;
    const unsigned L_AT = L + 131072, L_KM = L_AT + 8192, L_EX = L_KM + 4096, L_Y = L_EX + 16384, L_DEC = L_Y + 2048;
    if (tid < 2) lp<unsigned>(L_DEC)[tid] = 0;
    unsigned* const fG = a.ctl + C_G + h * 128;
    if (m == 2) {
#pragma unroll 1
        for (int i = 0; i < 16; ++i) glds(a.x + (size_t)(64 * h + 8 * w8) * DM + i * 512, l * 16, L + (w8 * 16 + i) * 1024);
        v4u gw[2], wr[2];
#pragma unroll
        for (int i = 0; i < 2; ++i) { gw[i] = *(const v4u*)(a.gw + 512 * i + 8 * l); wr[i] = *(const v4u*)(a.wr + 512 * i + 8 * l); }
        WAITV(0);
        float res = 0.f;
#pragma unroll 1
        for (int t = 0; t < 8; ++t) { float sg = 0.f, sr = 0.f;
#pragma unroll
            for (int i = 0; i < 2; ++i) { const v4u xv = *lp<v4u>(L + (w8 * 16 + 2 * t + i) * 1024 + l * 16);
#pragma unroll
                for (int e = 0; e < 4; ++e) { const float x0 = lo16(xv[e]), x1 = hi16(xv[e]); const unsigned p = cvt_pk(x0 * lo16(gw[i][e]), x1 * hi16(gw[i][e]));
                    sg += lo16(p) + hi16(p); sr = __builtin_fmaf(x0, lo16(wr[i][e]), __builtin_fmaf(x1, hi16(wr[i][e]), sr)); } }
            float v = tadd32(sg, sr);
            v = tadd16(v, v); v += dppf<ROR8>(v); v += dppf<0x124>(v); v += dppf<0x122>(v); v += dppf<0x121>(v);
            if ((l & 7) == t) res = v; }
        const unsigned rb = cvt_pk(res, 0.f), tok = 64 * h + 8 * w8 + (l & 7);
        const unsigned val = l < 32 ? __float_as_uint(rbf(1.0f / (1.0f + __expf(-lo16(rb))))) : (((rb & 0x8000u) ? ~rb : rb | 0x8000u) << 16) | (4095u - tok);
        if ((l & 31) < 8) gst(a.gate + (l >> 5) * S_TOK + tok, val);
        WAITV(0); BAR();
        if (tid == 0) gst(a.ctl + C_R + h * 32, 1u);
        MARK(90);
        if (w8 == 0) flags_wait<8>(fG, 1, 1, l, err);
        BAR();
    }
    const unsigned xo = (l ^ (8 * (w8 & 1))) * 16;
    auto xissue = [&](int s, int i0, int i1) {
        const bf16_t* p = a.x + (size_t)(32 * s + 8 * (w8 & 3)) * DM + (~w8 & 4) * 128; const unsigned d = L + (s & 1) * 65536 + (w8 ^ 4) * 8192;
#pragma unroll
        for (int i = i0; i < i1; ++i) {
            if (i == i0 || i == 4) asm volatile("s_mov_b32 m0, %0" :: "s"(d + (i & 4) * 1024));
            unsigned t; asm volatile("v_xor_b32 %0, %4, %1\n\tglobal_load_lds_dwordx4 %0, %2 offset:%3" : "=&v"(t) : "v"(xo), "s"(p), "n"((i & 3) * 1024), "n"(i * 1040 + (i & 4) * 1024) : "memory"); }
    };
    v4u wf[32];
    { const Tile T{(m == 0 ? a.Wk : (m == 1 ? a.Wv : a.Wq)) + h * DH, HD, 0, 0};
      tile_issue(T, 0, L, w8, l); tile_issue(T, 1, L, w8, l);
      tile_load(T, wf, L, w8, l, [&](int b) { if (b == 0 || w8 >= 4) xissue(b, 0, 8); return true; }); }
    MARK(0);
    const unsigned rb0 = L + (w8 >> 2) * 32768 + n * 1024 + (q ^ n) * 16;
    const rsrc_t rq = mkrs(a.qkv, NSEG * NH * 24576), ro_ = mkrs(a.ox, NSEG * 262144);
    const int sto = h * 24576 + m * 8192 + (m == 1 ? (w8 & 3) * 64 + l : ((w8 & 2) + (q & 1)) * 64 + (2 * (w8 & 1) + (q >> 1)) * 16 + n) * 16;
    const unsigned* fM = a.ctl + C_M + h * 128; unsigned* const fA = a.ctl + C_A + h * 32 + m;
    auto fD = [&]() { return (l >= 8 && l < 11 && l - 8 != m) ? fG + (l - 8) * 32 : fM + (l & 3) * 32; };
    const int rt = w8 & 1, eh = (w8 >> 1) & 1;
    auto vissue = [&](int s) { glds1<2>(a.qkv + ((size_t)s * NH + h) * 24576 + 8192 + (w8 & 3) * 2048, l * 16, L_AT + (w8 & 3) * 2048); };
    struct KQ { v4u k[8], q[4]; };
    auto kqload = [&](int s) { KQ r;
#pragma unroll
        for (int i = 0; i < 4; ++i) { const int so = (s * NH + h) * 24576; r.q[i] = __builtin_amdgcn_raw_buffer_load_b128(rq, l * 16 + i * 1024, so + 16384 + rt * 4096, 16);
            r.k[i] = __builtin_amdgcn_raw_buffer_load_b128(rq, l * 16 + i * 1024, so, 16); r.k[4 + i] = __builtin_amdgcn_raw_buffer_load_b128(rq, l * 16 + i * 1024, so + 4096, 16); }
        FENCE();
        return r; };
    auto attend = [&](int s, const KQ& kq) {
        frag_cd sc[3] = {};
#pragma unroll
        for (int i = 0; i < 4; ++i) {
            sc[0] = MFMA(kq.k[i], kq.q[i], sc[0]);
            sc[1] = MFMA(kq.k[4 + i], kq.q[i], sc[1]);
            sc[2] = MFMA(*lp<v4u>(L_KM + (8 * (i >> 1) + 2 * q + (i & 1)) * 128 + (n & 7) * 16), kq.q[i], sc[2]);
        }
        v4u vf[4]; v2u vm[4];
#pragma unroll
        for (int et = 0; et < 4; ++et) { const int ck = 8 * eh + 2 * et + ((l >> 1) & 1);
            const unsigned p = L_AT + (ck * 16 + 4 * q + (n >> 2)) * 16 + (l & 1) * 8;
            vf[et] = cat2(trd(p), trd(p + 4096)); vm[et] = trd(L_KM + 2048 + ck * 128 + (4 * (q & 1) + (n >> 2)) * 16 + (l & 1) * 8); }
        float mx = -INFINITY;
#pragma unroll
        for (int t = 0; t < 3; ++t)
#pragma unroll
            for (int e = 0; e < 4; e += 2) { unsigned p = cvt_pk(sc[t][e], sc[t][e + 1]); p = cvt_pk(lo16(p) * SCALE, hi16(p) * SCALE); float v0 = lo16(p), v1 = hi16(p);
                if (t < 2 && 16 * t + 4 * q + e > 16 * rt + n) v0 = -999292928.0f;
                if (t < 2 && 16 * t + 4 * q + e + 1 > 16 * rt + n) v1 = -999292928.0f;
                sc[t][e] = v0; sc[t][e + 1] = v1; mx = fmaxf(fmaxf(mx, v0), v1); }
        mx = tmax16(mx); mx = tmax32(mx);
        float sum = 0.f;
#pragma unroll
        for (int t = 0; t < 3; ++t)
#pragma unroll
            for (int e = 0; e < 4; ++e) { float v = __builtin_amdgcn_exp2f((sc[t][e] - mx) * LOG2E); if (t == 2 && q >= 2) v = 0.f; sc[t][e] = v; sum += v; }
        sum = tadd16(sum, sum); sum = tadd32(sum, sum);
        const float inv = __builtin_amdgcn_rcpf(sum);
        const v4u px = {cvt_pk(sc[0][0] * inv, sc[0][1] * inv), cvt_pk(sc[0][2] * inv, sc[0][3] * inv), cvt_pk(sc[1][0] * inv, sc[1][1] * inv), cvt_pk(sc[1][2] * inv, sc[1][3] * inv)};
        const v4u pm = {cvt_pk(sc[2][0] * inv, sc[2][1] * inv), cvt_pk(sc[2][2] * inv, sc[2][3] * inv), 0u, 0u};
        frag_cd o[4];
#pragma unroll
        for (int et = 0; et < 4; ++et) { o[et] = MFMA(vf[et], px, (frag_cd{})); o[et] = MFMA((v4u{vm[et].x, vm[et].y, 0u, 0u}), pm, o[et]); }
        MARK(65 + s / 3); FENCE();
        const int stx = (h >> 3) * 65536 + ((h & 7) * 2 + eh) * 4096 + rt * 2048 + ((2 * (q & 1) + (q >> 1)) * 16 + n) * 16;
        STB(pack_swap(o[0], o[1]), ro_, stx, s * 262144); STB(pack_swap(o[2], o[3]), ro_, stx + 1024, s * 262144);
    };
    int sa = m;
    unsigned fv = 0;
    unsigned pacc[8] = {}, plast = 0; (void)pacc; (void)plast;
    PT(7);
    if (w8 >= 4) {
        int staged = 0;
        const rsrc_t rf = mkrs(fG + m * 32, w8 == 4 ? 4 : 0);
        WAITV(8); BAR(); WAITV(0);
        for (int sg = 0; sg < NSEG; ++sg) {
            PT(0);
            BAR();
            PT(1);
            PRIO(2);
            if (sg <= GEARLY) __builtin_amdgcn_raw_buffer_store_b32(sg, rf, l * 4, 0, 16);
            frag_cd acc[4]; Half hx;
            seg_gemm<5, true>(wf, rb0 + (sg & 1) * 65536, acc, [&](int i) {
                if (i == BARI - 5) { gld_wait<3>(fv);
                    if (w8 == 4) { const bool ok = !staged && sa + 1 < sg && sg < NSEG - 2 && sa < SPL && __builtin_amdgcn_ballot_w64(fv <= (unsigned)sa) == 0; lp<unsigned>(L_DEC)[sg & 1] = ok; FENCE(); } }
                if (i == BARI) { PRIO(3); PT(5); BAR(); PT(6); }
                if (i == 26) half_req(L_EX + (w8 & 3) * 4096 + l * 16, L_DEC + 4 * (sg & 1), hx);
                if (i >= 26) xissue((sg + 2) & 63, i - 26, i - 25); });
            PRIO(0);
            PT(2);
            half_get(hx, acc, [&](int i) { if (i & 1) xissue((sg + 2) & 63, 6 + i / 2, 7 + i / 2); });
            v4u r[2];
#pragma unroll
            for (int mt = 0; mt < 2; ++mt) r[mt] = v4u{cvt_pk(acc[mt][0], acc[2 + mt][0]), cvt_pk(acc[mt][1], acc[2 + mt][1]), cvt_pk(acc[mt][2], acc[2 + mt][2]), cvt_pk(acc[mt][3], acc[2 + mt][3])};
            PT(3);
            staged = RFL(hx.dec);
            if (staged) { vissue(sa); glds1<1>(a.kvm + ((size_t)sa * NH + h) * 4096 + (w8 & 3) * 1024, l * 16, L_KM + (w8 & 3) * 1024); sa += 3; }
            if (w8 == 4 && fv <= (unsigned)sa) gld_req(fv, fD());
#pragma unroll
            for (int mt = 0; mt < 2; ++mt) STB(r[mt], rq, sto + mt * 4096, sg * (NH * 24576));
            __builtin_amdgcn_raw_buffer_store_b32(sg <= GEARLY ? sg : sg - 1, rf, l * 4, 0, 16);
            FENCE();
            if (sg < GEARLY) WAITV(0);
            PT(4);
        }
        PFLUSH();
        gld_wait<0>(fv); BAR();
        if (tid == 256) gst(fG + m * 32, NSEG);
    } else {
        int pend = 0, puba = 0;
        int now = 0;
        for (int sg = 0; sg < NSEG; ++sg) {
            frag_cd acc[4]; unsigned dec;
            auto round = [&](bool all) {
                WAITL();
                PT(0);
                BAR();
                PT(1);
                PRIO(2);
                seg_gemm<0, true>(wf, rb0 + (sg & 1) * 65536, acc, [&](int i) {
                    if (i == 3) asm volatile("ds_read_b32 %0, %1" : "=v"(dec) : "v"(L_DEC + 4 * (~sg & 1)) : "memory");
                    if (i == BARI) { PRIO(3); PT(5); if (all) WAITV(0); WAITV(2); BAR(); PT(6); }
                    if (i >= 32 - NT) xissue((sg + 1) & 63, i - 32 + NT, i - 31 + NT); });
                PRIO(1);
                PT(2);
                asm volatile("" : "+v"(dec));
                now = RFL(dec);
                half_put(L_EX, w8, l, acc);
                xissue((sg + 1) & 63, NT, 8);
                if (puba && tid == 0) gst(fA, puba);
                puba = pend; pend = 0;
                PT(3);
            };
            if (now) { const KQ kq = kqload(sa); round(true); attend(sa, kq); sa += 3; pend = sa; PT(4); }
            else round(!pend);
            MARK(sg + 1);
        }
        PFLUSH();
        WAITL(); BAR();
        WAITV(0); BAR();
        if ((pend | puba) && tid == 0) gst(fA, pend | puba);
    }
    asm volatile("v_mbcnt_lo_u32_b32 %0, -1, 0\n\tv_mbcnt_hi_u32_b32 %0, -1, %0" : "=v"(l));
    n = l & 15; q = l >> 4; tid = w8 * 64 + l; sa = RFL(sa);
    const int kq = h >> 3, nt = h & 7, j = nt * 4 + kq;
    { const Tile T{a.Wo + (size_t)kq * 1024 * DM + nt * 128, DM, 0, 1};
      tile_issue<true>(T, 0, L, w8, l); tile_issue<true>(T, 1, L, w8, l);
      tile_load<true>(T, wf, L, w8, l, [](int) { return false; }); }
    if (w8 == 0) { flags_wait<8>(a.ctl + C_Y + j * 32 + 1, 1, 1, l, err); glds1<2>(a.ypart + (32 * 28 + 2 * j) * 1024, l * 16, L_Y); WAITV(0); }
    BAR();
    MARK(91);
    unsigned* const fP = a.ctl + C_P + nt * 512 + 1 + m;
    auto project = [&](frag_cd (&acc)[4]) {
        flags_wait<2>(a.ctl + C_A + (kq * 8 + w8) * 32 + m, 1, sa + 3, l, err);
#pragma unroll
        for (int i = 0; i < 2; ++i) glds1<4>(a.ox + (size_t)sa * 262144 + kq * 65536 + w8 * 8192 + i * 4096, l * 16, L + w8 * 8192 + i * 4096);
        WAITV(0); BAR();
        seg_gemm<0, false>(wf, L + (w8 >> 2) * 32768 + l * 16, acc, [](int) {});
    };
    if (w8 >= 4) {
        unsigned gv = m == 1 ? NSEG : 0;
        const Finisher fz(a, j, w8 & 3, l, L_Y);
        for (;; sa += 3) {
            if (sa < NSEG) {
                flags_wait<4>(gv, fG + 32, sa + 1, err);
                vissue(sa);
                if (w8 == 4) { flags_wait<2, 16>(fv, fD(), sa + 1, err); glds1<4>(a.kvm + ((size_t)sa * NH + h) * 4096, l * 16, L_KM); }
                WAITV(0); BAR(); }
            if (sa >= SPL + 3) { unsigned pv = 0;
                flags_wait<2>(pv, fP + ((l & 3) * 4 + kq) * 32, sa - 2, err);
                fz.finish(sa - 3, fz.issue(sa - 3, true), [](int) {}); }
            if (sa >= NSEG) break;
            BAR();
            if (w8 == 4) gst1(fA, sa + 3);
            if (sa >= SPL) {
                frag_cd acc[4]; Half hx;
                project(acc);
                BAR();
                half_req(L_EX + (w8 & 3) * 4096 + l * 16, L_DEC, hx); half_get(hx, acc, [](int) {});
#pragma unroll
                for (int i = 0; i < 4; ++i) STB(__builtin_bit_cast(v4u, acc[i]), fz.rp, ((nt * 4 + (w8 & 3)) * 4 + kq) * 4096 + l * 16 + i * 1024, sa * 524288);
                WAITV(0);
                gst1(fP + (kq * 4 + (w8 & 3)) * 32, sa + 1);
            }
        }
    } else {
        unsigned gv = (l & 2) == m ? NSEG : 0;
        for (; sa < NSEG; sa += 3) {
            flags_wait<4>(gv, fG + (l & 2) * 32, sa + 1, err);
            const KQ kqv = kqload(sa);
            BAR();
            attend(sa, kqv);
            WAITV(0); BAR();
            if (sa >= SPL) {
                frag_cd acc[4];
                project(acc);
                half_put(L_EX, w8, l, acc); WAITL(); BAR();
                MARK(92 + sa / 3);
            }
        }
    }
    MARK(120);
}

__device__ __forceinline__ void owork(const Args& a, const int nt, const int kq, const unsigned L) {
    const int tid = threadIdx.x, w8 = RFL(tid >> 6), l = tid & 63, n = l & 15, q = l >> 4, j = nt * 4 + kq;
    unsigned* const err = a.ctl + C_ERR;
    const unsigned L_XS = L + 131072, L_EX = L_XS, L_DEC = L_EX + 16384, L_HS = L_DEC + 64, L_Y = L_HS + 2048, L_KEY = L_Y + 1024, L_GU = L_KEY + 64;
    if (tid < 4) lp<unsigned>(L_DEC)[tid] = 0;
    const rsrc_t rg = mkrs(a.gate, 2 * S_TOK * 4), ry = mkrs(a.ypart, 32 * 30 * 1024);
    if (w8 == 0) {
        unsigned char* p = a.ring + ((l >> 1) & 1) * CHB + (l & 1) * 8192 + j * 16384 + ((l >> 2) & 3) * 2048 + 1024;
        unsigned best = 0;
        if (l < 16) gst((unsigned*)p, ~0u);
        WAITV(0);
#pragma unroll 1
        for (int i = 0; i < 16; ++i) { u64 t1, t2; unsigned v;
            const u64 pi = ((u64)__builtin_amdgcn_readlane((unsigned)((u64)p >> 32), i) << 32) | (unsigned)__builtin_amdgcn_readlane((unsigned)(u64)p, i);
            asm volatile("s_nop 4\n\ts_memtime %0\n\ts_waitcnt lgkmcnt(0)\n\tglobal_load_dword %2, %3, %4 sc1\n\ts_waitcnt vmcnt(0)\n\ts_memtime %1\n\ts_waitcnt lgkmcnt(0)"
                         : "=&s"(t1), "=&s"(t2), "=&v"(v) : "v"(0u), "s"(pi) : "memory");
            if ((l & 3) == (i & 3)) best = max(best, ~(unsigned)(t2 - t1)); }
        if (l < 16) gst((unsigned*)p, ~0u);
        if (l < 4) __hip_atomic_fetch_max(a.ctl + C_CAL + l, best, __ATOMIC_RELAXED, __HIP_MEMORY_SCOPE_AGENT);
        WAITV(0);
        if (l == 0) __hip_atomic_fetch_add(a.ctl + C_READY, 1u, __ATOMIC_RELAXED, __HIP_MEMORY_SCOPE_AGENT);
        MARK(66);
        flags_wait<8>(a.ctl + C_G + j * 128, 1, 1, l, err);
    } else if (w8 == 1) {
        flags_wait<16>(a.ctl + C_R, 32, 1, l, err);
        v4u kv[8]; unsigned prev = ~0u, mine = 0;
#pragma unroll
        for (int i = 0; i < 8; ++i) kv[i] = __builtin_amdgcn_raw_buffer_load_b128(rg, S_TOK * 4 + l * 16, i * 1024, 16);
#pragma unroll 1
        for (int c = 0; c < TOPC; ++c) { unsigned mx = 0;
            FENCE();
#pragma unroll
            for (int i = 0; i < 8; ++i)
#pragma unroll
                for (int e = 0; e < 4; ++e) mx = max(mx, kv[i][e] < prev ? kv[i][e] : 0u);
            mx = max(mx, dppu<0x128>(mx)); mx = max(mx, dppu<0x124>(mx)); mx = max(mx, dppu<0x122>(mx)); mx = max(mx, dppu<0x121>(mx));
            { v2u r = sw16(mx, mx); mx = max(r.x, r.y); r = sw32(mx, mx); mx = max(r.x, r.y); }
            prev = RFL(mx); if ((l & 7) == c) mine = prev; }
        if (l < 8) lp<unsigned>(L_KEY)[l] = mine;
#pragma unroll 1
        for (int i = 0; i < 16; ++i) glds(a.x + i * 64, (4095u - (mine & 4095u)) * (DM * 2) + (l >> 3) * 16, L_XS + i * 1024);
        WAITV(0); WAITL();
    }
    BAR();
    v4u wf[32];
    {
      auto tile = [&](int t) { return t < 2 ? Tile{(t ? a.wu : a.wg) + j * 128, FF, 0, 0} : (t == 2 ? Tile{a.wd + (size_t)j * 128 * DM, DM, 1, 0} : Tile{a.Wo + (size_t)kq * 1024 * DM + nt * 128, DM, 0, 1}); };
      int ti = j < FF / 128 ? 0 : 3;
      Tile T = tile(ti);
      tile_issue(T, 0, L, w8, l); tile_issue(T, 1, L, w8, l);
      frag_cd gv[2] = {};
#pragma unroll 1
      for (;; ++ti) {
        const Tile N = tile(ti + 1);
        tile_load(T, wf, L, w8, l, [&](int b) { if (ti < 3) tile_issue(N, b, L, w8, l); return ti < 3; });
        T = N;
        if (ti == 3) break;
        if (ti < 2) {
            frag_cd acc[2] = {};
#pragma unroll
            for (int ks = 0; ks < 16; ++ks) { const v4u xf = *lp<v4u>(L_XS + ((w8 >> 2) * 16 + ks) * 512 + q * 128 + (n & 7) * 16); acc[0] = MFMA(wf[2 * ks], xf, acc[0]); acc[1] = MFMA(wf[2 * ks + 1], xf, acc[1]); }
            if (ti == 1) {
                frag_cd g = n < 8 ? gv[0] : gv[1], u = n < 8 ? acc[0] : acc[1];
                if (w8 >= 4) { *lp<frag_cd>(L_GU + (w8 & 3) * 2048 + l * 16) = g; *lp<frag_cd>(L_GU + (w8 & 3) * 2048 + 1024 + l * 16) = u; WAITL(); }
                BAR();
                if (w8 < 4) { g += *lp<frag_cd>(L_GU + w8 * 2048 + l * 16); u += *lp<frag_cd>(L_GU + w8 * 2048 + 1024 + l * 16);
                    float hv[4];
#pragma unroll
                    for (int e = 0; e < 4; ++e) { const float gg = rbf(g[e]); hv[e] = rbf(gg / (1.0f + __expf(-gg))) * rbf(u[e]); }
                    *lp<v2u>(L_HS + w8 * 512 + (2 * (n >> 3) + (q >> 1)) * 128 + (n & 7) * 16 + (q & 1) * 8) = v2u{cvt_pk(hv[0], hv[1]), cvt_pk(hv[2], hv[3])};
                    WAITL(); } }
            gv[0] = acc[0]; gv[1] = acc[1];
        } else {
#pragma unroll
            for (int cb = 0; cb < 4; ++cb) { frag_cd acc[2] = {};
#pragma unroll
                for (int i = 0; i < 4; ++i) { const v4u hf = *lp<v4u>(L_HS + i * 512 + q * 128 + (n & 7) * 16); acc[0] = MFMA(wf[8 * cb + 2 * i], hf, acc[0]); acc[1] = MFMA(wf[8 * cb + 2 * i + 1], hf, acc[1]); }
                STB(__builtin_bit_cast(v4u, n < 8 ? acc[0] : acc[1]), ry, ((w8 & 3) * 28 + j) * 1024 + ((n >> 3) * 32 + q * 8 + (n & 7)) * 16, ((w8 >> 2) * 4 + cb) * (4 * 28 * 1024)); }
        }
        MARK(67 + ti);
      }
      if (tid == 0) gst(a.ctl + C_Y + j * 32, 1u);
    }
    MARK(0);
    const unsigned mykey = lp<unsigned>(L_KEY)[l & 7];
    if (w8 == 0) {
        flags_wait<16>(a.ctl + C_Y, FF / 128, 1, l, err);
        frag_cd y = {};
#pragma unroll 1
        for (int i = 0; i < 28; i += 7) { v4u t[7];
#pragma unroll
            for (int u = 0; u < 7; ++u) t[u] = __builtin_amdgcn_raw_buffer_load_b128(ry, l * 16, (j * 28 + i + u) * 1024, 16);
#pragma unroll
            for (int u = 0; u < 7; ++u) y += __builtin_bit_cast(frag_cd, t[u]); }
        const float r = hi16((mykey & 0x80000000u) ? mykey & 0x7fffffffu : ~mykey), rs = rbf(1.0f / (1.0f + __expf(-r)));
#pragma unroll
        for (int e = 0; e < 4; ++e) y[e] = rbf(rbf(y[e]) * rs);
        *lp<frag_cd>(L_Y + l * 16) = y;
        STB(__builtin_bit_cast(v4u, y), ry, l * 16, (32 * 28 + 2 * j) * 1024); __builtin_amdgcn_raw_buffer_store_b32(mykey, ry, l * 4, (32 * 28 + 2 * j + 1) * 1024, 16);
        WAITV(0); WAITL();
        if (l == 0) gst(a.ctl + C_Y + j * 32 + 1, 1u);
    }
    unsigned* const fP = a.ctl + C_P + nt * 512;
    const unsigned rb = L + (w8 >> 2) * 32768 + l * 16;
    unsigned pacc[8] = {}, plast = 0; (void)pacc; (void)plast;
    PT(7);
    auto oissue = [&](int s, int wv, int i) { glds1<2>(a.ox + (size_t)s * 262144 + kq * 65536 + wv * 8192 + i * 2048, l * 16, L + (s & 1) * 65536 + wv * 8192 + i * 2048); };
    if (w8 >= 4) {
        const rsrc_t rf = mkrs(fP + (kq * 4 + (w8 & 3)) * 32, 4), rp = mkrs(a.part, NSEG * 524288);
        const int stp = ((nt * 4 + (w8 & 3)) * 4 + kq) * 4096 + l * 16;
        const unsigned* fW = l < 32 ? a.ctl + C_A + (kq * 8 + (l & 7)) * 32 + min(l >> 3, 2) : fP + ((l & 3) * 4 + kq) * 32;
        unsigned fv = 0; int pc = 0;
        auto a_ok = [&](int s) { return s < SPL && (unsigned)__builtin_amdgcn_ballot_w64(fv <= (unsigned)s) == 0; };
        auto p_upd = [&]() { if ((__builtin_amdgcn_ballot_w64(fv <= (unsigned)pc) >> 32) == 0) ++pc; if ((__builtin_amdgcn_ballot_w64(fv <= (unsigned)pc) >> 32) == 0) ++pc; lp<unsigned>(L_DEC)[2] = pc; };
        int have = 0, nxt = 0, late = 0;
        for (int s = 0; s <= SPL; ++s) {
            if (!have) {
                if (!late) {
                    gld_wait<0>(fv);
                    __builtin_amdgcn_raw_buffer_store_b32(s, rf, l * 4, 0, 16);
                    if (w8 == 4) {
                        for (int i = 0;; ++i) { if (fv <= (unsigned)(l < 32 ? s + 1 : s - 1)) fv = gld(fW); WAITV(0); p_upd(); if (a_ok(s) || pc == SPL) break; __builtin_amdgcn_s_sleep(2); if (i > GCAP) fatal(err); }
                        lp<unsigned>(L_DEC)[3] = s + 1; lp<unsigned>(L_DEC)[~s & 1] = a_ok(s + 1); FENCE(); }
                    if (s == SPL) break;
                    for (int i = 0; ldsr(L_DEC + 12) <= (unsigned)s; ++i) { __builtin_amdgcn_s_sleep(1); if (i > LCAP) endpgm(); }
                    FENCE();
                    PT(0);
#pragma unroll
                    for (int i = 0; i < 8; ++i) oissue(s, w8 ^ (~i & 4), i & 3);
                    WAITV(8);
                }
                WAITV(12); BAR();
                nxt = RFL(ldsr(L_DEC + 4 * (~s & 1))) & 1;
                if (late) WAITV(4); else WAITV(0);
                if (nxt) {
#pragma unroll
                    for (int i = 0; i < 4; ++i) oissue(s + 1, w8 ^ 4, i); }
                PT(1);
            }
            if (w8 == 4 && !nxt) {
                if (!have) { if (fv <= (unsigned)(s + 1)) gld_req(fv, fW); WAITV(0); }
                gld_wait<4>(fv);
                p_upd(); lp<unsigned>(L_DEC)[~s & 1] = 4 * a_ok(s + 1); WAITL();
                if (fv <= (unsigned)(s + 2)) gld_req(fv, fW); }
            BAR();
            PT(2);
            PRIO(2);
            frag_cd acc[4]; Half hx; unsigned mid;
            seg_gemm<5, false>(wf, rb + (s & 1) * 65536, acc, [&](int i) {
                if (i == BARI - 5) { if (!(have & nxt)) WAITV(0);
                    gld_wait<4>(fv);
                    if (w8 == 4) { p_upd(); const bool k = a_ok(s + 1); if (k) lp<unsigned>(L_DEC)[3] = s + 2; lp<unsigned>(L_DEC)[s & 1] = a_ok(s + 2) + 2 * k; }
                    __builtin_amdgcn_raw_buffer_store_b32(s - (have & nxt), rf, l * 4, 0, 16); FENCE(); }
                if (i == BARI) { PRIO(3); PT(5); BAR(); PT(6); }
                if (i == 3) asm volatile("ds_read_b32 %0, %1" : "=v"(mid) : "v"(L_DEC + 4 * (~s & 1)) : "memory");
                if (i == 26) half_req(L_EX + (w8 & 3) * 4096 + l * 16, L_DEC + 4 * (s & 1), hx);
                if (i == 28) { asm volatile("s_waitcnt lgkmcnt(4)" : "+v"(hx.dec), "+v"(mid)); const int d = RFL(hx.dec); have = nxt | RFL(mid) >> 2; nxt = have & d; late = ~have & d >> 1 & 1; }
                if (i >= 28 && (nxt | late)) oissue(s + 1 + nxt, w8 ^ 4, i - 28); });
            PRIO(0);
            PT(3);
            half_get(hx, acc, [&](int i) { if (late) oissue(s + 1, w8, i); });
            if (w8 == 4 && fv <= (unsigned)(s + 3)) gld_req(fv, fW);
#pragma unroll
            for (int i = 0; i < 4; ++i) STB(__builtin_bit_cast(v4u, acc[i]), rp, stp + i * 1024, s * 524288);
            FENCE();
            PT(4);
        }
    } else {
        const Finisher fz(a, j, w8, l, L_Y);
        int fin = 0, have = 0, pc = 0;
        for (int s = 0; s <= SPL; ++s) {
            if (!have) {
                for (int i = 0; fin < s; ++i) {
                    if (s < SPL && RFL(ldsr(L_DEC + 12)) > s) break;
                    pc = RFL(ldsr(L_DEC + 8));
                    if (fin < pc) { FENCE(); fz.finish(fin, fz.issue(fin, true), [](int) {}); ++fin; }
                    else { __builtin_amdgcn_s_sleep(1); if (i > LCAP) endpgm(); }
                }
                if (s == SPL) break;
            }
            FENCE();
            const bool fi = fin < pc;
            const Finisher::Fin f = fz.issue(fin, fi);
            WAITL(); WAITV(15);
            PT(0);
            BAR();
            PT(2);
            PRIO(2);
            frag_cd acc[4];
            v4u dec;
            seg_gemm<1, false>(wf, rb + (s & 1) * 65536, acc, [&](int i) {
                if (i == BARI) { PRIO(3); PT(1); WAITV(6); BAR(); PT(6); }
                if (i == 26) asm volatile("ds_read_b128 %0, %1" : "=v"(dec) : "v"(L_DEC) : "memory"); });
            PRIO(1);
            PT(3);
            asm volatile("s_waitcnt lgkmcnt(0)" : "+v"(dec));
            have = RFL(s & 1 ? dec.x : dec.y) & 5; pc = RFL(dec.z);
            half_put(L_EX, w8, l, acc);
            WAITV(0);
            if (have & 4) {
#pragma unroll
                for (int i = 0; i < 4; ++i) oissue(s + 1, w8, i); }
            if (have) { oissue(s + 1, w8 ^ 4, 0); oissue(s + 1, w8 ^ 4, 1); }
            PT(4);
            if (!have) { WAITL(); BAR(); }
            fz.finish(fin, f, [&](int i) { if (have) oissue(s + 1, w8 ^ 4, 2 + i); });
            fin += fi;
            MARK(1 + s);
            PT(5);
        }
    }
    PFLUSH();
    MARK(65);
}

__global__ void __launch_bounds__(512, 2) k_mega(Args a) {
    __shared__ __attribute__((aligned(1024))) unsigned char smem[163840];
    const unsigned L = (unsigned)(size_t)(LAS unsigned char*)smem;
    const int b = blockIdx.x, u = (b & 3) + 4 * (b >> 3);
    MARK(255);
    if (!(b & 4)) chain(a, u, L);
    else if (u < 96) gwork(a, (u >> 2) % 3, (u & 3) * 8 + (u >> 2) / 3, L);
    else owork(a, (u >> 2) - 24, u & 3, L);
}

static constexpr int g_coff = 0, g_force = 0; static constexpr int g_d1 = 780, g_d2 = 1620, g_per = 2100, g_dinit = 250; static constexpr unsigned* g_dbg = nullptr;
extern "C" void kernel_launch(void* const* d_in, const int* in_sizes, int n_in, void* d_out, int out_size, void* d_ws, size_t ws_size, hipStream_t stream) {
    auto in = [&](int i) { return (const bf16_t*)d_in[i]; };
    unsigned char* ws = (unsigned char*)d_ws; size_t off = g_coff;
    auto alloc = [&](size_t bytes) { unsigned char* p = ws + off; off += (bytes + 255) & ~(size_t)255; return p; };
    unsigned* ctl = (unsigned*)alloc(C_WORDS * 4);
    unsigned* gate = (unsigned*)alloc(2 * S_TOK * 4); unsigned char* ypart = alloc(32 * 30 * 1024);
    off += (size_t)(-(uintptr_t)(ws + off)) & ((2u << 20) - 1);
    unsigned char* ring = alloc(RING_BYTES);
    unsigned char* qkv = alloc((size_t)NSEG * NH * 24576); unsigned char* kvm = alloc((size_t)NSEG * NH * 4096);
    unsigned char* ox = alloc((size_t)NSEG * 262144); unsigned char* part = alloc((size_t)NSEG * 524288);
    (void)hipMemsetAsync(ctl, 0, C_WORDS * 4, stream);
    Args a{in(0), in(1), in(2), in(3), in(4), in(5), (bf16_t*)d_out, ctl, ring, qkv, kvm, ox, part, in(8), in(9), in(10), in(11), in(12), gate, ypart, g_dbg,
           g_d1 * 16 / 10, g_d2 * 16 / 10, g_per * 16 / 10, g_dinit * 16 / 10 + (g_force << 16)};
    k_mega<<<256, 512, 0, stream>>>(a);
}
```

```cpp
#include <hip/hip_runtime.h>
#include <stdint.h>
#include <type_traits>

typedef unsigned short bf16_t;
typedef unsigned long long u64;
typedef float frag_cd __attribute__((ext_vector_type(4)));
typedef unsigned v4u __attribute__((ext_vector_type(4)));
typedef unsigned v8u __attribute__((ext_vector_type(8)));
typedef unsigned v2u __attribute__((ext_vector_type(2)));
typedef float v2f __attribute__((ext_vector_type(2)));
typedef __bf16 bf16x2_t __attribute__((ext_vector_type(2)));
typedef __bf16 v8bf __attribute__((ext_vector_type(8)));
typedef __bf16 v16bf __attribute__((ext_vector_type(16)));
typedef short v4s_t __attribute__((ext_vector_type(4)));
typedef __amdgpu_buffer_rsrc_t rsrc_t;
#define LAS __attribute__((address_space(3)))
#define GAS __attribute__((address_space(1)))

static constexpr int S_TOK = 2048, DM = 1024, NH = 32, DH = 128, MM = 8, SEG = 32, NSEG = 64, HD = NH * DH, FF = 3584, TOPC = 8;
static constexpr int C_ERR = 0, C_READY = 32, C_CAL = 64  , C_G = 128  , C_M = C_G + 4096  ,
                     C_P = C_M + 4096  , C_R = C_P + 4096, C_Y = C_R + 1024  , C_WORDS = C_Y + 1024;
static constexpr int RING_BYTES = 4 << 20, SLOT = 1 << 20, CHB = 2 << 20, L_H3 = 192 << 10, L_TS = 256 << 10, SLOT3 = 192 << 10;
static constexpr unsigned SPIN_CAP = 1u << 18;
static constexpr int OOB = 0x7ffffff0, LCAP = 1 << 22, GCAP = 1 << 20;
static constexpr float SCALE = 0.08838834764831845f, LOG2E = 1.4426950408889634f;

struct Args {
    const bf16_t* x; const bf16_t* mem0; const bf16_t* Wq; const bf16_t* Wk; const bf16_t* Wv; const bf16_t* Wo;
    bf16_t* out; unsigned* ctl; unsigned char* ring; unsigned char* qkv; unsigned char* ox; unsigned char* part;
    const bf16_t* wr; const bf16_t* wg; const bf16_t* wu; const bf16_t* wd; const bf16_t* gw;
    unsigned* gate  ; unsigned char* ypart; unsigned* dbg;
    int d1, d2, per, dinit;
};

__device__ __forceinline__ unsigned cvt_pk(float lo, float hi) { v2f v = {lo, hi}; return __builtin_bit_cast(unsigned, __builtin_convertvector(v, bf16x2_t)); }
__device__ __forceinline__ float lo16(unsigned p) { return __uint_as_float(p << 16); }
__device__ __forceinline__ float hi16(unsigned p) { return __uint_as_float(p & 0xffff0000u); }
__device__ __forceinline__ float rbf(float f) { return lo16(cvt_pk(f, 0.f)); }
template <int C> __device__ __forceinline__ unsigned dppu(unsigned v) { return (unsigned)__builtin_amdgcn_update_dpp(0, (int)v, C, 0xf, 0xf, true); }
template <int C> __device__ __forceinline__ float dppf(float v) { return __uint_as_float(dppu<C>(__float_as_uint(v))); }
#define ROR8 0x128
#define HMIR 0x141
__device__ __forceinline__ v2u sw16(unsigned a, unsigned b) { auto r = __builtin_amdgcn_permlane16_swap(a, b, false, false); return v2u{r[0], r[1]}; }
__device__ __forceinline__ v2u sw32(unsigned a, unsigned b) { auto r = __builtin_amdgcn_permlane32_swap(a, b, false, false); return v2u{r[0], r[1]}; }
__device__ __forceinline__ float tadd16(float a, float b) { v2u r = sw16(__float_as_uint(a), __float_as_uint(b)); return __uint_as_float(r.x) + __uint_as_float(r.y); }
__device__ __forceinline__ float tadd32(float a, float b) { v2u r = sw32(__float_as_uint(a), __float_as_uint(b)); return __uint_as_float(r.x) + __uint_as_float(r.y); }
__device__ __forceinline__ float tmax16(float a) { v2u r = sw16(__float_as_uint(a), __float_as_uint(a)); return fmaxf(__uint_as_float(r.x), __uint_as_float(r.y)); }
__device__ __forceinline__ float tmax32(float a) { v2u r = sw32(__float_as_uint(a), __float_as_uint(a)); return fmaxf(__uint_as_float(r.x), __uint_as_float(r.y)); }
__device__ __forceinline__ unsigned umax3(unsigned a, unsigned b, unsigned c) { return max(max(a, b), c); }
__device__ __forceinline__ v4u pack_swap(frag_cd a, frag_cd b) { v2u x0 = sw16(cvt_pk(a[0], a[1]), cvt_pk(b[0], b[1])), x1 = sw16(cvt_pk(a[2], a[3]), cvt_pk(b[2], b[3])); return v4u{x0.x, x1.x, x0.y, x1.y}; }
#define MFMA(a, b, c) __builtin_amdgcn_mfma_f32_16x16x32_bf16(__builtin_bit_cast(v8bf, a), __builtin_bit_cast(v8bf, b), c, 0, 0, 0)
#define SMF(a, b, c, ix) __builtin_amdgcn_smfmac_f32_16x16x64_bf16(__builtin_bit_cast(v8bf, a), __builtin_bit_cast(v16bf, b), c, ix, 0, 0)
#define FENCE() asm volatile("" ::: "memory")
#define SB() __builtin_amdgcn_sched_barrier(0)
#define RFL(x) __builtin_amdgcn_readfirstlane(x)
#define BAR() do { FENCE(); __builtin_amdgcn_s_barrier(); FENCE(); } while (0)
#define WAITV(n) do { FENCE(); __builtin_amdgcn_s_waitcnt(0x0F70 | (n)); FENCE(); } while (0)
#define WAITL() asm volatile("s_waitcnt lgkmcnt(0)" ::: "memory")
__device__ __forceinline__ void STB(v4u v, rsrc_t rs, int off, int soff) { __builtin_amdgcn_raw_buffer_store_b128(v, rs, off, soff, 16); asm volatile("s_nop 1" :: "v"(v) : "memory"); }
template <class T> __device__ __forceinline__ LAS T* lp(unsigned a) { return (LAS T*)(size_t)a; }
__device__ __forceinline__ void glds(const void* g, unsigned voff, unsigned lds) { asm volatile("s_mov_b32 m0, %2\n\ts_nop 0\n\tglobal_load_lds_dwordx4 %0, %1" :: "v"(voff), "s"(g), "s"(lds) : "memory"); }
#define NTC " nt"
#define NTW " nt"
__device__ __forceinline__ void gldsc(const void* g, unsigned voff, unsigned lds) { asm volatile("s_mov_b32 m0, %2\n\ts_nop 0\n\tglobal_load_lds_dwordx4 %0, %1" NTC :: "v"(voff), "s"(g), "s"(lds) : "memory"); }
__device__ __forceinline__ void gldsw(const void* g, unsigned voff, unsigned lds) { asm volatile("s_mov_b32 m0, %2\n\ts_nop 0\n\tglobal_load_lds_dwordx4 %0, %1" NTW :: "v"(voff), "s"(g), "s"(lds) : "memory"); }
template <int N> __device__ __forceinline__ void glds1(const void* g, unsigned voff, unsigned lds) {
#define GL(o) "\n\tglobal_load_lds_dwordx4 %0, %1 offset:" #o " sc1"
    if constexpr (N == 1) asm volatile("s_mov_b32 m0, %2\n\ts_nop 0" GL(0) :: "v"(voff), "s"(g), "s"(lds) : "memory");
    if constexpr (N == 2) asm volatile("s_mov_b32 m0, %2\n\ts_nop 0" GL(0) GL(1024) :: "v"(voff), "s"(g), "s"(lds) : "memory");
    if constexpr (N == 4) asm volatile("s_mov_b32 m0, %2\n\ts_nop 0" GL(0) GL(1024) GL(2048) GL(3072) :: "v"(voff), "s"(g), "s"(lds) : "memory");
}
__device__ __forceinline__ rsrc_t mkrs(const void* p, int bytes) { return __builtin_amdgcn_make_buffer_rsrc((void*)p, 0, bytes, 0x00020000); }
__device__ __forceinline__ unsigned gld(const unsigned* p) { return __hip_atomic_load(p, __ATOMIC_RELAXED, __HIP_MEMORY_SCOPE_AGENT); }
__device__ __forceinline__ void gst(unsigned* p, unsigned v) { __hip_atomic_store(p, v, __ATOMIC_RELAXED, __HIP_MEMORY_SCOPE_AGENT); }
__device__ __forceinline__ void gst1(unsigned* p, unsigned v) { asm volatile("s_mov_b64 exec, 1\n\tglobal_store_dword %0, %1, %2 sc1\n\ts_mov_b64 exec, -1" :: "v"(0u), "v"(v), "s"(p) : "memory"); }
__device__ __forceinline__ unsigned rt16() { return (unsigned)__builtin_amdgcn_s_memrealtime() << 4; }

#define NSLOT 12
#define PT(k) do {} while (0)
#define PFLUSH() do {} while (0)
#define MARK(i) do {} while (0)
#define MARKC(i) do {} while (0)

#define VDEL1 6
#define VDEL2 8
#define AEARLY 1
#define XC 4
#define ODEL 12
#define LT 400
#define NS 8
#define GS 6
#define KC 520
#define GM 200
#define SGAP 60
#define HSL 6
#define CSL 7
#define WK 20
#define WKO 100
#define WB 200
#define WC1 0
#define WC2 64
#define WTHR 340
#define SLIP_THR 400
#define CKA 0.75
#define CKF 22.5
#define TG_THR 480
#define SLIP_ADD 640
#define SLIP_INC 10
__device__ __forceinline__ void endpgm() { asm volatile("s_endpgm" ::: "memory"); }
__device__ __forceinline__ void fatal(unsigned* err) { asm volatile("global_store_dword %0, %1, off sc1\n\ts_waitcnt vmcnt(0)\n\ts_endpgm" :: "v"(err), "v"(1u) : "memory"); }
__device__ __forceinline__ unsigned ldsr(unsigned p) { return __hip_atomic_load(lp<unsigned>(p), __ATOMIC_RELAXED, __HIP_MEMORY_SCOPE_WORKGROUP); }
__device__ __forceinline__ v2u trd(unsigned p) { return __builtin_bit_cast(v2u, __builtin_amdgcn_ds_read_tr16_b64_v4i16(lp<v4s_t>(p))); }
__device__ __forceinline__ v8u cat(v2u a, v2u b, v2u c, v2u d) { return v8u{a.x, a.y, b.x, b.y, c.x, c.y, d.x, d.y}; }
__device__ __forceinline__ v4u cat2(v2u a, v2u b) { return v4u{a.x, a.y, b.x, b.y}; }
template <int SL, int FAR = 0> __device__ __forceinline__ void flags_wait(unsigned& v, const unsigned* p, unsigned need, unsigned* err) {
    for (int i = 0; __builtin_amdgcn_ballot_w64(v < need) != 0; ++i) {
        if (i) { if (FAR && __builtin_amdgcn_ballot_w64(v + 1 < need) != 0) __builtin_amdgcn_s_sleep(FAR); __builtin_amdgcn_s_sleep(SL); }
        if (v < need) v = gld(p);
        if (i > GCAP) fatal(err); }
    FENCE();
}
template <int SL> __device__ __forceinline__ void flags_wait(const unsigned* p, int nw, unsigned need, int l, unsigned* err) { unsigned v = need - 1; flags_wait<SL>(v, p + min(l, nw - 1) * 32, need, err); }

typedef unsigned v16u __attribute__((ext_vector_type(16)));
typedef unsigned v32u __attribute__((ext_vector_type(32)));
#define STR_(x) #x
#define STR(x) STR_(x)
#define SET(x) "\n.set " #x ", " STR(x)
#define A_STAMPS "0"
#define A_BENCH "0"
static_assert(SLOT == 0x100000 && SLOT3 == 0x30000 && NH * 24576 == 0xC0000, "");
#define A_CHAIN ".set STAMPS, " A_STAMPS "\n.set BENCH, " A_BENCH "\n.set WB, " STR(WB) "\n.set WC1, " STR(WC1) "\n.set WC2, " STR(WC2) SET(LT) SET(NS) SET(GS) SET(KC) SET(GM) SET(SGAP) SET(HSL) "\n.set WK, " STR(WK) "\n.set WN, 48\n.set WTHR, " STR(WTHR) "\n" R"(
.macro SLED n
  s_getpc_b64 vcc
4: s_sub_u32 s77, (3f-4b)/4, s77
  s_lshl2_add_u32 vcc_lo, s77, vcc_lo
  s_addc_u32 vcc_hi, vcc_hi, 0
  s_setpc_b64 vcc
.rept \n
  s_nop 1
.endr
3: s_waitcnt lgkmcnt(0)
.endm
.macro WAITC T, thr
  s_movk_i32 s76, 0x1000
1: s_memrealtime vcc
  s_sub_u32 s76, s76, 1
  s_cbranch_scc1 3f
  s_waitcnt lgkmcnt(0)
  s_lshl_b32 s77, vcc_lo, 4
  s_sub_i32 s77, \T, s77
  s_cmp_ge_i32 s77, \thr
  s_cbranch_scc1 1b
.endm
.macro WAITT T
  WAITC \T, 64
3: s_waitcnt lgkmcnt(0)
.endm
.macro WAITP T
  WAITC \T, WTHR
  s_sub_i32 s77, s77, WC1
  s_cmp_lt_i32 s77, WB
  s_cbranch_scc1 5f
.irp r,30,32,34,36,38,94,96,98
  s_memrealtime s[\r:\r+1]
  s_nop 1
.endr
  s_sub_i32 s77, \T, WC2
  s_waitcnt lgkmcnt(0)
  s_add_u32 s30, s30, s32
  s_add_u32 s34, s34, s36
  s_add_u32 s38, s38, s94
  s_add_u32 s96, s96, s98
  s_add_u32 s30, s30, s34
  s_add_u32 s38, s38, s96
  s_add_u32 s30, s30, s38
  s_lshl_b32 s30, s30, 1
  s_sub_i32 s77, s77, s30
5: s_max_i32 s77, s77, 0
  s_mulk_i32 s77, 45
  s_lshr_b32 s77, s77, 8
  s_min_u32 s77, s77, WN
  SLED WN
.endm
.macro WAITK T
  s_movk_i32 s76, 0x1000
1: s_memtime vcc
  s_sub_u32 s76, s76, 1
  s_cbranch_scc1 3f
  s_waitcnt lgkmcnt(0)
  s_sub_i32 s77, \T, vcc_lo
  s_cmp_ge_i32 s77, 8*WK
  s_cbranch_scc1 1b
  s_max_i32 s77, s77, 0
  s_lshr_b32 s77, s77, 3
  SLED WK
.endm
.macro TMAX a, t
  v_readlane_b32 \t, v[\a], 0
.irp r,1,2,3
  v_readlane_b32 s77, v[\a], \r
  s_sub_i32 s77, s77, \t
  s_max_i32 s77, s77, 0
  s_add_i32 \t, \t, s77
.endr
.endm
.macro TGET a
  s_waitcnt vmcnt(0)
  v_mbcnt_lo_u32_b32 v[\a+1], 7, 0
  v_lshlrev_b32 v[\a+1], 7, v[\a+1]
  s_add_i32 s75, s88, s68
8: buffer_load_dword v[\a], v[\a+1], s[40:43], s75 offen sc1
  s_sub_u32 s71, s71, 1
  s_cbranch_scc1 L_fatal
  s_waitcnt vmcnt(0)
  v_cmp_eq_u32 vcc, -1, v[\a]
  s_cbranch_vccz 9f
  s_sleep 2
  s_branch 8b
9:
  TMAX \a, s75
  WAITT s75
  s_cmp_eq_u32 s76, 0xfff
  s_cselect_b32 s59, 1, 0
  s_mov_b32 s76, 0x400000
.endm
.macro HOLD
  s_cmp_eq_u32 s59, 0
  s_cbranch_scc1 6f
  s_sleep HSL
  s_mov_b32 s59, 0
6:
.endm
.macro PLAN
  s_mulk_i32 s74, 21
  s_ashr_i32 s74, s74, 3
  s_add_i32 s73, s73, s58
  s_add_i32 s73, s73, s74
.endm
.macro STAMP k
.if STAMPS
  s_memtime s[8+2*\k:9+2*\k]
.endif
.endm
.macro NEXT t
.if STAMPS
  s_waitcnt lgkmcnt(0)
.irp k,0,1,2,3,4,5,6,7,8,9,10
  v_writelane_b32 v[\t], s[8+2*\k], \k
.endr
  s_lshl_b32 s76, s64, 3
  s_add_i32 s76, s76, s63
  s_mul_i32 s76, s76, 48
  v_mbcnt_lo_u32_b32 v[\t+1], -1, 0
  v_lshl_add_u32 v[\t+1], v[\t+1], 2, s76
  s_mov_b64 exec, 0x7ff
  ds_write_b32 v[\t+1], v[\t]
  s_mov_b64 exec, -1
.endif
  s_lshl_b32 s72, s64, 18
  s_mov_b32 s64, s65
  s_add_i32 s65, s65, 1
  s_xor_b32 s66, s66, 0x100000
  s_xor_b32 s67, s67, 0x100000
  s_mov_b32 s76, s68
  s_mov_b32 s68, s69
  s_mov_b32 s69, s70
  s_mov_b32 s70, s76
.endm
.macro RETRY c
.if BENCH
  s_bitset1_b64 s[80+2*\c:81+2*\c], s64
.endif
  s_sub_u32 s71, s71, 1
  s_cbranch_scc1 L_fatal
  s_sleep 2
.endm
.macro LD d, a, so, o
  buffer_load_dwordx4 v[\d:\d+3], v[\a], s[40:43], \so offen offset:\o sc1
.endm
.macro SM d, a, b
  v_smfmac_f32_16x16x64_bf16 v[\d:\d+3], v[\a:\a+3], v[\b:\b+7], v[SIX]
.endm
.macro MX0 a
  v_max_u32 v[MX], v[\a], v[\a+1]
  v_max3_u32 v[MX], v[MX], v[\a+2], v[\a+3]
.endm
.macro MX1 a
  v_max3_u32 v[MX], v[MX], v[\a], v[\a+1]
  v_max3_u32 v[MX], v[MX], v[\a+2], v[\a+3]
.endm
.macro HS a
  v_pk_add_f32 v[\a:\a+1], v[\a:\a+1], v[\a+2:\a+3]
.endm
.macro ZR a, n
.set zi, 0
.rept \n
  v_mov_b64 v[\a+zi:\a+zi+1], 0
.set zi, zi+2
.endr
.endm
.macro PRE
  s_mov_b32 s64, 63
  s_mov_b32 s65, 0
  s_mov_b32 s66, 0x100000
  s_mov_b32 s67, 0
  s_mov_b32 s68, 0x30000
  s_mov_b32 s69, 0x60000
  s_mov_b32 s70, 0
.endm
.macro XSL r
  s_and_b32 s75, \r, 3
  s_mulk_i32 s75, 20480
.endm
.macro CAP
  s_sub_u32 s76, s76, 1
  s_cbranch_scc1 L_fatal
.endm
  s_mov_b32 s71, 0x40000
  s_mov_b32 s88, s64
  s_mov_b32 s59, 0
  s_mov_b32 s90, s65
  s_mov_b32 s91, s66
  s_mov_b64 s[80:81], 0
  s_mov_b64 s[82:83], 0
  s_mov_b64 s[84:85], 0
  s_mov_b64 s[86:87], 0
  s_cmp_gt_u32 s63, 3
  s_cbranch_scc1 LV_init
; ======== waves 0..3
.set O3,168
.set ST1,169
.set RW1,170
.set RW2,171
.set RW3,172
.set RR,173
.set FW,174
.set FR,175
.set SIX,176
.set STK,177
.set FL,183
.set MB,128
.set AC,144
.set TAG,160
.set MX,161
.set RES,162
.set FA,163
.set FB,164
.set RA,184
.set RB,190
.set KX,196
.set XF,177
.set XC,)" STR(XC) R"(
.macro PLQ f, r
  ds_read_b32 v[\f], v[FR]
  ds_read_b64 v[\r:\r+1], v[RR]
  ds_read_b64 v[\r+2:\r+3], v[RR] offset:2048
  ds_read_b64 v[\r+4:\r+5], v[RR] offset:4096
.endm
.macro FINQ r
  STAMP 7
  s_waitcnt lgkmcnt(6)
  v_pk_add_f32 v[AC+2:AC+3], v[AC+2:AC+3], v[\r:\r+1]
  s_waitcnt lgkmcnt(4)
  v_pk_add_f32 v[\r+2:\r+3], v[\r+2:\r+3], v[\r+4:\r+5]
  v_pk_add_f32 v[AC+2:AC+3], v[AC+2:AC+3], v[\r+2:\r+3]
  v_cvt_pk_bf16_f32 v[RES], v[AC+2], v[AC+3]
  buffer_store_dword v[RES], v[ST1], s[40:43], s66 offen sc1
.endm
.macro MEMQ
  LD MB, O3, s68, 0
  LD MB+4, O3, s68, 1024
  LD MB+8, O3, s68, 2048
  LD MB+12, O3, s68, 3072
.endm
.macro XCHK R
  s_add_i32 s75, s64, 2
  s_min_u32 s75, s75, 63
  v_cmp_ge_u32 vcc, s75, v[FL]
  s_cbranch_vccnz LX_w\R
LX_g\R:
  s_add_i32 s75, s64, 2
  XSL s75
  s_add_i32 m0, s75, s61
.endm
.macro XL4 a
.irp o,0,1024,2048,3072
  global_load_lds_dwordx4 v[\a], s[52:53] offset:\o sc1
.endr
  s_add_i32 m0, m0, 0x1000
.endm
.macro XREF R
  s_add_i32 s75, s64, 3
  s_min_u32 s75, s75, 63
  v_cmp_ge_u32 vcc, s75, v[FL]
  s_cbranch_vccz LX_n\R
  global_load_dword v[FL], v[XF], s[54:55] sc1
LX_n\R:
.endm
.macro XADV
  s_cmp_lt_u32 s64, 61
  s_cselect_b32 s75, 0xC0000, 0
  s_add_u32 s52, s52, s75
  s_addc_u32 s53, s53, 0
.endm
.macro XWAIT R
LX_w\R:
  s_mov_b32 s76, 0x100000
1: global_load_dword v[FL], v[XF], s[54:55] sc1
  s_waitcnt vmcnt(0)
  v_cmp_ge_u32 vcc, s75, v[FL]
  s_cbranch_vccz LX_g\R
  s_sleep 4
  CAP
  s_branch 1b
.endm
.macro OXST
  buffer_store_dwordx4 v[KX:KX+3], v[STK], s[44:47], s72 offen sc1
  buffer_store_dwordx4 v[KX+4:KX+7], v[STK], s[44:47], s72 offen offset:1024 sc1
.endm
.macro FLAGQ val
  v_mov_b32 v[MX], 0
  v_mov_b32 v[FA], \val
  s_mov_b64 exec, 1
  global_store_dword v[MX], v[FA], s[50:51] sc1
  s_mov_b64 exec, -1
.endm
.macro PHASEA R
LQ_loop\R:
  STAMP 0
  ZR AC, 8
  v_mov_b32 v[TAG], s65
.if \R == 0
  s_cmp_eq_u32 s60, 0
  s_cbranch_scc0 LQ_g0
  WAITP s73
LQ_g0:
.endif
LQ_bar\R:
  s_barrier
  STAMP 1
  MEMQ
.set XS, 0
.if \R == 2
.set XS, 2
.rept )" STR(ODEL) R"(
  s_nop 7
.endr
  OXST
.endif
  s_mov_b32 s76, 0x400000
LQ_a\R:
  s_waitcnt vmcnt(3+XS)
  SM AC+8, MB, 64
  MX0 MB
  SM AC+12, MB, 96
  SM AC, MB, 0
  SM AC+4, MB, 32
.irp i,1,2
  s_waitcnt vmcnt(3+XS-\i)
  SM AC+8, MB+4*\i, 64+8*\i
  MX1 MB+4*\i
  SM AC+12, MB+4*\i, 96+8*\i
  SM AC, MB+4*\i, 8*\i
  SM AC+4, MB+4*\i, 32+8*\i
.endr
  s_waitcnt vmcnt(XS)
  SM AC+8, MB+12, 88
  MX1 MB+12
  SM AC+12, MB+12, 120
  v_cmp_eq_u32 vcc, -1, v[MX]
  SM AC, MB+12, 24
  s_cbranch_vccnz LQ_ra\R
  SM AC+4, MB+12, 56
  STAMP 2
  HS AC+8
  HS AC+12
  ds_write2_b32 v[RW2], v[AC+8], v[AC+12] offset1:1
  ds_write2_b32 v[RW3], v[AC+9], v[AC+13] offset1:1
  HS AC
  s_nop 2
  HS AC+4
  ds_write2_b32 v[RW1], v[AC+1], v[AC+5] offset1:1
  ds_write_b32 v[FW], v[TAG]
  STAMP 6
  PLQ FA, RA
  v_mov_b32 v[AC+2], v[AC]
  v_mov_b32 v[AC+3], v[AC+4]
LQ_p\R:
  PLQ FB, RB
  s_waitcnt lgkmcnt(7)
  v_cmp_gt_u32 vcc, s65, v[FA]
  s_cbranch_vccnz LQ_pb\R
  FINQ RA
LQ_pe\R:
  STAMP 3
  HOLD
.if \R != 2
  s_cmp_eq_u32 s64, 63
  s_cbranch_scc1 L_last
.endif
  s_waitcnt lgkmcnt(0)
  s_barrier
  STAMP 4
.endm
.macro COLDA R
LQ_pb\R:
  PLQ FA, RA
  CAP
  s_waitcnt lgkmcnt(7)
  v_cmp_gt_u32 vcc, s65, v[FB]
  s_cbranch_vccnz LQ_p\R
  FINQ RB
  s_branch LQ_pe\R
LQ_ra\R:
  RETRY 2
  TGET RA
  MEMQ
  ZR AC, 8
  s_waitcnt vmcnt(0)
  s_branch LQ_a\R
.endm
  s_mov_b32 s64, 0
  s_mov_b32 s65, 1
  s_mov_b32 s66, 0
  s_mov_b32 s67, 0x100000
  s_mov_b32 s68, 0x60000
  s_mov_b32 s69, 0
  s_mov_b32 s70, 0x30000
  s_mov_b32 s72, 0xFC0000
  s_setprio 3
  s_cmp_eq_u32 s60, 2
  s_cbranch_scc1 LQ_init2
  s_cmp_eq_u32 s60, 3
  s_cbranch_scc1 LQ_init3
; ---- waves 0,1: scores against the x rows
  s_memrealtime vcc
  s_waitcnt lgkmcnt(0)
  s_lshl_b32 s73, vcc_lo, 4
  PHASEA 0
  LD MB, 178, s66, 0
  LD MB+4, 178, s66, 1024
LQ_b0:
  s_waitcnt vmcnt(1)
  v_mfma_f32_16x16x32_bf16 v[AC+4:AC+7], v[KX+4:KX+7], v[MB:MB+3], 0
  MX0 MB
  v_mfma_f32_16x16x32_bf16 v[AC:AC+3], v[KX:KX+3], v[MB:MB+3], 0
  s_waitcnt vmcnt(0)
  v_mfma_f32_16x16x32_bf16 v[AC+4:AC+7], v[KX+12:KX+15], v[MB+4:MB+7], v[AC+4:AC+7]
  MX1 MB+4
  v_mfma_f32_16x16x32_bf16 v[AC:AC+3], v[KX+8:KX+11], v[MB+4:MB+7], v[AC:AC+3]
  v_cmp_eq_u32 vcc, -1, v[MX]
  s_nop 5
  s_cbranch_vccnz LQ_rb0
.irp e,0,1,2,3
  v_add_f32_dpp v[AC+\e], v[AC+4+\e], v[AC+\e] quad_perm:[2,3,0,1] row_mask:0xf bank_mask:0xf
.endr
  ds_write_b128 v[180], v[AC:AC+3]
  ds_write_b32 v[FW], v[TAG] offset:2048
  STAMP 5
  XCHK 0
  s_nop 0
  XL4 181
.if XC == 4
  s_nop 0
  XL4 182
.endif
  XREF 0
  XADV
  XSL s65
  v_add_u32 v[FA], s75, v[179]
.irp i,0,1,2,3
  ds_read_b128 v[KX+4*\i:KX+4*\i+3], v[FA] offset:1024*\i
.endr
  v_mov_b32 v[FA], s62
  ds_read_b64 v[RA:RA+1], v[FA]
  s_waitcnt lgkmcnt(0)
  v_readfirstlane_b32 s73, v[RA]
  v_readfirstlane_b32 s74, v[RA+1]
  PLAN
  NEXT MB
  s_branch LQ_loop0
  COLDA 0
LQ_rb0:
  RETRY 0
  LD MB, 178, s66, 0
  LD MB+4, 178, s66, 1024
  s_branch LQ_b0
  XWAIT 0
; ---- wave 2: scores against the memory rows; attention of 16 x rows (64 columns)
.set AQ,165
.set AVF,166
.set AVM,167
.set QX,196
.set OA,212
.set MSK,228
.set LX,237
.set PB,238
.set S1,240
.set K7,244
.set P4,244
.macro MF d, a, b, c
  v_mfma_f32_16x16x32_bf16 v[\d:\d+3], v[\a:\a+3], v[\b:\b+3], \c
.endm
.macro XQ op, r
  v_mov_b32 v[FB], v[\r]
  s_nop 1
  v_permlane16_swap_b32 v[\r], v[FB]
  \op v[\r], v[\r], v[FB]
  v_mov_b32 v[FB], v[\r]
  s_nop 1
  v_permlane32_swap_b32 v[\r], v[FB]
  \op v[\r], v[\r], v[FB]
.endm
.macro QKQ
  LD MB, 178, s66, 0
  LD MB+4, 179, s66, 0
  LD MB+8, 178, s66, 1024
  LD MB+12, 179, s66, 1024
.endm
.macro SOFTX
  v_add_f32 v[AC+6], v[AC+4], v[S1+2]
  v_add_f32 v[AC+7], v[AC+5], v[S1+3]
  v_max3_f32 v[MX], v[LX], v[AC+6], v[AC+7]
  XQ v_max_f32, MX
  v_mul_f32 v[MX], s74, v[MX]
.irp i,6,7,8,9,10,11,12,13,14,15
  v_fma_f32 v[AC+\i], v[AC+\i], s73, v[MX]
.endr
.irp i,6,7,8,9,10,11,12,13,14,15
  v_exp_f32 v[AC+\i], v[AC+\i]
.endr
  v_add_f32 v[LX], v[AC+6], v[AC+7]
.irp i,8,9,10,11,12,13,14,15
  v_add_f32 v[LX], v[LX], v[AC+\i]
.endr
  XQ v_add_f32, LX
  v_rcp_f32 v[LX], v[LX]
  s_nop 0
.irp i,6,7,8,9,10,11,12,13,14,15
  v_mul_f32 v[AC+\i], v[AC+\i], v[LX]
.endr
.irp i,0,1,2,3
  v_cvt_pk_bf16_f32 v[P4+\i], v[AC+8+2*\i], v[AC+9+2*\i]
.endr
  v_cvt_pk_bf16_f32 v[PB], v[AC+6], v[AC+7]
.endm
LQ_init2:
  s_mov_b32 s73, 0x3e0293ee
  s_mov_b32 s74, 0xbe0293ee
.irp i,0,2,4,6
  v_mov_b64 v[MSK+\i:MSK+\i+1], v[QX+\i:QX+\i+1]
.endr
  v_mov_b32 v[AQ], v[QX+8]
  v_mov_b32 v[AVF], v[QX+9]
  v_mov_b32 v[AVM], v[QX+10]
  v_mov_b32 v[PB+1], 0
  PHASEA 2
  QKQ
  XSL s64
  v_add_u32 v[FB], s75, v[AQ]
  v_add_u32 v[RES], s75, v[AVF]
.irp i,0,1,2,3
  ds_read_b128 v[QX+4*\i:QX+4*\i+3], v[FB] offset:16384+1024*\i
  ds_read_b128 v[OA+4*\i:OA+4*\i+3], v[FB] offset:1024*\i
.endr
.irp i,0,1,2
  ds_read_b128 v[RA+4*\i:RA+4*\i+3], v[FB] offset:4096+1024*\i
.endr
  ds_read_b128 v[K7:K7+3], v[FB] offset:7168
  s_waitcnt lgkmcnt(10)
  MF AC+8, OA, QX, v[MSK:MSK+3]
.irp i,4,8,12
  s_waitcnt lgkmcnt(10-\i/2)
  MF AC+8, OA+\i, QX+\i, v[AC+8:AC+11]
.endr
  s_waitcnt lgkmcnt(0)
  MF AC+12, RA, QX, v[MSK+4:MSK+7]
  MF AC+12, RA+4, QX+4, v[AC+12:AC+15]
  MF AC+12, RA+8, QX+8, v[AC+12:AC+15]
  MF AC+12, K7, QX+12, v[AC+12:AC+15]
  v_max3_f32 v[LX], v[AC+8], v[AC+9], v[AC+10]
  s_nop 6
  v_max3_f32 v[LX], v[LX], v[AC+11], v[AC+12]
  v_max3_f32 v[LX], v[LX], v[AC+13], v[AC+14]
  v_max_f32 v[LX], v[LX], v[AC+15]
  STAMP 10
LQ_b2:
  s_waitcnt vmcnt(3)
  MX0 MB
  s_waitcnt vmcnt(2)
  v_mfma_f32_16x16x32_bf16 v[AC:AC+3], v[MB+4:MB+7], v[MB:MB+3], 0
  MX1 MB+4
  s_waitcnt vmcnt(1)
  MX1 MB+8
  s_waitcnt vmcnt(0)
  v_mfma_f32_16x16x32_bf16 v[AC:AC+3], v[MB+12:MB+15], v[MB+8:MB+11], v[AC:AC+3]
  MX1 MB+12
  v_cmp_eq_u32 vcc, -1, v[MX]
  s_nop 3
  s_cbranch_vccnz LQ_rb2
  v_add_f32_dpp v[AC], v[AC+2], v[AC] quad_perm:[2,3,0,1] row_mask:0xf bank_mask:0xf
  v_add_f32_dpp v[AC+1], v[AC+3], v[AC+1] quad_perm:[2,3,0,1] row_mask:0xf bank_mask:0xf
  ds_write_b64 v[180], v[AC:AC+1]
  ds_write_b32 v[FW], v[TAG] offset:2048
  STAMP 5
  s_setprio 0
  MF AC+4, MB+4, QX, 0
  MF S1, MB+4, QX+4, 0
  MF AC+4, MB+12, QX+8, v[AC+4:AC+7]
  MF S1, MB+12, QX+12, v[S1:S1+3]
  FLAGQ s64
.irp e,0,1,2,3
  ds_read_b64_tr_b16 v[MB+4*\e:MB+4*\e+1], v[RES] offset:512*\e
  ds_read_b64_tr_b16 v[MB+4*\e+2:MB+4*\e+3], v[RES] offset:512*\e+4096
.endr
.if )" STR(AEARLY) R"(
  SOFTX
.endif
  s_mov_b32 s76, 0x400000
LA_w:
  ds_read_b32 v[FA], v[FW] offset:2304
  CAP
  s_waitcnt lgkmcnt(0)
  v_cmp_gt_u32 vcc, s65, v[FA]
  s_cbranch_vccz LA_g
  s_sleep 1
  s_branch LA_w
LA_g:
  STAMP 8
  s_setprio 2
.irp e,0,1,2,3
  ds_read_b64_tr_b16 v[RA+2*\e:RA+2*\e+1], v[AVM] offset:256*\e
.endr
.if !)" STR(AEARLY) R"(
  SOFTX
  s_nop 0
.endif
.irp e,0,4,8,12
  MF OA+\e, MB+\e, P4, 0
.endr
  s_waitcnt lgkmcnt(0)
.irp e,0,1,2,3
  v_mfma_f32_16x16x16_bf16 v[OA+4*\e:OA+4*\e+3], v[RA+2*\e:RA+2*\e+1], v[PB:PB+1], v[OA+4*\e:OA+4*\e+3]
.endr
  s_nop 1
.irp i,0,1,2,3,4,5,6,7
  v_cvt_pk_bf16_f32 v[KX+\i], v[OA+2*\i], v[OA+2*\i+1]
.endr
.irp i,0,1,4,5
  v_permlane16_swap_b32 v[KX+\i], v[KX+\i+2]
.endr
  STAMP 9
  s_setprio 3
  NEXT MB
  s_cmp_lg_u32 s64, 64
  s_cbranch_scc1 LQ_loop2
  OXST
  s_waitcnt vmcnt(0)
  FLAGQ 64
  s_branch L_end
  COLDA 2
LQ_rb2:
  RETRY 0
  QKQ
  s_branch LQ_b2
; ---- wave 3: pacing, clearing, sum over the heads
.set CK,212
.macro PARTQ
  LD MB, 178, s66, 0
  LD MB+4, 178, s66, 1024
  LD MB+8, 178, s66, 2048
  LD MB+12, 178, s66, 3072
.endm
LQ_init3:
  s_memrealtime vcc
  v_mov_b64 v[KX+4:KX+5], -1
  v_mov_b64 v[KX+6:KX+7], -1
  v_mov_b32 v[KX+8], s62
  v_mov_b64 v[CK:CK+1], 0
  v_cvt_f32_u32 v[CK+4], s56
  s_mov_b32 s79, NS*GS
  s_sub_u32 s62, s57, s56
  s_sub_u32 s62, s62, KC
  s_waitcnt lgkmcnt(0)
  s_lshl_b32 s73, vcc_lo, 4
  PHASEA 3
  ds_read_b64 v[RA:RA+1], v[KX+8]
  s_waitcnt lgkmcnt(0)
  s_mov_b32 s78, s73
  v_readfirstlane_b32 s73, v[RA]
  v_readfirstlane_b32 s74, v[RA+1]
  s_sub_i32 s78, s73, s78
  s_cmp_gt_i32 s78, 16
  s_cbranch_scc1 LQ_slip
LQ_b5:
  s_sleep )" STR(CSL) R"(
  buffer_store_dwordx4 v[KX+4:KX+7], v[178], s[40:43], s67 offen sc1
  buffer_store_dwordx4 v[KX+4:KX+7], v[178], s[40:43], s67 offen offset:1024 sc1
  buffer_store_dwordx4 v[KX+4:KX+7], v[178], s[40:43], s67 offen offset:2048 sc1
  buffer_store_dwordx4 v[KX+4:KX+7], v[178], s[40:43], s67 offen offset:3072 sc1
  buffer_store_dwordx4 v[KX+4:KX+7], v[179], s[40:43], s67 offen sc1
  buffer_store_dwordx2 v[KX+4:KX+5], v[182], s[40:43], s67 offen sc1
  buffer_store_dwordx2 v[KX+4:KX+5], v[180], s[40:43], s70 offen sc1
  buffer_store_dwordx2 v[KX+4:KX+5], v[181], s[40:43], s70 offen sc1
  XCHK 3
  XREF 3
  STAMP 5
  s_memtime s[92:93]
  s_memrealtime vcc
  s_lshl1_add_u32 s75, s74, s73
  s_add_i32 s75, s75, s57
  PLAN
  s_waitcnt lgkmcnt(0)
  s_sub_u32 s76, s92, s90
  s_sub_u32 s77, vcc_lo, s91
  s_mov_b32 s90, s92
  s_mov_b32 s91, vcc_lo
  s_lshl4_add_u32 s78, vcc_lo, s62
  s_sub_i32 s78, s78, s75
  s_cmp_gt_i32 s78, LT
  s_cselect_b32 s78, s78, -GM
  s_add_i32 s78, s78, s75
  s_add_i32 s78, s78, GM
  s_cmp_lt_u32 s64, NS
  s_cselect_b32 s92, GS, 0
  s_sub_u32 s79, s79, s92
  v_cvt_f32_u32 v[CK+2], s76
  v_cvt_f32_u32 v[CK+3], s77
  v_fmac_f32 v[CK+2], )" STR(CKA) R"(, v[CK]
  v_fmac_f32 v[CK+3], )" STR(CKA) R"(, v[CK+1]
  v_rcp_f32 v[CK+5], v[CK+2]
  v_mov_b64 v[CK:CK+1], v[CK+2:CK+3]
  v_mul_f32 v[CK+3], )" STR(CKF) R"(, v[CK+3]
  v_fma_f32 v[CK+3], v[CK+3], v[CK+5], -1.0
  v_med3_f32 v[CK+3], v[CK+3], -0.5, 1.0
  v_mul_f32 v[CK+3], v[CK+3], v[CK+4]
  v_cvt_i32_f32 v[CK+3], v[CK+3]
  v_add_u32 v[CK+3], s79, v[CK+3]
  WAITP s78
  STAMP 8
  PARTQ
  s_memrealtime vcc
.if XC == 12
  XL4 KX+9
  s_nop 0
  XL4 KX+10
  s_nop 0
.endif
  XL4 KX+11
  s_mov_b32 s78, s75
  XADV
  s_mov_b32 s75, s78
  ZR AC, 8
  v_add_u32 v[TAG], 1, v[TAG]
LQ_b3:
  s_waitcnt lgkmcnt(0)
.if BENCH
  s_mov_b32 m0, s64
  s_mov_b32 s77, vcc_lo
  v_writelane_b32 v[KX+15], s77, m0
.endif
  s_lshl_b32 s89, vcc_lo, 4
  s_sub_i32 s89, s89, s75
  s_add_i32 s78, s89, )" STR(SLIP_ADD) R"(
  s_cmp_gt_i32 s89, )" STR(SLIP_THR) R"(
  s_cselect_b32 s78, s78, 0
  s_cselect_b32 s77, SGAP, 0
  s_add_i32 s78, s78, s73
  s_and_b32 s78, s78, -2
  v_mov_b32 v[RA], s78
  v_add_u32 v[RA+1], s77, v[CK+3]
  s_waitcnt vmcnt(3+XC)
  v_mfma_f32_16x16x32_bf16 v[RB:RB+3], v[KX:KX+3], v[MB:MB+3], 0
  MX0 MB
.irp i,1,2,3
  s_waitcnt vmcnt(3+XC-\i)
  v_mfma_f32_16x16x32_bf16 v[RB:RB+3], v[KX:KX+3], v[MB+4*\i:MB+4*\i+3], v[RB:RB+3]
  MX1 MB+4*\i
.endr
  v_cmp_eq_u32 vcc, -1, v[MX]
  STAMP 9
  s_nop 3
  s_cbranch_vccnz LQ_rb3
.irp e,0,1,2,3
  v_add_f32_dpp v[RB+\e], v[RB+\e], v[RB+\e] row_ror:8 row_mask:0xf bank_mask:0xf
.endr
  v_cvt_pk_bf16_f32 v[RB], v[RB], v[RB+1]
  v_cvt_pk_bf16_f32 v[RB+1], v[RB+2], v[RB+3]
  buffer_store_dwordx2 v[RB:RB+1], v[180], s[40:43], s69 offen sc1
  buffer_store_dwordx2 v[RA:RA+1], v[181], s[40:43], s69 offen sc1
  STAMP 10
  NEXT MB
  STAMP 0
  s_cmp_gt_i32 s89, )" STR(TG_THR) R"(
  s_cbranch_scc0 LQ_bar3
  TGET RA
  s_mov_b32 s59, 0
  s_branch LQ_bar3
  COLDA 3
  XWAIT 3
LQ_rb3:
  RETRY 1
  PARTQ
  s_memrealtime vcc
  s_waitcnt vmcnt(0)
  s_branch LQ_b3
LQ_slip:
.if BENCH
  s_bitset1_b64 s[86:87], s64
.endif
  s_cmp_lt_u32 s64, NS
  s_cbranch_scc1 LQ_b5
  s_add_i32 s79, s79, )" STR(SLIP_INC) R"(
  s_min_u32 s79, s79, 4000
  s_branch LQ_b5
; ======== waves 4..7
.set UB,128
.set B1,160
.set O3,168
.set O1,169
.set OT,170
.set ST1,171
.set ST2,172
.set AIM,173
.set RW,174
.set RR,175
.set FW,176
.set FR,177
.set FS,178
.set ASC,179
.set ASM,180
.set AVX,181
.set SIX,182
.set RES,184
.set TAG,185
.set MX,186
.set TM,187
.set MB,188
.set AC,204
.set TV,212
.set FA,214
.set RA,216
.set DD,188
.set SX,208
.set SMM,212
.set ZZ,224
.set VM,240
.set PA,248
.set VX,188
.set UU,220
.macro MEMV R
  LD MB, O3, s68, 0
  LD MB+4, O3, s68, 1024
  LD MB+8, O3, s68, 2048
  LD MB+12, O3, s68, 3072
.if \R == 3
  buffer_load_dwordx2 v[TV:TV+1], v[OT], s[40:43], s68 offen sc1
.endif
.endm
.macro SCV
  ds_read_b32 v[FA], v[FS]
  ds_read_b128 v[SX:SX+3], v[ASC]
  ds_read_b64 v[SMM:SMM+1], v[ASM]
.endm
.macro CZV t
  v_cvt_pk_bf16_f32 v[UB+8*\t+2], v[ZZ+4*\t], v[ZZ+4*\t+2]
  v_cvt_pk_bf16_f32 v[UB+8*\t+3], v[ZZ+4*\t+1], v[ZZ+4*\t+3]
.endm
.macro STEPV R
.set XL, (\R == 3) & 1
LV_loop\R:
  STAMP 0
  ZR AC, 4
  ZR ZZ, 8
  v_mov_b32 v[TAG], s65
  s_setprio 2
  s_barrier
.if \R == 3
  s_memtime s[92:93]
  s_nop 5
.else
  s_nop 7
.endif
.rept )" STR(VDEL1) R"(-1
  s_nop 7
.endr
  STAMP 1
  MEMV \R
  s_mov_b32 s76, 0x400000
LV_a\R:
  s_waitcnt vmcnt(3+XL)
  SM AC, MB, 64
  MX0 MB
  SM AC+4, MB, 96
.irp i,1,2,3
  s_waitcnt vmcnt(3+XL-\i)
  SM AC, MB+4*\i, 64+8*\i
  MX1 MB+4*\i
  SM AC+4, MB+4*\i, 96+8*\i
.endr
  v_cmp_eq_u32 vcc, -1, v[MX]
  STAMP 2
  s_nop 4
  s_cbranch_vccnz LV_ra\R
  HS AC
  HS AC+4
  ds_write2_b32 v[RW], v[AC], v[AC+4] offset1:1
  ds_write2_b32 v[RW], v[AC+1], v[AC+5] offset0:128 offset1:129
  ds_write_b32 v[FW], v[TAG]
.if \R == 0
LV_p:
  ds_read_b32 v[FA], v[FR]
  ds_read_b64 v[RA:RA+1], v[RR]
  ds_read_b64 v[RA+2:RA+3], v[RR] offset:1024
  ds_read_b64 v[RA+4:RA+5], v[RR] offset:2048
  ds_read_b64 v[RA+6:RA+7], v[RR] offset:3072
  CAP
  s_waitcnt lgkmcnt(4)
  v_cmp_gt_u32 vcc, s65, v[FA]
  s_cbranch_vccnz LV_p
  s_waitcnt lgkmcnt(2)
  HS RA
  s_waitcnt lgkmcnt(0)
  HS RA+4
  v_pk_add_f32 v[RA:RA+1], v[RA:RA+1], v[RA+4:RA+5]
  v_cvt_pk_bf16_f32 v[RES], v[RA], v[RA+1]
  buffer_store_dword v[RES], v[ST1], s[40:43], s66 offen sc1
.endif
  STAMP 3
  HOLD
.if \R == 0
  s_cmp_eq_u32 s64, 63
  s_cbranch_scc1 L_last
.endif
  s_waitcnt lgkmcnt(0)
.if \R == 3
  s_add_i32 s75, s92, s90
  s_cmp_eq_u32 s64, 0
  s_cbranch_scc1 LV_t0
LV_t1:
  s_waitcnt vmcnt(0)
  v_cmp_eq_u32 vcc, -1, v[TV]
  v_subrev_u32 v[TV], s73, v[TV]
  v_max_i32_dpp v[TV+1], v[TV+1], v[TV+1] quad_perm:[1,0,3,2] row_mask:0xf bank_mask:0xf
  s_cbranch_vccnz LV_rt
  v_max_i32_dpp v[TV], v[TV], v[TV] quad_perm:[1,0,3,2] row_mask:0xf bank_mask:0xf
  v_max_i32_dpp v[TV+1], v[TV+1], v[TV+1] quad_perm:[2,3,0,1] row_mask:0xf bank_mask:0xf
  s_nop 0
  v_max_i32_dpp v[TV], v[TV], v[TV] quad_perm:[2,3,0,1] row_mask:0xf bank_mask:0xf
  v_cmp_lt_i32 vcc, 16, v[TV]
  v_add_u32 v[TV], s73, v[TV]
  ds_write_b64 v[183], v[TV:TV+1]
  ZR DD, 10
  v_readfirstlane_b32 s73, v[TV]
  v_readfirstlane_b32 s74, v[TV+1]
  s_cbranch_vccnz LV_sl
  s_setprio 3
  WAITK s75
LV_t2:
  s_barrier
  s_cmp_eq_u32 s64, 63
  s_cbranch_scc1 L_end
  PLAN
  s_cmp_lt_u32 s64, NS
  s_cselect_b32 s75, GS*45/32, 0
  s_sub_u32 s90, s90, s75
.else
  ZR DD, 10
  s_setprio 3
  s_barrier
  s_nop 7
.endif
.rept )" STR(VDEL2) R"(-1
  s_nop 7
.endr
  STAMP 4
  LD VM, O1, s66, 0
  LD VM+4, O1, s66, 1024
  s_mov_b32 s76, 0x400000
LV_b\R:
  s_waitcnt vmcnt(1)
  SM ZZ, VM, 0
  MX0 VM
  SM ZZ+4, VM, 16
  SM ZZ+8, VM, 32
  SM ZZ+12, VM, 48
  s_waitcnt vmcnt(0)
  SM ZZ, VM+4, 8
  MX1 VM+4
  SM ZZ+4, VM+4, 24
  v_cmp_eq_u32 vcc, -1, v[MX]
  ds_read_b32 v[FA], v[FS]
  SM ZZ+8, VM+4, 40
  ds_read_b128 v[SX:SX+3], v[ASC]
  ds_read_b64 v[SMM:SMM+1], v[ASM]
  s_cmp_eq_u32 s64, 63
  SM ZZ+12, VM+4, 56
  s_cbranch_vccnz LV_rb\R
.if \R == 2
  s_cbranch_scc1 LV_img
.endif
  STAMP 5
LV_s\R:
  s_waitcnt lgkmcnt(2)
  v_cmp_gt_u32 vcc, s65, v[FA]
  s_cbranch_vccnz LV_rs\R
  s_waitcnt lgkmcnt(0)
  STAMP 6
  v_max3_f32 v[MX], v[SX], v[SX+1], v[SX+2]
  v_max3_f32 v[TM], v[SX+3], v[SMM], v[SMM+1]
  v_max_f32 v[MX], v[MX], v[TM]
  CZV 0
  v_max_f32_dpp v[MX], v[MX], v[MX] quad_perm:[2,3,0,1] row_mask:0xf bank_mask:0xf
  v_mov_b32 v[TM], v[MX]
  CZV 1
  v_permlane16_swap_b32 v[MX], v[TM]
  v_max_f32 v[MX], v[MX], v[TM]
  v_mov_b32 v[TM], v[MX]
  CZV 2
  v_permlane32_swap_b32 v[MX], v[TM]
  v_max_f32 v[MX], v[MX], v[TM]
  v_mul_f32 v[MX], s79, v[MX]
  v_pk_fma_f32 v[SX:SX+1], v[SX:SX+1], s[78:79], v[MX:MX+1] op_sel_hi:[1,0,0]
  v_pk_fma_f32 v[SMM:SMM+1], v[SMM:SMM+1], s[78:79], v[MX:MX+1] op_sel_hi:[1,0,0]
  v_pk_fma_f32 v[SX+2:SX+3], v[SX+2:SX+3], s[78:79], v[MX:MX+1] op_sel_hi:[1,0,0]
  v_exp_f32 v[SX], v[SX]
  v_exp_f32 v[SX+1], v[SX+1]
  v_exp_f32 v[SMM], v[SMM]
  v_cvt_pk_bf16_f32 v[PA], v[SX], v[SX+1]
  v_exp_f32 v[SMM+1], v[SMM+1]
  v_cvt_pk_bf16_f32 v[PA+1], v[SMM], v[SMM]
  v_exp_f32 v[SX+2], v[SX+2]
  v_exp_f32 v[SX+3], v[SX+3]
  v_cvt_pk_bf16_f32 v[PA+3], v[SMM+1], v[SMM+1]
  v_cvt_pk_bf16_f32 v[PA+2], v[SX+2], v[SX+3]
  CZV 3
  SM DD+16, PA, B1
  SM DD, PA, UB
  SM DD+4, PA, UB+8
  SM DD+8, PA, UB+16
  HS DD+16
  HS DD
  SM DD+12, PA, UB+24
  v_rcp_f32 v[DD+16], v[DD+16]
  v_rcp_f32 v[DD+17], v[DD+17]
  HS DD+4
  HS DD+8
  v_pk_mul_f32 v[DD:DD+1], v[DD:DD+1], v[DD+16:DD+17]
  v_pk_mul_f32 v[DD+4:DD+5], v[DD+4:DD+5], v[DD+16:DD+17]
  v_pk_mul_f32 v[DD+8:DD+9], v[DD+8:DD+9], v[DD+16:DD+17]
  v_cvt_pk_bf16_f32 v[SX], v[DD], v[DD+4]
  HS DD+12
  v_cvt_pk_bf16_f32 v[SX+2], v[DD+1], v[DD+5]
  v_pk_mul_f32 v[DD+12:DD+13], v[DD+12:DD+13], v[DD+16:DD+17]
  v_cvt_pk_bf16_f32 v[SX+1], v[DD+8], v[DD+12]
  v_cvt_pk_bf16_f32 v[SX+3], v[DD+9], v[DD+13]
  buffer_store_dwordx4 v[SX:SX+3], v[ST2], s[40:43], s66 offen sc1
  STAMP 7
  s_setprio 1
.if \R == 2
LV_img:
  v_add_u32 v[TM], 1024, v[AIM]
  ds_write2_b32 v[AIM], v[VM], v[VM+1] offset1:2
  ds_write2_b32 v[AIM], v[VM+2], v[VM+3] offset0:32 offset1:34
  ds_write2_b32 v[TM], v[VM+4], v[VM+5] offset1:2
  ds_write2_b32 v[TM], v[VM+6], v[VM+7] offset0:32 offset1:34
  ds_write_b32 v[FW], v[TAG] offset:1280
  s_cmp_eq_u32 s64, 63
  s_cbranch_scc1 L_end
.endif
LV_u\R:
  XSL s65
  v_add_u32 v[TM], s75, v[AVX]
.irp k,0,1,2,3,4,5,6,7
  ds_read_b128 v[VX+4*\k:VX+4*\k+3], v[TM] offset:1024*\k
.endr
.irp k,0,1,2,3,4,5,6,7
  s_waitcnt lgkmcnt(7-\k)
.irp t,0,1,2,3
.if (\k & 3) == 0
  v_mfma_f32_16x16x32_bf16 v[UU+4*\k+4*\t:UU+4*\k+4*\t+3], v[VX+4*\k:VX+4*\k+3], v[16*\t:16*\t+3], 0
.else
  v_mfma_f32_16x16x32_bf16 v[UU+4*(\k&4)+4*\t:UU+4*(\k&4)+4*\t+3], v[VX+4*\k:VX+4*\k+3], v[16*\t+4*(\k&3):16*\t+4*(\k&3)+3], v[UU+4*(\k&4)+4*\t:UU+4*(\k&4)+4*\t+3]
.endif
.endr
.endr
.irp k,0,4
.irp t,0,1,2,3
  v_cvt_pk_bf16_f32 v[UB+8*\t+\k], v[UU+4*\k+4*\t], v[UU+4*\k+4*\t+1]
  v_cvt_pk_bf16_f32 v[UB+8*\t+\k+1], v[UU+4*\k+4*\t+2], v[UU+4*\k+4*\t+3]
.endr
.endr
  STAMP 8
  NEXT MB
  s_branch LV_loop\R
LV_ra\R:
  RETRY 2
  TGET RA
  s_sleep 3
  MEMV \R
  ZR AC, 4
  s_branch LV_a\R
LV_rb\R:
  RETRY 0
  LD VM, O1, s66, 0
  LD VM+4, O1, s66, 1024
  ZR ZZ, 8
  s_branch LV_b\R
LV_rs\R:
  SCV
  CAP
  s_branch LV_s\R
.if \R == 3
LV_t0:
  s_memrealtime vcc
  s_waitcnt vmcnt(0) lgkmcnt(0)
  s_lshl_b32 s73, vcc_lo, 4
  s_sub_i32 s73, s73, 0x2000
  v_mov_b32 v[TV], s73
  v_mov_b32 v[TV+1], 0
  s_branch LV_t1
LV_sl:
  s_cmp_lt_u32 s64, NS
  s_cselect_b32 s75, 0, )" STR(SLIP_INC) R"(*45/32
  s_add_i32 s90, s90, s75
  s_add_i32 s75, s73, s56
  s_add_i32 s75, s75, s74
  s_setprio 3
  WAITP s75
  s_branch LV_t2
LV_rt:
  RETRY 2
  buffer_load_dwordx2 v[TV:TV+1], v[OT], s[40:43], s68 offen sc1
  s_branch LV_t1
.endif
.endm
LV_init:
  PRE
  s_mul_i32 s90, s56, 45
  s_lshr_b32 s90, s90, 5
  s_add_i32 s90, s90, NS*(GS*45/32)-)" STR(WKO) R"(
  s_mov_b32 s78, 0x3e0293ee
  s_mov_b32 s79, 0xbe0293ee
  ZR UB, 16
  v_mov_b32 v[B1], 0x3f803f80
  v_mov_b32 v[B1+1], 0x3f803f80
  v_mov_b32 v[B1+2], 0x3f80
  v_mov_b32 v[B1+3], 0x3f80
  v_mov_b32 v[B1+4], 0x3f803f80
  v_mov_b32 v[B1+5], 0x3f803f80
  v_mov_b64 v[B1+6:B1+7], 0
  s_cmp_eq_u32 s60, 2
  s_cbranch_scc1 LV_u2
  s_cmp_eq_u32 s60, 3
  s_cbranch_scc1 LV_u3
  s_branch LV_u0
  STEPV 0
  STEPV 2
  STEPV 3
L_fatal:
  v_mov_b32 v[MX], 0
  v_mov_b32 v[TAG], 1
  global_store_dword v[MX], v[TAG], s[48:49] sc1
  s_waitcnt vmcnt(0)
  s_endpgm
L_last:
  s_barrier
L_end:
  s_waitcnt vmcnt(0) lgkmcnt(0)
)"
static_assert(__builtin_bit_cast(unsigned, SCALE * LOG2E) == 0x3e0293eeu, "");
#define CL10(p) p "0", p "1", p "2", p "3", p "4", p "5", p "6", p "7", p "8", p "9"
#define CL_ST
#define CL_S CL_ST "s72", "s73", "s74", "s75", "s76", "s77", "s78", "s79", "s88", "s89", "s90", "s91", "s92", "s93", "s94", "s95", "s96", "s97", "s98", "s99", CL10("s3"), "vcc", "scc", "memory"

__device__ __forceinline__ void chain(const Args& a, const int c, const unsigned L) {
    const int tid = threadIdx.x, w8 = RFL(tid >> 6), w = w8 & 3, l = tid & 63;
    const int g = c & 3, aa = c >> 2, h = g * 8 + (aa >> 2), jj = aa & 3;
    const int n = l & 15, q = l >> 4, hf = (n >> 1) & 1, n2 = (n >> 2) & 1, n3 = n >> 3;
    const unsigned L_RED = L + 131072, L_RV = L_RED + 6144, L_SC = L_RV + 4096, L_SM = L_SC + 2048, L_FL = L_SM + 512, L_TB = L_FL + 3072, L_VM = L_TB + 256, L_KX = L + 32768, L_VX = L_KX + 8192;
    const rsrc_t rs = mkrs(a.ring, RING_BYTES);
    unsigned* const err = a.ctl + C_ERR;
    const v4u ones = {~0u, ~0u, ~0u, ~0u};
    const unsigned cyc0 = RFL((unsigned)__builtin_amdgcn_s_memtime()), rt0 = RFL((unsigned)__builtin_amdgcn_s_memrealtime());
    if (tid < 384) lp<v2u>(L_FL)[tid] = v2u{0u, 0u};
    if (c < 88 || (c >= 96 && c < 108)) {
#pragma unroll
        for (int i = 0; i < 4; ++i) STB(ones, rs, c * 32768 + i * 8192 + tid * 16, 0); }
    const int lg3 = L_H3 + ((q >> 1) * 2 + n3) * 16384 + c * 128 + n2 * 64 + hf * 32 + (q & 1) * 16 + (n & 1) * 8;
    if (w8 == 3) { const bf16_t* p = a.mem0 + ((c & 1) * 4 + n2 * 2 + (q & 1)) * DM + 16 * (c >> 1) + (n & 3);
        const v2u m = {min(p[0] | (unsigned)p[4] << 16, 0xFFFFFFFEu), min(p[8] | (unsigned)p[12] << 16, 0xFFFFFFFEu)};
#pragma unroll
        for (int i = 0; i < 2; ++i) __builtin_amdgcn_raw_buffer_store_b64(m, rs, CHB + ((lg3 >> 13) << 14) + (lg3 & 8191), 2 * SLOT3 + i * 8192, 16); }
    const unsigned stg = L + w8 * 16384;
    const int prr = 16 * (q >> 1) + (q & 1) + 4 * n2 + 2 * n3;
    v32u W[4];
    auto trd4 = [&](int k, unsigned p, int o1, int o2) { const v2u t[4] = {trd(p), trd(p + o1), trd(p + o2), trd(p + o2 + o1)};
#pragma unroll
        for (int i = 0; i < 8; ++i) W[k >> 2][(k & 3) * 8 + i] = t[i >> 1][i & 1]; };
    unsigned cal;
    auto phA = [&](int lg) { return ((lg >> 13) << 14) + (int)((cal & 1) << 13) + (lg & 8191); };
    auto phB = [&](int lg) { return CHB + ((lg >> 13) << 14) + (int)((cal & 2) << 12) + (lg & 8191); };
    auto h1o = [&](int inner) { return phB(h * 6144 + inner); };
    auto stream = [&](auto issue, auto read) {
        issue(0); issue(1);
        WAITV(8); BAR();
        if (w8 == 3) asm volatile("s_mov_b64 exec, 1\n\tglobal_atomic_add %0, %1, %2 sc1\n\ts_mov_b64 exec, -1" :: "v"(0u), "v"(1u), "s"(a.ctl + C_READY) : "memory");
        read(std::integral_constant<int, 0>{}); WAITL(); issue(2);
        WAITV(8); read(std::integral_constant<int, 1>{}); WAITL(); issue(3);
        WAITV(8); read(std::integral_constant<int, 2>{});
        WAITV(0); read(std::integral_constant<int, 3>{}); WAITL();
    };
    auto start = [&]() {
        if (w8 == 3) {
            flags_wait<4>(a.ctl + C_READY, 1, 128 + 32, l, err);
            unsigned v = gld(a.ctl + C_CAL + (l & 3));
            unsigned t = (__builtin_amdgcn_readlane(v, 1) < __builtin_amdgcn_readlane(v, 0) ? 1u : 0u) | (__builtin_amdgcn_readlane(v, 3) < __builtin_amdgcn_readlane(v, 2) ? 2u : 0u);
            lp<unsigned>(L_TB + 16)[0] = t; WAITL();
        }
        BAR();
        cal = RFL(ldsr(L_TB + 16));
    };
    const unsigned char* xp = a.qkv + h * 24576 + w * 8192 + (w == 2 ? (jj & 1) * 4096 : 0);
    const unsigned char* xq = a.qkv + h * 24576 + 2 * NH * 24576;
    const unsigned xw = w == 3 ? 20480 - XC * 1024 : w * (XC == 4 ? 8192 : 4096);
    const unsigned* fG = a.ctl + C_G + h * 128 + w * 32;
    auto lo = [](const void* p) { return (unsigned)(u64)p; };
    auto hi = [](const void* p) { return (unsigned)((u64)p >> 32); };
    v16u S0 = {lo(a.ring), hi(a.ring), RING_BYTES, 0x20000u, lo(a.ox), hi(a.ox), NSEG * 262144, 0x20000u, lo(err), hi(err), lo(a.ctl + C_M + h * 128 + jj * 32), hi(a.ctl + C_M + h * 128 + jj * 32),
               lo(xq), hi(xq), lo(fG - w * 32), hi(fG - w * 32)};
    v16u S1 = {(unsigned)a.d1, (unsigned)a.d2, (unsigned)a.per, a.dinit & 0xffffu, (unsigned)w, L_KX + xw, L_TB, (unsigned)w8};
    v8u cnts;
    v16u A, K;
    const unsigned six = hf ? 0xEEEE : 0x4444;

    if (w8 < 4) {
        { const bf16_t* Wm = (jj >> 1 ? a.Wk : a.Wq) + (size_t)256 * w * HD + h * DH + 64 * (jj & 1);
          const unsigned p = stg + prr * 128 + (((hf ^ (4 * n2 + 2 * n3)) * 16 + (n & 1) * 8) ^ ((w >> 1) * 64));
          stream([&](int i) { _Pragma("unroll 1") for (int j = 0; j < 8; ++j) gldsc(Wm + (size_t)(64 * i + 8 * j) * HD, (l >> 3) * (HD * 2) + ((l & 7) ^ (2 * q)) * 16, stg + (i & 1) * 8192 + j * 1024); },
                 [&](auto i) { _Pragma("unroll") for (int t = 0; t < 4; ++t) trd4(4 * t + i, (p ^ (t * 32)) + (i & 1) * 8192, 1024, 4096); }); }
        MARK(2);
#pragma unroll
        for (int i = 0; i < 4; ++i) K[i] = ((n & 7) >> 1) == i ? 0x3F80u << (16 * (n & 1)) : 0u;
        K[9] = 8192 + l * 16; K[10] = 12288 + l * 16; K[11] = 16384 + (jj & 1) * 4096 + l * 16;
        if (w == 2) { const int rt = jj & 1, ck = 8 * (jj >> 1) + hf;
#pragma unroll
            for (int i = 0; i < 8; ++i) K[i] = 16 * (i >> 2) + 4 * q + (i & 3) > 16 * rt + n ? 0xFF800000u : 0u;
            K[8] = L_KX + l * 16; K[9] = L_VX + (ck * 16 + 4 * q + (n >> 2)) * 16 + (l & 1) * 8; K[10] = L_VM + ck * 128 + (2 * q + n2) * 16 + (l & 1) * 8; }
        BAR();
        if (w < 3) {
            flags_wait<4>(fG, 1, 2, l, err);
#pragma unroll
            for (int i = 0; i < 4; ++i) if (w < 2 || !(i & 1)) glds1<4>(xp + (i >> 1) * (NH * 24576) + (i & 1) * 4096, l * 16, L_KX + (i >> 1) * 20480 + w * 8192 + (i & 1) * 4096);
            WAITV(0);
        }
        start();
        if (w < 2) {
#pragma unroll
            for (int i = 0; i < 4; ++i) { const v4u t = *lp<v4u>(L_KX + w * 4096 + i * 1024 + l * 16);
#pragma unroll
                for (int e = 0; e < 4; ++e) K[4 * i + e] = t[e]; } }
        MARK(0); MARKC(4);
        const int sl = 2 * (jj & 1) + (w >> 1);
        A[0] = phB(L_H3 + g * 16384 + w * 4096 + (l ^ (w & 1)) * 16);
        A[1] = h1o((jj >> 1) * 2048 + ((sl * 2 + n3) * 2 + (w & 1)) * 128 + (q >> 1) * 64 + n2 * 32 + (q & 1) * 16 + (n & 3) * 4);
        A[2] = L_RED + (w ^ 1) * 512 + l * 8; A[3] = L_RED + 2048 + (w ^ 2) * 512 + l * 8; A[4] = L_RED + 4096 + (w ^ 3) * 512 + l * 8; A[5] = L_RED + w * 512 + l * 8;
        A[6] = L_FL + w * 256 + l * 4; A[7] = L_FL + (l & 3) * 256 + l * 4; A[8] = six;
        A[9] = w == 2 ? h * 8192 + jj * 2048 + ((2 * (q & 1) + (q >> 1)) * 16 + n) * 16 : (XC == 4 ? min(w, 2) * 128 : (w == 3 ? 128 + (l & 1) * 128 : 0));
        A[10] = w < 3 ? h1o(l * 16) : phA(c * 4096 + l * 16);
        A[11] = w < 2 ? L_KX + w * 4096 + l * 16 : h1o((w == 2 ? 2048 : jj * 1024) + l * 16);
        A[12] = w < 2 ? L_SC + w * 1024 + ((l * 16) ^ ((q & 1) * 32)) : (w == 2 ? L_SM + ((l * 8) ^ ((q & 1) * 16)) : phB(lg3));
        A[13] = w < 3 ? xw + l * 16 : (c % 33 == 0 && l < 4 ? phB(L_TS + l * 512 + (c >> 5) * 128) : OOB);
        A[14] = w < 3 ? xw + 4096 + l * 16 : h1o(4096 + jj * 512 + l * 8); A[15] = 0;
    } else {
        { const bf16_t* Wo = a.Wo + (size_t)h * DH * DM + 256 * jj + 64 * w; const bf16_t* Wv = a.Wv + (size_t)256 * w * HD + h * DH + 32 * jj;
          const unsigned po = stg + (4 * q + n3 + 16 * n2) * 128 + (((n & 3) * 2) ^ (q & 1)) * 16, pv = stg + prr * 64 + (hf ^ (2 * n2)) * 16 + (n & 1) * 8;
          stream([&](int i) {
                     if (i < 2) { _Pragma("unroll 1") for (int j = 0; j < 8; ++j) gldsc(Wo + (size_t)(64 * i + 8 * j) * DM, (l >> 3) * (DM * 2) + ((l & 7) ^ (l >> 5)) * 16, stg + i * 8192 + j * 1024); }
                     else { _Pragma("unroll 1") for (int j = 0; j < 8; ++j) glds(Wv + (size_t)(128 * (i - 2) + 16 * j) * HD, (l >> 2) * (HD * 2) + ((l & 3) ^ (2 * (q & 1))) * 16, stg + (i & 1) * 8192 + j * 1024); } },
                 [&](auto i) { _Pragma("unroll") for (int t = 0; t < 4; ++t) {
                     if constexpr (i < 2) trd4(2 * t + i, (po ^ ((t >> 1) * 16)) + (t & 1) * 8 + i * 8192, 256, 4096);
                     else trd4(8 + 4 * (t & 1) + 2 * (i - 2) + (t >> 1), (pv ^ ((t & 1) * 32)) + (t >> 1) * 4096 + (i & 1) * 8192, 512, 2048); } }); }
        BAR();
        start();
        A[0] = phB(L_H3 + g * 16384 + w * 4096 + l * 16); A[1] = h1o(4096 + l * 16); A[2] = phB(L_TS + g * 512 + (l & 3) * 128);
        A[3] = h1o(4096 + ((jj * 2 + n3) * 2 + (w & 1)) * 128 + (q & 1) * 64 + (n & 1) * 32 + (q >> 1) * 16 + ((n >> 1) & 3) * 4);
        A[4] = phA((jj * 32 + w * 8 + (n >> 2) * 2 + (q >> 1)) * 4096 + h * 128 + ((q & 1) * 4 + (n & 3)) * 16);
        A[5] = L_VM + q * 256 + (4 * n3 + 2 * (n & 1) + n2) * 16 + hf * 4;
        A[6] = L_RV + w * 1024 + l * 8; A[7] = L_RV + (w & 1) * 512 + l * 8;
        A[8] = L_FL + 1024 + w * 256 + l * 4; A[9] = L_FL + 1024 + (l & 3) * 256 + l * 4; A[10] = L_FL + 2048 + min(l & 3, 2) * 256 + l * 4;
        A[11] = L_SC + q * 512 + hf * 256 + n * 16; A[12] = L_SM + (q & 1) * 256 + hf * 128 + n * 8;
        A[13] = L_VX + q * 256 + (8 * n3 + 4 * hf + 2 * n2 + (n & 1)) * 16; A[14] = six; A[15] = L_TB;
    }
    S1[8] = RFL(phB(L_TS + g * 512)); S1[9] = cyc0; S1[10] = rt0;
    asm volatile(A_CHAIN : "+{s[40:55]}"(S0), "+{s[56:71]}"(S1), "={s[80:87]}"(cnts), "+{v[168:183]}"(A), "+{v[196:211]}"(K) : "{v[0:31]}"(W[0]), "{v[32:63]}"(W[1]), "{v[64:95]}"(W[2]), "{v[96:127]}"(W[3])
                 : "v128", "v129", CL10("v13"), CL10("v14"), CL10("v15"), "v160", "v161", "v162", "v163", "v164", "v165", "v166", "v167", "v184", "v185", "v186", "v187", "v188", "v189",
                   "v190", "v191", "v192", "v193", "v194", "v195", "v212", "v213", "v214", "v215", "v216", "v217", "v218", "v219", CL10("v22"), CL10("v23"), CL10("v24"), "v250", "v251", CL_S);
}

struct Tile { const bf16_t* W; int ld, wide, perm; };
__device__ __forceinline__ void tile_issue(const Tile& T, int t, unsigned L, int w8, int l) {
    const bf16_t* p = T.W + (T.wide ? (w8 & 3) * 32 * T.ld + (2 * t + (w8 >> 2)) * 128 : (t * 256 + w8 * 32) * T.ld);
    const unsigned vo = (T.perm ? (l >> 5) + (l & 16) : l >> 4) * T.ld * 2 + ((l & 15) ^ (2 * (l >> 4))) * 16;
#pragma unroll 1
    for (int i = 0; i < 8; ++i) { const bf16_t* g = p + i * (T.perm ? 2 : 4) * T.ld; const unsigned v = vo ^ ((i & 2) << 6), d = L + (t & 1) * 65536 + (w8 * 8 + i) * 1024;
        gldsw(g, v, d); }
}
template <class F>
__device__ __forceinline__ void tile_load(const Tile& T, v4u (&wf)[32], unsigned L, int w8, int l, F next) {
    const int n = l & 15, q = l >> 4;
    const unsigned p = L + (8 * q + (n >> 2)) * 256 + ((4 * (w8 & 3) + ((l >> 1) & 1)) ^ (2 * ((n >> 2) | ((q & 1) << 2)))) * 16 + (l & 1) * 8;
    bool more = true;
#pragma unroll 1
    for (int hh = 0; hh < 2; ++hh)
#pragma unroll
    for (int u = 0; u < 2; ++u) {
        if (!more) WAITV(0);
        WAITV(8); BAR();
        if ((w8 >> 2) == hh) {
#pragma unroll
            for (int i = 0; i < 16; ++i) { const unsigned pp = (p ^ ((i & 1) * 32)) + u * 65536 + (i >> 1) * 8192; wf[u * 16 + i] = cat2(trd(pp), trd(pp + 1024)); }
            WAITL(); }
        BAR();
        if (hh == 0) tile_issue(T, u + 2, L, w8, l); else more = next(u);
    }
}
#define NB 8
#define GD 4
template <int XR, bool ROWS, class F>
__device__ __forceinline__ void seg_gemm(const v4u (&wf)[32], unsigned b0, frag_cd (&acc)[4], F side) {
    v4u f[2 * GD];
    const unsigned bb[4] = {b0, ROWS ? b0 ^ 64 : b0, ROWS ? b0 ^ 128 : b0, ROWS ? b0 ^ 192 : b0};
#define RD(ks, mt) asm volatile("ds_read_b128 %0, %1 offset:%2" : "=v"(f[((ks) % GD) * 2 + mt]) : "v"(bb[(ks) & 3]), "n"(ROWS ? ((ks) >> 2) * 256 + mt * 16384 : ((ks) >> 1) * 4096 + ((ks) & 1) * 1024 + mt * 2048) : "memory")
#define MM(ct, mt) do { if (ks == 0) asm volatile("v_mfma_f32_16x16x32_bf16 %0, %1, %2, 0" : "=&v"(acc[2 * ct + mt]) : "v"(wf[ct]), "v"(f[mt])); \
        else asm volatile("v_mfma_f32_16x16x32_bf16 %0, %1, %2, %0" : "+v"(acc[2 * ct + mt]) : "v"(wf[2 * ks + ct]), "v"(f[(ks % GD) * 2 + mt])); } while (0)
#pragma unroll
    for (int ks = 0; ks < GD - 1; ++ks) { RD(ks, 0); RD(ks, 1); }
#pragma unroll
    for (int ks = 0; ks < 16; ++ks) {
        side(2 * ks);
        asm volatile("s_waitcnt lgkmcnt(%0)" :: "n"(2 * (ks + GD - 1 <= 16 ? GD - 2 : 15 - ks) + (ks >= 13 ? XR : 0)));
        MM(0, 0);
        if (ks + GD - 1 < 16) RD(ks + GD - 1, 0);
        MM(0, 1);
        side(2 * ks + 1);
        MM(1, 0);
        if (ks + GD - 1 < 16) RD(ks + GD - 1, 1);
        MM(1, 1);
    }
    asm volatile("s_nop 7" : "+v"(acc[0]), "+v"(acc[1]), "+v"(acc[2]), "+v"(acc[3]));
#undef RD
#undef MM
}
static_assert(GD == 4, "");
__device__ __forceinline__ float fadd(float a, float b) { float r; asm("v_add_f32 %0, %1, %2" : "=v"(r) : "v"(a), "v"(b)); return r; }
__device__ __forceinline__ float fmul(float a, float b) { float r; asm("v_mul_f32 %0, %1, %2" : "=v"(r) : "v"(a), "v"(b)); return r; }
__device__ __forceinline__ void half_put(unsigned ex, int w8, int l, const frag_cd (&acc)[4]) {
#pragma unroll
    for (int i = 0; i < 4; ++i) *lp<frag_cd>(ex + w8 * 4096 + i * 1024 + l * 16) = acc[i];
    FENCE();
}
struct Half { frag_cd t[4]; unsigned dec; };
__device__ __forceinline__ void half_req(unsigned ex, unsigned dec, Half& x) {
    asm volatile("ds_read_b32 %0, %1" : "=v"(x.dec) : "v"(dec) : "memory");
#pragma unroll
    for (int i = 0; i < 4; ++i) asm volatile("ds_read_b128 %0, %1 offset:%2" : "=v"(x.t[i]) : "v"(ex), "n"(i * 1024) : "memory");
}
template <class F>
__device__ __forceinline__ void half_get(Half& x, frag_cd (&acc)[4], F side) {
    asm volatile("s_waitcnt lgkmcnt(0)" : "+v"(x.t[0]), "+v"(x.t[1]), "+v"(x.t[2]), "+v"(x.t[3]), "+v"(x.dec) :: "memory");
#pragma unroll
    for (int i = 0; i < 4; ++i) { side(i);
#pragma unroll
        for (int e = 0; e < 4; ++e) acc[i][e] = fadd(acc[i][e], x.t[i][e]);
        asm volatile("" : "+v"(acc[i])); }
}
__device__ __forceinline__ void gld_req(unsigned& v, const unsigned* p) { asm volatile("global_load_dword %0, %1, off sc1" : "+v"(v) : "v"(p) : "memory"); }
template <int N> __device__ __forceinline__ void gld_wait(unsigned& v) { asm volatile("s_waitcnt vmcnt(%1)" : "+v"(v) : "n"(N) : "memory"); }
#define GEARLY 2
#define BARI 25
#define PRIO(n) __builtin_amdgcn_s_setprio(n)

__device__ __forceinline__ unsigned topk(const Args& a, int l) {
    const rsrc_t rg = mkrs(a.gate, 2 * S_TOK * 4);
    flags_wait<16>(a.ctl + C_R, 32, 1, l, a.ctl + C_ERR);
    v4u kv[8]; unsigned prev = ~0u, mine = 0;
#pragma unroll
    for (int i = 0; i < 8; ++i) kv[i] = __builtin_amdgcn_raw_buffer_load_b128(rg, S_TOK * 4 + l * 16, i * 1024, 16);
#pragma unroll 1
    for (int c = 0; c < TOPC; ++c) { unsigned mx = 0;
        FENCE();
#pragma unroll
        for (int i = 0; i < 8; ++i)
#pragma unroll
            for (int e = 0; e < 4; ++e) mx = max(mx, kv[i][e] < prev ? kv[i][e] : 0u);
        mx = max(mx, dppu<0x128>(mx)); mx = max(mx, dppu<0x124>(mx)); mx = max(mx, dppu<0x122>(mx)); mx = max(mx, dppu<0x121>(mx));
        { v2u r = sw16(mx, mx); mx = max(r.x, r.y); r = sw32(mx, mx); mx = max(r.x, r.y); }
        prev = RFL(mx); if ((l & 7) == c) mine = prev; }
    return mine;
}
__device__ __forceinline__ unsigned mix(float gt, float omg, float t0, float t1, float b0, float b1) {
    unsigned av = cvt_pk(t0, t1); av = cvt_pk(fmul(gt, lo16(av)), fmul(gt, hi16(av))); const unsigned bv = cvt_pk(fmul(omg, b0), fmul(omg, b1));
    return cvt_pk(fadd(lo16(av), lo16(bv)), fadd(hi16(av), hi16(bv)));
}
struct Finisher {
    rsrc_t rp, rx, rg, ro; int po, frow, xo, w8; unsigned seltok, ly;
    struct Fin { v4u p[4]; v2u xv; float gt; int k; };
    __device__ __forceinline__ Finisher(const Args& a, int j, int w8_, int l, unsigned L_Y) {
        rp = mkrs(a.part, NSEG * 524288); rx = mkrs(a.x, S_TOK * DM * 2); rg = mkrs(a.gate, S_TOK * 4); ro = mkrs(a.out, S_TOK * DM * 2);
        w8 = w8_; po = j * 16384 + w8 * 1024 + l * 16; frow = 16 * (w8 & 1) + (l & 15); xo = (frow * DM + j * 32 + (w8 >> 1) * 16 + 4 * (l >> 4)) * 2;
        seltok = 4095u - (lp<unsigned>(L_Y + 1024)[l & 7] & 4095u); ly = L_Y + ((w8 >> 1) * 32 + (l >> 4) * 8) * 16;
    }
    __device__ __forceinline__ Fin issue(int s, bool on) const { Fin f; int k = on ? 0 : 0x40000000; asm("" : "+s"(k));
#pragma unroll
        for (int i = 0; i < 4; ++i) f.p[i] = __builtin_amdgcn_raw_buffer_load_b128(rp, k + po, s * 524288 + i * 4096, 16);
        f.k = k + xo;
        f.xv = __builtin_amdgcn_raw_buffer_load_b64(rx, f.k, s * 65536, 0);
        f.gt = __uint_as_float(__builtin_amdgcn_raw_buffer_load_b32(rg, k + frow * 4, s * 128, 16));
        return f;
    }
    template <class F> __device__ __forceinline__ void finish(int s, const Fin& f, F side) const {
        const int tok = 32 * s + frow; const float gt = f.gt, omg = rbf(1.0f - gt);
        frag_cd t; v2u o; int k = f.k;
#pragma unroll
        for (int e = 0; e < 4; e += 2) {
            side(e >> 1);
#pragma unroll
            for (int u = e; u < e + 2; ++u) t[u] = fadd(fadd(__uint_as_float(f.p[0][u]), __uint_as_float(f.p[1][u])), fadd(__uint_as_float(f.p[2][u]), __uint_as_float(f.p[3][u])));
            o[e >> 1] = mix(gt, omg, t[e], t[e + 1], lo16(f.xv[e >> 1]), hi16(f.xv[e >> 1])); asm volatile("" : "+v"(o[e >> 1])); }
        for (unsigned mk = (unsigned)__builtin_amdgcn_ballot_w64((seltok >> 4) == (unsigned)(2 * s + (w8 & 1))) & 255u; mk; mk &= mk - 1) {
            const int slot = __builtin_ctz(mk);
            if (__builtin_amdgcn_readlane(seltok, slot) == (unsigned)tok) { *lp<frag_cd>(ly + slot * 16) = t; k = 0x40000000; }
        }
        __builtin_amdgcn_raw_buffer_store_b64(o, ro, k, s * 65536, 0);
    }
};

__device__ __forceinline__ void gwork(const Args& a, const int m, const int h, const unsigned L) {
    const int w8 = RFL(threadIdx.x >> 6); int l = threadIdx.x & 63, n = l & 15, q = l >> 4;
    unsigned* const err = a.ctl + C_ERR;
    const unsigned L_EX = L + 131072, L_XS = L_EX, L_HS = L_XS + 16384, L_GU = L_HS + 2048;
    unsigned* const fG = a.ctl + C_G + h * 128 + m * 32;
    const unsigned xo = (l ^ (8 * (w8 & 1))) * 16;
    auto xissue = [&](int s, int i0, int i1) {
        const bf16_t* p = a.x + (size_t)(32 * s + 8 * (w8 & 3)) * DM + (~w8 & 4) * 128; const unsigned d = L + (s & 1) * 65536 + (w8 ^ 4) * 8192;
#pragma unroll
        for (int i = i0; i < i1; ++i) {
            if (i == i0 || i == 4) asm volatile("s_mov_b32 m0, %0" :: "s"(d + (i & 4) * 1024));
            unsigned t; asm volatile("v_xor_b32 %0, %4, %1\n\tglobal_load_lds_dwordx4 %0, %2 offset:%3" : "=&v"(t) : "v"(xo), "s"(p), "n"((i & 3) * 1024), "n"(i * 1040 + (i & 4) * 1024) : "memory"); }
    };
    v4u wf[32];
    { const Tile T{(m == 0 ? a.Wk : (m == 1 ? a.Wv : a.Wq)) + h * DH, HD, 0, 0};
      tile_issue(T, 0, L, w8, l); tile_issue(T, 1, L, w8, l);
      tile_load(T, wf, L, w8, l, [&](int b) { if (b == 0 || w8 >= 4) xissue(b, 0, 8); return true; }); }
    MARK(0);
    const unsigned rb0 = L + (w8 >> 2) * 32768 + n * 1024 + (q ^ n) * 16;
    unsigned pacc[8] = {}, plast = 0; (void)pacc; (void)plast;
    PT(7);
    if (w8 >= 4) {
        const rsrc_t rq = mkrs(a.qkv, NSEG * NH * 24576), rf = mkrs(fG, w8 == 4 ? 4 : 0);
        const int sto = h * 24576 + m * 8192 + (m == 1 ? (w8 & 3) * 64 + l : ((w8 & 2) + (q & 1)) * 64 + (2 * (w8 & 1) + (q >> 1)) * 16 + n) * 16;
        WAITV(8); BAR(); WAITV(0);
        for (int sg = 0; sg < NSEG; ++sg) {
            PT(0);
            BAR();
            PT(1);
            PRIO(2);
            if (sg <= GEARLY) __builtin_amdgcn_raw_buffer_store_b32(sg, rf, l * 4, 0, 16);
            frag_cd acc[4]; Half hx;
            seg_gemm<5, true>(wf, rb0 + (sg & 1) * 65536, acc, [&](int i) {
                if (i == BARI) { PRIO(3); PT(5); WAITV(3); BAR(); PT(6); }
                if (i == 26) half_req(L_EX + (w8 & 3) * 4096 + l * 16, L_EX, hx);
                if (i >= 26) xissue((sg + 2) & 63, i - 26, i - 25); });
            PRIO(0);
            PT(2);
            half_get(hx, acc, [&](int i) { if (i & 1) xissue((sg + 2) & 63, 6 + i / 2, 7 + i / 2); });
            v4u r[2];
#pragma unroll
            for (int mt = 0; mt < 2; ++mt) r[mt] = v4u{cvt_pk(acc[mt][0], acc[2 + mt][0]), cvt_pk(acc[mt][1], acc[2 + mt][1]), cvt_pk(acc[mt][2], acc[2 + mt][2]), cvt_pk(acc[mt][3], acc[2 + mt][3])};
            PT(3);
#pragma unroll
            for (int mt = 0; mt < 2; ++mt) STB(r[mt], rq, sto + mt * 4096, sg * (NH * 24576));
            __builtin_amdgcn_raw_buffer_store_b32(sg <= GEARLY ? sg : sg - 1, rf, l * 4, 0, 16);
            FENCE();
            if (sg < GEARLY) WAITV(0);
            PT(4);
        }
        PFLUSH();
        WAITV(0); BAR();
        if (w8 == 4) gst1(fG, NSEG);
    } else {
        for (int sg = 0; sg < NSEG; ++sg) {
            frag_cd acc[4];
            WAITL();
            PT(0);
            BAR();
            PT(1);
            PRIO(2);
            seg_gemm<0, true>(wf, rb0 + (sg & 1) * 65536, acc, [&](int i) { if (i == BARI) { PRIO(3); PT(5); WAITV(0); BAR(); PT(6); } });
            PRIO(1);
            PT(2);
            half_put(L_EX, w8, l, acc);
            xissue((sg + 1) & 63, 0, 8);
            PT(3);
            MARK(sg + 1);
        }
        PFLUSH();
        WAITL(); BAR();
        WAITV(0); BAR();
    }
    if (m != 2) return;
    asm volatile("v_mbcnt_lo_u32_b32 %0, -1, 0\n\tv_mbcnt_hi_u32_b32 %0, -1, %0" : "=v"(l));
    n = l & 15; q = l >> 4;
    const rsrc_t ry = mkrs(a.ypart, 32 * 29 * 1024);
    auto tile = [&](int t) { return t < 2 ? Tile{(t ? a.wu : a.wg) + h * 128, FF, 0, 0} : Tile{a.wd + (size_t)h * 128 * DM, DM, 1, 0}; };
    unsigned mykey = 0;
    if (w8 == 0) {
        mykey = topk(a, l);
#pragma unroll 1
        for (int i = 0; i < 16; ++i) glds(a.x + i * 64, (4095u - (mykey & 4095u)) * (DM * 2) + (l >> 3) * 16, L_XS + i * 1024);
    }
    if (h < FF / 128) {
      Tile T = tile(0);
      tile_issue(T, 0, L, w8, l); tile_issue(T, 1, L, w8, l);
      frag_cd gv[2] = {};
#pragma unroll 1
      for (int ti = 0; ti < 3; ++ti) {
        const Tile N = tile(ti + 1);
        tile_load(T, wf, L, w8, l, [&](int b) { if (ti < 2) tile_issue(N, b, L, w8, l); return ti < 2; });
        T = N;
        if (ti < 2) {
            frag_cd acc[2] = {};
#pragma unroll
            for (int ks = 0; ks < 16; ++ks) { const v4u xf = *lp<v4u>(L_XS + ((w8 >> 2) * 16 + ks) * 512 + q * 128 + (n & 7) * 16); acc[0] = MFMA(wf[2 * ks], xf, acc[0]); acc[1] = MFMA(wf[2 * ks + 1], xf, acc[1]); }
            if (ti == 1) {
                frag_cd g = n < 8 ? gv[0] : gv[1], u = n < 8 ? acc[0] : acc[1];
                if (w8 >= 4) { *lp<frag_cd>(L_GU + (w8 & 3) * 2048 + l * 16) = g; *lp<frag_cd>(L_GU + (w8 & 3) * 2048 + 1024 + l * 16) = u; WAITL(); }
                BAR();
                if (w8 < 4) { g += *lp<frag_cd>(L_GU + w8 * 2048 + l * 16); u += *lp<frag_cd>(L_GU + w8 * 2048 + 1024 + l * 16);
                    float hv[4];
#pragma unroll
                    for (int e = 0; e < 4; ++e) { const float gg = rbf(g[e]); hv[e] = rbf(gg / (1.0f + __expf(-gg))) * rbf(u[e]); }
                    *lp<v2u>(L_HS + w8 * 512 + (2 * (n >> 3) + (q >> 1)) * 128 + (n & 7) * 16 + (q & 1) * 8) = v2u{cvt_pk(hv[0], hv[1]), cvt_pk(hv[2], hv[3])};
                    WAITL(); } }
            gv[0] = acc[0]; gv[1] = acc[1];
        } else {
#pragma unroll
            for (int cb = 0; cb < 4; ++cb) { frag_cd acc[2] = {};
#pragma unroll
                for (int i = 0; i < 4; ++i) { const v4u hf = *lp<v4u>(L_HS + i * 512 + q * 128 + (n & 7) * 16); acc[0] = MFMA(wf[8 * cb + 2 * i], hf, acc[0]); acc[1] = MFMA(wf[8 * cb + 2 * i + 1], hf, acc[1]); }
                STB(__builtin_bit_cast(v4u, n < 8 ? acc[0] : acc[1]), ry, ((w8 & 3) * 28 + h) * 1024 + ((n >> 3) * 32 + q * 8 + (n & 7)) * 16, ((w8 >> 2) * 4 + cb) * (4 * 28 * 1024)); }
        }
        MARK(67 + ti);
      }
      WAITV(0); BAR();
    }
    if (w8 == 0) {
        gst1(a.ctl + C_Y + h * 32, 1u);
        flags_wait<16>(a.ctl + C_Y, FF / 128, 1, l, err);
        frag_cd y = {};
#pragma unroll 1
        for (int i = 0; i < 28; i += 7) { v4u t[7];
#pragma unroll
            for (int u = 0; u < 7; ++u) t[u] = __builtin_amdgcn_raw_buffer_load_b128(ry, l * 16, (h * 28 + i + u) * 1024, 16);
#pragma unroll
            for (int u = 0; u < 7; ++u) y += __builtin_bit_cast(frag_cd, t[u]); }
        const float r = hi16((mykey & 0x80000000u) ? mykey & 0x7fffffffu : ~mykey), rs = rbf(1.0f / (1.0f + __expf(-r)));
#pragma unroll
        for (int e = 0; e < 4; ++e) y[e] = rbf(rbf(y[e]) * rs);
        STB(__builtin_bit_cast(v4u, y), ry, l * 16, (32 * 28 + h) * 1024);
        WAITV(0);
        gst1(a.ctl + C_Y + h * 32 + 1, 1u);
        MARK(120);
    }
}

__device__ __forceinline__ void owork(const Args& a, const int nt, const int kq, const unsigned L) {
    const int tid = threadIdx.x, w8 = RFL(tid >> 6), l = tid & 63, j = nt * 4 + kq;
    unsigned* const err = a.ctl + C_ERR;
    const unsigned L_EX = L + 131072, L_DEC = L_EX + 16384, L_Y = L_DEC + 64, L_KEY = L_Y + 1024;
    if (tid < 4) lp<unsigned>(L_DEC)[tid] = 0;
    if (w8 == 0) {
        unsigned char* p = a.ring + ((l >> 1) & 1) * CHB + (l & 1) * 8192 + j * 16384 + ((l >> 2) & 3) * 2048 + 1024;
        unsigned best = 0;
        if (l < 16) gst((unsigned*)p, ~0u);
        WAITV(0);
#pragma unroll 1
        for (int i = 0; i < 16; ++i) { u64 t1, t2; unsigned v;
            const u64 pi = ((u64)__builtin_amdgcn_readlane((unsigned)((u64)p >> 32), i) << 32) | (unsigned)__builtin_amdgcn_readlane((unsigned)(u64)p, i);
            asm volatile("s_nop 4\n\ts_memtime %0\n\ts_waitcnt lgkmcnt(0)\n\tglobal_load_dword %2, %3, %4 sc1\n\ts_waitcnt vmcnt(0)\n\ts_memtime %1\n\ts_waitcnt lgkmcnt(0)"
                         : "=&s"(t1), "=&s"(t2), "=&v"(v) : "v"(0u), "s"(pi) : "memory");
            if ((l & 3) == (i & 3)) best = max(best, ~(unsigned)(t2 - t1)); }
        if (l < 16) gst((unsigned*)p, ~0u);
        if (l < 4) __hip_atomic_fetch_max(a.ctl + C_CAL + l, best, __ATOMIC_RELAXED, __HIP_MEMORY_SCOPE_AGENT);
        WAITV(0);
        if (l == 0) __hip_atomic_fetch_add(a.ctl + C_READY, 1u, __ATOMIC_RELAXED, __HIP_MEMORY_SCOPE_AGENT);
        MARK(66);
    }
    BAR();
    {
#pragma unroll 1
        for (int i = 0; i < 16; ++i) glds(a.x + (size_t)(64 * j + 8 * w8) * DM + i * 512, l * 16, L + (w8 * 16 + i) * 1024);
        v4u gw[2], wr[2];
#pragma unroll
        for (int i = 0; i < 2; ++i) { gw[i] = *(const v4u*)(a.gw + 512 * i + 8 * l); wr[i] = *(const v4u*)(a.wr + 512 * i + 8 * l); }
        WAITV(0);
        float res = 0.f;
#pragma unroll 1
        for (int t = 0; t < 8; ++t) { float sg = 0.f, sr = 0.f;
#pragma unroll
            for (int i = 0; i < 2; ++i) { const v4u xv = *lp<v4u>(L + (w8 * 16 + 2 * t + i) * 1024 + l * 16);
#pragma unroll
                for (int e = 0; e < 4; ++e) { const float x0 = lo16(xv[e]), x1 = hi16(xv[e]); const unsigned p = cvt_pk(x0 * lo16(gw[i][e]), x1 * hi16(gw[i][e]));
                    sg += lo16(p) + hi16(p); sr = __builtin_fmaf(x0, lo16(wr[i][e]), __builtin_fmaf(x1, hi16(wr[i][e]), sr)); } }
            float v = tadd32(sg, sr);
            v = tadd16(v, v); v += dppf<ROR8>(v); v += dppf<0x124>(v); v += dppf<0x122>(v); v += dppf<0x121>(v);
            if ((l & 7) == t) res = v; }
        const unsigned rb = cvt_pk(res, 0.f), tok = 64 * j + 8 * w8 + (l & 7);
        const unsigned val = l < 32 ? __float_as_uint(rbf(1.0f / (1.0f + __expf(-lo16(rb))))) : (((rb & 0x8000u) ? ~rb : rb | 0x8000u) << 16) | (4095u - tok);
        if ((l & 31) < 8) gst(a.gate + (l >> 5) * S_TOK + tok, val);
        WAITV(0); BAR();
        if (w8 == 0) gst1(a.ctl + C_R + j * 32, 1u);
        MARK(67);
    }
    v4u wf[32];
    { const Tile T{a.Wo + (size_t)kq * 1024 * DM + nt * 128, DM, 0, 1};
      tile_issue(T, 0, L, w8, l); tile_issue(T, 1, L, w8, l);
      tile_load(T, wf, L, w8, l, [](int) { return false; }); }
    MARK(0);
    if (w8 == 1) { const unsigned mine = topk(a, l); if (l < 8) lp<unsigned>(L_KEY)[l] = mine; WAITL(); }
    BAR();
    MARK(68);
    unsigned* const fP = a.ctl + C_P + nt * 512;
    const unsigned rb = L + (w8 >> 2) * 32768 + l * 16;
    unsigned pacc[8] = {}, plast = 0; (void)pacc; (void)plast;
    PT(7);
    auto oissue = [&](int s, int wv, int i) { glds1<2>(a.ox + (size_t)s * 262144 + kq * 65536 + wv * 8192 + i * 2048, l * 16, L + (s & 1) * 65536 + wv * 8192 + i * 2048); };
    if (w8 >= 4) {
        const rsrc_t rf = mkrs(fP + (kq * 4 + (w8 & 3)) * 32, 4), rp = mkrs(a.part, NSEG * 524288);
        const int stp = ((nt * 4 + (w8 & 3)) * 4 + kq) * 4096 + l * 16;
        const unsigned* fW = l < 32 ? a.ctl + C_M + (kq * 32 + l) * 32 : fP + ((l & 3) * 4 + kq) * 32;
        unsigned fv = 0; int pc = 0;
        auto a_ok = [&](int s) { return s < NSEG && (unsigned)__builtin_amdgcn_ballot_w64(fv <= (unsigned)s) == 0; };
        auto p_upd = [&]() { if ((__builtin_amdgcn_ballot_w64(fv <= (unsigned)pc) >> 32) == 0) ++pc; if ((__builtin_amdgcn_ballot_w64(fv <= (unsigned)pc) >> 32) == 0) ++pc; lp<unsigned>(L_DEC)[2] = pc; };
        int have = 0, nxt = 0, late = 0;
        for (int s = 0; s <= NSEG; ++s) {
            if (!have) {
                if (!late) {
                    gld_wait<0>(fv);
                    __builtin_amdgcn_raw_buffer_store_b32(s, rf, l * 4, 0, 16);
                    if (w8 == 4) {
                        for (int i = 0;; ++i) { if (fv <= (unsigned)(l < 32 ? s + 1 : s - 1)) fv = gld(fW); WAITV(0); p_upd(); if (a_ok(s) || pc == NSEG) break; __builtin_amdgcn_s_sleep(2); if (i > GCAP) fatal(err); }
                        lp<unsigned>(L_DEC)[3] = s + 1; lp<unsigned>(L_DEC)[~s & 1] = a_ok(s + 1); FENCE(); }
                    if (s == NSEG) break;
                    for (int i = 0; ldsr(L_DEC + 12) <= (unsigned)s; ++i) { __builtin_amdgcn_s_sleep(1); if (i > LCAP) endpgm(); }
                    FENCE();
                    PT(0);
#pragma unroll
                    for (int i = 0; i < 8; ++i) oissue(s, w8 ^ (~i & 4), i & 3);
                    WAITV(8);
                }
                WAITV(12); BAR();
                nxt = RFL(ldsr(L_DEC + 4 * (~s & 1))) & 1;
                if (late) WAITV(4); else WAITV(0);
                if (nxt) {
#pragma unroll
                    for (int i = 0; i < 4; ++i) oissue(s + 1, w8 ^ 4, i); }
                PT(1);
            }
            if (w8 == 4 && !nxt) {
                if (!have) { if (fv <= (unsigned)(s + 1)) gld_req(fv, fW); WAITV(0); }
                gld_wait<4>(fv);
                p_upd(); lp<unsigned>(L_DEC)[~s & 1] = 4 * a_ok(s + 1); WAITL();
                if (fv <= (unsigned)(s + 2)) gld_req(fv, fW); }
            BAR();
            PT(2);
            PRIO(2);
            frag_cd acc[4]; Half hx; unsigned mid;
            seg_gemm<5, false>(wf, rb + (s & 1) * 65536, acc, [&](int i) {
                if (i == BARI - 5) { if (!(have & nxt)) WAITV(0);
                    gld_wait<4>(fv);
                    if (w8 == 4) { p_upd(); const bool k = a_ok(s + 1); if (k) lp<unsigned>(L_DEC)[3] = s + 2; lp<unsigned>(L_DEC)[s & 1] = a_ok(s + 2) + 2 * k; }
                    __builtin_amdgcn_raw_buffer_store_b32(s - (have & nxt), rf, l * 4, 0, 16); FENCE(); }
                if (i == BARI) { PRIO(3); PT(5); BAR(); PT(6); }
                if (i == 3) asm volatile("ds_read_b32 %0, %1" : "=v"(mid) : "v"(L_DEC + 4 * (~s & 1)) : "memory");
                if (i == 26) half_req(L_EX + (w8 & 3) * 4096 + l * 16, L_DEC + 4 * (s & 1), hx);
                if (i == 28) { asm volatile("s_waitcnt lgkmcnt(4)" : "+v"(hx.dec), "+v"(mid)); const int d = RFL(hx.dec); have = nxt | RFL(mid) >> 2; nxt = have & d; late = ~have & d >> 1 & 1; }
                if (i >= 28 && (nxt | late)) oissue(s + 1 + nxt, w8 ^ 4, i - 28); });
            PRIO(0);
            PT(3);
            half_get(hx, acc, [&](int i) { if (late) oissue(s + 1, w8, i); });
            if (w8 == 4 && fv <= (unsigned)(s + 3)) gld_req(fv, fW);
#pragma unroll
            for (int i = 0; i < 4; ++i) STB(__builtin_bit_cast(v4u, acc[i]), rp, stp + i * 1024, s * 524288);
            FENCE();
            PT(4);
        }
        if (w8 == 5) {
            flags_wait<4>(a.ctl + C_Y + j * 32 + 1, 1, 1, l, err);
            const unsigned tok = 4095u - (lp<unsigned>(L_KEY)[l & 7] & 4095u); const int xo = tok * DM + j * 32 + (l >> 3) * 4;
            const frag_cd y = __builtin_bit_cast(frag_cd, __builtin_amdgcn_raw_buffer_load_b128(mkrs(a.ypart + (32 * 28 + j) * 1024, 1024), l * 16, 0, 16));
            const v2u xv = *(const v2u*)(a.x + xo); const float gt = __uint_as_float(gld(a.gate + tok)), omg = rbf(1.0f - gt);
            const float b[4] = {rbf(lo16(xv.x) + y[0]), rbf(hi16(xv.x) + y[1]), rbf(lo16(xv.y) + y[2]), rbf(hi16(xv.y) + y[3])};
            asm volatile("" :: "v"(b[0]), "v"(b[1]), "v"(b[2]), "v"(b[3]), "v"(omg));
            BAR();
            const frag_cd t = *lp<frag_cd>(L_Y + l * 16);
            *(v2u*)(a.out + xo) = v2u{mix(gt, omg, t[0], t[1], b[0], b[1]), mix(gt, omg, t[2], t[3], b[2], b[3])};
        } else BAR();
    } else {
        const Finisher fz(a, j, w8, l, L_Y);
        int fin = 0, have = 0, pc = 0;
        for (int s = 0; s <= NSEG; ++s) {
            if (!have) {
                for (int i = 0; fin < s; ++i) {
                    if (s < NSEG && RFL(ldsr(L_DEC + 12)) > s) break;
                    pc = RFL(ldsr(L_DEC + 8));
                    if (fin < pc) { FENCE(); fz.finish(fin, fz.issue(fin, true), [](int) {}); ++fin; }
                    else { __builtin_amdgcn_s_sleep(1); if (i > LCAP) endpgm(); }
                }
                if (s == NSEG) break;
            }
            FENCE();
            const bool fi = fin < pc;
            const Finisher::Fin f = fz.issue(fin, fi);
            WAITL(); WAITV(15);
            PT(0);
            BAR();
            PT(2);
            PRIO(2);
            frag_cd acc[4];
            v4u dec;
            seg_gemm<1, false>(wf, rb + (s & 1) * 65536, acc, [&](int i) {
                if (i == BARI) { PRIO(3); PT(1); WAITV(6); BAR(); PT(6); }
                if (i == 26) asm volatile("ds_read_b128 %0, %1" : "=v"(dec) : "v"(L_DEC) : "memory"); });
            PRIO(1);
            PT(3);
            asm volatile("s_waitcnt lgkmcnt(0)" : "+v"(dec));
            have = RFL(s & 1 ? dec.x : dec.y) & 5; pc = RFL(dec.z);
            half_put(L_EX, w8, l, acc);
            WAITV(0);
            if (have & 4) {
#pragma unroll
                for (int i = 0; i < 4; ++i) oissue(s + 1, w8, i); }
            if (have) { oissue(s + 1, w8 ^ 4, 0); oissue(s + 1, w8 ^ 4, 1); }
            PT(4);
            if (!have) { WAITL(); BAR(); }
            fz.finish(fin, f, [&](int i) { if (have) oissue(s + 1, w8 ^ 4, 2 + i); });
            fin += fi;
            MARK(1 + s);
            PT(5);
        }
        WAITL(); BAR();
    }
    PFLUSH();
    MARK(65);
}

__global__ void __launch_bounds__(512, 2) k_mega(Args a) {
    __shared__ __attribute__((aligned(1024))) unsigned char smem[163840];
    const unsigned L = (unsigned)(size_t)(LAS unsigned char*)smem;
    const int b = blockIdx.x, u = (b & 3) + 4 * (b >> 3);
    MARK(255); MARKC(254);
    if (!(b & 4)) chain(a, u, L);
    else if (u < 96) gwork(a, (u >> 2) % 3, (u & 3) * 8 + (u >> 2) / 3, L);
    else owork(a, (u >> 2) - 24, u & 3, L);
}

static constexpr int g_coff = 0, g_force = 0; static constexpr int g_d1 = 765, g_d2 = 1555, g_per = 2045, g_dinit = 250; static constexpr unsigned* g_dbg = nullptr;
extern "C" void kernel_launch(void* const* d_in, const int* in_sizes, int n_in, void* d_out, int out_size, void* d_ws, size_t ws_size, hipStream_t stream) {
    auto in = [&](int i) { return (const bf16_t*)d_in[i]; };
    unsigned char* ws = (unsigned char*)d_ws; size_t off = g_coff;
    auto alloc = [&](size_t bytes) { unsigned char* p = ws + off; off += (bytes + 255) & ~(size_t)255; return p; };
    unsigned* ctl = (unsigned*)alloc(C_WORDS * 4);
    unsigned* gate = (unsigned*)alloc(2 * S_TOK * 4); unsigned char* ypart = alloc(32 * 29 * 1024);
    off += (size_t)(-(uintptr_t)(ws + off)) & ((2u << 20) - 1);
    unsigned char* ring = alloc(RING_BYTES);
    unsigned char* qkv = alloc((size_t)NSEG * NH * 24576);
    unsigned char* ox = alloc((size_t)NSEG * 262144); unsigned char* part = alloc((size_t)NSEG * 524288);
    (void)hipMemsetAsync(ctl, 0, C_WORDS * 4, stream);
    Args a{in(0), in(1), in(2), in(3), in(4), in(5), (bf16_t*)d_out, ctl, ring, qkv, ox, part, in(8), in(9), in(10), in(11), in(12), gate, ypart, g_dbg,
           g_d1 * 16 / 10, g_d2 * 16 / 10, g_per * 16 / 10, g_dinit * 16 / 10 + (g_force << 16)};
    k_mega<<<256, 512, 0, stream>>>(a);
}
```

```cpp
#include <hip/hip_runtime.h>
#include <stdint.h>
#include <type_traits>

typedef unsigned short bf16_t;
typedef unsigned long long u64;
typedef float frag_cd __attribute__((ext_vector_type(4)));
typedef unsigned v4u __attribute__((ext_vector_type(4)));
typedef unsigned v8u __attribute__((ext_vector_type(8)));
typedef unsigned v2u __attribute__((ext_vector_type(2)));
typedef float v2f __attribute__((ext_vector_type(2)));
typedef __bf16 bf16x2_t __attribute__((ext_vector_type(2)));
typedef __bf16 v8bf __attribute__((ext_vector_type(8)));
typedef __bf16 v16bf __attribute__((ext_vector_type(16)));
typedef short v4s_t __attribute__((ext_vector_type(4)));
typedef __amdgpu_buffer_rsrc_t rsrc_t;
#define LAS __attribute__((address_space(3)))
#define GAS __attribute__((address_space(1)))

static constexpr int S_TOK = 2048, DM = 1024, NH = 32, DH = 128, MM = 8, SEG = 32, NSEG = 64, HD = NH * DH, FF = 3584, TOPC = 8;
static constexpr int C_ERR = 0, C_READY = 32, C_CAL = 64  , C_G = 128  , C_M = C_G + 4096  ,
                     C_P = C_M + 4096  , C_R = C_P + 4096, C_Y = C_R + 1024  , C_HB = C_Y + 1024, C_WORDS = C_HB + 32;
static constexpr int RING_BYTES = 4 << 20, SLOT = 1 << 20, CHB = 2 << 20, L_H3 = 192 << 10, L_TS = 256 << 10, SLOT3 = 192 << 10;
static constexpr unsigned SPIN_CAP = 1u << 18;
static constexpr int OOB = 0x7ffffff0, LCAP = 1 << 22, GCAP = 1 << 20;
static constexpr float SCALE = 0.08838834764831845f, LOG2E = 1.4426950408889634f;

struct Args {
    const bf16_t* x; const bf16_t* mem0; const bf16_t* Wq; const bf16_t* Wk; const bf16_t* Wv; const bf16_t* Wo;
    bf16_t* out; unsigned* ctl; unsigned char* ring; unsigned char* qkv; unsigned char* ox; unsigned char* part;
    const bf16_t* wr; const bf16_t* wg; const bf16_t* wu; const bf16_t* wd; const bf16_t* gw;
    unsigned* gate  ; unsigned char* ypart; unsigned* dbg;
    int d1, d2, per, dinit;
};

__device__ __forceinline__ unsigned cvt_pk(float lo, float hi) { v2f v = {lo, hi}; return __builtin_bit_cast(unsigned, __builtin_convertvector(v, bf16x2_t)); }
__device__ __forceinline__ float lo16(unsigned p) { return __uint_as_float(p << 16); }
__device__ __forceinline__ float hi16(unsigned p) { return __uint_as_float(p & 0xffff0000u); }
__device__ __forceinline__ float rbf(float f) { return lo16(cvt_pk(f, 0.f)); }
template <int C> __device__ __forceinline__ unsigned dppu(unsigned v) { return (unsigned)__builtin_amdgcn_update_dpp(0, (int)v, C, 0xf, 0xf, true); }
template <int C> __device__ __forceinline__ float dppf(float v) { return __uint_as_float(dppu<C>(__float_as_uint(v))); }
#define ROR8 0x128
#define HMIR 0x141
__device__ __forceinline__ v2u sw16(unsigned a, unsigned b) { auto r = __builtin_amdgcn_permlane16_swap(a, b, false, false); return v2u{r[0], r[1]}; }
__device__ __forceinline__ v2u sw32(unsigned a, unsigned b) { auto r = __builtin_amdgcn_permlane32_swap(a, b, false, false); return v2u{r[0], r[1]}; }
__device__ __forceinline__ float tadd16(float a, float b) { v2u r = sw16(__float_as_uint(a), __float_as_uint(b)); return __uint_as_float(r.x) + __uint_as_float(r.y); }
__device__ __forceinline__ float tadd32(float a, float b) { v2u r = sw32(__float_as_uint(a), __float_as_uint(b)); return __uint_as_float(r.x) + __uint_as_float(r.y); }
__device__ __forceinline__ float tmax16(float a) { v2u r = sw16(__float_as_uint(a), __float_as_uint(a)); return fmaxf(__uint_as_float(r.x), __uint_as_float(r.y)); }
__device__ __forceinline__ float tmax32(float a) { v2u r = sw32(__float_as_uint(a), __float_as_uint(a)); return fmaxf(__uint_as_float(r.x), __uint_as_float(r.y)); }
__device__ __forceinline__ unsigned umax3(unsigned a, unsigned b, unsigned c) { return max(max(a, b), c); }
__device__ __forceinline__ v4u pack_swap(frag_cd a, frag_cd b) { v2u x0 = sw16(cvt_pk(a[0], a[1]), cvt_pk(b[0], b[1])), x1 = sw16(cvt_pk(a[2], a[3]), cvt_pk(b[2], b[3])); return v4u{x0.x, x1.x, x0.y, x1.y}; }
#define MFMA(a, b, c) __builtin_amdgcn_mfma_f32_16x16x32_bf16(__builtin_bit_cast(v8bf, a), __builtin_bit_cast(v8bf, b), c, 0, 0, 0)
#define SMF(a, b, c, ix) __builtin_amdgcn_smfmac_f32_16x16x64_bf16(__builtin_bit_cast(v8bf, a), __builtin_bit_cast(v16bf, b), c, ix, 0, 0)
#define FENCE() asm volatile("" ::: "memory")
#define SB() __builtin_amdgcn_sched_barrier(0)
#define RFL(x) __builtin_amdgcn_readfirstlane(x)
#define BAR() do { FENCE(); __builtin_amdgcn_s_barrier(); FENCE(); } while (0)
#define WAITV(n) do { FENCE(); __builtin_amdgcn_s_waitcnt(0x0F70 | (n)); FENCE(); } while (0)
#define WAITL() asm volatile("s_waitcnt lgkmcnt(0)" ::: "memory")
__device__ __forceinline__ void STB(v4u v, rsrc_t rs, int off, int soff) { __builtin_amdgcn_raw_buffer_store_b128(v, rs, off, soff, 16); asm volatile("s_nop 1" :: "v"(v) : "memory"); }
template <class T> __device__ __forceinline__ LAS T* lp(unsigned a) { return (LAS T*)(size_t)a; }
__device__ __forceinline__ void glds(const void* g, unsigned voff, unsigned lds) { asm volatile("s_mov_b32 m0, %2\n\ts_nop 0\n\tglobal_load_lds_dwordx4 %0, %1" :: "v"(voff), "s"(g), "s"(lds) : "memory"); }
#define NTC " nt"
#define NTW " nt"
__device__ __forceinline__ void gldsc(const void* g, unsigned voff, unsigned lds) { asm volatile("s_mov_b32 m0, %2\n\ts_nop 0\n\tglobal_load_lds_dwordx4 %0, %1" NTC :: "v"(voff), "s"(g), "s"(lds) : "memory"); }
__device__ __forceinline__ void gldsw(const void* g, unsigned voff, unsigned lds) { asm volatile("s_mov_b32 m0, %2\n\ts_nop 0\n\tglobal_load_lds_dwordx4 %0, %1" NTW :: "v"(voff), "s"(g), "s"(lds) : "memory"); }
template <int N> __device__ __forceinline__ void glds1(const void* g, unsigned voff, unsigned lds) {
#define GL(o) "\n\tglobal_load_lds_dwordx4 %0, %1 offset:" #o " sc1"
    if constexpr (N == 1) asm volatile("s_mov_b32 m0, %2\n\ts_nop 0" GL(0) :: "v"(voff), "s"(g), "s"(lds) : "memory");
    if constexpr (N == 2) asm volatile("s_mov_b32 m0, %2\n\ts_nop 0" GL(0) GL(1024) :: "v"(voff), "s"(g), "s"(lds) : "memory");
    if constexpr (N == 4) asm volatile("s_mov_b32 m0, %2\n\ts_nop 0" GL(0) GL(1024) GL(2048) GL(3072) :: "v"(voff), "s"(g), "s"(lds) : "memory");
}
__device__ __forceinline__ rsrc_t mkrs(const void* p, int bytes) { return __builtin_amdgcn_make_buffer_rsrc((void*)p, 0, bytes, 0x00020000); }
__device__ __forceinline__ unsigned gld(const unsigned* p) { return __hip_atomic_load(p, __ATOMIC_RELAXED, __HIP_MEMORY_SCOPE_AGENT); }
__device__ __forceinline__ void gst(unsigned* p, unsigned v) { __hip_atomic_store(p, v, __ATOMIC_RELAXED, __HIP_MEMORY_SCOPE_AGENT); }
__device__ __forceinline__ void gst1(unsigned* p, unsigned v) { asm volatile("s_mov_b64 exec, 1\n\tglobal_store_dword %0, %1, %2 sc1\n\ts_mov_b64 exec, -1" :: "v"(0u), "v"(v), "s"(p) : "memory"); }
__device__ __forceinline__ unsigned rt16() { return (unsigned)__builtin_amdgcn_s_memrealtime() << 4; }

#define NSLOT 12
#define PT(k) do {} while (0)
#define PFLUSH() do {} while (0)
#define MARK(i) do {} while (0)
#define MARKC(i) do {} while (0)

#define VDEL1 6
#define VDEL2 8
#define AEARLY 1
#define XC 4
#define ODEL 12
#define LT 400
#define NS 8
#define GS 6
#define KC 520
#define GM 200
#define SGAP 60
#define HSL 6
#define CSL 7
#define WK 20
#define WKO 100
#define WB 200
#define WC1 0
#define WC2 64
#define WTHR 340
#define SLIP_THR 400
#define CKA 0.75
#define CKF 22.5
#define TG_THR 480
#define SLIP_ADD 640
#define SLIP_INC 10
__device__ __forceinline__ void endpgm() { asm volatile("s_endpgm" ::: "memory"); }
__device__ __forceinline__ void fatal(unsigned* err) { asm volatile("global_store_dword %0, %1, off sc1\n\ts_waitcnt vmcnt(0)\n\ts_endpgm" :: "v"(err), "v"(1u) : "memory"); }
__device__ __forceinline__ unsigned ldsr(unsigned p) { return __hip_atomic_load(lp<unsigned>(p), __ATOMIC_RELAXED, __HIP_MEMORY_SCOPE_WORKGROUP); }
__device__ __forceinline__ v2u trd(unsigned p) { return __builtin_bit_cast(v2u, __builtin_amdgcn_ds_read_tr16_b64_v4i16(lp<v4s_t>(p))); }
__device__ __forceinline__ v8u cat(v2u a, v2u b, v2u c, v2u d) { return v8u{a.x, a.y, b.x, b.y, c.x, c.y, d.x, d.y}; }
__device__ __forceinline__ v4u cat2(v2u a, v2u b) { return v4u{a.x, a.y, b.x, b.y}; }
template <int SL, int FAR = 0> __device__ __forceinline__ void flags_wait(unsigned& v, const unsigned* p, unsigned need, unsigned* err) {
    for (int i = 0; __builtin_amdgcn_ballot_w64(v < need) != 0; ++i) {
        if (i) { if (FAR && __builtin_amdgcn_ballot_w64(v + 1 < need) != 0) __builtin_amdgcn_s_sleep(FAR); __builtin_amdgcn_s_sleep(SL); }
        if (v < need) v = gld(p);
        if (i > GCAP) fatal(err); }
    FENCE();
}
template <int SL> __device__ __forceinline__ void flags_wait(const unsigned* p, int nw, unsigned need, int l, unsigned* err) { unsigned v = need - 1; flags_wait<SL>(v, p + min(l, nw - 1) * 32, need, err); }

__device__ __forceinline__ void gadd1(unsigned* p) { asm volatile("s_mov_b64 exec, 1\n\tglobal_atomic_add %0, %1, %2 sc1\n\ts_mov_b64 exec, -1" :: "v"(0u), "v"(1u), "s"(p) : "memory"); }
__device__ __forceinline__ void ctl_wait(const Args& a, unsigned hb) {
    for (int i = 0; gld(a.ctl + C_HB) == hb; ++i) { __builtin_amdgcn_s_sleep(8); if (i > GCAP) fatal(a.ctl + C_ERR); }
    FENCE();
}

typedef unsigned v16u __attribute__((ext_vector_type(16)));
typedef unsigned v32u __attribute__((ext_vector_type(32)));
#define STR_(x) #x
#define STR(x) STR_(x)
#define SET(x) "\n.set " #x ", " STR(x)
#define A_STAMPS "0"
#define A_BENCH "0"
static_assert(SLOT == 0x100000 && SLOT3 == 0x30000 && NH * 24576 == 0xC0000, "");
#define A_CHAIN ".set STAMPS, " A_STAMPS "\n.set BENCH, " A_BENCH "\n.set WB, " STR(WB) "\n.set WC1, " STR(WC1) "\n.set WC2, " STR(WC2) SET(LT) SET(NS) SET(GS) SET(KC) SET(GM) SET(SGAP) SET(HSL) "\n.set WK, " STR(WK) "\n.set WN, 48\n.set WTHR, " STR(WTHR) "\n" R"(
.macro SLED n
  s_getpc_b64 vcc
4: s_sub_u32 s77, (3f-4b)/4, s77
  s_lshl2_add_u32 vcc_lo, s77, vcc_lo
  s_addc_u32 vcc_hi, vcc_hi, 0
  s_setpc_b64 vcc
.rept \n
  s_nop 1
.endr
3: s_waitcnt lgkmcnt(0)
.endm
.macro WAITC T, thr
  s_movk_i32 s76, 0x1000
1: s_memrealtime vcc
  s_sub_u32 s76, s76, 1
  s_cbranch_scc1 3f
  s_waitcnt lgkmcnt(0)
  s_lshl_b32 s77, vcc_lo, 4
  s_sub_i32 s77, \T, s77
  s_cmp_ge_i32 s77, \thr
  s_cbranch_scc1 1b
.endm
.macro WAITT T
  WAITC \T, 64
3: s_waitcnt lgkmcnt(0)
.endm
.macro WAITP T
  WAITC \T, WTHR
  s_sub_i32 s77, s77, WC1
  s_cmp_lt_i32 s77, WB
  s_cbranch_scc1 5f
.irp r,30,32,34,36,38,94,96,98
  s_memrealtime s[\r:\r+1]
  s_nop 1
.endr
  s_sub_i32 s77, \T, WC2
  s_waitcnt lgkmcnt(0)
  s_add_u32 s30, s30, s32
  s_add_u32 s34, s34, s36
  s_add_u32 s38, s38, s94
  s_add_u32 s96, s96, s98
  s_add_u32 s30, s30, s34
  s_add_u32 s38, s38, s96
  s_add_u32 s30, s30, s38
  s_lshl_b32 s30, s30, 1
  s_sub_i32 s77, s77, s30
5: s_max_i32 s77, s77, 0
  s_mulk_i32 s77, 45
  s_lshr_b32 s77, s77, 8
  s_min_u32 s77, s77, WN
  SLED WN
.endm
.macro WAITK T
  s_movk_i32 s76, 0x1000
1: s_memtime vcc
  s_sub_u32 s76, s76, 1
  s_cbranch_scc1 3f
  s_waitcnt lgkmcnt(0)
  s_sub_i32 s77, \T, vcc_lo
  s_cmp_ge_i32 s77, 8*WK
  s_cbranch_scc1 1b
  s_max_i32 s77, s77, 0
  s_lshr_b32 s77, s77, 3
  SLED WK
.endm
.macro TMAX a, t
  v_readlane_b32 \t, v[\a], 0
.irp r,1,2,3
  v_readlane_b32 s77, v[\a], \r
  s_sub_i32 s77, s77, \t
  s_max_i32 s77, s77, 0
  s_add_i32 \t, \t, s77
.endr
.endm
.macro TGET a
  s_waitcnt vmcnt(0)
  v_mbcnt_lo_u32_b32 v[\a+1], 7, 0
  v_lshlrev_b32 v[\a+1], 7, v[\a+1]
  s_add_i32 s75, s88, s68
8: buffer_load_dword v[\a], v[\a+1], s[40:43], s75 offen sc1
  s_sub_u32 s71, s71, 1
  s_cbranch_scc1 L_fatal
  s_waitcnt vmcnt(0)
  v_cmp_eq_u32 vcc, -1, v[\a]
  s_cbranch_vccz 9f
  s_sleep 2
  s_branch 8b
9:
  TMAX \a, s75
  WAITT s75
  s_cmp_eq_u32 s76, 0xfff
  s_cselect_b32 s59, 1, 0
  s_mov_b32 s76, 0x400000
.endm
.macro HOLD
  s_cmp_eq_u32 s59, 0
  s_cbranch_scc1 6f
  s_sleep HSL
  s_mov_b32 s59, 0
6:
.endm
.macro PLAN
  s_mulk_i32 s74, 21
  s_ashr_i32 s74, s74, 3
  s_add_i32 s73, s73, s58
  s_add_i32 s73, s73, s74
.endm
.macro STAMP k
.if STAMPS
  s_memtime s[8+2*\k:9+2*\k]
.endif
.endm
.macro NEXT t
.if STAMPS
  s_waitcnt lgkmcnt(0)
.irp k,0,1,2,3,4,5,6,7,8,9,10
  v_writelane_b32 v[\t], s[8+2*\k], \k
.endr
  s_lshl_b32 s76, s64, 3
  s_add_i32 s76, s76, s63
  s_mul_i32 s76, s76, 48
  v_mbcnt_lo_u32_b32 v[\t+1], -1, 0
  v_lshl_add_u32 v[\t+1], v[\t+1], 2, s76
  s_mov_b64 exec, 0x7ff
  ds_write_b32 v[\t+1], v[\t]
  s_mov_b64 exec, -1
.endif
  s_lshl_b32 s72, s64, 18
  s_mov_b32 s64, s65
  s_add_i32 s65, s65, 1
  s_xor_b32 s66, s66, 0x100000
  s_xor_b32 s67, s67, 0x100000
  s_mov_b32 s76, s68
  s_mov_b32 s68, s69
  s_mov_b32 s69, s70
  s_mov_b32 s70, s76
.endm
.macro RETRY c
.if BENCH
  s_bitset1_b64 s[80+2*\c:81+2*\c], s64
.endif
  s_sub_u32 s71, s71, 1
  s_cbranch_scc1 L_fatal
  s_sleep 2
.endm
.macro LD d, a, so, o
  buffer_load_dwordx4 v[\d:\d+3], v[\a], s[40:43], \so offen offset:\o sc1
.endm
.macro SM d, a, b
  v_smfmac_f32_16x16x64_bf16 v[\d:\d+3], v[\a:\a+3], v[\b:\b+7], v[SIX]
.endm
.macro MX0 a
  v_max_u32 v[MX], v[\a], v[\a+1]
  v_max3_u32 v[MX], v[MX], v[\a+2], v[\a+3]
.endm
.macro MX1 a
  v_max3_u32 v[MX], v[MX], v[\a], v[\a+1]
  v_max3_u32 v[MX], v[MX], v[\a+2], v[\a+3]
.endm
.macro HS a
  v_pk_add_f32 v[\a:\a+1], v[\a:\a+1], v[\a+2:\a+3]
.endm
.macro ZR a, n
.set zi, 0
.rept \n
  v_mov_b64 v[\a+zi:\a+zi+1], 0
.set zi, zi+2
.endr
.endm
.macro PRE
  s_mov_b32 s64, 63
  s_mov_b32 s65, 0
  s_mov_b32 s66, 0x100000
  s_mov_b32 s67, 0
  s_mov_b32 s68, 0x30000
  s_mov_b32 s69, 0x60000
  s_mov_b32 s70, 0
.endm
.macro XSL r
  s_and_b32 s75, \r, 3
  s_mulk_i32 s75, 20480
.endm
.macro CAP
  s_sub_u32 s76, s76, 1
  s_cbranch_scc1 L_fatal
.endm
  s_mov_b32 s71, 0x40000
  s_mov_b32 s88, s64
  s_mov_b32 s59, 0
  s_mov_b32 s90, s65
  s_mov_b32 s91, s66
  s_mov_b64 s[80:81], 0
  s_mov_b64 s[82:83], 0
  s_mov_b64 s[84:85], 0
  s_mov_b64 s[86:87], 0
  s_cmp_gt_u32 s63, 3
  s_cbranch_scc1 LV_init
; ======== waves 0..3
.set O3,168
.set ST1,169
.set RW1,170
.set RW2,171
.set RW3,172
.set RR,173
.set FW,174
.set FR,175
.set SIX,176
.set STK,177
.set FL,183
.set MB,128
.set AC,144
.set TAG,160
.set MX,161
.set RES,162
.set FA,163
.set FB,164
.set RA,184
.set RB,190
.set KX,196
.set XF,177
.set XC,)" STR(XC) R"(
.macro PLQ f, r
  ds_read_b32 v[\f], v[FR]
  ds_read_b64 v[\r:\r+1], v[RR]
  ds_read_b64 v[\r+2:\r+3], v[RR] offset:2048
  ds_read_b64 v[\r+4:\r+5], v[RR] offset:4096
.endm
.macro FINQ r
  STAMP 7
  s_waitcnt lgkmcnt(6)
  v_pk_add_f32 v[AC+2:AC+3], v[AC+2:AC+3], v[\r:\r+1]
  s_waitcnt lgkmcnt(4)
  v_pk_add_f32 v[\r+2:\r+3], v[\r+2:\r+3], v[\r+4:\r+5]
  v_pk_add_f32 v[AC+2:AC+3], v[AC+2:AC+3], v[\r+2:\r+3]
  v_cvt_pk_bf16_f32 v[RES], v[AC+2], v[AC+3]
  buffer_store_dword v[RES], v[ST1], s[40:43], s66 offen sc1
.endm
.macro MEMQ
  LD MB, O3, s68, 0
  LD MB+4, O3, s68, 1024
  LD MB+8, O3, s68, 2048
  LD MB+12, O3, s68, 3072
.endm
.macro XCHK R
  s_add_i32 s75, s64, 2
  s_min_u32 s75, s75, 63
  v_cmp_ge_u32 vcc, s75, v[FL]
  s_cbranch_vccnz LX_w\R
LX_g\R:
  s_add_i32 s75, s64, 2
  XSL s75
  s_add_i32 m0, s75, s61
.endm
.macro XL4 a
.irp o,0,1024,2048,3072
  global_load_lds_dwordx4 v[\a], s[52:53] offset:\o sc1
.endr
  s_add_i32 m0, m0, 0x1000
.endm
.macro XREF R
  s_add_i32 s75, s64, 3
  s_min_u32 s75, s75, 63
  v_cmp_ge_u32 vcc, s75, v[FL]
  s_cbranch_vccz LX_n\R
  global_load_dword v[FL], v[XF], s[54:55] sc1
LX_n\R:
.endm
.macro XADV
  s_cmp_lt_u32 s64, 61
  s_cselect_b32 s75, 0xC0000, 0
  s_add_u32 s52, s52, s75
  s_addc_u32 s53, s53, 0
.endm
.macro XWAIT R
LX_w\R:
  s_mov_b32 s76, 0x100000
1: global_load_dword v[FL], v[XF], s[54:55] sc1
  s_waitcnt vmcnt(0)
  v_cmp_ge_u32 vcc, s75, v[FL]
  s_cbranch_vccz LX_g\R
  s_sleep 4
  CAP
  s_branch 1b
.endm
.macro OXST
  buffer_store_dwordx4 v[KX:KX+3], v[STK], s[44:47], s72 offen sc1
  buffer_store_dwordx4 v[KX+4:KX+7], v[STK], s[44:47], s72 offen offset:1024 sc1
.endm
.macro FLAGQ val
  v_mov_b32 v[MX], 0
  v_mov_b32 v[FA], \val
  s_mov_b64 exec, 1
  global_store_dword v[MX], v[FA], s[50:51] sc1
  s_mov_b64 exec, -1
.endm
.macro PHASEA R
LQ_loop\R:
  STAMP 0
  ZR AC, 8
  v_mov_b32 v[TAG], s65
.if \R == 0
  s_cmp_eq_u32 s60, 0
  s_cbranch_scc0 LQ_g0
  WAITP s73
LQ_g0:
.endif
LQ_bar\R:
  s_barrier
  STAMP 1
  MEMQ
.set XS, 0
.if \R == 2
.set XS, 2
.rept )" STR(ODEL) R"(
  s_nop 7
.endr
  OXST
.endif
  s_mov_b32 s76, 0x400000
LQ_a\R:
  s_waitcnt vmcnt(3+XS)
  SM AC+8, MB, 64
  MX0 MB
  SM AC+12, MB, 96
  SM AC, MB, 0
  SM AC+4, MB, 32
.irp i,1,2
  s_waitcnt vmcnt(3+XS-\i)
  SM AC+8, MB+4*\i, 64+8*\i
  MX1 MB+4*\i
  SM AC+12, MB+4*\i, 96+8*\i
  SM AC, MB+4*\i, 8*\i
  SM AC+4, MB+4*\i, 32+8*\i
.endr
  s_waitcnt vmcnt(XS)
  SM AC+8, MB+12, 88
  MX1 MB+12
  SM AC+12, MB+12, 120
  v_cmp_eq_u32 vcc, -1, v[MX]
  SM AC, MB+12, 24
  s_cbranch_vccnz LQ_ra\R
  SM AC+4, MB+12, 56
  STAMP 2
  HS AC+8
  HS AC+12
  ds_write2_b32 v[RW2], v[AC+8], v[AC+12] offset1:1
  ds_write2_b32 v[RW3], v[AC+9], v[AC+13] offset1:1
  HS AC
  s_nop 2
  HS AC+4
  ds_write2_b32 v[RW1], v[AC+1], v[AC+5] offset1:1
  ds_write_b32 v[FW], v[TAG]
  STAMP 6
  PLQ FA, RA
  v_mov_b32 v[AC+2], v[AC]
  v_mov_b32 v[AC+3], v[AC+4]
LQ_p\R:
  PLQ FB, RB
  s_waitcnt lgkmcnt(7)
  v_cmp_gt_u32 vcc, s65, v[FA]
  s_cbranch_vccnz LQ_pb\R
  FINQ RA
LQ_pe\R:
  STAMP 3
  HOLD
.if \R != 2
  s_cmp_eq_u32 s64, 63
  s_cbranch_scc1 L_last
.endif
  s_waitcnt lgkmcnt(0)
  s_barrier
  STAMP 4
.endm
.macro COLDA R
LQ_pb\R:
  PLQ FA, RA
  CAP
  s_waitcnt lgkmcnt(7)
  v_cmp_gt_u32 vcc, s65, v[FB]
  s_cbranch_vccnz LQ_p\R
  FINQ RB
  s_branch LQ_pe\R
LQ_ra\R:
  RETRY 2
  TGET RA
  MEMQ
  ZR AC, 8
  s_waitcnt vmcnt(0)
  s_branch LQ_a\R
.endm
  s_mov_b32 s64, 0
  s_mov_b32 s65, 1
  s_mov_b32 s66, 0
  s_mov_b32 s67, 0x100000
  s_mov_b32 s68, 0x60000
  s_mov_b32 s69, 0
  s_mov_b32 s70, 0x30000
  s_mov_b32 s72, 0xFC0000
  s_setprio 3
  s_cmp_eq_u32 s60, 2
  s_cbranch_scc1 LQ_init2
  s_cmp_eq_u32 s60, 3
  s_cbranch_scc1 LQ_init3
; ---- waves 0,1: scores against the x rows
  s_memrealtime vcc
  s_waitcnt lgkmcnt(0)
  s_lshl_b32 s73, vcc_lo, 4
  PHASEA 0
  LD MB, 178, s66, 0
  LD MB+4, 178, s66, 1024
LQ_b0:
  s_waitcnt vmcnt(1)
  v_mfma_f32_16x16x32_bf16 v[AC+4:AC+7], v[KX+4:KX+7], v[MB:MB+3], 0
  MX0 MB
  v_mfma_f32_16x16x32_bf16 v[AC:AC+3], v[KX:KX+3], v[MB:MB+3], 0
  s_waitcnt vmcnt(0)
  v_mfma_f32_16x16x32_bf16 v[AC+4:AC+7], v[KX+12:KX+15], v[MB+4:MB+7], v[AC+4:AC+7]
  MX1 MB+4
  v_mfma_f32_16x16x32_bf16 v[AC:AC+3], v[KX+8:KX+11], v[MB+4:MB+7], v[AC:AC+3]
  v_cmp_eq_u32 vcc, -1, v[MX]
  s_nop 5
  s_cbranch_vccnz LQ_rb0
.irp e,0,1,2,3
  v_add_f32_dpp v[AC+\e], v[AC+4+\e], v[AC+\e] quad_perm:[2,3,0,1] row_mask:0xf bank_mask:0xf
.endr
  ds_write_b128 v[180], v[AC:AC+3]
  ds_write_b32 v[FW], v[TAG] offset:2048
  STAMP 5
  XCHK 0
  s_nop 0
  XL4 181
.if XC == 4
  s_nop 0
  XL4 182
.endif
  XREF 0
  XADV
  XSL s65
  v_add_u32 v[FA], s75, v[179]
.irp i,0,1,2,3
  ds_read_b128 v[KX+4*\i:KX+4*\i+3], v[FA] offset:1024*\i
.endr
  v_mov_b32 v[FA], s62
  ds_read_b64 v[RA:RA+1], v[FA]
  s_waitcnt lgkmcnt(0)
  v_readfirstlane_b32 s73, v[RA]
  v_readfirstlane_b32 s74, v[RA+1]
  PLAN
  NEXT MB
  s_branch LQ_loop0
  COLDA 0
LQ_rb0:
  RETRY 0
  LD MB, 178, s66, 0
  LD MB+4, 178, s66, 1024
  s_branch LQ_b0
  XWAIT 0
; ---- wave 2: scores against the memory rows; attention of 16 x rows (64 columns)
.set AQ,165
.set AVF,166
.set AVM,167
.set QX,196
.set OA,212
.set MSK,228
.set LX,237
.set PB,238
.set S1,240
.set K7,244
.set P4,244
.macro MF d, a, b, c
  v_mfma_f32_16x16x32_bf16 v[\d:\d+3], v[\a:\a+3], v[\b:\b+3], \c
.endm
.macro XQ op, r
  v_mov_b32 v[FB], v[\r]
  s_nop 1
  v_permlane16_swap_b32 v[\r], v[FB]
  \op v[\r], v[\r], v[FB]
  v_mov_b32 v[FB], v[\r]
  s_nop 1
  v_permlane32_swap_b32 v[\r], v[FB]
  \op v[\r], v[\r], v[FB]
.endm
.macro QKQ
  LD MB, 178, s66, 0
  LD MB+4, 179, s66, 0
  LD MB+8, 178, s66, 1024
  LD MB+12, 179, s66, 1024
.endm
.macro SOFTX
  v_add_f32 v[AC+6], v[AC+4], v[S1+2]
  v_add_f32 v[AC+7], v[AC+5], v[S1+3]
  v_max3_f32 v[MX], v[LX], v[AC+6], v[AC+7]
  XQ v_max_f32, MX
  v_mul_f32 v[MX], s74, v[MX]
.irp i,6,7,8,9,10,11,12,13,14,15
  v_fma_f32 v[AC+\i], v[AC+\i], s73, v[MX]
.endr
.irp i,6,7,8,9,10,11,12,13,14,15
  v_exp_f32 v[AC+\i], v[AC+\i]
.endr
  v_add_f32 v[LX], v[AC+6], v[AC+7]
.irp i,8,9,10,11,12,13,14,15
  v_add_f32 v[LX], v[LX], v[AC+\i]
.endr
  XQ v_add_f32, LX
  v_rcp_f32 v[LX], v[LX]
  s_nop 0
.irp i,6,7,8,9,10,11,12,13,14,15
  v_mul_f32 v[AC+\i], v[AC+\i], v[LX]
.endr
.irp i,0,1,2,3
  v_cvt_pk_bf16_f32 v[P4+\i], v[AC+8+2*\i], v[AC+9+2*\i]
.endr
  v_cvt_pk_bf16_f32 v[PB], v[AC+6], v[AC+7]
.endm
LQ_init2:
  s_mov_b32 s73, 0x3e0293ee
  s_mov_b32 s74, 0xbe0293ee
.irp i,0,2,4,6
  v_mov_b64 v[MSK+\i:MSK+\i+1], v[QX+\i:QX+\i+1]
.endr
  v_mov_b32 v[AQ], v[QX+8]
  v_mov_b32 v[AVF], v[QX+9]
  v_mov_b32 v[AVM], v[QX+10]
  v_mov_b32 v[PB+1], 0
  PHASEA 2
  QKQ
  XSL s64
  v_add_u32 v[FB], s75, v[AQ]
  v_add_u32 v[RES], s75, v[AVF]
.irp i,0,1,2,3
  ds_read_b128 v[QX+4*\i:QX+4*\i+3], v[FB] offset:16384+1024*\i
  ds_read_b128 v[OA+4*\i:OA+4*\i+3], v[FB] offset:1024*\i
.endr
.irp i,0,1,2
  ds_read_b128 v[RA+4*\i:RA+4*\i+3], v[FB] offset:4096+1024*\i
.endr
  ds_read_b128 v[K7:K7+3], v[FB] offset:7168
  s_waitcnt lgkmcnt(10)
  MF AC+8, OA, QX, v[MSK:MSK+3]
.irp i,4,8,12
  s_waitcnt lgkmcnt(10-\i/2)
  MF AC+8, OA+\i, QX+\i, v[AC+8:AC+11]
.endr
  s_waitcnt lgkmcnt(0)
  MF AC+12, RA, QX, v[MSK+4:MSK+7]
  MF AC+12, RA+4, QX+4, v[AC+12:AC+15]
  MF AC+12, RA+8, QX+8, v[AC+12:AC+15]
  MF AC+12, K7, QX+12, v[AC+12:AC+15]
  v_max3_f32 v[LX], v[AC+8], v[AC+9], v[AC+10]
  s_nop 6
  v_max3_f32 v[LX], v[LX], v[AC+11], v[AC+12]
  v_max3_f32 v[LX], v[LX], v[AC+13], v[AC+14]
  v_max_f32 v[LX], v[LX], v[AC+15]
  STAMP 10
LQ_b2:
  s_waitcnt vmcnt(3)
  MX0 MB
  s_waitcnt vmcnt(2)
  v_mfma_f32_16x16x32_bf16 v[AC:AC+3], v[MB+4:MB+7], v[MB:MB+3], 0
  MX1 MB+4
  s_waitcnt vmcnt(1)
  MX1 MB+8
  s_waitcnt vmcnt(0)
  v_mfma_f32_16x16x32_bf16 v[AC:AC+3], v[MB+12:MB+15], v[MB+8:MB+11], v[AC:AC+3]
  MX1 MB+12
  v_cmp_eq_u32 vcc, -1, v[MX]
  s_nop 3
  s_cbranch_vccnz LQ_rb2
  v_add_f32_dpp v[AC], v[AC+2], v[AC] quad_perm:[2,3,0,1] row_mask:0xf bank_mask:0xf
  v_add_f32_dpp v[AC+1], v[AC+3], v[AC+1] quad_perm:[2,3,0,1] row_mask:0xf bank_mask:0xf
  ds_write_b64 v[180], v[AC:AC+1]
  ds_write_b32 v[FW], v[TAG] offset:2048
  STAMP 5
  s_setprio 0
  MF AC+4, MB+4, QX, 0
  MF S1, MB+4, QX+4, 0
  MF AC+4, MB+12, QX+8, v[AC+4:AC+7]
  MF S1, MB+12, QX+12, v[S1:S1+3]
  FLAGQ s64
.irp e,0,1,2,3
  ds_read_b64_tr_b16 v[MB+4*\e:MB+4*\e+1], v[RES] offset:512*\e
  ds_read_b64_tr_b16 v[MB+4*\e+2:MB+4*\e+3], v[RES] offset:512*\e+4096
.endr
.if )" STR(AEARLY) R"(
  SOFTX
.endif
  s_mov_b32 s76, 0x400000
LA_w:
  ds_read_b32 v[FA], v[FW] offset:2304
  CAP
  s_waitcnt lgkmcnt(0)
  v_cmp_gt_u32 vcc, s65, v[FA]
  s_cbranch_vccz LA_g
  s_sleep 1
  s_branch LA_w
LA_g:
  STAMP 8
  s_setprio 2
.irp e,0,1,2,3
  ds_read_b64_tr_b16 v[RA+2*\e:RA+2*\e+1], v[AVM] offset:256*\e
.endr
.if !)" STR(AEARLY) R"(
  SOFTX
  s_nop 0
.endif
.irp e,0,4,8,12
  MF OA+\e, MB+\e, P4, 0
.endr
  s_waitcnt lgkmcnt(0)
.irp e,0,1,2,3
  v_mfma_f32_16x16x16_bf16 v[OA+4*\e:OA+4*\e+3], v[RA+2*\e:RA+2*\e+1], v[PB:PB+1], v[OA+4*\e:OA+4*\e+3]
.endr
  s_nop 1
.irp i,0,1,2,3,4,5,6,7
  v_cvt_pk_bf16_f32 v[KX+\i], v[OA+2*\i], v[OA+2*\i+1]
.endr
.irp i,0,1,4,5
  v_permlane16_swap_b32 v[KX+\i], v[KX+\i+2]
.endr
  STAMP 9
  s_setprio 3
  NEXT MB
  s_cmp_lg_u32 s64, 64
  s_cbranch_scc1 LQ_loop2
  OXST
  s_waitcnt vmcnt(0)
  FLAGQ 64
  s_branch L_end
  COLDA 2
LQ_rb2:
  RETRY 0
  QKQ
  s_branch LQ_b2
; ---- wave 3: pacing, clearing, sum over the heads
.set CK,212
.macro PARTQ
  LD MB, 178, s66, 0
  LD MB+4, 178, s66, 1024
  LD MB+8, 178, s66, 2048
  LD MB+12, 178, s66, 3072
.endm
LQ_init3:
  s_memrealtime vcc
  v_mov_b64 v[KX+4:KX+5], -1
  v_mov_b64 v[KX+6:KX+7], -1
  v_mov_b32 v[KX+8], s62
  v_mov_b64 v[CK:CK+1], 0
  v_cvt_f32_u32 v[CK+4], s56
  s_mov_b32 s79, NS*GS
  s_sub_u32 s62, s57, s56
  s_sub_u32 s62, s62, KC
  s_waitcnt lgkmcnt(0)
  s_lshl_b32 s73, vcc_lo, 4
  PHASEA 3
  ds_read_b64 v[RA:RA+1], v[KX+8]
  s_waitcnt lgkmcnt(0)
  s_mov_b32 s78, s73
  v_readfirstlane_b32 s73, v[RA]
  v_readfirstlane_b32 s74, v[RA+1]
  s_sub_i32 s78, s73, s78
  s_cmp_gt_i32 s78, 16
  s_cbranch_scc1 LQ_slip
LQ_b5:
  s_sleep )" STR(CSL) R"(
  buffer_store_dwordx4 v[KX+4:KX+7], v[178], s[40:43], s67 offen sc1
  buffer_store_dwordx4 v[KX+4:KX+7], v[178], s[40:43], s67 offen offset:1024 sc1
  buffer_store_dwordx4 v[KX+4:KX+7], v[178], s[40:43], s67 offen offset:2048 sc1
  buffer_store_dwordx4 v[KX+4:KX+7], v[178], s[40:43], s67 offen offset:3072 sc1
  buffer_store_dwordx4 v[KX+4:KX+7], v[179], s[40:43], s67 offen sc1
  buffer_store_dwordx2 v[KX+4:KX+5], v[182], s[40:43], s67 offen sc1
  buffer_store_dwordx2 v[KX+4:KX+5], v[180], s[40:43], s70 offen sc1
  buffer_store_dwordx2 v[KX+4:KX+5], v[181], s[40:43], s70 offen sc1
  XCHK 3
  XREF 3
  STAMP 5
  s_memtime s[92:93]
  s_memrealtime vcc
  s_lshl1_add_u32 s75, s74, s73
  s_add_i32 s75, s75, s57
  PLAN
  s_waitcnt lgkmcnt(0)
  s_sub_u32 s76, s92, s90
  s_sub_u32 s77, vcc_lo, s91
  s_mov_b32 s90, s92
  s_mov_b32 s91, vcc_lo
  s_lshl4_add_u32 s78, vcc_lo, s62
  s_sub_i32 s78, s78, s75
  s_cmp_gt_i32 s78, LT
  s_cselect_b32 s78, s78, -GM
  s_add_i32 s78, s78, s75
  s_add_i32 s78, s78, GM
  s_cmp_lt_u32 s64, NS
  s_cselect_b32 s92, GS, 0
  s_sub_u32 s79, s79, s92
  v_cvt_f32_u32 v[CK+2], s76
  v_cvt_f32_u32 v[CK+3], s77
  v_fmac_f32 v[CK+2], )" STR(CKA) R"(, v[CK]
  v_fmac_f32 v[CK+3], )" STR(CKA) R"(, v[CK+1]
  v_rcp_f32 v[CK+5], v[CK+2]
  v_mov_b64 v[CK:CK+1], v[CK+2:CK+3]
  v_mul_f32 v[CK+3], )" STR(CKF) R"(, v[CK+3]
  v_fma_f32 v[CK+3], v[CK+3], v[CK+5], -1.0
  v_med3_f32 v[CK+3], v[CK+3], -0.5, 1.0
  v_mul_f32 v[CK+3], v[CK+3], v[CK+4]
  v_cvt_i32_f32 v[CK+3], v[CK+3]
  v_add_u32 v[CK+3], s79, v[CK+3]
  WAITP s78
  STAMP 8
  PARTQ
  s_memrealtime vcc
.if XC == 12
  XL4 KX+9
  s_nop 0
  XL4 KX+10
  s_nop 0
.endif
  XL4 KX+11
  s_mov_b32 s78, s75
  XADV
  s_mov_b32 s75, s78
  ZR AC, 8
  v_add_u32 v[TAG], 1, v[TAG]
LQ_b3:
  s_waitcnt lgkmcnt(0)
.if BENCH
  s_mov_b32 m0, s64
  s_mov_b32 s77, vcc_lo
  v_writelane_b32 v[KX+15], s77, m0
.endif
  s_lshl_b32 s89, vcc_lo, 4
  s_sub_i32 s89, s89, s75
  s_add_i32 s78, s89, )" STR(SLIP_ADD) R"(
  s_cmp_gt_i32 s89, )" STR(SLIP_THR) R"(
  s_cselect_b32 s78, s78, 0
  s_cselect_b32 s77, SGAP, 0
  s_add_i32 s78, s78, s73
  s_and_b32 s78, s78, -2
  v_mov_b32 v[RA], s78
  v_add_u32 v[RA+1], s77, v[CK+3]
  s_waitcnt vmcnt(3+XC)
  v_mfma_f32_16x16x32_bf16 v[RB:RB+3], v[KX:KX+3], v[MB:MB+3], 0
  MX0 MB
.irp i,1,2,3
  s_waitcnt vmcnt(3+XC-\i)
  v_mfma_f32_16x16x32_bf16 v[RB:RB+3], v[KX:KX+3], v[MB+4*\i:MB+4*\i+3], v[RB:RB+3]
  MX1 MB+4*\i
.endr
  v_cmp_eq_u32 vcc, -1, v[MX]
  STAMP 9
  s_nop 3
  s_cbranch_vccnz LQ_rb3
.irp e,0,1,2,3
  v_add_f32_dpp v[RB+\e], v[RB+\e], v[RB+\e] row_ror:8 row_mask:0xf bank_mask:0xf
.endr
  v_cvt_pk_bf16_f32 v[RB], v[RB], v[RB+1]
  v_cvt_pk_bf16_f32 v[RB+1], v[RB+2], v[RB+3]
  buffer_store_dwordx2 v[RB:RB+1], v[180], s[40:43], s69 offen sc1
  buffer_store_dwordx2 v[RA:RA+1], v[181], s[40:43], s69 offen sc1
  STAMP 10
  NEXT MB
  STAMP 0
  s_cmp_gt_i32 s89, )" STR(TG_THR) R"(
  s_cbranch_scc0 LQ_bar3
  TGET RA
  s_mov_b32 s59, 0
  s_branch LQ_bar3
  COLDA 3
  XWAIT 3
LQ_rb3:
  RETRY 1
  PARTQ
  s_memrealtime vcc
  s_waitcnt vmcnt(0)
  s_branch LQ_b3
LQ_slip:
.if BENCH
  s_bitset1_b64 s[86:87], s64
.endif
  s_cmp_lt_u32 s64, NS
  s_cbranch_scc1 LQ_b5
  s_add_i32 s79, s79, )" STR(SLIP_INC) R"(
  s_min_u32 s79, s79, 4000
  s_branch LQ_b5
; ======== waves 4..7
.set UB,128
.set B1,160
.set O3,168
.set O1,169
.set OT,170
.set ST1,171
.set ST2,172
.set AIM,173
.set RW,174
.set RR,175
.set FW,176
.set FR,177
.set FS,178
.set ASC,179
.set ASM,180
.set AVX,181
.set SIX,182
.set RES,184
.set TAG,185
.set MX,186
.set TM,187
.set MB,188
.set AC,204
.set TV,212
.set FA,214
.set RA,216
.set DD,188
.set SX,208
.set SMM,212
.set ZZ,224
.set VM,240
.set PA,248
.set VX,188
.set UU,220
.macro MEMV R
  LD MB, O3, s68, 0
  LD MB+4, O3, s68, 1024
  LD MB+8, O3, s68, 2048
  LD MB+12, O3, s68, 3072
.if \R == 3
  buffer_load_dwordx2 v[TV:TV+1], v[OT], s[40:43], s68 offen sc1
.endif
.endm
.macro SCV
  ds_read_b32 v[FA], v[FS]
  ds_read_b128 v[SX:SX+3], v[ASC]
  ds_read_b64 v[SMM:SMM+1], v[ASM]
.endm
.macro CZV t
  v_cvt_pk_bf16_f32 v[UB+8*\t+2], v[ZZ+4*\t], v[ZZ+4*\t+2]
  v_cvt_pk_bf16_f32 v[UB+8*\t+3], v[ZZ+4*\t+1], v[ZZ+4*\t+3]
.endm
.macro STEPV R
.set XL, (\R == 3) & 1
LV_loop\R:
  STAMP 0
  ZR AC, 4
  ZR ZZ, 8
  v_mov_b32 v[TAG], s65
  s_setprio 2
  s_barrier
.if \R == 3
  s_memtime s[92:93]
  s_nop 5
.else
  s_nop 7
.endif
.rept )" STR(VDEL1) R"(-1
  s_nop 7
.endr
  STAMP 1
  MEMV \R
  s_mov_b32 s76, 0x400000
LV_a\R:
  s_waitcnt vmcnt(3+XL)
  SM AC, MB, 64
  MX0 MB
  SM AC+4, MB, 96
.irp i,1,2,3
  s_waitcnt vmcnt(3+XL-\i)
  SM AC, MB+4*\i, 64+8*\i
  MX1 MB+4*\i
  SM AC+4, MB+4*\i, 96+8*\i
.endr
  v_cmp_eq_u32 vcc, -1, v[MX]
  STAMP 2
  s_nop 4
  s_cbranch_vccnz LV_ra\R
  HS AC
  HS AC+4
  ds_write2_b32 v[RW], v[AC], v[AC+4] offset1:1
  ds_write2_b32 v[RW], v[AC+1], v[AC+5] offset0:128 offset1:129
  ds_write_b32 v[FW], v[TAG]
.if \R == 0
LV_p:
  ds_read_b32 v[FA], v[FR]
  ds_read_b64 v[RA:RA+1], v[RR]
  ds_read_b64 v[RA+2:RA+3], v[RR] offset:1024
  ds_read_b64 v[RA+4:RA+5], v[RR] offset:2048
  ds_read_b64 v[RA+6:RA+7], v[RR] offset:3072
  CAP
  s_waitcnt lgkmcnt(4)
  v_cmp_gt_u32 vcc, s65, v[FA]
  s_cbranch_vccnz LV_p
  s_waitcnt lgkmcnt(2)
  HS RA
  s_waitcnt lgkmcnt(0)
  HS RA+4
  v_pk_add_f32 v[RA:RA+1], v[RA:RA+1], v[RA+4:RA+5]
  v_cvt_pk_bf16_f32 v[RES], v[RA], v[RA+1]
  buffer_store_dword v[RES], v[ST1], s[40:43], s66 offen sc1
.endif
  STAMP 3
  HOLD
.if \R == 0
  s_cmp_eq_u32 s64, 63
  s_cbranch_scc1 L_last
.endif
  s_waitcnt lgkmcnt(0)
.if \R == 3
  s_add_i32 s75, s92, s90
  s_cmp_eq_u32 s64, 0
  s_cbranch_scc1 LV_t0
LV_t1:
  s_waitcnt vmcnt(0)
  v_cmp_eq_u32 vcc, -1, v[TV]
  v_subrev_u32 v[TV], s73, v[TV]
  v_max_i32_dpp v[TV+1], v[TV+1], v[TV+1] quad_perm:[1,0,3,2] row_mask:0xf bank_mask:0xf
  s_cbranch_vccnz LV_rt
  v_max_i32_dpp v[TV], v[TV], v[TV] quad_perm:[1,0,3,2] row_mask:0xf bank_mask:0xf
  v_max_i32_dpp v[TV+1], v[TV+1], v[TV+1] quad_perm:[2,3,0,1] row_mask:0xf bank_mask:0xf
  s_nop 0
  v_max_i32_dpp v[TV], v[TV], v[TV] quad_perm:[2,3,0,1] row_mask:0xf bank_mask:0xf
  v_cmp_lt_i32 vcc, 16, v[TV]
  v_add_u32 v[TV], s73, v[TV]
  ds_write_b64 v[183], v[TV:TV+1]
  ZR DD, 10
  v_readfirstlane_b32 s73, v[TV]
  v_readfirstlane_b32 s74, v[TV+1]
  s_cbranch_vccnz LV_sl
  s_setprio 3
  WAITK s75
LV_t2:
  s_barrier
  s_cmp_eq_u32 s64, 63
  s_cbranch_scc1 L_end
  PLAN
  s_cmp_lt_u32 s64, NS
  s_cselect_b32 s75, GS*45/32, 0
  s_sub_u32 s90, s90, s75
.else
  ZR DD, 10
  s_setprio 3
  s_barrier
  s_nop 7
.endif
.rept )" STR(VDEL2) R"(-1
  s_nop 7
.endr
  STAMP 4
  LD VM, O1, s66, 0
  LD VM+4, O1, s66, 1024
  s_mov_b32 s76, 0x400000
LV_b\R:
  s_waitcnt vmcnt(1)
  SM ZZ, VM, 0
  MX0 VM
  SM ZZ+4, VM, 16
  SM ZZ+8, VM, 32
  SM ZZ+12, VM, 48
  s_waitcnt vmcnt(0)
  SM ZZ, VM+4, 8
  MX1 VM+4
  SM ZZ+4, VM+4, 24
  v_cmp_eq_u32 vcc, -1, v[MX]
  ds_read_b32 v[FA], v[FS]
  SM ZZ+8, VM+4, 40
  ds_read_b128 v[SX:SX+3], v[ASC]
  ds_read_b64 v[SMM:SMM+1], v[ASM]
  s_cmp_eq_u32 s64, 63
  SM ZZ+12, VM+4, 56
  s_cbranch_vccnz LV_rb\R
.if \R == 2
  s_cbranch_scc1 LV_img
.endif
  STAMP 5
LV_s\R:
  s_waitcnt lgkmcnt(2)
  v_cmp_gt_u32 vcc, s65, v[FA]
  s_cbranch_vccnz LV_rs\R
  s_waitcnt lgkmcnt(0)
  STAMP 6
  v_max3_f32 v[MX], v[SX], v[SX+1], v[SX+2]
  v_max3_f32 v[TM], v[SX+3], v[SMM], v[SMM+1]
  v_max_f32 v[MX], v[MX], v[TM]
  CZV 0
  v_max_f32_dpp v[MX], v[MX], v[MX] quad_perm:[2,3,0,1] row_mask:0xf bank_mask:0xf
  v_mov_b32 v[TM], v[MX]
  CZV 1
  v_permlane16_swap_b32 v[MX], v[TM]
  v_max_f32 v[MX], v[MX], v[TM]
  v_mov_b32 v[TM], v[MX]
  CZV 2
  v_permlane32_swap_b32 v[MX], v[TM]
  v_max_f32 v[MX], v[MX], v[TM]
  v_mul_f32 v[MX], s79, v[MX]
  v_pk_fma_f32 v[SX:SX+1], v[SX:SX+1], s[78:79], v[MX:MX+1] op_sel_hi:[1,0,0]
  v_pk_fma_f32 v[SMM:SMM+1], v[SMM:SMM+1], s[78:79], v[MX:MX+1] op_sel_hi:[1,0,0]
  v_pk_fma_f32 v[SX+2:SX+3], v[SX+2:SX+3], s[78:79], v[MX:MX+1] op_sel_hi:[1,0,0]
  v_exp_f32 v[SX], v[SX]
  v_exp_f32 v[SX+1], v[SX+1]
  v_exp_f32 v[SMM], v[SMM]
  v_cvt_pk_bf16_f32 v[PA], v[SX], v[SX+1]
  v_exp_f32 v[SMM+1], v[SMM+1]
  v_cvt_pk_bf16_f32 v[PA+1], v[SMM], v[SMM]
  v_exp_f32 v[SX+2], v[SX+2]
  v_exp_f32 v[SX+3], v[SX+3]
  v_cvt_pk_bf16_f32 v[PA+3], v[SMM+1], v[SMM+1]
  v_cvt_pk_bf16_f32 v[PA+2], v[SX+2], v[SX+3]
  CZV 3
  SM DD+16, PA, B1
  SM DD, PA, UB
  SM DD+4, PA, UB+8
  SM DD+8, PA, UB+16
  HS DD+16
  HS DD
  SM DD+12, PA, UB+24
  v_rcp_f32 v[DD+16], v[DD+16]
  v_rcp_f32 v[DD+17], v[DD+17]
  HS DD+4
  HS DD+8
  v_pk_mul_f32 v[DD:DD+1], v[DD:DD+1], v[DD+16:DD+17]
  v_pk_mul_f32 v[DD+4:DD+5], v[DD+4:DD+5], v[DD+16:DD+17]
  v_pk_mul_f32 v[DD+8:DD+9], v[DD+8:DD+9], v[DD+16:DD+17]
  v_cvt_pk_bf16_f32 v[SX], v[DD], v[DD+4]
  HS DD+12
  v_cvt_pk_bf16_f32 v[SX+2], v[DD+1], v[DD+5]
  v_pk_mul_f32 v[DD+12:DD+13], v[DD+12:DD+13], v[DD+16:DD+17]
  v_cvt_pk_bf16_f32 v[SX+1], v[DD+8], v[DD+12]
  v_cvt_pk_bf16_f32 v[SX+3], v[DD+9], v[DD+13]
  buffer_store_dwordx4 v[SX:SX+3], v[ST2], s[40:43], s66 offen sc1
  STAMP 7
  s_setprio 1
.if \R == 2
LV_img:
  v_add_u32 v[TM], 1024, v[AIM]
  ds_write2_b32 v[AIM], v[VM], v[VM+1] offset1:2
  ds_write2_b32 v[AIM], v[VM+2], v[VM+3] offset0:32 offset1:34
  ds_write2_b32 v[TM], v[VM+4], v[VM+5] offset1:2
  ds_write2_b32 v[TM], v[VM+6], v[VM+7] offset0:32 offset1:34
  ds_write_b32 v[FW], v[TAG] offset:1280
  s_cmp_eq_u32 s64, 63
  s_cbranch_scc1 L_end
.endif
LV_u\R:
  XSL s65
  v_add_u32 v[TM], s75, v[AVX]
.irp k,0,1,2,3,4,5,6,7
  ds_read_b128 v[VX+4*\k:VX+4*\k+3], v[TM] offset:1024*\k
.endr
.irp k,0,1,2,3,4,5,6,7
  s_waitcnt lgkmcnt(7-\k)
.irp t,0,1,2,3
.if (\k & 3) == 0
  v_mfma_f32_16x16x32_bf16 v[UU+4*\k+4*\t:UU+4*\k+4*\t+3], v[VX+4*\k:VX+4*\k+3], v[16*\t:16*\t+3], 0
.else
  v_mfma_f32_16x16x32_bf16 v[UU+4*(\k&4)+4*\t:UU+4*(\k&4)+4*\t+3], v[VX+4*\k:VX+4*\k+3], v[16*\t+4*(\k&3):16*\t+4*(\k&3)+3], v[UU+4*(\k&4)+4*\t:UU+4*(\k&4)+4*\t+3]
.endif
.endr
.endr
.irp k,0,4
.irp t,0,1,2,3
  v_cvt_pk_bf16_f32 v[UB+8*\t+\k], v[UU+4*\k+4*\t], v[UU+4*\k+4*\t+1]
  v_cvt_pk_bf16_f32 v[UB+8*\t+\k+1], v[UU+4*\k+4*\t+2], v[UU+4*\k+4*\t+3]
.endr
.endr
  STAMP 8
  NEXT MB
  s_branch LV_loop\R
LV_ra\R:
  RETRY 2
  TGET RA
  s_sleep 3
  MEMV \R
  ZR AC, 4
  s_branch LV_a\R
LV_rb\R:
  RETRY 0
  LD VM, O1, s66, 0
  LD VM+4, O1, s66, 1024
  ZR ZZ, 8
  s_branch LV_b\R
LV_rs\R:
  SCV
  CAP
  s_branch LV_s\R
.if \R == 3
LV_t0:
  s_memrealtime vcc
  s_waitcnt vmcnt(0) lgkmcnt(0)
  s_lshl_b32 s73, vcc_lo, 4
  s_sub_i32 s73, s73, 0x2000
  v_mov_b32 v[TV], s73
  v_mov_b32 v[TV+1], 0
  s_branch LV_t1
LV_sl:
  s_cmp_lt_u32 s64, NS
  s_cselect_b32 s75, 0, )" STR(SLIP_INC) R"(*45/32
  s_add_i32 s90, s90, s75
  s_add_i32 s75, s73, s56
  s_add_i32 s75, s75, s74
  s_setprio 3
  WAITP s75
  s_branch LV_t2
LV_rt:
  RETRY 2
  buffer_load_dwordx2 v[TV:TV+1], v[OT], s[40:43], s68 offen sc1
  s_branch LV_t1
.endif
.endm
LV_init:
  PRE
  s_mul_i32 s90, s56, 45
  s_lshr_b32 s90, s90, 5
  s_add_i32 s90, s90, NS*(GS*45/32)-)" STR(WKO) R"(
  s_mov_b32 s78, 0x3e0293ee
  s_mov_b32 s79, 0xbe0293ee
  ZR UB, 16
  v_mov_b32 v[B1], 0x3f803f80
  v_mov_b32 v[B1+1], 0x3f803f80
  v_mov_b32 v[B1+2], 0x3f80
  v_mov_b32 v[B1+3], 0x3f80
  v_mov_b32 v[B1+4], 0x3f803f80
  v_mov_b32 v[B1+5], 0x3f803f80
  v_mov_b64 v[B1+6:B1+7], 0
  s_cmp_eq_u32 s60, 2
  s_cbranch_scc1 LV_u2
  s_cmp_eq_u32 s60, 3
  s_cbranch_scc1 LV_u3
  s_branch LV_u0
  STEPV 0
  STEPV 2
  STEPV 3
L_fatal:
  v_mov_b32 v[MX], 0
  v_mov_b32 v[TAG], 1
  global_store_dword v[MX], v[TAG], s[48:49] sc1
  s_waitcnt vmcnt(0)
  s_endpgm
L_last:
  s_barrier
L_end:
  s_waitcnt vmcnt(0) lgkmcnt(0)
)"
static_assert(__builtin_bit_cast(unsigned, SCALE * LOG2E) == 0x3e0293eeu, "");
#define CL10(p) p "0", p "1", p "2", p "3", p "4", p "5", p "6", p "7", p "8", p "9"
#define CL_ST
#define CL_S CL_ST "s72", "s73", "s74", "s75", "s76", "s77", "s78", "s79", "s88", "s89", "s90", "s91", "s92", "s93", "s94", "s95", "s96", "s97", "s98", "s99", CL10("s3"), "vcc", "scc", "memory"

__device__ __forceinline__ void chain(const Args& a, const int c, const unsigned L, const unsigned hb) {
    const int tid = threadIdx.x, w8 = RFL(tid >> 6), w = w8 & 3, l = tid & 63;
    const int g = c & 3, aa = c >> 2, h = g * 8 + (aa >> 2), jj = aa & 3;
    const int n = l & 15, q = l >> 4, hf = (n >> 1) & 1, n2 = (n >> 2) & 1, n3 = n >> 3;
    const unsigned L_RED = L + 131072, L_RV = L_RED + 6144, L_SC = L_RV + 4096, L_SM = L_SC + 2048, L_FL = L_SM + 512, L_TB = L_FL + 3072, L_VM = L_TB + 256, L_KX = L + 32768, L_VX = L_KX + 8192;
    const rsrc_t rs = mkrs(a.ring, RING_BYTES);
    unsigned* const err = a.ctl + C_ERR;
    const v4u ones = {~0u, ~0u, ~0u, ~0u};
    const unsigned cyc0 = RFL((unsigned)__builtin_amdgcn_s_memtime()), rt0 = RFL((unsigned)__builtin_amdgcn_s_memrealtime());
    if (tid < 384) lp<v2u>(L_FL)[tid] = v2u{0u, 0u};
    if (c == 0 && w8 == 3) {
#pragma unroll
        for (int i = 0; i < 8; ++i) STB(v4u{}, mkrs(a.ctl, C_HB * 4), l * 128, i * 8192); }
    if (c < 88 || (c >= 96 && c < 108)) {
#pragma unroll
        for (int i = 0; i < 4; ++i) STB(ones, rs, c * 32768 + i * 8192 + tid * 16, 0); }
    const int lg3 = L_H3 + ((q >> 1) * 2 + n3) * 16384 + c * 128 + n2 * 64 + hf * 32 + (q & 1) * 16 + (n & 1) * 8;
    if (w8 == 3) { const bf16_t* p = a.mem0 + ((c & 1) * 4 + n2 * 2 + (q & 1)) * DM + 16 * (c >> 1) + (n & 3);
        const v2u m = {min(p[0] | (unsigned)p[4] << 16, 0xFFFFFFFEu), min(p[8] | (unsigned)p[12] << 16, 0xFFFFFFFEu)};
#pragma unroll
        for (int i = 0; i < 2; ++i) __builtin_amdgcn_raw_buffer_store_b64(m, rs, CHB + ((lg3 >> 13) << 14) + (lg3 & 8191), 2 * SLOT3 + i * 8192, 16); }
    const unsigned stg = L + w8 * 16384;
    const int prr = 16 * (q >> 1) + (q & 1) + 4 * n2 + 2 * n3;
    v32u W[4];
    auto trd4 = [&](int k, unsigned p, int o1, int o2) { const v2u t[4] = {trd(p), trd(p + o1), trd(p + o2), trd(p + o2 + o1)};
#pragma unroll
        for (int i = 0; i < 8; ++i) W[k >> 2][(k & 3) * 8 + i] = t[i >> 1][i & 1]; };
    unsigned cal;
    auto phA = [&](int lg) { return ((lg >> 13) << 14) + (int)((cal & 1) << 13) + (lg & 8191); };
    auto phB = [&](int lg) { return CHB + ((lg >> 13) << 14) + (int)((cal & 2) << 12) + (lg & 8191); };
    auto h1o = [&](int inner) { return phB(h * 6144 + inner); };
    auto stream = [&](auto issue, auto read) {
        issue(0); issue(1);
        WAITV(8); BAR();
        if (c == 0 && w8 == 3) gadd1(a.ctl + C_HB);
        read(std::integral_constant<int, 0>{}); WAITL(); issue(2);
        WAITV(8); read(std::integral_constant<int, 1>{}); WAITL(); issue(3);
        WAITV(8); read(std::integral_constant<int, 2>{});
        WAITV(0); read(std::integral_constant<int, 3>{}); WAITL();
        if (w8 == 3) { if (c) ctl_wait(a, hb); gadd1(a.ctl + C_READY); }
    };
    auto start = [&]() {
        if (w8 == 3) {
            for (int i = 0; gld(a.ctl + C_READY) < 256; ++i) { if (c == 0) gadd1(a.ctl + C_HB); __builtin_amdgcn_s_sleep(4); if (i > GCAP) fatal(err); }
            FENCE();
            unsigned v = gld(a.ctl + C_CAL + (l & 3));
            unsigned t = (__builtin_amdgcn_readlane(v, 1) < __builtin_amdgcn_readlane(v, 0) ? 1u : 0u) | (__builtin_amdgcn_readlane(v, 3) < __builtin_amdgcn_readlane(v, 2) ? 2u : 0u);
            lp<unsigned>(L_TB + 16)[0] = t; WAITL();
        }
        BAR();
        cal = RFL(ldsr(L_TB + 16));
    };
    const unsigned char* xp = a.qkv + h * 24576 + w * 8192 + (w == 2 ? (jj & 1) * 4096 : 0);
    const unsigned char* xq = a.qkv + h * 24576 + 2 * NH * 24576;
    const unsigned xw = w == 3 ? 20480 - XC * 1024 : w * (XC == 4 ? 8192 : 4096);
    const unsigned* fG = a.ctl + C_G + h * 128 + w * 32;
    auto lo = [](const void* p) { return (unsigned)(u64)p; };
    auto hi = [](const void* p) { return (unsigned)((u64)p >> 32); };
    v16u S0 = {lo(a.ring), hi(a.ring), RING_BYTES, 0x20000u, lo(a.ox), hi(a.ox), NSEG * 262144, 0x20000u, lo(err), hi(err), lo(a.ctl + C_M + h * 128 + jj * 32), hi(a.ctl + C_M + h * 128 + jj * 32),
               lo(xq), hi(xq), lo(fG - w * 32), hi(fG - w * 32)};
    v16u S1 = {(unsigned)a.d1, (unsigned)a.d2, (unsigned)a.per, a.dinit & 0xffffu, (unsigned)w, L_KX + xw, L_TB, (unsigned)w8};
    v8u cnts;
    v16u A, K;
    const unsigned six = hf ? 0xEEEE : 0x4444;

    if (w8 < 4) {
        { const bf16_t* Wm = (jj >> 1 ? a.Wk : a.Wq) + (size_t)256 * w * HD + h * DH + 64 * (jj & 1);
          const unsigned p = stg + prr * 128 + (((hf ^ (4 * n2 + 2 * n3)) * 16 + (n & 1) * 8) ^ ((w >> 1) * 64));
          stream([&](int i) { _Pragma("unroll 1") for (int j = 0; j < 8; ++j) gldsc(Wm + (size_t)(64 * i + 8 * j) * HD, (l >> 3) * (HD * 2) + ((l & 7) ^ (2 * q)) * 16, stg + (i & 1) * 8192 + j * 1024); },
                 [&](auto i) { _Pragma("unroll") for (int t = 0; t < 4; ++t) trd4(4 * t + i, (p ^ (t * 32)) + (i & 1) * 8192, 1024, 4096); }); }
        MARK(2);
#pragma unroll
        for (int i = 0; i < 4; ++i) K[i] = ((n & 7) >> 1) == i ? 0x3F80u << (16 * (n & 1)) : 0u;
        K[9] = 8192 + l * 16; K[10] = 12288 + l * 16; K[11] = 16384 + (jj & 1) * 4096 + l * 16;
        if (w == 2) { const int rt = jj & 1, ck = 8 * (jj >> 1) + hf;
#pragma unroll
            for (int i = 0; i < 8; ++i) K[i] = 16 * (i >> 2) + 4 * q + (i & 3) > 16 * rt + n ? 0xFF800000u : 0u;
            K[8] = L_KX + l * 16; K[9] = L_VX + (ck * 16 + 4 * q + (n >> 2)) * 16 + (l & 1) * 8; K[10] = L_VM + ck * 128 + (2 * q + n2) * 16 + (l & 1) * 8; }
        BAR();
        if (w < 3) {
            flags_wait<4>(fG, 1, 2, l, err);
#pragma unroll
            for (int i = 0; i < 4; ++i) if (w < 2 || !(i & 1)) glds1<4>(xp + (i >> 1) * (NH * 24576) + (i & 1) * 4096, l * 16, L_KX + (i >> 1) * 20480 + w * 8192 + (i & 1) * 4096);
            WAITV(0);
        }
        start();
        if (w < 2) {
#pragma unroll
            for (int i = 0; i < 4; ++i) { const v4u t = *lp<v4u>(L_KX + w * 4096 + i * 1024 + l * 16);
#pragma unroll
                for (int e = 0; e < 4; ++e) K[4 * i + e] = t[e]; } }
        MARK(0); MARKC(4);
        const int sl = 2 * (jj & 1) + (w >> 1);
        A[0] = phB(L_H3 + g * 16384 + w * 4096 + (l ^ (w & 1)) * 16);
        A[1] = h1o((jj >> 1) * 2048 + ((sl * 2 + n3) * 2 + (w & 1)) * 128 + (q >> 1) * 64 + n2 * 32 + (q & 1) * 16 + (n & 3) * 4);
        A[2] = L_RED + (w ^ 1) * 512 + l * 8; A[3] = L_RED + 2048 + (w ^ 2) * 512 + l * 8; A[4] = L_RED + 4096 + (w ^ 3) * 512 + l * 8; A[5] = L_RED + w * 512 + l * 8;
        A[6] = L_FL + w * 256 + l * 4; A[7] = L_FL + (l & 3) * 256 + l * 4; A[8] = six;
        A[9] = w == 2 ? h * 8192 + jj * 2048 + ((2 * (q & 1) + (q >> 1)) * 16 + n) * 16 : (XC == 4 ? min(w, 2) * 128 : (w == 3 ? 128 + (l & 1) * 128 : 0));
        A[10] = w < 3 ? h1o(l * 16) : phA(c * 4096 + l * 16);
        A[11] = w < 2 ? L_KX + w * 4096 + l * 16 : h1o((w == 2 ? 2048 : jj * 1024) + l * 16);
        A[12] = w < 2 ? L_SC + w * 1024 + ((l * 16) ^ ((q & 1) * 32)) : (w == 2 ? L_SM + ((l * 8) ^ ((q & 1) * 16)) : phB(lg3));
        A[13] = w < 3 ? xw + l * 16 : (c % 33 == 0 && l < 4 ? phB(L_TS + l * 512 + (c >> 5) * 128) : OOB);
        A[14] = w < 3 ? xw + 4096 + l * 16 : h1o(4096 + jj * 512 + l * 8); A[15] = 0;
    } else {
        { const bf16_t* Wo = a.Wo + (size_t)h * DH * DM + 256 * jj + 64 * w; const bf16_t* Wv = a.Wv + (size_t)256 * w * HD + h * DH + 32 * jj;
          const unsigned po = stg + (4 * q + n3 + 16 * n2) * 128 + (((n & 3) * 2) ^ (q & 1)) * 16, pv = stg + prr * 64 + (hf ^ (2 * n2)) * 16 + (n & 1) * 8;
          stream([&](int i) {
                     if (i < 2) { _Pragma("unroll 1") for (int j = 0; j < 8; ++j) gldsc(Wo + (size_t)(64 * i + 8 * j) * DM, (l >> 3) * (DM * 2) + ((l & 7) ^ (l >> 5)) * 16, stg + i * 8192 + j * 1024); }
                     else { _Pragma("unroll 1") for (int j = 0; j < 8; ++j) glds(Wv + (size_t)(128 * (i - 2) + 16 * j) * HD, (l >> 2) * (HD * 2) + ((l & 3) ^ (2 * (q & 1))) * 16, stg + (i & 1) * 8192 + j * 1024); } },
                 [&](auto i) { _Pragma("unroll") for (int t = 0; t < 4; ++t) {
                     if constexpr (i < 2) trd4(2 * t + i, (po ^ ((t >> 1) * 16)) + (t & 1) * 8 + i * 8192, 256, 4096);
                     else trd4(8 + 4 * (t & 1) + 2 * (i - 2) + (t >> 1), (pv ^ ((t & 1) * 32)) + (t >> 1) * 4096 + (i & 1) * 8192, 512, 2048); } }); }
        BAR();
        start();
        A[0] = phB(L_H3 + g * 16384 + w * 4096 + l * 16); A[1] = h1o(4096 + l * 16); A[2] = phB(L_TS + g * 512 + (l & 3) * 128);
        A[3] = h1o(4096 + ((jj * 2 + n3) * 2 + (w & 1)) * 128 + (q & 1) * 64 + (n & 1) * 32 + (q >> 1) * 16 + ((n >> 1) & 3) * 4);
        A[4] = phA((jj * 32 + w * 8 + (n >> 2) * 2 + (q >> 1)) * 4096 + h * 128 + ((q & 1) * 4 + (n & 3)) * 16);
        A[5] = L_VM + q * 256 + (4 * n3 + 2 * (n & 1) + n2) * 16 + hf * 4;
        A[6] = L_RV + w * 1024 + l * 8; A[7] = L_RV + (w & 1) * 512 + l * 8;
        A[8] = L_FL + 1024 + w * 256 + l * 4; A[9] = L_FL + 1024 + (l & 3) * 256 + l * 4; A[10] = L_FL + 2048 + min(l & 3, 2) * 256 + l * 4;
        A[11] = L_SC + q * 512 + hf * 256 + n * 16; A[12] = L_SM + (q & 1) * 256 + hf * 128 + n * 8;
        A[13] = L_VX + q * 256 + (8 * n3 + 4 * hf + 2 * n2 + (n & 1)) * 16; A[14] = six; A[15] = L_TB;
    }
    S1[8] = RFL(phB(L_TS + g * 512)); S1[9] = cyc0; S1[10] = rt0;
    asm volatile(A_CHAIN : "+{s[40:55]}"(S0), "+{s[56:71]}"(S1), "={s[80:87]}"(cnts), "+{v[168:183]}"(A), "+{v[196:211]}"(K) : "{v[0:31]}"(W[0]), "{v[32:63]}"(W[1]), "{v[64:95]}"(W[2]), "{v[96:127]}"(W[3])
                 : "v128", "v129", CL10("v13"), CL10("v14"), CL10("v15"), "v160", "v161", "v162", "v163", "v164", "v165", "v166", "v167", "v184", "v185", "v186", "v187", "v188", "v189",
                   "v190", "v191", "v192", "v193", "v194", "v195", "v212", "v213", "v214", "v215", "v216", "v217", "v218", "v219", CL10("v22"), CL10("v23"), CL10("v24"), "v250", "v251", CL_S);
}

struct Tile { const bf16_t* W; int ld, wide, perm; };
__device__ __forceinline__ void tile_issue(const Tile& T, int t, unsigned L, int w8, int l) {
    const bf16_t* p = T.W + (T.wide ? (w8 & 3) * 32 * T.ld + (2 * t + (w8 >> 2)) * 128 : (t * 256 + w8 * 32) * T.ld);
    const unsigned vo = (T.perm ? (l >> 5) + (l & 16) : l >> 4) * T.ld * 2 + ((l & 15) ^ (2 * (l >> 4))) * 16;
#pragma unroll 1
    for (int i = 0; i < 8; ++i) { const bf16_t* g = p + i * (T.perm ? 2 : 4) * T.ld; const unsigned v = vo ^ ((i & 2) << 6), d = L + (t & 1) * 65536 + (w8 * 8 + i) * 1024;
        gldsw(g, v, d); }
}
template <class F>
__device__ __forceinline__ void tile_load(const Tile& T, v4u (&wf)[32], unsigned L, int w8, int l, F next) {
    const int n = l & 15, q = l >> 4;
    const unsigned p = L + (8 * q + (n >> 2)) * 256 + ((4 * (w8 & 3) + ((l >> 1) & 1)) ^ (2 * ((n >> 2) | ((q & 1) << 2)))) * 16 + (l & 1) * 8;
    bool more = true;
#pragma unroll 1
    for (int hh = 0; hh < 2; ++hh)
#pragma unroll
    for (int u = 0; u < 2; ++u) {
        if (!more) WAITV(0);
        WAITV(8); BAR();
        if ((w8 >> 2) == hh) {
#pragma unroll
            for (int i = 0; i < 16; ++i) { const unsigned pp = (p ^ ((i & 1) * 32)) + u * 65536 + (i >> 1) * 8192; wf[u * 16 + i] = cat2(trd(pp), trd(pp + 1024)); }
            WAITL(); }
        BAR();
        if (hh == 0) tile_issue(T, u + 2, L, w8, l); else more = next(u);
    }
}
#define NB 8
#define GD 4
template <int XR, bool ROWS, class F>
__device__ __forceinline__ void seg_gemm(const v4u (&wf)[32], unsigned b0, frag_cd (&acc)[4], F side) {
    v4u f[2 * GD];
    const unsigned bb[4] = {b0, ROWS ? b0 ^ 64 : b0, ROWS ? b0 ^ 128 : b0, ROWS ? b0 ^ 192 : b0};
#define RD(ks, mt) asm volatile("ds_read_b128 %0, %1 offset:%2" : "=v"(f[((ks) % GD) * 2 + mt]) : "v"(bb[(ks) & 3]), "n"(ROWS ? ((ks) >> 2) * 256 + mt * 16384 : ((ks) >> 1) * 4096 + ((ks) & 1) * 1024 + mt * 2048) : "memory")
#define MM(ct, mt) do { if (ks == 0) asm volatile("v_mfma_f32_16x16x32_bf16 %0, %1, %2, 0" : "=&v"(acc[2 * ct + mt]) : "v"(wf[ct]), "v"(f[mt])); \
        else asm volatile("v_mfma_f32_16x16x32_bf16 %0, %1, %2, %0" : "+v"(acc[2 * ct + mt]) : "v"(wf[2 * ks + ct]), "v"(f[(ks % GD) * 2 + mt])); } while (0)
#pragma unroll
    for (int ks = 0; ks < GD - 1; ++ks) { RD(ks, 0); RD(ks, 1); }
#pragma unroll
    for (int ks = 0; ks < 16; ++ks) {
        side(2 * ks);
        asm volatile("s_waitcnt lgkmcnt(%0)" :: "n"(2 * (ks + GD - 1 <= 16 ? GD - 2 : 15 - ks) + (ks >= 13 ? XR : 0)));
        MM(0, 0);
        if (ks + GD - 1 < 16) RD(ks + GD - 1, 0);
        MM(0, 1);
        side(2 * ks + 1);
        MM(1, 0);
        if (ks + GD - 1 < 16) RD(ks + GD - 1, 1);
        MM(1, 1);
    }
    asm volatile("s_nop 7" : "+v"(acc[0]), "+v"(acc[1]), "+v"(acc[2]), "+v"(acc[3]));
#undef RD
#undef MM
}
static_assert(GD == 4, "");
__device__ __forceinline__ float fadd(float a, float b) { float r; asm("v_add_f32 %0, %1, %2" : "=v"(r) : "v"(a), "v"(b)); return r; }
__device__ __forceinline__ float fmul(float a, float b) { float r; asm("v_mul_f32 %0, %1, %2" : "=v"(r) : "v"(a), "v"(b)); return r; }
__device__ __forceinline__ void half_put(unsigned ex, int w8, int l, const frag_cd (&acc)[4]) {
#pragma unroll
    for (int i = 0; i < 4; ++i) *lp<frag_cd>(ex + w8 * 4096 + i * 1024 + l * 16) = acc[i];
    FENCE();
}
struct Half { frag_cd t[4]; unsigned dec; };
__device__ __forceinline__ void half_req(unsigned ex, unsigned dec, Half& x) {
    asm volatile("ds_read_b32 %0, %1" : "=v"(x.dec) : "v"(dec) : "memory");
#pragma unroll
    for (int i = 0; i < 4; ++i) asm volatile("ds_read_b128 %0, %1 offset:%2" : "=v"(x.t[i]) : "v"(ex), "n"(i * 1024) : "memory");
}
template <class F>
__device__ __forceinline__ void half_get(Half& x, frag_cd (&acc)[4], F side) {
    asm volatile("s_waitcnt lgkmcnt(0)" : "+v"(x.t[0]), "+v"(x.t[1]), "+v"(x.t[2]), "+v"(x.t[3]), "+v"(x.dec) :: "memory");
#pragma unroll
    for (int i = 0; i < 4; ++i) { side(i);
#pragma unroll
        for (int e = 0; e < 4; ++e) acc[i][e] = fadd(acc[i][e], x.t[i][e]);
        asm volatile("" : "+v"(acc[i])); }
}
__device__ __forceinline__ void gld_req(unsigned& v, const unsigned* p) { asm volatile("global_load_dword %0, %1, off sc1" : "+v"(v) : "v"(p) : "memory"); }
template <int N> __device__ __forceinline__ void gld_wait(unsigned& v) { asm volatile("s_waitcnt vmcnt(%1)" : "+v"(v) : "n"(N) : "memory"); }
#define GEARLY 2
#define BARI 25
#define PRIO(n) __builtin_amdgcn_s_setprio(n)

__device__ __forceinline__ unsigned topk(const Args& a, int l) {
    const rsrc_t rg = mkrs(a.gate, 2 * S_TOK * 4);
    flags_wait<16>(a.ctl + C_R, 32, 1, l, a.ctl + C_ERR);
    v4u kv[8]; unsigned prev = ~0u, mine = 0;
#pragma unroll
    for (int i = 0; i < 8; ++i) kv[i] = __builtin_amdgcn_raw_buffer_load_b128(rg, S_TOK * 4 + l * 16, i * 1024, 16);
#pragma unroll 1
    for (int c = 0; c < TOPC; ++c) { unsigned mx = 0;
        FENCE();
#pragma unroll
        for (int i = 0; i < 8; ++i)
#pragma unroll
            for (int e = 0; e < 4; ++e) mx = max(mx, kv[i][e] < prev ? kv[i][e] : 0u);
        mx = max(mx, dppu<0x128>(mx)); mx = max(mx, dppu<0x124>(mx)); mx = max(mx, dppu<0x122>(mx)); mx = max(mx, dppu<0x121>(mx));
        { v2u r = sw16(mx, mx); mx = max(r.x, r.y); r = sw32(mx, mx); mx = max(r.x, r.y); }
        prev = RFL(mx); if ((l & 7) == c) mine = prev; }
    return mine;
}
__device__ __forceinline__ unsigned mix(float gt, float omg, float t0, float t1, float b0, float b1) {
    unsigned av = cvt_pk(t0, t1); av = cvt_pk(fmul(gt, lo16(av)), fmul(gt, hi16(av))); const unsigned bv = cvt_pk(fmul(omg, b0), fmul(omg, b1));
    return cvt_pk(fadd(lo16(av), lo16(bv)), fadd(hi16(av), hi16(bv)));
}
struct Finisher {
    rsrc_t rp, rx, rg, ro; int po, frow, xo, w8; unsigned seltok, ly;
    struct Fin { v4u p[4]; v2u xv; float gt; int k; };
    __device__ __forceinline__ Finisher(const Args& a, int j, int w8_, int l, unsigned L_Y) {
        rp = mkrs(a.part, NSEG * 524288); rx = mkrs(a.x, S_TOK * DM * 2); rg = mkrs(a.gate, S_TOK * 4); ro = mkrs(a.out, S_TOK * DM * 2);
        w8 = w8_; po = j * 16384 + w8 * 1024 + l * 16; frow = 16 * (w8 & 1) + (l & 15); xo = (frow * DM + j * 32 + (w8 >> 1) * 16 + 4 * (l >> 4)) * 2;
        seltok = 4095u - (lp<unsigned>(L_Y + 1024)[l & 7] & 4095u); ly = L_Y + ((w8 >> 1) * 32 + (l >> 4) * 8) * 16;
    }
    __device__ __forceinline__ Fin issue(int s, bool on) const { Fin f; int k = on ? 0 : 0x40000000; asm("" : "+s"(k));
#pragma unroll
        for (int i = 0; i < 4; ++i) f.p[i] = __builtin_amdgcn_raw_buffer_load_b128(rp, k + po, s * 524288 + i * 4096, 16);
        f.k = k + xo;
        f.xv = __builtin_amdgcn_raw_buffer_load_b64(rx, f.k, s * 65536, 0);
        f.gt = __uint_as_float(__builtin_amdgcn_raw_buffer_load_b32(rg, k + frow * 4, s * 128, 16));
        return f;
    }
    template <class F> __device__ __forceinline__ void finish(int s, const Fin& f, F side) const {
        const int tok = 32 * s + frow; const float gt = f.gt, omg = rbf(1.0f - gt);
        frag_cd t; v2u o; int k = f.k;
#pragma unroll
        for (int e = 0; e < 4; e += 2) {
            side(e >> 1);
#pragma unroll
            for (int u = e; u < e + 2; ++u) t[u] = fadd(fadd(__uint_as_float(f.p[0][u]), __uint_as_float(f.p[1][u])), fadd(__uint_as_float(f.p[2][u]), __uint_as_float(f.p[3][u])));
            o[e >> 1] = mix(gt, omg, t[e], t[e + 1], lo16(f.xv[e >> 1]), hi16(f.xv[e >> 1])); asm volatile("" : "+v"(o[e >> 1])); }
        for (unsigned mk = (unsigned)__builtin_amdgcn_ballot_w64((seltok >> 4) == (unsigned)(2 * s + (w8 & 1))) & 255u; mk; mk &= mk - 1) {
            const int slot = __builtin_ctz(mk);
            if (__builtin_amdgcn_readlane(seltok, slot) == (unsigned)tok) { *lp<frag_cd>(ly + slot * 16) = t; k = 0x40000000; }
        }
        __builtin_amdgcn_raw_buffer_store_b64(o, ro, k, s * 65536, 0);
    }
};

__device__ __forceinline__ void gwork(const Args& a, const int m, const int h, const unsigned L, const unsigned hb) {
    const int w8 = RFL(threadIdx.x >> 6); int l = threadIdx.x & 63, n = l & 15, q = l >> 4;
    unsigned* const err = a.ctl + C_ERR;
    const unsigned L_EX = L + 131072, L_XS = L_EX, L_HS = L_XS + 16384, L_GU = L_HS + 2048;
    unsigned* const fG = a.ctl + C_G + h * 128 + m * 32;
    const unsigned xo = (l ^ (8 * (w8 & 1))) * 16;
    auto xissue = [&](int s, int i0, int i1) {
        const bf16_t* p = a.x + (size_t)(32 * s + 8 * (w8 & 3)) * DM + (~w8 & 4) * 128; const unsigned d = L + (s & 1) * 65536 + (w8 ^ 4) * 8192;
#pragma unroll
        for (int i = i0; i < i1; ++i) {
            if (i == i0 || i == 4) asm volatile("s_mov_b32 m0, %0" :: "s"(d + (i & 4) * 1024));
            unsigned t; asm volatile("v_xor_b32 %0, %4, %1\n\tglobal_load_lds_dwordx4 %0, %2 offset:%3" : "=&v"(t) : "v"(xo), "s"(p), "n"((i & 3) * 1024), "n"(i * 1040 + (i & 4) * 1024) : "memory"); }
    };
    v4u wf[32];
    { const Tile T{(m == 0 ? a.Wk : (m == 1 ? a.Wv : a.Wq)) + h * DH, HD, 0, 0};
      tile_issue(T, 0, L, w8, l); tile_issue(T, 1, L, w8, l);
      tile_load(T, wf, L, w8, l, [&](int b) { if (b == 0 || w8 >= 4) xissue(b, 0, 8); return true; }); }
    MARK(0);
    const unsigned rb0 = L + (w8 >> 2) * 32768 + n * 1024 + (q ^ n) * 16;
    unsigned pacc[8] = {}, plast = 0; (void)pacc; (void)plast;
    PT(7);
    if (w8 >= 4) {
        const rsrc_t rq = mkrs(a.qkv, NSEG * NH * 24576), rf = mkrs(fG, w8 == 4 ? 4 : 0);
        const int sto = h * 24576 + m * 8192 + (m == 1 ? (w8 & 3) * 64 + l : ((w8 & 2) + (q & 1)) * 64 + (2 * (w8 & 1) + (q >> 1)) * 16 + n) * 16;
        WAITV(8); BAR(); WAITV(0);
        if (w8 == 4) { ctl_wait(a, hb); gadd1(a.ctl + C_READY); }
        for (int sg = 0; sg < NSEG; ++sg) {
            PT(0);
            BAR();
            PT(1);
            PRIO(2);
            if (sg <= GEARLY) __builtin_amdgcn_raw_buffer_store_b32(sg, rf, l * 4, 0, 16);
            frag_cd acc[4]; Half hx;
            seg_gemm<5, true>(wf, rb0 + (sg & 1) * 65536, acc, [&](int i) {
                if (i == BARI) { PRIO(3); PT(5); WAITV(3); BAR(); PT(6); }
                if (i == 26) half_req(L_EX + (w8 & 3) * 4096 + l * 16, L_EX, hx);
                if (i >= 26) xissue((sg + 2) & 63, i - 26, i - 25); });
            PRIO(0);
            PT(2);
            half_get(hx, acc, [&](int i) { if (i & 1) xissue((sg + 2) & 63, 6 + i / 2, 7 + i / 2); });
            v4u r[2];
#pragma unroll
            for (int mt = 0; mt < 2; ++mt) r[mt] = v4u{cvt_pk(acc[mt][0], acc[2 + mt][0]), cvt_pk(acc[mt][1], acc[2 + mt][1]), cvt_pk(acc[mt][2], acc[2 + mt][2]), cvt_pk(acc[mt][3], acc[2 + mt][3])};
            PT(3);
#pragma unroll
            for (int mt = 0; mt < 2; ++mt) STB(r[mt], rq, sto + mt * 4096, sg * (NH * 24576));
            __builtin_amdgcn_raw_buffer_store_b32(sg <= GEARLY ? sg : sg - 1, rf, l * 4, 0, 16);
            FENCE();
            if (sg < GEARLY) WAITV(0);
            PT(4);
        }
        PFLUSH();
        WAITV(0); BAR();
        if (w8 == 4) gst1(fG, NSEG);
    } else {
        for (int sg = 0; sg < NSEG; ++sg) {
            frag_cd acc[4];
            WAITL();
            PT(0);
            BAR();
            PT(1);
            PRIO(2);
            seg_gemm<0, true>(wf, rb0 + (sg & 1) * 65536, acc, [&](int i) { if (i == BARI) { PRIO(3); PT(5); WAITV(0); BAR(); PT(6); } });
            PRIO(1);
            PT(2);
            half_put(L_EX, w8, l, acc);
            xissue((sg + 1) & 63, 0, 8);
            PT(3);
            MARK(sg + 1);
        }
        PFLUSH();
        WAITL(); BAR();
        WAITV(0); BAR();
    }
    if (m != 2) return;
    asm volatile("v_mbcnt_lo_u32_b32 %0, -1, 0\n\tv_mbcnt_hi_u32_b32 %0, -1, %0" : "=v"(l));
    n = l & 15; q = l >> 4;
    const rsrc_t ry = mkrs(a.ypart, 32 * 29 * 1024);
    auto tile = [&](int t) { return t < 2 ? Tile{(t ? a.wu : a.wg) + h * 128, FF, 0, 0} : Tile{a.wd + (size_t)h * 128 * DM, DM, 1, 0}; };
    unsigned mykey = 0;
    if (w8 == 0) {
        mykey = topk(a, l);
#pragma unroll 1
        for (int i = 0; i < 16; ++i) glds(a.x + i * 64, (4095u - (mykey & 4095u)) * (DM * 2) + (l >> 3) * 16, L_XS + i * 1024);
    }
    if (h < FF / 128) {
      Tile T = tile(0);
      tile_issue(T, 0, L, w8, l); tile_issue(T, 1, L, w8, l);
      frag_cd gv[2] = {};
#pragma unroll 1
      for (int ti = 0; ti < 3; ++ti) {
        const Tile N = tile(ti + 1);
        tile_load(T, wf, L, w8, l, [&](int b) { if (ti < 2) tile_issue(N, b, L, w8, l); return ti < 2; });
        T = N;
        if (ti < 2) {
            frag_cd acc[2] = {};
#pragma unroll
            for (int ks = 0; ks < 16; ++ks) { const v4u xf = *lp<v4u>(L_XS + ((w8 >> 2) * 16 + ks) * 512 + q * 128 + (n & 7) * 16); acc[0] = MFMA(wf[2 * ks], xf, acc[0]); acc[1] = MFMA(wf[2 * ks + 1], xf, acc[1]); }
            if (ti == 1) {
                frag_cd g = n < 8 ? gv[0] : gv[1], u = n < 8 ? acc[0] : acc[1];
                if (w8 >= 4) { *lp<frag_cd>(L_GU + (w8 & 3) * 2048 + l * 16) = g; *lp<frag_cd>(L_GU + (w8 & 3) * 2048 + 1024 + l * 16) = u; WAITL(); }
                BAR();
                if (w8 < 4) { g += *lp<frag_cd>(L_GU + w8 * 2048 + l * 16); u += *lp<frag_cd>(L_GU + w8 * 2048 + 1024 + l * 16);
                    float hv[4];
#pragma unroll
                    for (int e = 0; e < 4; ++e) { const float gg = rbf(g[e]); hv[e] = rbf(gg / (1.0f + __expf(-gg))) * rbf(u[e]); }
                    *lp<v2u>(L_HS + w8 * 512 + (2 * (n >> 3) + (q >> 1)) * 128 + (n & 7) * 16 + (q & 1) * 8) = v2u{cvt_pk(hv[0], hv[1]), cvt_pk(hv[2], hv[3])};
                    WAITL(); } }
            gv[0] = acc[0]; gv[1] = acc[1];
        } else {
#pragma unroll
            for (int cb = 0; cb < 4; ++cb) { frag_cd acc[2] = {};
#pragma unroll
                for (int i = 0; i < 4; ++i) { const v4u hf = *lp<v4u>(L_HS + i * 512 + q * 128 + (n & 7) * 16); acc[0] = MFMA(wf[8 * cb + 2 * i], hf, acc[0]); acc[1] = MFMA(wf[8 * cb + 2 * i + 1], hf, acc[1]); }
                STB(__builtin_bit_cast(v4u, n < 8 ? acc[0] : acc[1]), ry, ((w8 & 3) * 28 + h) * 1024 + ((n >> 3) * 32 + q * 8 + (n & 7)) * 16, ((w8 >> 2) * 4 + cb) * (4 * 28 * 1024)); }
        }
        MARK(67 + ti);
      }
      WAITV(0); BAR();
    }
    if (w8 == 0) {
        gst1(a.ctl + C_Y + h * 32, 1u);
        flags_wait<16>(a.ctl + C_Y, FF / 128, 1, l, err);
        frag_cd y = {};
#pragma unroll 1
        for (int i = 0; i < 28; i += 7) { v4u t[7];
#pragma unroll
            for (int u = 0; u < 7; ++u) t[u] = __builtin_amdgcn_raw_buffer_load_b128(ry, l * 16, (h * 28 + i + u) * 1024, 16);
#pragma unroll
            for (int u = 0; u < 7; ++u) y += __builtin_bit_cast(frag_cd, t[u]); }
        const float r = hi16((mykey & 0x80000000u) ? mykey & 0x7fffffffu : ~mykey), rs = rbf(1.0f / (1.0f + __expf(-r)));
#pragma unroll
        for (int e = 0; e < 4; ++e) y[e] = rbf(rbf(y[e]) * rs);
        STB(__builtin_bit_cast(v4u, y), ry, l * 16, (32 * 28 + h) * 1024);
        WAITV(0);
        gst1(a.ctl + C_Y + h * 32 + 1, 1u);
        MARK(120);
    }
}

__device__ __forceinline__ void owork(const Args& a, const int nt, const int kq, const unsigned L, const unsigned hb) {
    const int tid = threadIdx.x, w8 = RFL(tid >> 6), l = tid & 63, j = nt * 4 + kq;
    unsigned* const err = a.ctl + C_ERR;
    const unsigned L_EX = L + 131072, L_DEC = L_EX + 16384, L_Y = L_DEC + 64, L_KEY = L_Y + 1024;
    if (tid < 4) lp<unsigned>(L_DEC)[tid] = 0;
    if (w8 == 0) {
        unsigned char* p = a.ring + ((l >> 1) & 1) * CHB + (l & 1) * 8192 + j * 16384 + ((l >> 2) & 3) * 2048 + 1024;
        unsigned best = 0;
        if (l < 16) gst((unsigned*)p, ~0u);
        WAITV(0);
#pragma unroll 1
        for (int i = 0; i < 16; ++i) { u64 t1, t2; unsigned v;
            const u64 pi = ((u64)__builtin_amdgcn_readlane((unsigned)((u64)p >> 32), i) << 32) | (unsigned)__builtin_amdgcn_readlane((unsigned)(u64)p, i);
            asm volatile("s_nop 4\n\ts_memtime %0\n\ts_waitcnt lgkmcnt(0)\n\tglobal_load_dword %2, %3, %4 sc1\n\ts_waitcnt vmcnt(0)\n\ts_memtime %1\n\ts_waitcnt lgkmcnt(0)"
                         : "=&s"(t1), "=&s"(t2), "=&v"(v) : "v"(0u), "s"(pi) : "memory");
            if ((l & 3) == (i & 3)) best = max(best, ~(unsigned)(t2 - t1)); }
        if (l < 16) gst((unsigned*)p, ~0u);
        ctl_wait(a, hb);
        if (l < 4) __hip_atomic_fetch_max(a.ctl + C_CAL + l, best, __ATOMIC_RELAXED, __HIP_MEMORY_SCOPE_AGENT);
        WAITV(0);
        if (l == 0) __hip_atomic_fetch_add(a.ctl + C_READY, 1u, __ATOMIC_RELAXED, __HIP_MEMORY_SCOPE_AGENT);
        MARK(66);
    }
    BAR();
    {
#pragma unroll 1
        for (int i = 0; i < 16; ++i) glds(a.x + (size_t)(64 * j + 8 * w8) * DM + i * 512, l * 16, L + (w8 * 16 + i) * 1024);
        v4u gw[2], wr[2];
#pragma unroll
        for (int i = 0; i < 2; ++i) { gw[i] = *(const v4u*)(a.gw + 512 * i + 8 * l); wr[i] = *(const v4u*)(a.wr + 512 * i + 8 * l); }
        WAITV(0);
        float res = 0.f;
#pragma unroll 1
        for (int t = 0; t < 8; ++t) { float sg = 0.f, sr = 0.f;
#pragma unroll
            for (int i = 0; i < 2; ++i) { const v4u xv = *lp<v4u>(L + (w8 * 16 + 2 * t + i) * 1024 + l * 16);
#pragma unroll
                for (int e = 0; e < 4; ++e) { const float x0 = lo16(xv[e]), x1 = hi16(xv[e]); const unsigned p = cvt_pk(x0 * lo16(gw[i][e]), x1 * hi16(gw[i][e]));
                    sg += lo16(p) + hi16(p); sr = __builtin_fmaf(x0, lo16(wr[i][e]), __builtin_fmaf(x1, hi16(wr[i][e]), sr)); } }
            float v = tadd32(sg, sr);
            v = tadd16(v, v); v += dppf<ROR8>(v); v += dppf<0x124>(v); v += dppf<0x122>(v); v += dppf<0x121>(v);
            if ((l & 7) == t) res = v; }
        const unsigned rb = cvt_pk(res, 0.f), tok = 64 * j + 8 * w8 + (l & 7);
        const unsigned val = l < 32 ? __float_as_uint(rbf(1.0f / (1.0f + __expf(-lo16(rb))))) : (((rb & 0x8000u) ? ~rb : rb | 0x8000u) << 16) | (4095u - tok);
        if ((l & 31) < 8) gst(a.gate + (l >> 5) * S_TOK + tok, val);
        WAITV(0); BAR();
        if (w8 == 0) gst1(a.ctl + C_R + j * 32, 1u);
        MARK(67);
    }
    v4u wf[32];
    { const Tile T{a.Wo + (size_t)kq * 1024 * DM + nt * 128, DM, 0, 1};
      tile_issue(T, 0, L, w8, l); tile_issue(T, 1, L, w8, l);
      tile_load(T, wf, L, w8, l, [](int) { return false; }); }
    MARK(0);
    if (w8 == 1) { const unsigned mine = topk(a, l); if (l < 8) lp<unsigned>(L_KEY)[l] = mine; WAITL(); }
    BAR();
    MARK(68);
    unsigned* const fP = a.ctl + C_P + nt * 512;
    const unsigned rb = L + (w8 >> 2) * 32768 + l * 16;
    unsigned pacc[8] = {}, plast = 0; (void)pacc; (void)plast;
    PT(7);
    auto oissue = [&](int s, int wv, int i) { glds1<2>(a.ox + (size_t)s * 262144 + kq * 65536 + wv * 8192 + i * 2048, l * 16, L + (s & 1) * 65536 + wv * 8192 + i * 2048); };
    if (w8 >= 4) {
        const rsrc_t rf = mkrs(fP + (kq * 4 + (w8 & 3)) * 32, 4), rp = mkrs(a.part, NSEG * 524288);
        const int stp = ((nt * 4 + (w8 & 3)) * 4 + kq) * 4096 + l * 16;
        const unsigned* fW = l < 32 ? a.ctl + C_M + (kq * 32 + l) * 32 : fP + ((l & 3) * 4 + kq) * 32;
        unsigned fv = 0; int pc = 0;
        auto a_ok = [&](int s) { return s < NSEG && (unsigned)__builtin_amdgcn_ballot_w64(fv <= (unsigned)s) == 0; };
        auto p_upd = [&]() { if ((__builtin_amdgcn_ballot_w64(fv <= (unsigned)pc) >> 32) == 0) ++pc; if ((__builtin_amdgcn_ballot_w64(fv <= (unsigned)pc) >> 32) == 0) ++pc; lp<unsigned>(L_DEC)[2] = pc; };
        int have = 0, nxt = 0, late = 0;
        for (int s = 0; s <= NSEG; ++s) {
            if (!have) {
                if (!late) {
                    gld_wait<0>(fv);
                    __builtin_amdgcn_raw_buffer_store_b32(s, rf, l * 4, 0, 16);
                    if (w8 == 4) {
                        for (int i = 0;; ++i) { if (fv <= (unsigned)(l < 32 ? s + 1 : s - 1)) fv = gld(fW); WAITV(0); p_upd(); if (a_ok(s) || pc == NSEG) break; __builtin_amdgcn_s_sleep(2); if (i > GCAP) fatal(err); }
                        lp<unsigned>(L_DEC)[3] = s + 1; lp<unsigned>(L_DEC)[~s & 1] = a_ok(s + 1); FENCE(); }
                    if (s == NSEG) break;
                    for (int i = 0; ldsr(L_DEC + 12) <= (unsigned)s; ++i) { __builtin_amdgcn_s_sleep(1); if (i > LCAP) endpgm(); }
                    FENCE();
                    PT(0);
#pragma unroll
                    for (int i = 0; i < 8; ++i) oissue(s, w8 ^ (~i & 4), i & 3);
                    WAITV(8);
                }
                WAITV(12); BAR();
                nxt = RFL(ldsr(L_DEC + 4 * (~s & 1))) & 1;
                if (late) WAITV(4); else WAITV(0);
                if (nxt) {
#pragma unroll
                    for (int i = 0; i < 4; ++i) oissue(s + 1, w8 ^ 4, i); }
                PT(1);
            }
            if (w8 == 4 && !nxt) {
                if (!have) { if (fv <= (unsigned)(s + 1)) gld_req(fv, fW); WAITV(0); }
                gld_wait<4>(fv);
                p_upd(); lp<unsigned>(L_DEC)[~s & 1] = 4 * a_ok(s + 1); WAITL();
                if (fv <= (unsigned)(s + 2)) gld_req(fv, fW); }
            BAR();
            PT(2);
            PRIO(2);
            frag_cd acc[4]; Half hx; unsigned mid;
            seg_gemm<5, false>(wf, rb + (s & 1) * 65536, acc, [&](int i) {
                if (i == BARI - 5) { if (!(have & nxt)) WAITV(0);
                    gld_wait<4>(fv);
                    if (w8 == 4) { p_upd(); const bool k = a_ok(s + 1); if (k) lp<unsigned>(L_DEC)[3] = s + 2; lp<unsigned>(L_DEC)[s & 1] = a_ok(s + 2) + 2 * k; }
                    __builtin_amdgcn_raw_buffer_store_b32(s - (have & nxt), rf, l * 4, 0, 16); FENCE(); }
                if (i == BARI) { PRIO(3); PT(5); BAR(); PT(6); }
                if (i == 3) asm volatile("ds_read_b32 %0, %1" : "=v"(mid) : "v"(L_DEC + 4 * (~s & 1)) : "memory");
                if (i == 26) half_req(L_EX + (w8 & 3) * 4096 + l * 16, L_DEC + 4 * (s & 1), hx);
                if (i == 28) { asm volatile("s_waitcnt lgkmcnt(4)" : "+v"(hx.dec), "+v"(mid)); const int d = RFL(hx.dec); have = nxt | RFL(mid) >> 2; nxt = have & d; late = ~have & d >> 1 & 1; }
                if (i >= 28 && (nxt | late)) oissue(s + 1 + nxt, w8 ^ 4, i - 28); });
            PRIO(0);
            PT(3);
            half_get(hx, acc, [&](int i) { if (late) oissue(s + 1, w8, i); });
            if (w8 == 4 && fv <= (unsigned)(s + 3)) gld_req(fv, fW);
#pragma unroll
            for (int i = 0; i < 4; ++i) STB(__builtin_bit_cast(v4u, acc[i]), rp, stp + i * 1024, s * 524288);
            FENCE();
            PT(4);
        }
        if (w8 == 5) {
            flags_wait<4>(a.ctl + C_Y + j * 32 + 1, 1, 1, l, err);
            const unsigned tok = 4095u - (lp<unsigned>(L_KEY)[l & 7] & 4095u); const int xo = tok * DM + j * 32 + (l >> 3) * 4;
            const frag_cd y = __builtin_bit_cast(frag_cd, __builtin_amdgcn_raw_buffer_load_b128(mkrs(a.ypart + (32 * 28 + j) * 1024, 1024), l * 16, 0, 16));
            const v2u xv = *(const v2u*)(a.x + xo); const float gt = __uint_as_float(gld(a.gate + tok)), omg = rbf(1.0f - gt);
            const float b[4] = {rbf(lo16(xv.x) + y[0]), rbf(hi16(xv.x) + y[1]), rbf(lo16(xv.y) + y[2]), rbf(hi16(xv.y) + y[3])};
            asm volatile("" :: "v"(b[0]), "v"(b[1]), "v"(b[2]), "v"(b[3]), "v"(omg));
            BAR();
            const frag_cd t = *lp<frag_cd>(L_Y + l * 16);
            *(v2u*)(a.out + xo) = v2u{mix(gt, omg, t[0], t[1], b[0], b[1]), mix(gt, omg, t[2], t[3], b[2], b[3])};
        } else BAR();
    } else {
        const Finisher fz(a, j, w8, l, L_Y);
        int fin = 0, have = 0, pc = 0;
        for (int s = 0; s <= NSEG; ++s) {
            if (!have) {
                for (int i = 0; fin < s; ++i) {
                    if (s < NSEG && RFL(ldsr(L_DEC + 12)) > s) break;
                    pc = RFL(ldsr(L_DEC + 8));
                    if (fin < pc) { FENCE(); fz.finish(fin, fz.issue(fin, true), [](int) {}); ++fin; }
                    else { __builtin_amdgcn_s_sleep(1); if (i > LCAP) endpgm(); }
                }
                if (s == NSEG) break;
            }
            FENCE();
            const bool fi = fin < pc;
            const Finisher::Fin f = fz.issue(fin, fi);
            WAITL(); WAITV(15);
            PT(0);
            BAR();
            PT(2);
            PRIO(2);
            frag_cd acc[4];
            v4u dec;
            seg_gemm<1, false>(wf, rb + (s & 1) * 65536, acc, [&](int i) {
                if (i == BARI) { PRIO(3); PT(1); WAITV(6); BAR(); PT(6); }
                if (i == 26) asm volatile("ds_read_b128 %0, %1" : "=v"(dec) : "v"(L_DEC) : "memory"); });
            PRIO(1);
            PT(3);
            asm volatile("s_waitcnt lgkmcnt(0)" : "+v"(dec));
            have = RFL(s & 1 ? dec.x : dec.y) & 5; pc = RFL(dec.z);
            half_put(L_EX, w8, l, acc);
            WAITV(0);
            if (have & 4) {
#pragma unroll
                for (int i = 0; i < 4; ++i) oissue(s + 1, w8, i); }
            if (have) { oissue(s + 1, w8 ^ 4, 0); oissue(s + 1, w8 ^ 4, 1); }
            PT(4);
            if (!have) { WAITL(); BAR(); }
            fz.finish(fin, f, [&](int i) { if (have) oissue(s + 1, w8 ^ 4, 2 + i); });
            fin += fi;
            MARK(1 + s);
            PT(5);
        }
        WAITL(); BAR();
    }
    PFLUSH();
    MARK(65);
}

__global__ void __launch_bounds__(512, 2) k_mega(Args a) {
    __shared__ __attribute__((aligned(1024))) unsigned char smem[163840];
    const unsigned L = (unsigned)(size_t)(LAS unsigned char*)smem;
    const int b = blockIdx.x, u = (b & 3) + 4 * (b >> 3);
    MARK(255); MARKC(254);
    const unsigned hb = __builtin_amdgcn_raw_buffer_load_b32(mkrs(a.ctl + C_HB, 4), threadIdx.x >> 6 == (b & 4 ? (u < 96 ? 4 : 0) : 3) ? 0 : OOB, 0, 16);
    FENCE();
    if (!(b & 4)) chain(a, u, L, hb);
    else if (u < 96) gwork(a, (u >> 2) % 3, (u & 3) * 8 + (u >> 2) / 3, L, hb);
    else owork(a, (u >> 2) - 24, u & 3, L, hb);
}

static constexpr int g_coff = 0, g_force = 0; static constexpr int g_d1 = 765, g_d2 = 1555, g_per = 2045, g_dinit = 250; static constexpr unsigned* g_dbg = nullptr;
extern "C" void kernel_launch(void* const* d_in, const int* in_sizes, int n_in, void* d_out, int out_size, void* d_ws, size_t ws_size, hipStream_t stream) {
    auto in = [&](int i) { return (const bf16_t*)d_in[i]; };
    unsigned char* ws = (unsigned char*)d_ws; size_t off = g_coff;
    auto alloc = [&](size_t bytes) { unsigned char* p = ws + off; off += (bytes + 255) & ~(size_t)255; return p; };
    unsigned* ctl = (unsigned*)alloc(C_WORDS * 4);
    unsigned* gate = (unsigned*)alloc(2 * S_TOK * 4); unsigned char* ypart = alloc(32 * 29 * 1024);
    off += (size_t)(-(uintptr_t)(ws + off)) & ((2u << 20) - 1);
    unsigned char* ring = alloc(RING_BYTES);
    unsigned char* qkv = alloc((size_t)NSEG * NH * 24576);
    unsigned char* ox = alloc((size_t)NSEG * 262144); unsigned char* part = alloc((size_t)NSEG * 524288);
    Args a{in(0), in(1), in(2), in(3), in(4), in(5), (bf16_t*)d_out, ctl, ring, qkv, ox, part, in(8), in(9), in(10), in(11), in(12), gate, ypart, g_dbg,
           g_d1 * 16 / 10, g_d2 * 16 / 10, g_per * 16 / 10, g_dinit * 16 / 10 + (g_force << 16)};
    k_mega<<<256, 512, 0, stream>>>(a);
}
```

```cpp
#include <hip/hip_runtime.h>
#include <stdint.h>
#include <type_traits>

typedef unsigned short bf16_t;
typedef unsigned long long u64;
typedef float frag_cd __attribute__((ext_vector_type(4)));
typedef unsigned v4u __attribute__((ext_vector_type(4)));
typedef unsigned v8u __attribute__((ext_vector_type(8)));
typedef unsigned v2u __attribute__((ext_vector_type(2)));
typedef float v2f __attribute__((ext_vector_type(2)));
typedef __bf16 bf16x2_t __attribute__((ext_vector_type(2)));
typedef __bf16 v8bf __attribute__((ext_vector_type(8)));
typedef __bf16 v16bf __attribute__((ext_vector_type(16)));
typedef short v4s_t __attribute__((ext_vector_type(4)));
typedef __amdgpu_buffer_rsrc_t rsrc_t;
#define LAS __attribute__((address_space(3)))
#define GAS __attribute__((address_space(1)))

static constexpr int S_TOK = 2048, DM = 1024, NH = 32, DH = 128, MM = 8, SEG = 32, NSEG = 64, HD = NH * DH, FF = 3584, TOPC = 8;
static constexpr int C_ERR = 0, C_READY = 32, C_CAL = 64  , C_ACK = 96, C_G = 128  , C_M = C_G + 4096  ,
                     C_P = C_M + 4096  , C_R = C_P + 4096, C_Y = C_R + 1024  , C_HB = C_Y + 1024, C_WORDS = C_HB + 32;
static constexpr int RING_BYTES = 4 << 20, B_H1 = 64, B_H3 = 96, B_TS = 112;
static constexpr unsigned SPIN_CAP = 1u << 18;
static constexpr int OOB = 0x7ffffff0, LCAP = 1 << 22, GCAP = 1 << 20;
static constexpr float SCALE = 0.08838834764831845f, LOG2E = 1.4426950408889634f;

struct Args {
    const bf16_t* x; const bf16_t* mem0; const bf16_t* Wq; const bf16_t* Wk; const bf16_t* Wv; const bf16_t* Wo;
    bf16_t* out; unsigned* ctl; unsigned char* ring; unsigned char* qkv; unsigned char* ox; unsigned char* part;
    const bf16_t* wr; const bf16_t* wg; const bf16_t* wu; const bf16_t* wd; const bf16_t* gw;
    unsigned* gate  ; unsigned char* ypart; unsigned* dbg;
    int d1, d2, per, dinit;
};

__device__ __forceinline__ unsigned cvt_pk(float lo, float hi) { v2f v = {lo, hi}; return __builtin_bit_cast(unsigned, __builtin_convertvector(v, bf16x2_t)); }
__device__ __forceinline__ float lo16(unsigned p) { return __uint_as_float(p << 16); }
__device__ __forceinline__ float hi16(unsigned p) { return __uint_as_float(p & 0xffff0000u); }
__device__ __forceinline__ float rbf(float f) { return lo16(cvt_pk(f, 0.f)); }
template <int C> __device__ __forceinline__ unsigned dppu(unsigned v) { return (unsigned)__builtin_amdgcn_update_dpp(0, (int)v, C, 0xf, 0xf, true); }
template <int C> __device__ __forceinline__ float dppf(float v) { return __uint_as_float(dppu<C>(__float_as_uint(v))); }
#define ROR8 0x128
#define HMIR 0x141
__device__ __forceinline__ v2u sw16(unsigned a, unsigned b) { auto r = __builtin_amdgcn_permlane16_swap(a, b, false, false); return v2u{r[0], r[1]}; }
__device__ __forceinline__ v2u sw32(unsigned a, unsigned b) { auto r = __builtin_amdgcn_permlane32_swap(a, b, false, false); return v2u{r[0], r[1]}; }
__device__ __forceinline__ float tadd16(float a, float b) { v2u r = sw16(__float_as_uint(a), __float_as_uint(b)); return __uint_as_float(r.x) + __uint_as_float(r.y); }
__device__ __forceinline__ float tadd32(float a, float b) { v2u r = sw32(__float_as_uint(a), __float_as_uint(b)); return __uint_as_float(r.x) + __uint_as_float(r.y); }
__device__ __forceinline__ float tmax16(float a) { v2u r = sw16(__float_as_uint(a), __float_as_uint(a)); return fmaxf(__uint_as_float(r.x), __uint_as_float(r.y)); }
__device__ __forceinline__ float tmax32(float a) { v2u r = sw32(__float_as_uint(a), __float_as_uint(a)); return fmaxf(__uint_as_float(r.x), __uint_as_float(r.y)); }
__device__ __forceinline__ unsigned umax3(unsigned a, unsigned b, unsigned c) { return max(max(a, b), c); }
__device__ __forceinline__ v4u pack_swap(frag_cd a, frag_cd b) { v2u x0 = sw16(cvt_pk(a[0], a[1]), cvt_pk(b[0], b[1])), x1 = sw16(cvt_pk(a[2], a[3]), cvt_pk(b[2], b[3])); return v4u{x0.x, x1.x, x0.y, x1.y}; }
#define MFMA(a, b, c) __builtin_amdgcn_mfma_f32_16x16x32_bf16(__builtin_bit_cast(v8bf, a), __builtin_bit_cast(v8bf, b), c, 0, 0, 0)
#define SMF(a, b, c, ix) __builtin_amdgcn_smfmac_f32_16x16x64_bf16(__builtin_bit_cast(v8bf, a), __builtin_bit_cast(v16bf, b), c, ix, 0, 0)
#define FENCE() asm volatile("" ::: "memory")
#define SB() __builtin_amdgcn_sched_barrier(0)
#define RFL(x) __builtin_amdgcn_readfirstlane(x)
#define BAR() do { FENCE(); __builtin_amdgcn_s_barrier(); FENCE(); } while (0)
#define WAITV(n) do { FENCE(); __builtin_amdgcn_s_waitcnt(0x0F70 | (n)); FENCE(); } while (0)
#define WAITL() asm volatile("s_waitcnt lgkmcnt(0)" ::: "memory")
__device__ __forceinline__ void STB(v4u v, rsrc_t rs, int off, int soff) { __builtin_amdgcn_raw_buffer_store_b128(v, rs, off, soff, 16); asm volatile("s_nop 1" :: "v"(v) : "memory"); }
template <class T> __device__ __forceinline__ LAS T* lp(unsigned a) { return (LAS T*)(size_t)a; }
__device__ __forceinline__ void glds(const void* g, unsigned voff, unsigned lds) { asm volatile("s_mov_b32 m0, %2\n\ts_nop 0\n\tglobal_load_lds_dwordx4 %0, %1" :: "v"(voff), "s"(g), "s"(lds) : "memory"); }
#define NTC " nt"
#define NTW " nt"
__device__ __forceinline__ void gldsc(const void* g, unsigned voff, unsigned lds) { asm volatile("s_mov_b32 m0, %2\n\ts_nop 0\n\tglobal_load_lds_dwordx4 %0, %1" NTC :: "v"(voff), "s"(g), "s"(lds) : "memory"); }
__device__ __forceinline__ void gldsw(const void* g, unsigned voff, unsigned lds) { asm volatile("s_mov_b32 m0, %2\n\ts_nop 0\n\tglobal_load_lds_dwordx4 %0, %1" NTW :: "v"(voff), "s"(g), "s"(lds) : "memory"); }
template <int N> __device__ __forceinline__ void glds1(const void* g, unsigned voff, unsigned lds) {
#define GL(o) "\n\tglobal_load_lds_dwordx4 %0, %1 offset:" #o " sc1"
    if constexpr (N == 1) asm volatile("s_mov_b32 m0, %2\n\ts_nop 0" GL(0) :: "v"(voff), "s"(g), "s"(lds) : "memory");
    if constexpr (N == 2) asm volatile("s_mov_b32 m0, %2\n\ts_nop 0" GL(0) GL(1024) :: "v"(voff), "s"(g), "s"(lds) : "memory");
    if constexpr (N == 4) asm volatile("s_mov_b32 m0, %2\n\ts_nop 0" GL(0) GL(1024) GL(2048) GL(3072) :: "v"(voff), "s"(g), "s"(lds) : "memory");
}
__device__ __forceinline__ rsrc_t mkrs(const void* p, int bytes) { return __builtin_amdgcn_make_buffer_rsrc((void*)p, 0, bytes, 0x00020000); }
__device__ __forceinline__ unsigned gld(const unsigned* p) { return __hip_atomic_load(p, __ATOMIC_RELAXED, __HIP_MEMORY_SCOPE_AGENT); }
__device__ __forceinline__ void gst(unsigned* p, unsigned v) { __hip_atomic_store(p, v, __ATOMIC_RELAXED, __HIP_MEMORY_SCOPE_AGENT); }
__device__ __forceinline__ void gst1(unsigned* p, unsigned v) { asm volatile("s_mov_b64 exec, 1\n\tglobal_store_dword %0, %1, %2 sc1\n\ts_mov_b64 exec, -1" :: "v"(0u), "v"(v), "s"(p) : "memory"); }
__device__ __forceinline__ unsigned rt16() { return (unsigned)__builtin_amdgcn_s_memrealtime() << 4; }

#define NSLOT 12
#define PT(k) do {} while (0)
#define PFLUSH() do {} while (0)
#define MARK(i) do {} while (0)
#define MARKC(i) do {} while (0)

#define H3X 0
#define EW 30
#define E0 32
#define E2 60
#define E3 120
#define AT 0
#define XH 0
#define XP 0
#define VR 0
#define DWM 4
#define VDEL1 15
#define AEARLY 1
#define XC 4
#define ODEL 1
#define SMQ 0x53b
#define SMV 0x79b
#define AL1 1
#define AL2 0
#define AL3 0
#define AL4 1
#define AL5 7
#define AL6 0
#define HB 0
#define HP 0
#define H3 0
#define HSM 0x2d
#define PF 22
#define US 1
#define ROT 0
#define VL 1
#define VP 0x3911
#define VE 0
#define VN3 36
#define CV 0
#define CD 60
#define XS1 0
#define XS2 6
#define PLO 0
#define MSP 1
#define VD 260
#define LK 300
#define LT 400
#define NS 16
#define GS 6
#define KC 560
#define GM 200
#define SGAP 60
#define HSL 6
#define CSL 3
#define WK 40
#define WKO 100
#define WB 200
#define WC1 0
#define WC2 64
#define WTHR 340
#define SLIP_THR 400
#define CKA 0.75
#define CKF 22.5
#define TG_THR 480
#define SLIP_ADD 640
#define SLIP_INC 10
__device__ __forceinline__ void endpgm() { asm volatile("s_endpgm" ::: "memory"); }
__device__ __forceinline__ void fatal(unsigned* err) { asm volatile("global_store_dword %0, %1, off sc1\n\ts_waitcnt vmcnt(0)\n\ts_endpgm" :: "v"(err), "v"(1u) : "memory"); }
__device__ __forceinline__ unsigned ldsr(unsigned p) { return __hip_atomic_load(lp<unsigned>(p), __ATOMIC_RELAXED, __HIP_MEMORY_SCOPE_WORKGROUP); }
__device__ __forceinline__ v2u trd(unsigned p) { return __builtin_bit_cast(v2u, __builtin_amdgcn_ds_read_tr16_b64_v4i16(lp<v4s_t>(p))); }
__device__ __forceinline__ v8u cat(v2u a, v2u b, v2u c, v2u d) { return v8u{a.x, a.y, b.x, b.y, c.x, c.y, d.x, d.y}; }
__device__ __forceinline__ v4u cat2(v2u a, v2u b) { return v4u{a.x, a.y, b.x, b.y}; }
template <int SL, int FAR = 0> __device__ __forceinline__ void flags_wait(unsigned& v, const unsigned* p, unsigned need, unsigned* err) {
    for (int i = 0; __builtin_amdgcn_ballot_w64(v < need) != 0; ++i) {
        if (i) { if (FAR && __builtin_amdgcn_ballot_w64(v + 1 < need) != 0) __builtin_amdgcn_s_sleep(FAR); __builtin_amdgcn_s_sleep(SL); }
        if (v < need) v = gld(p);
        if (i > GCAP) fatal(err); }
    FENCE();
}
template <int SL> __device__ __forceinline__ void flags_wait(const unsigned* p, int nw, unsigned need, int l, unsigned* err) { unsigned v = need - 1; flags_wait<SL>(v, p + min(l, nw - 1) * 32, need, err); }

__device__ __forceinline__ void gadd1(unsigned* p) { asm volatile("s_mov_b64 exec, 1\n\tglobal_atomic_add %0, %1, off sc1\n\ts_mov_b64 exec, -1" :: "v"(p), "v"(1u) : "memory"); }
__device__ __forceinline__ void gld_req(unsigned& v, const unsigned* p) { asm volatile("global_load_dword %0, %1, off sc1" : "+v"(v) : "v"(p) : "memory"); }
template <int N> __device__ __forceinline__ void gld_wait(unsigned& v) { asm volatile("s_waitcnt vmcnt(%1)" : "+v"(v) : "n"(N) : "memory"); }
__device__ __forceinline__ void ctl_wait(const Args& a, unsigned hb) {
    for (int i = 0; RFL(gld(a.ctl + C_HB)) == RFL(hb); ++i) { __builtin_amdgcn_s_sleep(8); if (i > GCAP) fatal(a.ctl + C_ERR); }
    FENCE();
}

typedef unsigned v16u __attribute__((ext_vector_type(16)));
typedef unsigned v32u __attribute__((ext_vector_type(32)));
#define STR_(x) #x
#define STR(x) STR_(x)
#define SET(x) "\n.set " #x ", " STR(x)
#define A_STAMPS "0"
#define A_BENCH "0"
static_assert(NH * 24576 == 0xC0000, "");
#define A_CHAIN ".set STAMPS, " A_STAMPS "\n.set BENCH, " A_BENCH "\n.set WB, " STR(WB) "\n.set WC1, " STR(WC1) "\n.set WC2, " STR(WC2) SET(SMQ) SET(SMV) SET(AL1) SET(AL2) SET(AL3) SET(AL4) SET(AL5) SET(AL6) SET(HB) SET(US) SET(PF) SET(HP) SET(H3) "\n.set HS, " STR(HSM) SET(VL) SET(VP) SET(VE) SET(VN3) SET(CV) SET(CD) SET(XS1) SET(XS2) SET(PLO) SET(CSL) SET(MSP) SET(VD) SET(LK) SET(LT) SET(NS) SET(GS) SET(GM) SET(SGAP) SET(VR) SET(DWM) SET(AT) SET(EW) SET(E0) SET(E2) SET(E3) SET(HSL) "\n.set WK, " STR(WK) "\n.set WN, 64\n.set WTHR, " STR(WTHR) "\n" R"(
.macro SLED n
  s_getpc_b64 vcc
4: s_sub_u32 s77, (3f-4b)/4, s77
  s_lshl2_add_u32 vcc_lo, s77, vcc_lo
  s_addc_u32 vcc_hi, vcc_hi, 0
  s_setpc_b64 vcc
.rept \n
  s_nop 1
.endr
3: s_waitcnt lgkmcnt(0)
.endm
.macro WAITC T, thr
  s_movk_i32 s76, 0x1000
1: s_memrealtime vcc
  s_memtime s[6:7]
  s_sub_u32 s76, s76, 1
  s_cbranch_scc1 3f
  s_waitcnt lgkmcnt(0)
  s_lshl_b32 s77, vcc_lo, 4
  s_sub_i32 s77, \T, s77
  s_cmp_ge_i32 s77, \thr
  s_cbranch_scc1 1b
.endm
.macro WAITT T
  WAITC \T, 64
3: s_waitcnt lgkmcnt(0)
.endm
.macro WAITP T
  WAITC \T, WTHR
  s_sub_i32 s77, s77, WC1
  s_cmp_lt_i32 s77, WB
  s_cbranch_scc1 5f
.irp r,30,32,34,36,38,94,96,98
  s_memrealtime s[\r:\r+1]
  s_nop 1
.endr
  s_memtime s[6:7]
  s_sub_i32 s77, \T, WC2
  s_sub_i32 s4, s4, EW
  s_waitcnt lgkmcnt(0)
  s_add_u32 s30, s30, s32
  s_add_u32 s34, s34, s36
  s_add_u32 s38, s38, s94
  s_add_u32 s96, s96, s98
  s_add_u32 s30, s30, s34
  s_add_u32 s38, s38, s96
  s_add_u32 s30, s30, s38
  s_lshl_b32 s30, s30, 1
  s_sub_i32 s77, s77, s30
5: s_max_i32 s77, s77, 0
  s_mulk_i32 s77, 45
  s_lshr_b32 s77, s77, 8
  s_sub_i32 s6, s4, s6
  s_ashr_i32 s6, s6, 3
  s_max_i32 s77, s77, s6
  s_min_u32 s77, s77, WN
  SLED WN
.endm
.macro WAITK x
  s_add_i32 s75, s92, s59
  s_movk_i32 s76, 0x1000
  s_add_i32 s75, s75, \x
1: s_memtime vcc
  s_sub_u32 s76, s76, 1
  s_cbranch_scc1 3f
  s_waitcnt lgkmcnt(0)
  s_sub_i32 s77, s75, vcc_lo
  s_cmp_ge_i32 s77, 8*WK
  s_cbranch_scc1 1b
  s_cmp_gt_i32 s77, -LK
  s_cbranch_scc1 2f
  s_sleep HSL
2: s_max_i32 s77, s77, 0
  s_lshr_b32 s77, s77, 3
  SLED WK
.endm
.macro H1ST
.if AT == 1
  buffer_atomic_swap v[RES], v[ST1], s[40:43], s66 offen sc1
.elseif AT == 2
  buffer_atomic_and v[RES], v[ST1], s[40:43], s66 offen sc1
.elseif AT == 3
  buffer_store_dword v[RES], v[ST1], s[40:43], s66 offen sc0 sc1
.elseif AT == 4
  buffer_store_dword v[RES], v[ST1], s[40:43], s66 offen sc1 nt
.else
  buffer_store_dword v[RES], v[ST1], s[40:43], s66 offen sc1
.endif
.endm
.macro ALN k
.p2align 6
.rept \k
  s_nop 0
.endr
.endm
.macro HT n, r
.rept \n
  v_mov_b32 v[\r], 0
  s_nop 1
.endr
.endm
.macro TMAX a, t
  v_readlane_b32 \t, v[\a], 0
.irp r,1,2,3
  v_readlane_b32 s77, v[\a], \r
  s_sub_i32 s77, s77, \t
  s_max_i32 s77, s77, 0
  s_add_i32 \t, \t, s77
.endr
.endm
.macro TGET a
  s_waitcnt vmcnt(0)
  v_mbcnt_lo_u32_b32 v[\a+1], 7, 0
  v_lshlrev_b32 v[\a+1], 7, v[\a+1]
  s_add_i32 s75, s88, s68
8: buffer_load_dword v[\a], v[\a+1], s[40:43], s75 offen sc1
  s_sub_u32 s71, s71, 1
  s_cbranch_scc1 L_fatal
  s_waitcnt vmcnt(0)
  v_cmp_eq_u32 vcc, -1, v[\a]
  s_cbranch_vccz 9f
  s_sleep 2
  s_branch 8b
9:
  TMAX \a, s75
  WAITT s75
  s_mov_b32 s76, 0x400000
.endm
.macro PLAN
  s_mulk_i32 s74, PF
  s_ashr_i32 s74, s74, 3
  s_add_i32 s73, s73, s58
  s_add_i32 s73, s73, s74
.endm
.macro STAMP k
.if STAMPS && ((SMK >> \k) & 1)
  s_memtime s[8+2*\k:9+2*\k]
.endif
.endm
.macro NEXT t
.if STAMPS
  s_waitcnt lgkmcnt(0)
.irp k,0,1,2,3,4,5,6,7,8,9,10
  v_writelane_b32 v[\t], s[8+2*\k], \k
.endr
  s_lshl_b32 s76, s64, 3
  s_add_i32 s76, s76, s63
  s_mul_i32 s76, s76, 48
  v_mbcnt_lo_u32_b32 v[\t+1], -1, 0
  v_lshl_add_u32 v[\t+1], v[\t+1], 2, s76
  s_mov_b64 exec, 0x7ff
  ds_write_b32 v[\t+1], v[\t]
  s_mov_b64 exec, -1
.endif
  s_lshl_b32 s72, s64, 18
  s_mov_b32 s64, s65
  s_add_i32 s65, s65, 1
  s_xor_b32 s66, s66, 0x8000
  s_xor_b32 s67, s67, 0x8000
  s_mov_b32 s76, s68
  s_mov_b32 s68, s69
  s_mov_b32 s69, s70
  s_mov_b32 s70, s76
.endm
.macro RETRY c
.if BENCH
  s_bitset1_b64 s[80+2*\c:81+2*\c], s64
.endif
  s_sub_u32 s71, s71, 1
  s_cbranch_scc1 L_fatal
  s_sleep 2
.endm
.macro LD d, a, so, o
  buffer_load_dwordx4 v[\d:\d+3], v[\a], s[40:43], \so offen offset:\o sc1
.endm
.macro SM d, a, b
  v_smfmac_f32_16x16x64_bf16 v[\d:\d+3], v[\a:\a+3], v[\b:\b+7], v[SIX]
.endm
.macro MX0 a
  v_max_u32 v[MX], v[\a], v[\a+1]
  v_max3_u32 v[MX], v[MX], v[\a+2], v[\a+3]
.endm
.macro MX1 a
  v_max3_u32 v[MX], v[MX], v[\a], v[\a+1]
  v_max3_u32 v[MX], v[MX], v[\a+2], v[\a+3]
.endm
.macro AW r, k
  ds_write_addtid_b32 v[\r] offset:63744+256*\k
.endm
.macro HS a
  v_pk_add_f32 v[\a:\a+1], v[\a:\a+1], v[\a+2:\a+3]
.endm
.macro ZR a, n
.set zi, 0
.rept \n
  v_mov_b64 v[\a+zi:\a+zi+1], 0
.set zi, zi+2
.endr
.endm
.macro PRE
  s_mov_b32 s64, 63
  s_mov_b32 s65, 0
  s_mov_b32 s66, 0x8000
  s_mov_b32 s67, 0
  s_mov_b32 s68, 0x8000
  s_mov_b32 s69, 0x10000
  s_mov_b32 s70, 0
.endm
.macro XSL r
  s_and_b32 s75, \r, 3
  s_mulk_i32 s75, 20480
.endm
.macro CAP
  s_sub_u32 s76, s76, 1
  s_cbranch_scc1 L_fatal
.endm
  s_mov_b32 s71, 0x40000
  s_mov_b32 s88, s64
  s_mov_b32 s90, s65
  s_mov_b32 s91, s66
  s_mov_b64 s[80:81], 0
  s_mov_b64 s[82:83], 0
  s_mov_b64 s[84:85], 0
  s_mov_b64 s[86:87], 0
  s_mov_b32 s100, s67
  s_mov_b32 s101, s68
  s_cmp_gt_u32 s63, 3
  s_cbranch_scc1 LV_init
; ======== waves 0..3
.set SMK, SMQ
.set O3,168
.set ST1,169
.set RW1,170
.set RW2,171
.set RW3,172
.set XA,173
.set FW,174
.set FR,175
.set SIX,176
.set STK,177
.set FL,183
.set MB,128
.set AC,144
.set TAG,160
.set MX,161
.set RES,162
.set FA,163
.set FB,164
.set VA,164
.set VB,184
.set WV,188
.set RA,134
.set KX,196
.set XF,177
.set XC,)" STR(XC) R"(
.macro MEMQ
  LD MB, O3, s96, 0
  LD MB+4, O3, s97, 0
  LD MB+8, O3, s98, 0
  LD MB+12, O3, s99, 0
.endm
.macro XCHK R
  s_add_i32 s75, s64, 2
  s_min_u32 s75, s75, 63
  v_cmp_ge_u32 vcc, s75, v[FL]
  s_cbranch_vccnz LX_w\R
LX_g\R:
  s_add_i32 s75, s64, 2
  XSL s75
  s_add_i32 m0, s75, s61
.endm
.macro XL4 a
.irp o,0,1024,2048,3072
  global_load_lds_dwordx4 v[\a], s[52:53] offset:\o sc1
.endr
  s_add_i32 m0, m0, 0x1000
.endm
.macro XREF R
  s_add_i32 s75, s64, 3
  s_min_u32 s75, s75, 63
  v_cmp_ge_u32 vcc, s75, v[FL]
  s_cbranch_vccz LX_n\R
  global_load_dword v[FL], v[XF], s[54:55] sc1
LX_n\R:
.endm
.macro XADV
  s_cmp_lt_u32 s64, 61
  s_cselect_b32 s75, 0xC0000, 0
  s_add_u32 s52, s52, s75
  s_addc_u32 s53, s53, 0
.endm
.macro XWAIT R
LX_w\R:
  s_mov_b32 s76, 0x100000
1: global_load_dword v[FL], v[XF], s[54:55] sc1
  s_waitcnt vmcnt(0)
  v_cmp_ge_u32 vcc, s75, v[FL]
  s_cbranch_vccz LX_g\R
  s_sleep 4
  CAP
  s_branch 1b
.endm
.macro OXST
  buffer_store_dwordx4 v[KX:KX+3], v[STK], s[44:47], s72 offen sc1
  buffer_store_dwordx4 v[KX+4:KX+7], v[STK], s[44:47], s72 offen offset:1024 sc1
.endm
.macro FLAGQ val
  v_mov_b32 v[MX], 0
  v_mov_b32 v[FA], \val
  s_mov_b64 exec, 1
  global_store_dword v[MX], v[FA], s[50:51] sc1
  s_mov_b64 exec, -1
.endm
.macro SMW i
  SM VA, MB+4*\i, WV+8*\i
  SM VB, MB+4*\i, WV+32+8*\i
.endm
.macro SMN k
.if (VP >> \k) & 1
  SM VA+(VB-VA)*(vn&1), MB+4*(vn>>1), WV+32*(vn&1)+8*(vn>>1)
.set vn, vn+1
.endif
.endm
.macro VOUT
  v_add_f32 v[VA], v[VA], v[VA+2]
  v_add_f32 v[VA+1], v[VA+1], v[VA+3]
  v_add_f32 v[VB], v[VB], v[VB+2]
  v_add_f32 v[VB+1], v[VB+1], v[VB+3]
  AW VA, 0
  AW VA+1, 2
  AW VB, 1
  AW VB+1, 3
  AW TAG, 6
.endm
.macro DM k
.if (HS >> \k) & 1
  v_mfma_f32_16x16x32_bf16 v[VA:VA+3], v[MB:MB+3], v[MB:MB+3], 0
.endif
.endm
.macro ZA
  ZR AC, 8
  ZR VA, 2
  ZR VB, 2
.endm
.macro PHASEA R
LQ_loop\R:
  STAMP 0
  ZA
  v_mov_b32 v[TAG], s65
LQ_bar\R:
  s_or_b32 s96, s68, s56
  s_xor_b32 s97, s96, 0x400
  s_xor_b32 s98, s96, 0x800
  s_xor_b32 s99, s96, 0xc00
  s_barrier
  STAMP 1
.if MSP
  LD MB, O3, s96, 0
  s_memtime s[92:93]
  s_nop MSP-1
  LD MB+4, O3, s97, 0
  s_mov_b32 m0, s100
  s_nop MSP-1
  LD MB+8, O3, s98, 0
  s_nop MSP
  LD MB+12, O3, s99, 0
.else
  s_memtime s[92:93]
  MEMQ
  s_mov_b32 m0, s100
.endif
.set XS, 0
.if \R == 2
.set XS, 2
.irp i,0,1,4,5,6,7,8,9,10,11,12,13,14,15
  ds_read_b128 v[WV+4*\i:WV+4*\i+3], v[XA] offset:4096+1024*\i
.if \i == 9
  s_waitcnt lgkmcnt(6)
.endif
.endr
.rept )" STR(ODEL) R"(
  s_nop 7
.endr
  OXST
  s_nop 1
.irp i,2,3
  ds_read_b128 v[WV+4*\i:WV+4*\i+3], v[XA] offset:4096+1024*\i
.endr
  s_waitcnt lgkmcnt(0)
.endif
  s_mov_b32 s76, 0x400000
  ALN AL1
LQ_a\R:
  s_waitcnt vmcnt(3+XS)
  SM AC+8, MB, 64
  v_max_u32 v[MX], v[MB], v[MB+1]
  SM AC+12, MB, 96
  v_max3_u32 v[MX], v[MX], v[MB+2], v[MB+3]
  SM AC, MB, 0
  SM AC+4, MB, 32
  SMW 0
.irp i,1,2,3
  s_waitcnt vmcnt(3+XS-\i)
  SM AC+8, MB+4*\i, 64+8*\i
  v_max3_u32 v[MX], v[MX], v[MB+4*\i], v[MB+4*\i+1]
  SM AC+12, MB+4*\i, 96+8*\i
  v_max3_u32 v[MX], v[MX], v[MB+4*\i+2], v[MB+4*\i+3]
  SM AC, MB+4*\i, 8*\i
.if \i == 3
  v_cmp_eq_u32 vcc, -1, v[MX]
.endif
  SM AC+4, MB+4*\i, 32+8*\i
.if \i < VL
  SMW \i
.endif
.endr
  STAMP 2
  v_add_f32 v[AC+8], v[AC+8], v[AC+10]
  v_add_f32 v[AC+12], v[AC+12], v[AC+14]
  v_add_f32 v[AC+9], v[AC+9], v[AC+11]
  v_add_f32 v[AC+13], v[AC+13], v[AC+15]
  v_add_f32 v[AC+3], v[AC+1], v[AC+3]
  v_add_f32 v[AC], v[AC], v[AC+2]
  v_add_f32 v[AC+5], v[AC+5], v[AC+7]
  s_cbranch_vccnz LQ_ra\R
.set vn, 2*VL
  AW AC+8, 0
  SMN 0
  AW AC+12, 1
  SMN 1
  AW AC+9, 2
  SMN 2
  AW AC+13, 3
  SMN 3
  AW AC+3, 4
  SMN 4
  AW AC+5, 5
  SMN 5
  AW TAG, 6
  SMN 6
  STAMP 6
  s_barrier
  SMN 7
  ds_read_b32 v[FA], v[FR]
  SMN 8
  ds_read2st64_b32 v[AC+8:AC+9], v[RW1] offset1:1
  SMN 9
  ds_read2st64_b32 v[AC+10:AC+11], v[RW2] offset1:1
  SMN 10
  ds_read2st64_b32 v[AC+12:AC+13], v[RW3] offset1:1
  SMN 11
  s_add_i32 m0, s100, 0x2000
  v_add_f32 v[AC+1], v[AC+4], v[AC+6]
  SMN 12
  SMN 13
  SMN 14
.if vn != 8
.error "v projection incomplete"
.endif
.if VE
  VOUT
.endif
  s_waitcnt lgkmcnt(5*VE)
LQ_p\R:
  v_cmp_gt_u32 vcc, s65, v[FA]
  v_add_f32 v[AC+8], v[AC+8], v[AC]
  v_add_f32 v[AC+9], v[AC+9], v[AC+1]
  v_add_f32 v[AC+8], v[AC+8], v[AC+10]
  v_add_f32 v[AC+9], v[AC+9], v[AC+11]
  v_add_f32 v[AC+8], v[AC+8], v[AC+12]
  v_add_f32 v[AC+9], v[AC+9], v[AC+13]
  v_cvt_pk_bf16_f32 v[RES], v[AC+8], v[AC+9]
  s_cbranch_vccnz LQ_pb\R
  H1ST
  STAMP 3
.if !VE
  VOUT
.endif
.if \R != 2
  s_cmp_eq_u32 s64, 63
  s_cbranch_scc1 L_last
.endif
.if \R != 3
  WAITK 0
.endif
  STAMP 4
.endm
.macro COLDA R
LQ_pb\R:
.if BENCH && \R != 3
  s_bitset1_b64 s[86:87], s64
.endif
  CAP
  ds_read_b32 v[FA], v[FR]
  ds_read2st64_b32 v[AC+8:AC+9], v[RW1] offset1:1
  ds_read2st64_b32 v[AC+10:AC+11], v[RW2] offset1:1
  ds_read2st64_b32 v[AC+12:AC+13], v[RW3] offset1:1
  s_waitcnt lgkmcnt(0)
  s_branch LQ_p\R
LQ_ra\R:
  RETRY 2
  TGET RES
  MEMQ
  ZA
  s_waitcnt vmcnt(0)
  s_branch LQ_a\R
.endm
  s_mov_b32 s64, 0
  s_mov_b32 s65, 1
  s_mov_b32 s66, 0
  s_mov_b32 s67, 0x8000
  s_mov_b32 s68, 0x10000
  s_mov_b32 s69, 0
  s_mov_b32 s70, 0x8000
  s_mov_b32 s72, 0xFC0000
  s_setprio 3
  s_cmp_eq_u32 s60, 2
  s_cbranch_scc1 LQ_init2
  s_cmp_eq_u32 s60, 3
  s_cbranch_scc1 LQ_init3
; ---- waves 0,1: scores against the x rows
  PHASEA 0
  LD MB, 178, s66, 0
  LD MB+4, 178, s66, 1024
  XSL s64
  v_add_u32 v[FA], s75, v[179]
  ds_read_b128 v[MB+8:MB+11], v[FA]
  ds_read_b128 v[MB+12:MB+15], v[FA] offset:1024
  ds_read_b128 v[AC+8:AC+11], v[FA] offset:2048
  ds_read_b128 v[AC+12:AC+15], v[FA] offset:3072
  s_waitcnt lgkmcnt(0)
  ALN AL2
LQ_b0:
  s_waitcnt vmcnt(1)
  v_mfma_f32_16x16x32_bf16 v[AC+4:AC+7], v[MB+12:MB+15], v[MB:MB+3], 0
  MX0 MB
  v_mfma_f32_16x16x32_bf16 v[AC:AC+3], v[MB+8:MB+11], v[MB:MB+3], 0
  s_waitcnt vmcnt(0)
  v_mfma_f32_16x16x32_bf16 v[AC+4:AC+7], v[AC+12:AC+15], v[MB+4:MB+7], v[AC+4:AC+7]
  MX1 MB+4
  v_mfma_f32_16x16x32_bf16 v[AC:AC+3], v[AC+8:AC+11], v[MB+4:MB+7], v[AC:AC+3]
  v_cmp_eq_u32 vcc, -1, v[MX]
  s_nop 5
  s_cbranch_vccnz LQ_rb0
.irp e,0,1,2,3
  v_add_f32_dpp v[AC+\e], v[AC+4+\e], v[AC+\e] quad_perm:[2,3,0,1] row_mask:0xf bank_mask:0xf
.endr
  DM 0
  ds_write_b128 v[180], v[AC:AC+3]
  DM 1
  ds_write_b32 v[FW], v[TAG] offset:2048
  DM 2
  s_barrier
  STAMP 5
  s_setprio PLO
  HT HP, VB
.if XS1
  s_sleep XS1
.endif
  XCHK 0
  s_nop 0
  XL4 181
.if XC == 4
  s_nop 0
  XL4 182
.endif
  XREF 0
.if XS2
  s_sleep XS2
.endif
  XADV
  v_mov_b32 v[FA], s62
  ds_read_b32 v[RA], v[FA] offset:8
  s_waitcnt lgkmcnt(0)
  v_readfirstlane_b32 s59, v[RA]
  NEXT MB
  s_setprio 3
  s_branch LQ_loop0
  COLDA 0
LQ_rb0:
  RETRY 0
  LD MB, 178, s66, 0
  LD MB+4, 178, s66, 1024
  s_branch LQ_b0
  XWAIT 0
; ---- wave 2: scores against the memory rows; attention of 16 x rows (64 columns)
.set AQ,181
.set AVF,182
.set AVM,183
.set RA,184
.set QX,196
.set OA,212
.set MSK,228
.set LX,237
.set PB,238
.set S1,240
.set K7,244
.set P4,244
.macro MF d, a, b, c
  v_mfma_f32_16x16x32_bf16 v[\d:\d+3], v[\a:\a+3], v[\b:\b+3], \c
.endm
.macro XQ op, r
  v_mov_b32 v[FB], v[\r]
  s_nop 1
  v_permlane16_swap_b32 v[\r], v[FB]
  \op v[\r], v[\r], v[FB]
  v_mov_b32 v[FB], v[\r]
  s_nop 1
  v_permlane32_swap_b32 v[\r], v[FB]
  \op v[\r], v[\r], v[FB]
.endm
.macro QKQ
  LD MB, 178, s66, 0
  LD MB+4, 179, s66, 0
  LD MB+8, 178, s66, 1024
  LD MB+12, 179, s66, 1024
.endm
.macro SOFTX
  v_add_f32 v[AC+6], v[AC+4], v[S1+2]
  v_add_f32 v[AC+7], v[AC+5], v[S1+3]
  v_max3_f32 v[MX], v[LX], v[AC+6], v[AC+7]
  XQ v_max_f32, MX
  v_mul_f32 v[MX], s74, v[MX]
.irp i,6,7,8,9,10,11,12,13,14,15
  v_fma_f32 v[AC+\i], v[AC+\i], s73, v[MX]
.endr
.irp i,6,7,8,9,10,11,12,13,14,15
  v_exp_f32 v[AC+\i], v[AC+\i]
.endr
  v_add_f32 v[LX], v[AC+6], v[AC+7]
.irp i,8,9,10,11,12,13,14,15
  v_add_f32 v[LX], v[LX], v[AC+\i]
.endr
  XQ v_add_f32, LX
  v_rcp_f32 v[LX], v[LX]
  s_nop 0
.irp i,6,7,8,9,10,11,12,13,14,15
  v_mul_f32 v[AC+\i], v[AC+\i], v[LX]
.endr
.irp i,0,1,2,3
  v_cvt_pk_bf16_f32 v[P4+\i], v[AC+8+2*\i], v[AC+9+2*\i]
.endr
  v_cvt_pk_bf16_f32 v[PB], v[AC+6], v[AC+7]
.endm
LQ_init2:
  s_mov_b32 s73, 0x3e0293ee
  s_mov_b32 s74, 0xbe0293ee
  PHASEA 2
  QKQ
  XSL s64
  ds_read_b128 v[MSK:MSK+3], v[XA]
  ds_read_b128 v[MSK+4:MSK+7], v[XA] offset:1024
  v_add_u32 v[FB], s75, v[AQ]
  v_add_u32 v[RES], s75, v[AVF]
  v_mov_b32 v[PB+1], 0
.irp i,0,1,2,3
  ds_read_b128 v[QX+4*\i:QX+4*\i+3], v[FB] offset:16384+1024*\i
  ds_read_b128 v[OA+4*\i:OA+4*\i+3], v[FB] offset:1024*\i
.endr
.irp i,0,1,2
  ds_read_b128 v[RA+4*\i:RA+4*\i+3], v[FB] offset:4096+1024*\i
.endr
  ds_read_b128 v[K7:K7+3], v[FB] offset:7168
  s_waitcnt lgkmcnt(10)
  MF AC+8, OA, QX, v[MSK:MSK+3]
.irp i,4,8,12
  s_waitcnt lgkmcnt(10-\i/2)
  MF AC+8, OA+\i, QX+\i, v[AC+8:AC+11]
.endr
  s_waitcnt lgkmcnt(0)
  MF AC+12, RA, QX, v[MSK+4:MSK+7]
  MF AC+12, RA+4, QX+4, v[AC+12:AC+15]
  MF AC+12, RA+8, QX+8, v[AC+12:AC+15]
  MF AC+12, K7, QX+12, v[AC+12:AC+15]
  v_max3_f32 v[LX], v[AC+8], v[AC+9], v[AC+10]
  s_nop 6
  v_max3_f32 v[LX], v[LX], v[AC+11], v[AC+12]
  v_max3_f32 v[LX], v[LX], v[AC+13], v[AC+14]
  v_max_f32 v[LX], v[LX], v[AC+15]
  STAMP 10
  ALN AL3
LQ_b2:
  s_waitcnt vmcnt(3)
  MX0 MB
  s_waitcnt vmcnt(2)
  v_mfma_f32_16x16x32_bf16 v[AC:AC+3], v[MB+4:MB+7], v[MB:MB+3], 0
  MX1 MB+4
  s_waitcnt vmcnt(1)
  MX1 MB+8
  s_waitcnt vmcnt(0)
  v_mfma_f32_16x16x32_bf16 v[AC:AC+3], v[MB+12:MB+15], v[MB+8:MB+11], v[AC:AC+3]
  MX1 MB+12
  v_cmp_eq_u32 vcc, -1, v[MX]
  s_nop 3
  s_cbranch_vccnz LQ_rb2
  v_add_f32_dpp v[AC], v[AC+2], v[AC] quad_perm:[2,3,0,1] row_mask:0xf bank_mask:0xf
  v_add_f32_dpp v[AC+1], v[AC+3], v[AC+1] quad_perm:[2,3,0,1] row_mask:0xf bank_mask:0xf
  DM 3
  ds_write_b64 v[180], v[AC:AC+1]
  DM 4
  ds_write_b32 v[FW], v[TAG] offset:2048
  DM 5
  s_barrier
  STAMP 5
  s_setprio 0
  MF AC+4, MB+4, QX, 0
  MF S1, MB+4, QX+4, 0
  MF AC+4, MB+12, QX+8, v[AC+4:AC+7]
  MF S1, MB+12, QX+12, v[S1:S1+3]
  FLAGQ s64
.irp e,0,1,2,3
  ds_read_b64_tr_b16 v[MB+4*\e:MB+4*\e+1], v[RES] offset:512*\e
  ds_read_b64_tr_b16 v[MB+4*\e+2:MB+4*\e+3], v[RES] offset:512*\e+4096
.endr
.if )" STR(AEARLY) R"(
  SOFTX
.endif
  v_mov_b32 v[FA], s62
  ds_read_b32 v[FB], v[FA] offset:8
  s_mov_b32 s76, 0x400000
LA_w:
  ds_read_b32 v[FA], v[FW] offset:2304
  CAP
  s_waitcnt lgkmcnt(0)
  v_cmp_gt_u32 vcc, s65, v[FA]
  s_cbranch_vccz LA_g
  s_sleep 1
  s_branch LA_w
LA_g:
  STAMP 8
  v_readfirstlane_b32 s59, v[FB]
  s_setprio 2
.irp e,0,1,2,3
  ds_read_b64_tr_b16 v[RA+2*\e:RA+2*\e+1], v[AVM] offset:256*\e
.endr
.if !)" STR(AEARLY) R"(
  SOFTX
  s_nop 0
.endif
.irp e,0,4,8,12
  MF OA+\e, MB+\e, P4, 0
.endr
  s_waitcnt lgkmcnt(0)
.irp e,0,1,2,3
  v_mfma_f32_16x16x16_bf16 v[OA+4*\e:OA+4*\e+3], v[RA+2*\e:RA+2*\e+1], v[PB:PB+1], v[OA+4*\e:OA+4*\e+3]
.endr
  s_nop 1
.irp i,0,1,2,3,4,5,6,7
  v_cvt_pk_bf16_f32 v[KX+\i], v[OA+2*\i], v[OA+2*\i+1]
.endr
.irp i,0,1,4,5
  v_permlane16_swap_b32 v[KX+\i], v[KX+\i+2]
.endr
  STAMP 9
  s_setprio 3
  NEXT MB
  s_cmp_lg_u32 s64, 64
  s_cbranch_scc1 LQ_loop2
  OXST
  s_waitcnt vmcnt(0)
  FLAGQ 64
  s_branch L_end
  COLDA 2
LQ_rb2:
  RETRY 0
  QKQ
  s_branch LQ_b2
; ---- wave 3: pacing, clearing, sum over the heads
.set CK,144
.set RA,162
.set RC,184
.macro PARTQ
  LD MB, 178, s66, 0
  LD MB+4, 178, s66, 1024
  LD MB+8, 178, s66, 2048
  LD MB+12, 178, s66, 3072
.endm
LQ_init3:
  s_memrealtime vcc
  s_mov_b64 s[44:45], 0
  s_mov_b32 s46, s62
  s_mov_b32 s79, NS*GS
  s_mov_b32 s62, s101
  s_waitcnt lgkmcnt(0)
  s_lshl_b32 s73, vcc_lo, 4
  PHASEA 3
  s_setprio PLO
  v_mov_b32 v[MB+4], s46
  s_memtime s[96:97]
  s_memrealtime s[94:95]
  ds_read_b128 v[VA:VA+3], v[XA] offset:8192
  v_mov_b64 v[MB:MB+1], -1
  v_mov_b64 v[MB+2:MB+3], -1
  v_rcp_f32 v[CK+6], s45
  v_mov_b64 v[CK:CK+1], s[44:45]
  v_cvt_f32_u32 v[CK+4], s57
  v_mul_f32 v[CK+6], s44, v[CK+6]
  s_waitcnt lgkmcnt(0)
  s_sub_u32 s76, s96, s90
  s_sub_u32 s77, s94, s91
  s_mov_b32 s90, s96
  s_mov_b32 s91, s94
  v_mul_f32 v[CK+6], 256.0, v[CK+6]
  v_cvt_f32_u32 v[CK+2], s76
  v_cvt_f32_u32 v[CK+3], s77
  v_cvt_i32_f32 v[CK+6], v[CK+6]
  v_fmac_f32 v[CK+2], )" STR(CKA) R"(, v[CK]
  v_fmac_f32 v[CK+3], )" STR(CKA) R"(, v[CK+1]
  v_rcp_f32 v[CK+5], v[CK+2]
  v_readfirstlane_b32 s51, v[CK+6]
  v_readfirstlane_b32 s44, v[CK+2]
  v_readfirstlane_b32 s45, v[CK+3]
  v_mul_f32 v[CK+3], )" STR(CKF) R"(, v[CK+3]
  v_fma_f32 v[CK+3], v[CK+3], v[CK+5], -1.0
  v_med3_f32 v[CK+3], v[CK+3], -0.5, 1.0
  v_mul_f32 v[CK+3], v[CK+3], v[CK+4]
  v_cvt_i32_f32 v[CK+3], v[CK+3]
  STAMP 7
  s_nop 0
  v_readfirstlane_b32 s47, v[CK+3]
  WAITK VD+CD
  buffer_store_dwordx4 v[MB:MB+3], v[179], s[40:43], s67 offen sc1
  buffer_store_dwordx2 v[MB:MB+1], v[182], s[40:43], s67 offen sc1
  buffer_store_dwordx2 v[MB:MB+1], v[180], s[40:43], s70 offen sc1
  buffer_store_dwordx2 v[MB:MB+1], v[181], s[40:43], s70 offen sc1
  ds_read_b128 v[RC:RC+3], v[MB+4]
  ds_read_b32 v[RA], v[MB+4] offset:24
  XCHK 3
  XREF 3
  STAMP 5
  s_barrier
  s_memrealtime s[94:95]
  s_mov_b32 s76, 0x400000
  s_mov_b32 s78, s73
LQ_t3:
  s_waitcnt lgkmcnt(0)
  v_cmp_gt_u32 vcc, s65, v[RC+3]
  s_cbranch_vccnz LQ_t4
  v_readfirstlane_b32 s73, v[RC]
  v_readfirstlane_b32 s74, v[RC+1]
  v_readfirstlane_b32 s59, v[RC+2]
  v_readfirstlane_b32 s92, v[RA]
  s_sub_i32 s78, s73, s78
  s_cmp_gt_i32 s78, 512
  s_cbranch_scc1 LQ_slip
LQ_b5:
  s_lshl1_add_u32 s4, s74, s57
  s_add_i32 s50, s4, s73
  PLAN
  s_sub_i32 s5, s73, s50
  s_mul_i32 s4, s4, s51
  s_mul_i32 s5, s5, s51
  s_lshr_b32 s4, s4, 12
  s_lshr_b32 s5, s5, 12
  s_add_i32 s4, s4, s92
  s_sub_i32 s4, s4, E2
  s_sub_i32 s5, s5, E3
  s_cmp_lt_u32 s64, NS
  s_cselect_b32 s78, GS, 0
  s_sub_u32 s79, s79, s78
  s_ashr_i32 s47, s47, 1
  s_add_i32 s47, s47, s79
.if CSL
  s_sleep CSL
.endif
  s_lshl4_add_u32 s78, s94, s62
  s_sub_i32 s78, s78, s50
  s_cmp_gt_i32 s78, LT
  s_cselect_b32 s78, s78, -GM
  s_add_i32 s78, s78, s50
  s_add_i32 s78, s78, GM
  s_setprio 3
  WAITP s78
  STAMP 8
  PARTQ
  s_memrealtime vcc
  s_memtime s[6:7]
  v_mov_b32 v[RA], s46
  XL4 XA
  XADV
  ZR AC, 8
  v_add_u32 v[TAG], 1, v[TAG]
  s_waitcnt lgkmcnt(0)
  s_add_i32 s6, s6, s5
  v_mov_b32 v[RA+1], s6
  ds_write_b32 v[RA], v[RA+1] offset:20
LQ_b3:
  s_waitcnt lgkmcnt(0)
.if BENCH
  s_mov_b32 m0, s64
  s_mov_b32 s77, vcc_lo
  v_writelane_b32 v[FW], s77, m0
.endif
  s_lshl_b32 s89, vcc_lo, 4
  s_sub_i32 s89, s89, s50
  s_add_i32 s78, s89, )" STR(SLIP_ADD) R"(
  s_sub_i32 s77, s89, E0
  s_max_i32 s77, s77, 0
  s_cmp_gt_i32 s89, )" STR(SLIP_THR) R"(
  s_cselect_b32 s78, s78, s77
  s_cselect_b32 s77, SGAP, 0
  s_add_i32 s78, s78, s73
  s_and_b32 s78, s78, -2
  s_add_i32 s77, s77, s47
  v_mov_b32 v[RA], s78
  v_mov_b32 v[RA+1], s77
  ALN AL4
  s_waitcnt vmcnt(3+XC)
  v_mfma_f32_16x16x32_bf16 v[RC:RC+3], v[VA:VA+3], v[MB:MB+3], 0
  MX0 MB
.irp i,1,2,3
  s_waitcnt vmcnt(3+XC-\i)
  v_mfma_f32_16x16x32_bf16 v[RC:RC+3], v[VA:VA+3], v[MB+4*\i:MB+4*\i+3], v[RC:RC+3]
  MX1 MB+4*\i
.endr
  v_cmp_eq_u32 vcc, -1, v[MX]
  STAMP 9
  s_nop 3
.irp e,0,1,2,3
  v_add_f32_dpp v[RC+\e], v[RC+\e], v[RC+\e] row_ror:8 row_mask:0xf bank_mask:0xf
.endr
  v_cvt_pk_bf16_f32 v[RC], v[RC], v[RC+1]
  v_cvt_pk_bf16_f32 v[RC+1], v[RC+2], v[RC+3]
  s_cbranch_vccnz LQ_rb3
  buffer_store_dwordx2 v[RC:RC+1], v[180], s[40:43], s69 offen sc1
  buffer_store_dwordx2 v[RA:RA+1], v[181], s[40:43], s69 offen sc1
  STAMP 10
  ZR VA, 2
  ZR VB, 2
  NEXT MB
  STAMP 0
  s_cmp_gt_i32 s89, )" STR(TG_THR) R"(
  s_cbranch_scc0 LQ_bar3
  TGET RA
  s_branch LQ_bar3
  COLDA 3
  XWAIT 3
LQ_rb3:
  RETRY 1
  PARTQ
  s_memrealtime vcc
  s_waitcnt vmcnt(0)
  s_branch LQ_b3
LQ_t4:
  ds_read_b128 v[RC:RC+3], v[MB+4]
  CAP
  s_branch LQ_t3
LQ_slip:
.if BENCH
  s_bitset1_b64 s[86:87], s64
.endif
  s_cmp_lt_u32 s64, NS
  s_cbranch_scc1 LQ_b5
  s_add_i32 s79, s79, )" STR(SLIP_INC) R"(
  s_min_u32 s79, s79, 4000
  s_branch LQ_b5
; ======== waves 4..7
.set SMK, SMV
.set UB,128
.set B1,160
.set O3,168
.set O1,169
.set OT,170
.set ST1,171
.set ST2,172
.set AIM,173
.set RR,175
.set FW,176
.set FR,177
.set FS,178
.set ASC,179
.set ASM,180
.set AVX,181
.set SIX,182
.set RES,184
.set TAG,185
.set MX,186
.set TM,187
.set MB,188
.set AC,204
.set TV,212
.set FA,214
.set RA,216
.set DD,188
.set SX,208
.set SMM,212
.set ZZ,224
.set VM,240
.set PA,248
.set VX,188
.set UU,220
.macro SCV
  ds_read_b32 v[FA], v[FS]
  ds_read_b128 v[SX:SX+3], v[ASC]
  ds_read_b64 v[SMM:SMM+1], v[ASM]
.endm
.macro CZV t
  v_cvt_pk_bf16_f32 v[UB+8*\t+2], v[ZZ+4*\t], v[ZZ+4*\t+2]
  v_cvt_pk_bf16_f32 v[UB+8*\t+3], v[ZZ+4*\t+1], v[ZZ+4*\t+3]
.endm
.macro URD h, r
.irp k,0,1,2,3
  ds_read_b128 v[VX+\r+4*\k:VX+\r+4*\k+3], v[TM] offset:1024*(\h+\k)
.endr
.endm
.macro UMM h, r, n
.irp k,0,1,2,3
  s_waitcnt lgkmcnt(\n+3-\k)
.irp t,0,1,2,3
.if \k == 0
  v_mfma_f32_16x16x32_bf16 v[UU+\r+4*\t:UU+\r+4*\t+3], v[VX+\r:VX+\r+3], v[16*\t:16*\t+3], 0
.else
  v_mfma_f32_16x16x32_bf16 v[UU+\r+4*\t:UU+\r+4*\t+3], v[VX+\r+4*\k:VX+\r+4*\k+3], v[16*\t+4*\k:16*\t+4*\k+3], v[UU+\r+4*\t:UU+\r+4*\t+3]
.endif
.endr
.endr
  s_nop 1
.irp t,0,1,2,3
  v_cvt_pk_bf16_f32 v[UB+8*\t+\h], v[UU+\r+4*\t], v[UU+\r+4*\t+1]
  v_cvt_pk_bf16_f32 v[UB+8*\t+\h+1], v[UU+\r+4*\t+2], v[UU+\r+4*\t+3]
.endr
.endm
.macro UREST
.if US
  UMM 4, 0, 0
.endif
  ZR ZZ, 8
  ZR DD, 10
  v_mov_b32 v[TAG], s65
.endm
.macro STEPV R
LV_loop\R:
  STAMP 0
.if US
  URD 4, 0
.if \R == 3
  ds_read_b32 v[MX], v[183] offset:20
.endif
  s_waitcnt lgkmcnt(0)
.endif
.if \R == 3
  v_readfirstlane_b32 s4, v[MX]
  s_setprio 3
  WAITP s73
  s_memtime s[6:7]
.endif
  s_barrier
  s_memtime s[92:93]
  s_setprio 1
  s_nop )" STR(VDEL1) R"(
  STAMP 1
  UREST
.if \R == 3
  v_mov_b32 v[TV+2], s6
  buffer_load_dwordx2 v[TV:TV+1], v[OT], s[40:43], s68 offen sc1
  ds_write_b32 v[183], v[TV+2] offset:24
.endif
.if \R == 2
  v_mov_b64 v[PA:PA+1], -1
  v_mov_b64 v[PA+2:PA+3], -1
.rept CV
  s_nop 3
.endr
.irp o,0,1024,2048,3072
  buffer_store_dwordx4 v[PA:PA+3], v[ST1], s[40:43], s67 offen offset:\o sc1
.endr
.endif
  s_mov_b32 s76, 0x400000
  s_setprio 2
  STAMP 9
  ALN AL6
  s_barrier
  STAMP 10
.if \R == 3
  s_cmp_eq_u32 s64, 0
  s_cbranch_scc1 LV_t0
LV_t1:
  s_waitcnt vmcnt(0)
  v_cmp_eq_u32 vcc, -1, v[TV]
  v_subrev_u32 v[TV], s73, v[TV]
  v_max_i32_dpp v[TV+1], v[TV+1], v[TV+1] quad_perm:[1,0,3,2] row_mask:0xf bank_mask:0xf
  s_cbranch_vccnz LV_rt
  v_max_i32_dpp v[TV], v[TV], v[TV] quad_perm:[1,0,3,2] row_mask:0xf bank_mask:0xf
  v_max_i32_dpp v[TV+1], v[TV+1], v[TV+1] quad_perm:[2,3,0,1] row_mask:0xf bank_mask:0xf
  s_nop 0
  v_max_i32_dpp v[TV], v[TV], v[TV] quad_perm:[2,3,0,1] row_mask:0xf bank_mask:0xf
  s_nop 0
  v_readfirstlane_b32 s75, v[TV]
  v_add_u32 v[TV], s73, v[TV]
  v_readfirstlane_b32 s74, v[TV+1]
  s_add_i32 s73, s73, s75
  s_cmp_gt_i32 s75, 512
  s_cselect_b32 s75, )" STR(SLIP_INC) R"(*45/32, 0
  s_cmp_lt_u32 s64, NS
  s_cselect_b32 s75, -(GS*45/32), s75
  s_add_i32 s75, s75, s59
  v_mov_b32 v[TV+2], s75
  v_mov_b32 v[TV+3], s65
  ds_write_b128 v[183], v[TV:TV+3]
  PLAN
.endif
.if \R == 2
  HT HB, FA
.endif
.if \R == 0
.rept VN3
  s_nop 1
.endr
LV_p:
  ds_read_b32 v[FA], v[FR]
.irp k,0,1,2,3
  ds_read2st64_b32 v[RA+2*\k:RA+2*\k+1], v[RR-(\k&1)] offset0:8*\k offset1:8*\k+1
.endr
  s_waitcnt lgkmcnt(4)
  v_cmp_gt_u32 vcc, s65, v[FA]
  s_waitcnt lgkmcnt(2)
  v_add_f32 v[RA], v[RA], v[RA+2]
  v_add_f32 v[RA+1], v[RA+1], v[RA+3]
  s_waitcnt lgkmcnt(0)
  v_add_f32 v[RA+4], v[RA+4], v[RA+6]
  v_add_f32 v[RA+5], v[RA+5], v[RA+7]
  v_add_f32 v[RA], v[RA], v[RA+4]
  v_add_f32 v[RA+1], v[RA+1], v[RA+5]
  v_cvt_pk_bf16_f32 v[RES], v[RA], v[RA+1]
  s_cbranch_vccnz LV_pb
  H1ST
.endif
  STAMP 3
.if \R != 2
  s_cmp_eq_u32 s64, 63
  s_cbranch_scc1 L_last
.endif
  s_waitcnt lgkmcnt(0)
.if \R == 2
  s_waitcnt vmcnt(0)
.endif
  s_setprio 3
  WAITK VD
  STAMP 4
  LD VM, O1, s66, 0
  LD VM+4, O1, s66, 1024
  s_mov_b32 s76, 0x400000
.if \R == 2
  s_cmp_eq_u32 s64, 63
  s_cbranch_scc1 LV_lst
.endif
  ALN AL5
  s_barrier
  SCV
  STAMP 5
.if VR == 0
  s_waitcnt vmcnt(1)
  SM ZZ, VM, 0
  v_max_u32 v[RES], v[VM], v[VM+1]
  v_max3_u32 v[RES], v[RES], v[VM+2], v[VM+3]
  SM ZZ+4, VM, 16
  SM ZZ+8, VM, 32
  SM ZZ+12, VM, 48
LV_s\R:
  s_waitcnt lgkmcnt(0)
  STAMP 6
  v_cmp_gt_u32 vcc, s65, v[FA]
  v_max3_f32 v[MX], v[SX], v[SX+1], v[SX+2]
  v_max3_f32 v[TM], v[SX+3], v[SMM], v[SMM+1]
  v_max_f32 v[MX], v[MX], v[TM]
  s_cbranch_vccnz LV_rs\R
  s_nop 0
  v_max_f32_dpp v[MX], v[MX], v[MX] quad_perm:[2,3,0,1] row_mask:0xf bank_mask:0xf
  v_mov_b32 v[TM], v[MX]
  s_nop 1
  v_permlane16_swap_b32 v[MX], v[TM]
  v_max_f32 v[MX], v[MX], v[TM]
  v_mov_b32 v[TM], v[MX]
  s_waitcnt vmcnt(0)
  SM ZZ, VM+4, 8
  v_permlane32_swap_b32 v[MX], v[TM]
  v_max_f32 v[MX], v[MX], v[TM]
  v_mul_f32 v[MX], s79, v[MX]
  SM ZZ+4, VM+4, 24
  v_fma_f32 v[SX], v[SX], s78, v[MX]
  v_fma_f32 v[SX+1], v[SX+1], s78, v[MX]
  v_fma_f32 v[SMM], v[SMM], s78, v[MX]
  SM ZZ+8, VM+4, 40
  v_fma_f32 v[SMM+1], v[SMM+1], s78, v[MX]
  v_fma_f32 v[SX+2], v[SX+2], s78, v[MX]
  v_fma_f32 v[SX+3], v[SX+3], s78, v[MX]
  SM ZZ+12, VM+4, 56
  v_exp_f32 v[SX], v[SX]
  v_exp_f32 v[SX+1], v[SX+1]
  v_exp_f32 v[SMM], v[SMM]
  v_cvt_pk_bf16_f32 v[PA], v[SX], v[SX+1]
  v_exp_f32 v[SMM+1], v[SMM+1]
  v_cvt_pk_bf16_f32 v[PA+1], v[SMM], v[SMM]
  v_exp_f32 v[SX+2], v[SX+2]
  v_exp_f32 v[SX+3], v[SX+3]
  v_cvt_pk_bf16_f32 v[PA+3], v[SMM+1], v[SMM+1]
  v_cvt_pk_bf16_f32 v[PA+2], v[SX+2], v[SX+3]
.else
.rept DWM
  v_mfma_f32_16x16x32_bf16 v[PA:PA+3], v[B1:B1+3], v[B1:B1+3], 0
.endr
LV_s\R:
  s_waitcnt lgkmcnt(0)
  STAMP 6
  v_cmp_gt_u32 vcc, s65, v[FA]
  v_max3_f32 v[MX], v[SX], v[SX+1], v[SX+2]
  v_max3_f32 v[TM], v[SX+3], v[SMM], v[SMM+1]
  v_max_f32 v[MX], v[MX], v[TM]
  s_cbranch_vccnz LV_rs\R
.if VR == 2
  s_waitcnt vmcnt(1)
  SM ZZ, VM, 0
.else
  s_nop 0
.endif
  v_max_f32_dpp v[MX], v[MX], v[MX] quad_perm:[2,3,0,1] row_mask:0xf bank_mask:0xf
  v_mov_b32 v[TM], v[MX]
.if VR == 2
  SM ZZ+4, VM, 16
  s_nop 0
.else
  s_waitcnt vmcnt(1)
  SM ZZ, VM, 0
.endif
  v_permlane16_swap_b32 v[MX], v[TM]
  v_max_f32 v[MX], v[MX], v[TM]
  v_mov_b32 v[TM], v[MX]
  v_max_u32 v[RES], v[VM], v[VM+1]
.if VR == 2
  SM ZZ+8, VM, 32
.else
  SM ZZ+4, VM, 16
.endif
  v_permlane32_swap_b32 v[MX], v[TM]
  v_max_f32 v[MX], v[MX], v[TM]
  v_mul_f32 v[MX], s79, v[MX]
.if VR == 2
  SM ZZ+12, VM, 48
.else
  SM ZZ+8, VM, 32
.endif
  v_fma_f32 v[SX], v[SX], s78, v[MX]
  v_fma_f32 v[SX+1], v[SX+1], s78, v[MX]
  v_fma_f32 v[SMM], v[SMM], s78, v[MX]
.if VR == 2
  s_waitcnt vmcnt(0)
  SM ZZ, VM+4, 8
.else
  SM ZZ+12, VM, 48
.endif
  v_fma_f32 v[SMM+1], v[SMM+1], s78, v[MX]
  v_fma_f32 v[SX+2], v[SX+2], s78, v[MX]
  v_fma_f32 v[SX+3], v[SX+3], s78, v[MX]
.if VR == 2
  SM ZZ+4, VM+4, 24
.else
  s_waitcnt vmcnt(0)
  SM ZZ, VM+4, 8
.endif
  v_exp_f32 v[SX], v[SX]
.if VR == 2
  SM ZZ+8, VM+4, 40
.else
  SM ZZ+4, VM+4, 24
.endif
  v_exp_f32 v[SX+1], v[SX+1]
.if VR == 2
  SM ZZ+12, VM+4, 56
.else
  SM ZZ+8, VM+4, 40
.endif
  v_exp_f32 v[SMM], v[SMM]
.if VR == 1
  SM ZZ+12, VM+4, 56
.endif
  v_cvt_pk_bf16_f32 v[PA], v[SX], v[SX+1]
  v_exp_f32 v[SMM+1], v[SMM+1]
  v_cvt_pk_bf16_f32 v[PA+1], v[SMM], v[SMM]
  v_exp_f32 v[SX+2], v[SX+2]
  v_exp_f32 v[SX+3], v[SX+3]
  v_cvt_pk_bf16_f32 v[PA+3], v[SMM+1], v[SMM+1]
  v_cvt_pk_bf16_f32 v[PA+2], v[SX+2], v[SX+3]
  v_max3_u32 v[RES], v[RES], v[VM+2], v[VM+3]
.endif
LV_z\R:
  CZV 0
  SM DD+16, PA, B1
  CZV 1
  SM DD, PA, UB
  CZV 2
  v_max3_u32 v[RES], v[RES], v[VM+4], v[VM+5]
  SM DD+4, PA, UB+8
  CZV 3
  v_max3_u32 v[RES], v[RES], v[VM+6], v[VM+7]
  SM DD+8, PA, UB+16
  v_add_f32 v[DD+16], v[DD+16], v[DD+18]
  v_add_f32 v[DD+17], v[DD+17], v[DD+19]
  v_cmp_eq_u32 vcc, -1, v[RES]
  SM DD+12, PA, UB+24
  v_rcp_f32 v[DD+16], v[DD+16]
  v_rcp_f32 v[DD+17], v[DD+17]
  s_cbranch_vccnz LV_rb\R
  HS DD
  HS DD+4
  HS DD+8
  v_pk_mul_f32 v[DD:DD+1], v[DD:DD+1], v[DD+16:DD+17]
  v_pk_mul_f32 v[DD+4:DD+5], v[DD+4:DD+5], v[DD+16:DD+17]
  HS DD+12
  v_pk_mul_f32 v[DD+8:DD+9], v[DD+8:DD+9], v[DD+16:DD+17]
  v_cvt_pk_bf16_f32 v[SX], v[DD], v[DD+4]
  v_pk_mul_f32 v[DD+12:DD+13], v[DD+12:DD+13], v[DD+16:DD+17]
  v_cvt_pk_bf16_f32 v[SX+2], v[DD+1], v[DD+5]
  v_cvt_pk_bf16_f32 v[SX+1], v[DD+8], v[DD+12]
  v_cvt_pk_bf16_f32 v[SX+3], v[DD+9], v[DD+13]
  buffer_store_dwordx4 v[SX:SX+3], v[ST2], s[40:43], s66 offen sc1
  STAMP 7
  s_setprio 1
.if \R == 2
LV_img:
  v_add_u32 v[TM], 1024, v[AIM]
  ds_write2_b32 v[AIM], v[VM], v[VM+1] offset1:2
  ds_write2_b32 v[AIM], v[VM+2], v[VM+3] offset0:32 offset1:34
  ds_write2_b32 v[TM], v[VM+4], v[VM+5] offset1:2
  ds_write2_b32 v[TM], v[VM+6], v[VM+7] offset0:32 offset1:34
  ds_write_b32 v[FW], v[TAG] offset:2304
  s_cmp_eq_u32 s64, 63
  s_cbranch_scc1 L_end
.endif
LV_u\R:
  XSL s65
  v_add_u32 v[TM], s75, v[AVX]
  ds_read_b32 v[MX], v[183] offset:8
  URD 0, 0
.if US
  UMM 0, 0, 0
.else
  URD 4, 16
  UMM 0, 0, 4
  UMM 4, 16, 0
.endif
  v_readfirstlane_b32 s59, v[MX]
  STAMP 8
  NEXT MB
  s_branch LV_loop\R
.if \R == 0
LV_pb:
.if BENCH
  s_bitset1_b64 s[86:87], s64
.endif
  CAP
  s_branch LV_p
.endif
LV_rb\R:
  RETRY 0
  LD VM, O1, s66, 0
  LD VM+4, O1, s66, 1024
  ZR ZZ, 8
  ZR DD, 10
  s_waitcnt vmcnt(0)
.irp t,0,1,2,3
  SM ZZ+4*\t, VM, 16*\t
  s_nop 7
  SM ZZ+4*\t, VM+4, 16*\t+8
.endr
  v_max_u32 v[RES], v[VM], v[VM+1]
  v_max3_u32 v[RES], v[RES], v[VM+2], v[VM+3]
  s_nop 7
  s_branch LV_z\R
.if \R == 2
LV_lst:
  s_barrier
LV_63:
  s_waitcnt vmcnt(0)
  MX0 VM
  MX1 VM+4
  v_cmp_eq_u32 vcc, -1, v[MX]
  s_cbranch_vccz LV_img
  RETRY 0
  LD VM, O1, s66, 0
  LD VM+4, O1, s66, 1024
  s_branch LV_63
.endif
LV_rs\R:
.if BENCH
  s_bitset1_b64 s[86:87], s64
.endif
  SCV
  CAP
  s_branch LV_s\R
.if \R == 3
LV_t0:
  s_memrealtime vcc
  s_waitcnt vmcnt(0) lgkmcnt(0)
  s_lshl_b32 s73, vcc_lo, 4
  s_sub_i32 s73, s73, 0x2000
  v_mov_b32 v[TV], s73
  v_mov_b32 v[TV+1], 0
  s_branch LV_t1
LV_rt:
  RETRY 2
  buffer_load_dwordx2 v[TV:TV+1], v[OT], s[40:43], s68 offen sc1
  s_branch LV_t1
.endif
.endm
LV_init:
  s_memrealtime vcc
  PRE
  s_waitcnt lgkmcnt(0)
  s_lshl_b32 s73, vcc_lo, 4
  s_mov_b32 s78, 0x3e0293ee
  s_mov_b32 s79, 0xbe0293ee
  ZR UB, 16
  v_mov_b32 v[B1], 0x3f803f80
  v_mov_b32 v[B1+1], 0x3f803f80
  v_mov_b32 v[B1+2], 0x3f80
  v_mov_b32 v[B1+3], 0x3f80
  v_mov_b32 v[B1+4], 0x3f803f80
  v_mov_b32 v[B1+5], 0x3f803f80
  v_mov_b64 v[B1+6:B1+7], 0
  s_cmp_eq_u32 s60, 2
  s_cbranch_scc1 LV_u2
  s_cmp_eq_u32 s60, 3
  s_cbranch_scc1 LV_u3
  s_branch LV_u0
  STEPV 0
  STEPV 2
  STEPV 3
L_fatal:
  v_mov_b32 v[MX], 0
  v_mov_b32 v[TAG], 1
  global_store_dword v[MX], v[TAG], s[48:49] sc1
  s_waitcnt vmcnt(0)
  s_endpgm
L_last:
  s_barrier
L_end:
  s_waitcnt vmcnt(0) lgkmcnt(0)
)"
static_assert(__builtin_bit_cast(unsigned, SCALE * LOG2E) == 0x3e0293eeu, "");
#define CL10(p) p "0", p "1", p "2", p "3", p "4", p "5", p "6", p "7", p "8", p "9"
#define CL_ST
#define CL_S CL_ST "s4", "s5", "s6", "s7", "s72", "s73", "s74", "s75", "s76", "s77", "s78", "s79", "s88", "s89", "s90", "s91", "s92", "s93", "s94", "s95", "s96", "s97", "s98", "s99", "s100", "s101", CL10("s3"), "vcc", "scc", "memory"

__device__ __forceinline__ void chain(const Args& a, const int c, const unsigned L, const unsigned hb) {
    const int tid = threadIdx.x, w8 = RFL(tid >> 6), w = w8 & 3, l = tid & 63;
    const int g = c & 3, aa = c >> 2, h = g * 8 + (aa >> 2), hp = h ^ XH * 8, jj = aa & 3;
    const int n = l & 15, q = l >> 4, hf = (n >> 1) & 1, n2 = (n >> 2) & 1, n3 = n >> 3;
    const unsigned L_EX = L + 114688, L_SC = L + 131072, L_SM = L_SC + 2048, L_FL = L_SM + 512, L_TB = L_FL + 3072, L_VM = L_TB + 256, L_W2 = L + 143360, L_KX = L + 32768, L_VX = L_KX + 8192;
    const rsrc_t rs = mkrs(a.ring, RING_BYTES);
    unsigned* const err = a.ctl + C_ERR;
    const v4u ones = {~0u, ~0u, ~0u, ~0u};
    const unsigned cyc0 = RFL((unsigned)__builtin_amdgcn_s_memtime()), rt0 = RFL((unsigned)__builtin_amdgcn_s_memrealtime());
    if (tid < 384) lp<v2u>(L_FL)[tid] = v2u{0u, 0u};
    if (c < B_H3 || (c < B_TS + 2 && !(c & 2))) {
#pragma unroll
        for (int i = 0; i < 4; ++i) STB(ones, rs, c * 32768 + i * 8192 + tid * 16, 0); }
    const int lg3 = (c & 31) * 128 + n2 * 64 + hf * 32 + (q & 1) * 16 + (n & 1) * 8;
    if (w8 == 3) { const bf16_t* p = a.mem0 + ((c & 1) * 4 + n2 * 2 + (q & 1)) * DM + 16 * (c >> 1) + (n & 3);
        const v2u m = {min(p[0] | (unsigned)p[4] << 16, 0xFFFFFFFEu), min(p[8] | (unsigned)p[12] << 16, 0xFFFFFFFEu)};
#pragma unroll
        for (int i = 0; i < 2; ++i) __builtin_amdgcn_raw_buffer_store_b64(m, rs, (B_H3 + 4 * (c >> 5) + 2) * 32768 + ((q >> 1) * 2 + n3) * 4096 + lg3, i * 16384, 16); }
    const unsigned stg = L + w8 * 16384;
    const int rot = (ROT) & 3;
    const int prr = 16 * (q >> 1) + (q & 1) + 4 * n2 + 2 * n3;
    v32u W[6];
    auto trd4 = [&](int k, unsigned p, int o1, int o2) { const v2u t[4] = {trd(p), trd(p + o1), trd(p + o2), trd(p + o2 + o1)};
#pragma unroll
        for (int i = 0; i < 8; ++i) W[k >> 2][(k & 3) * 8 + i] = t[i >> 1][i & 1]; };
    unsigned cal;
    const unsigned d1c = (a.d1 * 45 >> 5) + NS * (GS * 45 / 32) - WKO;
    auto pg = [&](int blk, int x) { return blk * 32768 + (int)((((cal >> (blk >> 6) * 3) ^ ((x ^ blk >> 4) & 1) * 4 ^ ((x >> 1 ^ blk >> 5) & 1)) & 7) << 12); };
    auto h1o = [&](int inner, int x = 0) { return pg(B_H1 + (aa & 28) + (inner >> 12) * 2, g ^ x ^ XP) + (inner & 4095); };
    auto stream = [&](auto issue, auto read) {
        issue(0); issue(1);
        WAITV(8); BAR();
        unsigned h2 = hb;
        auto arrive = [&]() { gadd1(a.ctl + C_READY); if (c == 0) gst1(a.ctl + C_ACK, 1u); };
        if (w8 == 3) gld_req(h2, a.ctl + C_HB);
        read(std::integral_constant<int, 0>{}); WAITL(); issue(2);
        gld_wait<8>(h2);
        const bool seen = RFL(h2) != RFL(hb);
        if (seen) arrive();
        read(std::integral_constant<int, 1>{}); WAITL(); issue(3);
        WAITV(8); read(std::integral_constant<int, 2>{});
        WAITV(0); read(std::integral_constant<int, 3>{}); WAITL();
        if (w8 == 3 && !seen) { ctl_wait(a, hb); arrive(); }
    };
    auto start = [&]() {
        if (w8 == 3) {
            for (int i = 0; RFL(gld(a.ctl + C_READY)) < 256; ++i) { if (c == 0) gadd1(a.ctl + C_HB); __builtin_amdgcn_s_sleep(4); if (i > GCAP) fatal(err); }
            FENCE();
            const unsigned v = ~gld(a.ctl + C_CAL + (l & 15)) & 0xffffu;
            unsigned u = (2 * v + dppu<0xB1>(v) + dppu<0x1B>(dppu<HMIR>(v))) * 8 + (l & 7);
            u = min(u, dppu<0xB1>(u)); u = min(u, dppu<0x4E>(u)); u = min(u, dppu<HMIR>(u));
            unsigned t = ((__builtin_amdgcn_readlane(u, 0) & 7) | (__builtin_amdgcn_readlane(u, 8) & 7) << 3) ^ 022;
            *lp<v2u>(L_TB + 16) = v2u{t, cyc0}; *lp<v2u>(L_TB + 8) = v2u{d1c, 0u}; WAITL();
        }
        lp<unsigned>(L_EX + w8 * 2048 + 1536)[l] = 0; WAITL();
        BAR();
        cal = RFL(ldsr(L_TB + 16));
    };
    const unsigned char* xp = a.qkv + h * 24576 + w * 8192 + (w == 2 ? (jj & 1) * 4096 : 0);
    const unsigned char* xq = a.qkv + h * 24576 + 2 * NH * 24576;
    const unsigned xw = w == 3 ? 20480 - XC * 1024 : w * (XC == 4 ? 8192 : 4096);
    const unsigned* fG = a.ctl + C_G + h * 128 + w * 32;
    auto lo = [](const void* p) { return (unsigned)(u64)p; };
    auto hi = [](const void* p) { return (unsigned)((u64)p >> 32); };
    v16u S0 = {lo(a.ring), hi(a.ring), RING_BYTES, 0x20000u, lo(a.ox), hi(a.ox), NSEG * 262144, 0x20000u, lo(err), hi(err), lo(a.ctl + C_M + h * 128 + jj * 32), hi(a.ctl + C_M + h * 128 + jj * 32),
               lo(xq), hi(xq), lo(fG - w * 32), hi(fG - w * 32)};
    v16u S1 = {(unsigned)rot << 10, (unsigned)a.d2, (unsigned)a.per, d1c, (unsigned)w, L_KX + xw, L_TB, (unsigned)w8};
    v8u cnts;
    v16u A;
    const unsigned six = hf ? 0xEEEE : 0x4444;

    if (w8 < 4) {
        { const bf16_t* Wm = (jj >> 1 ? a.Wk : a.Wq) + (size_t)256 * w * HD + hp * DH + 64 * (jj & 1);
          const unsigned p = stg + prr * 128 + (((hf ^ (4 * n2 + 2 * n3)) * 16 + (n & 1) * 8) ^ ((w >> 1) * 64));
          stream([&](int i) { _Pragma("unroll 1") for (int j = 0; j < 8; ++j) gldsc(Wm + (size_t)(64 * (i ^ rot) + 8 * j) * HD, (l >> 3) * (HD * 2) + ((l & 7) ^ (2 * q)) * 16, stg + (i & 1) * 8192 + j * 1024); },
                 [&](auto i) { _Pragma("unroll") for (int t = 0; t < 4; ++t) trd4(4 * t + i, (p ^ (t * 32)) + (i & 1) * 8192, 1024, 4096); }); }
        MARK(2);
        BAR();
        { const unsigned pv = stg + 65536 + prr * 64 + (hf ^ (2 * n2)) * 16 + (n & 1) * 8;
#pragma unroll
          for (int k = 0; k < 8; ++k) trd4(16 + k, (pv ^ ((k >> 2) * 32)) + ((k & 3) ^ rot) * 4096, 512, 2048); }
        WAITL();
        BAR();
        if (w < 3) {
            flags_wait<4>(fG, 1, 2, l, err);
#pragma unroll
            for (int i = 0; i < 4; ++i) if (w < 2 || !(i & 1)) glds1<4>(xp + (i >> 1) * (NH * 24576) + (i & 1) * 4096, l * 16, L_KX + (i >> 1) * 20480 + w * 8192 + (i & 1) * 4096);
            WAITV(0);
        }
        A[5] = w == 2 ? L_W2 + l * 16 : 16384 + (jj & 1) * 4096 + l * 16;
        A[13] = w < 3 ? xw + l * 16 : 0; A[14] = xw + 4096 + l * 16; A[15] = 0;
        if (w == 2) { const int ck = 8 * (jj >> 1) + hf;
#pragma unroll
            for (int i = 0; i < 8; ++i) lp<unsigned>(A[5] + (i >> 2) * 1024)[i & 3] = 16 * (i >> 2) + 4 * q + (i & 3) > 16 * (jj & 1) + n ? 0xFF800000u : 0u;
#pragma unroll
            for (int i = 0; i < 16; ++i) *lp<v4u>(A[5] + 4096 + i * 1024) = v4u{W[4 + i / 8][i % 8 * 4], W[4 + i / 8][i % 8 * 4 + 1], W[4 + i / 8][i % 8 * 4 + 2], W[4 + i / 8][i % 8 * 4 + 3]};
            A[13] = L_KX + l * 16; A[14] = L_VX + (ck * 16 + 4 * q + (n >> 2)) * 16 + (l & 1) * 8; A[15] = L_VM + ck * 128 + (2 * q + n2) * 16 + (l & 1) * 8; }
        if (w == 3) {
#pragma unroll
            for (int i = 0; i < 4; ++i) lp<unsigned>(A[5] + 8192)[i] = ((n & 7) >> 1) == i ? 0x3F80u << (16 * (n & 1)) : 0u; }
        start();
        MARK(0); MARKC(4);
        const int sl = 2 * (jj & 1) + (w >> 1);
        A[0] = pg(B_H3 + 4 * w, g ^ (H3X)) + (l ^ (w & 1)) * 16;
        A[1] = h1o((jj >> 1) * 2048 + ((sl * 2 + n3) * 2 + (w & 1)) * 128 + (q >> 1) * 64 + n2 * 32 + (q & 1) * 16 + (n & 3) * 4, XH);
        A[2] = L_EX + (w ^ 1) * 2048 + 1024 + l * 4; A[3] = L_EX + (w ^ 2) * 2048 + l * 4; A[4] = L_EX + (w ^ 3) * 2048 + 512 + l * 4;
        A[6] = L_FL + w * 256 + l * 4; A[7] = L_EX + (l & 3) * 2048 + 1536 + l * 4; A[8] = six;
        A[9] = w == 2 ? h * 8192 + jj * 2048 + ((2 * (q & 1) + (q >> 1)) * 16 + n) * 16 : (XC == 4 ? min(w, 2) * 128 : (w == 3 ? 128 + (l & 1) * 128 : 0));
        A[10] = w < 3 ? h1o(l * 16) : pg(2 * aa, g) + l * 16;
        A[11] = w < 2 ? L_KX + w * 4096 + l * 16 : h1o((w == 2 ? 2048 : jj * 1024) + l * 16);
        A[12] = w < 2 ? L_SC + w * 1024 + ((l * 16) ^ ((q & 1) * 32)) : (w == 2 ? L_SM + ((l * 8) ^ ((q & 1) * 16)) : pg(B_H3 + 4 * (c >> 5), (q >> 1) * 2 + n3) + lg3);
        if (w == 3) { A[13] = c % 33 == 0 && l < 4 ? pg(B_TS, l) + (c >> 5) * 128 : OOB; A[14] = h1o(4096 + jj * 512 + l * 8); }
    } else {
        { const bf16_t* Wo = a.Wo + (size_t)h * DH * DM + 256 * jj + 64 * w; const bf16_t* Wv = a.Wv + (size_t)256 * w * HD + hp * DH + 32 * jj;
          const unsigned po = stg + (4 * q + n3 + 16 * n2) * 128 + (((n & 3) * 2) ^ (q & 1)) * 16;
          stream([&](int i) {
                     if (i < 2) { _Pragma("unroll 1") for (int j = 0; j < 8; ++j) gldsc(Wo + (size_t)(64 * i + 8 * j) * DM, (l >> 3) * (DM * 2) + ((l & 7) ^ (l >> 5)) * 16, stg + i * 8192 + j * 1024); }
                     else { _Pragma("unroll 1") for (int j = 0; j < 8; ++j) glds(Wv + (size_t)(128 * (i - 2) + 16 * j) * HD, (l >> 2) * (HD * 2) + ((l & 3) ^ (2 * (q & 1))) * 16, stg + (i & 1) * 8192 + j * 1024); } },
                 [&](auto i) { _Pragma("unroll") for (int t = 0; t < 4; ++t) if constexpr (i < 2) trd4(2 * t + i, (po ^ ((t >> 1) * 16)) + (t & 1) * 8 + i * 8192, 256, 4096); }); }
        BAR(); BAR();
        start();
        A[0] = 0; A[1] = h1o(4096 + l * 16); A[2] = pg(B_TS, g) + (l & 3) * 128;
        A[3] = w >= 2 ? pg(2 * aa, g) + l * 16 : h1o(4096 + ((jj * 2 + n3) * 2 + (w & 1)) * 128 + (q & 1) * 64 + (n & 1) * 32 + (q >> 1) * 16 + ((n >> 1) & 3) * 4, XH);
        A[4] = pg(jj * 16 + w * 4 + n3 * 2, n2 * 2 + (q >> 1)) + h * 128 + ((q & 1) * 4 + (n & 3)) * 16;
        A[5] = L_VM + q * 256 + (4 * n3 + 2 * (n & 1) + n2) * 16 + hf * 4;
        A[7] = L_EX + 8192 + (w & 1) * 512 + l * 4; A[6] = A[7] ^ 512;
        A[8] = L_FL + w * 256 + l * 4; A[9] = L_EX + 8192 + (l & 3) * 2048 + 1536 + l * 4; A[10] = L_FL + 2048 + min(l & 3, 2) * 256 + l * 4;
        A[11] = L_SC + q * 512 + hf * 256 + n * 16; A[12] = L_SM + (q & 1) * 256 + hf * 128 + n * 8;
        A[13] = L_VX + q * 256 + (8 * n3 + 4 * hf + 2 * n2 + (n & 1)) * 16; A[14] = six; A[15] = L_TB;
    }
    S1[12] = a.d2 - a.d1 - KC; S1[8] = RFL(pg(B_TS, g)); S1[9] = cyc0; S1[10] = rt0; S1[11] = RFL(L_EX - 63744 + w8 * 2048);
    asm volatile(A_CHAIN : "+{s[40:55]}"(S0), "+{s[56:71]}"(S1), "={s[80:87]}"(cnts), "+{v[168:183]}"(A), "+{v[188:219]}"(W[4]), "+{v[220:251]}"(W[5]) : "{v[0:31]}"(W[0]), "{v[32:63]}"(W[1]), "{v[64:95]}"(W[2]), "{v[96:127]}"(W[3])
                 : "v128", "v129", CL10("v13"), CL10("v14"), CL10("v15"), "v160", "v161", "v162", "v163", "v164", "v165", "v166", "v167", "v184", "v185", "v186", "v187", CL_S);
}

struct Tile { const bf16_t* W; int ld, wide, perm; };
__device__ __forceinline__ void tile_issue(const Tile& T, int t, unsigned L, int w8, int l) {
    const bf16_t* p = T.W + (T.wide ? (w8 & 3) * 32 * T.ld + (2 * t + (w8 >> 2)) * 128 : (t * 256 + w8 * 32) * T.ld);
    const unsigned vo = (T.perm ? (l >> 5) + (l & 16) : l >> 4) * T.ld * 2 + ((l & 15) ^ (2 * (l >> 4))) * 16;
#pragma unroll 1
    for (int i = 0; i < 8; ++i) { const bf16_t* g = p + i * (T.perm ? 2 : 4) * T.ld; const unsigned v = vo ^ ((i & 2) << 6), d = L + (t & 1) * 65536 + (w8 * 8 + i) * 1024;
        gldsw(g, v, d); }
}
template <class F>
__device__ __forceinline__ void tile_load(const Tile& T, v4u (&wf)[32], unsigned L, int w8, int l, F next) {
    const int n = l & 15, q = l >> 4;
    const unsigned p = L + (8 * q + (n >> 2)) * 256 + ((4 * (w8 & 3) + ((l >> 1) & 1)) ^ (2 * ((n >> 2) | ((q & 1) << 2)))) * 16 + (l & 1) * 8;
    bool more = true;
#pragma unroll 1
    for (int hh = 0; hh < 2; ++hh)
#pragma unroll
    for (int u = 0; u < 2; ++u) {
        if (!more) WAITV(0);
        WAITV(8); BAR();
        if ((w8 >> 2) == hh) {
#pragma unroll
            for (int i = 0; i < 16; ++i) { const unsigned pp = (p ^ ((i & 1) * 32)) + u * 65536 + (i >> 1) * 8192; wf[u * 16 + i] = cat2(trd(pp), trd(pp + 1024)); }
            WAITL(); }
        BAR();
        if (hh == 0) tile_issue(T, u + 2, L, w8, l); else more = next(u);
    }
}
#define NB 8
#define GD 4
template <int XR, bool ROWS, class F>
__device__ __forceinline__ void seg_gemm(const v4u (&wf)[32], unsigned b0, frag_cd (&acc)[4], F side) {
    v4u f[2 * GD];
    const unsigned bb[4] = {b0, ROWS ? b0 ^ 64 : b0, ROWS ? b0 ^ 128 : b0, ROWS ? b0 ^ 192 : b0};
#define RD(ks, mt) asm volatile("ds_read_b128 %0, %1 offset:%2" : "=v"(f[((ks) % GD) * 2 + mt]) : "v"(bb[(ks) & 3]), "n"(ROWS ? ((ks) >> 2) * 256 + mt * 16384 : ((ks) >> 1) * 4096 + ((ks) & 1) * 1024 + mt * 2048) : "memory")
#define MM(ct, mt) do { if (ks == 0) asm volatile("v_mfma_f32_16x16x32_bf16 %0, %1, %2, 0" : "=&v"(acc[2 * ct + mt]) : "v"(wf[ct]), "v"(f[mt])); \
        else asm volatile("v_mfma_f32_16x16x32_bf16 %0, %1, %2, %0" : "+v"(acc[2 * ct + mt]) : "v"(wf[2 * ks + ct]), "v"(f[(ks % GD) * 2 + mt])); } while (0)
#pragma unroll
    for (int ks = 0; ks < GD - 1; ++ks) { RD(ks, 0); RD(ks, 1); }
#pragma unroll
    for (int ks = 0; ks < 16; ++ks) {
        side(2 * ks);
        asm volatile("s_waitcnt lgkmcnt(%0)" :: "n"(2 * (ks + GD - 1 <= 16 ? GD - 2 : 15 - ks) + (ks >= 13 ? XR : 0)));
        MM(0, 0);
        if (ks + GD - 1 < 16) RD(ks + GD - 1, 0);
        MM(0, 1);
        side(2 * ks + 1);
        MM(1, 0);
        if (ks + GD - 1 < 16) RD(ks + GD - 1, 1);
        MM(1, 1);
    }
    asm volatile("s_nop 7" : "+v"(acc[0]), "+v"(acc[1]), "+v"(acc[2]), "+v"(acc[3]));
#undef RD
#undef MM
}
static_assert(GD == 4, "");
__device__ __forceinline__ float fadd(float a, float b) { float r; asm("v_add_f32 %0, %1, %2" : "=v"(r) : "v"(a), "v"(b)); return r; }
__device__ __forceinline__ float fmul(float a, float b) { float r; asm("v_mul_f32 %0, %1, %2" : "=v"(r) : "v"(a), "v"(b)); return r; }
__device__ __forceinline__ void half_put(unsigned ex, int w8, int l, const frag_cd (&acc)[4]) {
#pragma unroll
    for (int i = 0; i < 4; ++i) *lp<frag_cd>(ex + w8 * 4096 + i * 1024 + l * 16) = acc[i];
    FENCE();
}
struct Half { frag_cd t[4]; unsigned dec; };
__device__ __forceinline__ void half_req(unsigned ex, unsigned dec, Half& x) {
    asm volatile("ds_read_b32 %0, %1" : "=v"(x.dec) : "v"(dec) : "memory");
#pragma unroll
    for (int i = 0; i < 4; ++i) asm volatile("ds_read_b128 %0, %1 offset:%2" : "=v"(x.t[i]) : "v"(ex), "n"(i * 1024) : "memory");
}
template <class F>
__device__ __forceinline__ void half_get(Half& x, frag_cd (&acc)[4], F side) {
    asm volatile("s_waitcnt lgkmcnt(0)" : "+v"(x.t[0]), "+v"(x.t[1]), "+v"(x.t[2]), "+v"(x.t[3]), "+v"(x.dec) :: "memory");
#pragma unroll
    for (int i = 0; i < 4; ++i) { side(i);
#pragma unroll
        for (int e = 0; e < 4; ++e) acc[i][e] = fadd(acc[i][e], x.t[i][e]);
        asm volatile("" : "+v"(acc[i])); }
}
#define GEARLY 2
#define BARI 25
#define PRIO(n) __builtin_amdgcn_s_setprio(n)

__device__ __forceinline__ unsigned topk(const Args& a, int l) {
    const rsrc_t rg = mkrs(a.gate, 2 * S_TOK * 4);
    flags_wait<16>(a.ctl + C_R, 32, 1, l, a.ctl + C_ERR);
    v4u kv[8]; unsigned prev = ~0u, mine = 0;
#pragma unroll
    for (int i = 0; i < 8; ++i) kv[i] = __builtin_amdgcn_raw_buffer_load_b128(rg, S_TOK * 4 + l * 16, i * 1024, 16);
#pragma unroll 1
    for (int c = 0; c < TOPC; ++c) { unsigned mx = 0;
        FENCE();
#pragma unroll
        for (int i = 0; i < 8; ++i)
#pragma unroll
            for (int e = 0; e < 4; ++e) mx = max(mx, kv[i][e] < prev ? kv[i][e] : 0u);
        mx = max(mx, dppu<0x128>(mx)); mx = max(mx, dppu<0x124>(mx)); mx = max(mx, dppu<0x122>(mx)); mx = max(mx, dppu<0x121>(mx));
        { v2u r = sw16(mx, mx); mx = max(r.x, r.y); r = sw32(mx, mx); mx = max(r.x, r.y); }
        prev = RFL(mx); if ((l & 7) == c) mine = prev; }
    return mine;
}
__device__ __forceinline__ unsigned mix(float gt, float omg, float t0, float t1, float b0, float b1) {
    unsigned av = cvt_pk(t0, t1); av = cvt_pk(fmul(gt, lo16(av)), fmul(gt, hi16(av))); const unsigned bv = cvt_pk(fmul(omg, b0), fmul(omg, b1));
    return cvt_pk(fadd(lo16(av), lo16(bv)), fadd(hi16(av), hi16(bv)));
}
struct Finisher {
    rsrc_t rp, rx, rg, ro; int po, frow, xo, w8; unsigned seltok, ly;
    struct Fin { v4u p[4]; v2u xv; float gt; int k; };
    __device__ __forceinline__ Finisher(const Args& a, int j, int w8_, int l, unsigned L_Y) {
        rp = mkrs(a.part, NSEG * 524288); rx = mkrs(a.x, S_TOK * DM * 2); rg = mkrs(a.gate, S_TOK * 4); ro = mkrs(a.out, S_TOK * DM * 2);
        w8 = w8_; po = j * 16384 + w8 * 1024 + l * 16; frow = 16 * (w8 & 1) + (l & 15); xo = (frow * DM + j * 32 + (w8 >> 1) * 16 + 4 * (l >> 4)) * 2;
        seltok = 4095u - (lp<unsigned>(L_Y + 1024)[l & 7] & 4095u); ly = L_Y + ((w8 >> 1) * 32 + (l >> 4) * 8) * 16;
    }
    __device__ __forceinline__ Fin issue(int s, bool on) const { Fin f; int k = on ? 0 : 0x40000000; asm("" : "+s"(k));
#pragma unroll
        for (int i = 0; i < 4; ++i) f.p[i] = __builtin_amdgcn_raw_buffer_load_b128(rp, k + po, s * 524288 + i * 4096, 16);
        f.k = k + xo;
        f.xv = __builtin_amdgcn_raw_buffer_load_b64(rx, f.k, s * 65536, 0);
        f.gt = __uint_as_float(__builtin_amdgcn_raw_buffer_load_b32(rg, k + frow * 4, s * 128, 16));
        return f;
    }
    template <class F> __device__ __forceinline__ void finish(int s, const Fin& f, F side) const {
        const int tok = 32 * s + frow; const float gt = f.gt, omg = rbf(1.0f - gt);
        frag_cd t; v2u o; int k = f.k;
#pragma unroll
        for (int e = 0; e < 4; e += 2) {
            side(e >> 1);
#pragma unroll
            for (int u = e; u < e + 2; ++u) t[u] = fadd(fadd(__uint_as_float(f.p[0][u]), __uint_as_float(f.p[1][u])), fadd(__uint_as_float(f.p[2][u]), __uint_as_float(f.p[3][u])));
            o[e >> 1] = mix(gt, omg, t[e], t[e + 1], lo16(f.xv[e >> 1]), hi16(f.xv[e >> 1])); asm volatile("" : "+v"(o[e >> 1])); }
        for (unsigned mk = (unsigned)__builtin_amdgcn_ballot_w64((seltok >> 4) == (unsigned)(2 * s + (w8 & 1))) & 255u; mk; mk &= mk - 1) {
            const int slot = __builtin_ctz(mk);
            if (__builtin_amdgcn_readlane(seltok, slot) == (unsigned)tok) { *lp<frag_cd>(ly + slot * 16) = t; k = 0x40000000; }
        }
        __builtin_amdgcn_raw_buffer_store_b64(o, ro, k, s * 65536, 0);
    }
};

__device__ __forceinline__ void gwork(const Args& a, const int m, const int h, const unsigned L, const unsigned hb) {
    const int w8 = RFL(threadIdx.x >> 6); int l = threadIdx.x & 63, n = l & 15, q = l >> 4;
    unsigned* const err = a.ctl + C_ERR;
    const unsigned L_EX = L + 131072, L_XS = L_EX, L_HS = L_XS + 16384, L_GU = L_HS + 2048;
    unsigned* const fG = a.ctl + C_G + h * 128 + m * 32;
    const unsigned xo = (l ^ (8 * (w8 & 1))) * 16;
    auto xissue = [&](int s, int i0, int i1) {
        const bf16_t* p = a.x + (size_t)(32 * s + 8 * (w8 & 3)) * DM + (~w8 & 4) * 128; const unsigned d = L + (s & 1) * 65536 + (w8 ^ 4) * 8192;
#pragma unroll
        for (int i = i0; i < i1; ++i) {
            if (i == i0 || i == 4) asm volatile("s_mov_b32 m0, %0" :: "s"(d + (i & 4) * 1024));
            unsigned t; asm volatile("v_xor_b32 %0, %4, %1\n\tglobal_load_lds_dwordx4 %0, %2 offset:%3" : "=&v"(t) : "v"(xo), "s"(p), "n"((i & 3) * 1024), "n"(i * 1040 + (i & 4) * 1024) : "memory"); }
    };
    v4u wf[32];
    { const Tile T{(m == 0 ? a.Wk : (m == 1 ? a.Wv : a.Wq)) + h * DH, HD, 0, 0};
      tile_issue(T, 0, L, w8, l); tile_issue(T, 1, L, w8, l);
      tile_load(T, wf, L, w8, l, [&](int b) { if (b == 0 || w8 >= 4) xissue(b, 0, 8); return true; }); }
    MARK(0);
    const unsigned rb0 = L + (w8 >> 2) * 32768 + n * 1024 + (q ^ n) * 16;
    unsigned pacc[8] = {}, plast = 0; (void)pacc; (void)plast;
    PT(7);
    if (w8 >= 4) {
        const rsrc_t rq = mkrs(a.qkv, NSEG * NH * 24576), rf = mkrs(fG, w8 == 4 ? 4 : 0);
        const int sto = h * 24576 + m * 8192 + (m == 1 ? (w8 & 3) * 64 + l : ((w8 & 2) + (q & 1)) * 64 + (2 * (w8 & 1) + (q >> 1)) * 16 + n) * 16;
        WAITV(8); BAR(); WAITV(0);
        if (w8 == 4) { ctl_wait(a, hb); gadd1(a.ctl + C_READY); }
        for (int sg = 0; sg < NSEG; ++sg) {
            PT(0);
            BAR();
            PT(1);
            PRIO(2);
            if (sg <= GEARLY) __builtin_amdgcn_raw_buffer_store_b32(sg, rf, l * 4, 0, 16);
            frag_cd acc[4]; Half hx;
            seg_gemm<5, true>(wf, rb0 + (sg & 1) * 65536, acc, [&](int i) {
                if (i == BARI) { PRIO(3); PT(5); WAITV(3); BAR(); PT(6); }
                if (i == 26) half_req(L_EX + (w8 & 3) * 4096 + l * 16, L_EX, hx);
                if (i >= 26) xissue((sg + 2) & 63, i - 26, i - 25); });
            PRIO(0);
            PT(2);
            half_get(hx, acc, [&](int i) { if (i & 1) xissue((sg + 2) & 63, 6 + i / 2, 7 + i / 2); });
            v4u r[2];
#pragma unroll
            for (int mt = 0; mt < 2; ++mt) r[mt] = v4u{cvt_pk(acc[mt][0], acc[2 + mt][0]), cvt_pk(acc[mt][1], acc[2 + mt][1]), cvt_pk(acc[mt][2], acc[2 + mt][2]), cvt_pk(acc[mt][3], acc[2 + mt][3])};
            PT(3);
#pragma unroll
            for (int mt = 0; mt < 2; ++mt) STB(r[mt], rq, sto + mt * 4096, sg * (NH * 24576));
            __builtin_amdgcn_raw_buffer_store_b32(sg <= GEARLY ? sg : sg - 1, rf, l * 4, 0, 16);
            FENCE();
            if (sg < GEARLY) WAITV(0);
            PT(4);
        }
        PFLUSH();
        WAITV(0); BAR();
        if (w8 == 4) gst1(fG, NSEG);
    } else {
        for (int sg = 0; sg < NSEG; ++sg) {
            frag_cd acc[4];
            WAITL();
            PT(0);
            BAR();
            PT(1);
            PRIO(2);
            seg_gemm<0, true>(wf, rb0 + (sg & 1) * 65536, acc, [&](int i) { if (i == BARI) { PRIO(3); PT(5); WAITV(0); BAR(); PT(6); } });
            PRIO(1);
            PT(2);
            half_put(L_EX, w8, l, acc);
            xissue((sg + 1) & 63, 0, 8);
            PT(3);
            MARK(sg + 1);
        }
        PFLUSH();
        WAITL(); BAR();
        WAITV(0); BAR();
    }
    if (m != 2) return;
    asm volatile("v_mbcnt_lo_u32_b32 %0, -1, 0\n\tv_mbcnt_hi_u32_b32 %0, -1, %0" : "=v"(l));
    n = l & 15; q = l >> 4;
    const rsrc_t ry = mkrs(a.ypart, 32 * 29 * 1024);
    auto tile = [&](int t) { return t < 2 ? Tile{(t ? a.wu : a.wg) + h * 128, FF, 0, 0} : Tile{a.wd + (size_t)h * 128 * DM, DM, 1, 0}; };
    unsigned mykey = 0;
    if (w8 == 0) {
        mykey = topk(a, l);
#pragma unroll 1
        for (int i = 0; i < 16; ++i) glds(a.x + i * 64, (4095u - (mykey & 4095u)) * (DM * 2) + (l >> 3) * 16, L_XS + i * 1024);
    }
    if (h < FF / 128) {
      Tile T = tile(0);
      tile_issue(T, 0, L, w8, l); tile_issue(T, 1, L, w8, l);
      frag_cd gv[2] = {};
#pragma unroll 1
      for (int ti = 0; ti < 3; ++ti) {
        const Tile N = tile(ti + 1);
        tile_load(T, wf, L, w8, l, [&](int b) { if (ti < 2) tile_issue(N, b, L, w8, l); return ti < 2; });
        T = N;
        if (ti < 2) {
            frag_cd acc[2] = {};
#pragma unroll
            for (int ks = 0; ks < 16; ++ks) { const v4u xf = *lp<v4u>(L_XS + ((w8 >> 2) * 16 + ks) * 512 + q * 128 + (n & 7) * 16); acc[0] = MFMA(wf[2 * ks], xf, acc[0]); acc[1] = MFMA(wf[2 * ks + 1], xf, acc[1]); }
            if (ti == 1) {
                frag_cd g = n < 8 ? gv[0] : gv[1], u = n < 8 ? acc[0] : acc[1];
                if (w8 >= 4) { *lp<frag_cd>(L_GU + (w8 & 3) * 2048 + l * 16) = g; *lp<frag_cd>(L_GU + (w8 & 3) * 2048 + 1024 + l * 16) = u; WAITL(); }
                BAR();
                if (w8 < 4) { g += *lp<frag_cd>(L_GU + w8 * 2048 + l * 16); u += *lp<frag_cd>(L_GU + w8 * 2048 + 1024 + l * 16);
                    float hv[4];
#pragma unroll
                    for (int e = 0; e < 4; ++e) { const float gg = rbf(g[e]); hv[e] = rbf(gg / (1.0f + __expf(-gg))) * rbf(u[e]); }
                    *lp<v2u>(L_HS + w8 * 512 + (2 * (n >> 3) + (q >> 1)) * 128 + (n & 7) * 16 + (q & 1) * 8) = v2u{cvt_pk(hv[0], hv[1]), cvt_pk(hv[2], hv[3])};
                    WAITL(); } }
            gv[0] = acc[0]; gv[1] = acc[1];
        } else {
#pragma unroll
            for (int cb = 0; cb < 4; ++cb) { frag_cd acc[2] = {};
#pragma unroll
                for (int i = 0; i < 4; ++i) { const v4u hf = *lp<v4u>(L_HS + i * 512 + q * 128 + (n & 7) * 16); acc[0] = MFMA(wf[8 * cb + 2 * i], hf, acc[0]); acc[1] = MFMA(wf[8 * cb + 2 * i + 1], hf, acc[1]); }
                STB(__builtin_bit_cast(v4u, n < 8 ? acc[0] : acc[1]), ry, ((w8 & 3) * 28 + h) * 1024 + ((n >> 3) * 32 + q * 8 + (n & 7)) * 16, ((w8 >> 2) * 4 + cb) * (4 * 28 * 1024)); }
        }
        MARK(67 + ti);
      }
      WAITV(0); BAR();
    }
    if (w8 == 0) {
        gst1(a.ctl + C_Y + h * 32, 1u);
        flags_wait<16>(a.ctl + C_Y, FF / 128, 1, l, err);
        frag_cd y = {};
#pragma unroll 1
        for (int i = 0; i < 28; i += 7) { v4u t[7];
#pragma unroll
            for (int u = 0; u < 7; ++u) t[u] = __builtin_amdgcn_raw_buffer_load_b128(ry, l * 16, (h * 28 + i + u) * 1024, 16);
#pragma unroll
            for (int u = 0; u < 7; ++u) y += __builtin_bit_cast(frag_cd, t[u]); }
        const float r = hi16((mykey & 0x80000000u) ? mykey & 0x7fffffffu : ~mykey), rs = rbf(1.0f / (1.0f + __expf(-r)));
#pragma unroll
        for (int e = 0; e < 4; ++e) y[e] = rbf(rbf(y[e]) * rs);
        STB(__builtin_bit_cast(v4u, y), ry, l * 16, (32 * 28 + h) * 1024);
        WAITV(0);
        gst1(a.ctl + C_Y + h * 32 + 1, 1u);
        MARK(120);
    }
}

__device__ __forceinline__ void owork(const Args& a, const int nt, const int kq, const unsigned L, const unsigned hb) {
    const int tid = threadIdx.x, w8 = RFL(tid >> 6), l = tid & 63, j = nt * 4 + kq;
    unsigned* const err = a.ctl + C_ERR;
    const unsigned L_EX = L + 131072, L_DEC = L_EX + 16384, L_Y = L_DEC + 64, L_KEY = L_Y + 1024;
    if (tid < 4) lp<unsigned>(L_DEC)[tid] = 0;
    {
        const bool lead = j == 0 && w8 == 1;
        if (lead) { const rsrc_t rc = mkrs(a.ctl, C_HB * 4);
#pragma unroll
            for (int i = 0; i < 8; ++i) STB(v4u{}, rc, l * 128, i * 8192);
            STB(v4u{}, rc, l * 16, 0); }
        unsigned char* p = a.ring + ((l >> 3 & 1) * 64 + (j >> 1)) * 32768 + ((l ^ (kq & 1) * 4 ^ kq >> 1) & 7) * 4096 + (w8 * 2 + (nt & 1)) * 256 + (kq & 1) * 128;
        unsigned best = 0;
        if (l < 16) gst((unsigned*)p, ~0u);
        WAITV(0);
#pragma unroll 1
        for (int i = 0; i < 16; ++i) { u64 t1, t2; unsigned v;
            if (lead && !(i & 3)) gadd1(a.ctl + C_HB);
            const u64 pi = ((u64)__builtin_amdgcn_readlane((unsigned)((u64)p >> 32), i) << 32) | (unsigned)__builtin_amdgcn_readlane((unsigned)(u64)p, i);
            asm volatile("s_nop 4\n\ts_memtime %0\n\ts_waitcnt lgkmcnt(0)\n\tglobal_load_dword %2, %3, %4 sc1\n\ts_waitcnt vmcnt(0)\n\ts_memtime %1\n\ts_waitcnt lgkmcnt(0)"
                         : "=&s"(t1), "=&s"(t2), "=&v"(v) : "v"(0u), "s"(pi) : "memory");
            if (l == i) best = ~(unsigned)(t2 - t1); }
        if (l < 16) { gst((unsigned*)p, ~0u); lp<unsigned>(L)[w8 * 16 + l] = best; }
        WAITV(0); WAITL(); BAR();
        if (w8 == 0) {
#pragma unroll
            for (int i = 1; i < 8; ++i) best = max(best, lp<unsigned>(L)[i * 16 + (l & 15)]);
            if (j) ctl_wait(a, hb);
            if (l < 16) __hip_atomic_fetch_max(a.ctl + C_CAL + l, best, __ATOMIC_RELAXED, __HIP_MEMORY_SCOPE_AGENT);
            WAITV(0);
            gadd1(a.ctl + C_READY);
            MARK(66);
        }
        if (lead) for (int i = 0; !RFL(gld(a.ctl + C_ACK)); ++i) { gadd1(a.ctl + C_HB); __builtin_amdgcn_s_sleep(8); if (i > GCAP) fatal(err); }
    }
    BAR();
    {
#pragma unroll 1
        for (int i = 0; i < 16; ++i) glds(a.x + (size_t)(64 * j + 8 * w8) * DM + i * 512, l * 16, L + (w8 * 16 + i) * 1024);
        v4u gw[2], wr[2];
#pragma unroll
        for (int i = 0; i < 2; ++i) { gw[i] = *(const v4u*)(a.gw + 512 * i + 8 * l); wr[i] = *(const v4u*)(a.wr + 512 * i + 8 * l); }
        WAITV(0);
        float res = 0.f;
#pragma unroll 1
        for (int t = 0; t < 8; ++t) { float sg = 0.f, sr = 0.f;
#pragma unroll
            for (int i = 0; i < 2; ++i) { const v4u xv = *lp<v4u>(L + (w8 * 16 + 2 * t + i) * 1024 + l * 16);
#pragma unroll
                for (int e = 0; e < 4; ++e) { const float x0 = lo16(xv[e]), x1 = hi16(xv[e]); const unsigned p = cvt_pk(x0 * lo16(gw[i][e]), x1 * hi16(gw[i][e]));
                    sg += lo16(p) + hi16(p); sr = __builtin_fmaf(x0, lo16(wr[i][e]), __builtin_fmaf(x1, hi16(wr[i][e]), sr)); } }
            float v = tadd32(sg, sr);
            v = tadd16(v, v); v += dppf<ROR8>(v); v += dppf<0x124>(v); v += dppf<0x122>(v); v += dppf<0x121>(v);
            if ((l & 7) == t) res = v; }
        const unsigned rb = cvt_pk(res, 0.f), tok = 64 * j + 8 * w8 + (l & 7);
        const unsigned val = l < 32 ? __float_as_uint(rbf(1.0f / (1.0f + __expf(-lo16(rb))))) : (((rb & 0x8000u) ? ~rb : rb | 0x8000u) << 16) | (4095u - tok);
        if ((l & 31) < 8) gst(a.gate + (l >> 5) * S_TOK + tok, val);
        WAITV(0); BAR();
        if (w8 == 0) gst1(a.ctl + C_R + j * 32, 1u);
        MARK(67);
    }
    v4u wf[32];
    { const Tile T{a.Wo + (size_t)kq * 1024 * DM + nt * 128, DM, 0, 1};
      tile_issue(T, 0, L, w8, l); tile_issue(T, 1, L, w8, l);
      tile_load(T, wf, L, w8, l, [](int) { return false; }); }
    MARK(0);
    if (w8 == 1) { const unsigned mine = topk(a, l); if (l < 8) lp<unsigned>(L_KEY)[l] = mine; WAITL(); }
    BAR();
    MARK(68);
    unsigned* const fP = a.ctl + C_P + nt * 512;
    const unsigned rb = L + (w8 >> 2) * 32768 + l * 16;
    unsigned pacc[8] = {}, plast = 0; (void)pacc; (void)plast;
    PT(7);
    auto oissue = [&](int s, int wv, int i) { glds1<2>(a.ox + (size_t)s * 262144 + kq * 65536 + wv * 8192 + i * 2048, l * 16, L + (s & 1) * 65536 + wv * 8192 + i * 2048); };
    if (w8 >= 4) {
        const rsrc_t rf = mkrs(fP + (kq * 4 + (w8 & 3)) * 32, 4), rp = mkrs(a.part, NSEG * 524288);
        const int stp = ((nt * 4 + (w8 & 3)) * 4 + kq) * 4096 + l * 16;
        const unsigned* fW = l < 32 ? a.ctl + C_M + (kq * 32 + l) * 32 : fP + ((l & 3) * 4 + kq) * 32;
        unsigned fv = 0; int pc = 0;
        auto a_ok = [&](int s) { return s < NSEG && (unsigned)__builtin_amdgcn_ballot_w64(fv <= (unsigned)s) == 0; };
        auto p_upd = [&]() { if ((__builtin_amdgcn_ballot_w64(fv <= (unsigned)pc) >> 32) == 0) ++pc; if ((__builtin_amdgcn_ballot_w64(fv <= (unsigned)pc) >> 32) == 0) ++pc; lp<unsigned>(L_DEC)[2] = pc; };
        int have = 0, nxt = 0, late = 0;
        for (int s = 0; s <= NSEG; ++s) {
            if (!have) {
                if (!late) {
                    gld_wait<0>(fv);
                    __builtin_amdgcn_raw_buffer_store_b32(s, rf, l * 4, 0, 16);
                    if (w8 == 4) {
                        for (int i = 0;; ++i) { if (fv <= (unsigned)(l < 32 ? s + 1 : s - 1)) fv = gld(fW); WAITV(0); p_upd(); if (a_ok(s) || pc == NSEG) break; __builtin_amdgcn_s_sleep(2); if (i > GCAP) fatal(err); }
                        lp<unsigned>(L_DEC)[3] = s + 1; lp<unsigned>(L_DEC)[~s & 1] = a_ok(s + 1); FENCE(); }
                    if (s == NSEG) break;
                    for (int i = 0; ldsr(L_DEC + 12) <= (unsigned)s; ++i) { __builtin_amdgcn_s_sleep(1); if (i > LCAP) endpgm(); }
                    FENCE();
                    PT(0);
#pragma unroll
                    for (int i = 0; i < 8; ++i) oissue(s, w8 ^ (~i & 4), i & 3);
                    WAITV(8);
                }
                WAITV(12); BAR();
                nxt = RFL(ldsr(L_DEC + 4 * (~s & 1))) & 1;
                if (late) WAITV(4); else WAITV(0);
                if (nxt) {
#pragma unroll
                    for (int i = 0; i < 4; ++i) oissue(s + 1, w8 ^ 4, i); }
                PT(1);
            }
            if (w8 == 4 && !nxt) {
                if (!have) { if (fv <= (unsigned)(s + 1)) gld_req(fv, fW); WAITV(0); }
                gld_wait<4>(fv);
                p_upd(); lp<unsigned>(L_DEC)[~s & 1] = 4 * a_ok(s + 1); WAITL();
                if (fv <= (unsigned)(s + 2)) gld_req(fv, fW); }
            BAR();
            PT(2);
            PRIO(2);
            frag_cd acc[4]; Half hx; unsigned mid;
            seg_gemm<5, false>(wf, rb + (s & 1) * 65536, acc, [&](int i) {
                if (i == BARI - 5) { if (!(have & nxt)) WAITV(0);
                    gld_wait<4>(fv);
                    if (w8 == 4) { p_upd(); const bool k = a_ok(s + 1); if (k) lp<unsigned>(L_DEC)[3] = s + 2; lp<unsigned>(L_DEC)[s & 1] = a_ok(s + 2) + 2 * k; }
                    __builtin_amdgcn_raw_buffer_store_b32(s - (have & nxt), rf, l * 4, 0, 16); FENCE(); }
                if (i == BARI) { PRIO(3); PT(5); BAR(); PT(6); }
                if (i == 3) asm volatile("ds_read_b32 %0, %1" : "=v"(mid) : "v"(L_DEC + 4 * (~s & 1)) : "memory");
                if (i == 26) half_req(L_EX + (w8 & 3) * 4096 + l * 16, L_DEC + 4 * (s & 1), hx);
                if (i == 28) { asm volatile("s_waitcnt lgkmcnt(4)" : "+v"(hx.dec), "+v"(mid)); const int d = RFL(hx.dec); have = nxt | RFL(mid) >> 2; nxt = have & d; late = ~have & d >> 1 & 1; }
                if (i >= 28 && (nxt | late)) oissue(s + 1 + nxt, w8 ^ 4, i - 28); });
            PRIO(0);
            PT(3);
            half_get(hx, acc, [&](int i) { if (late) oissue(s + 1, w8, i); });
            if (w8 == 4 && fv <= (unsigned)(s + 3)) gld_req(fv, fW);
#pragma unroll
            for (int i = 0; i < 4; ++i) STB(__builtin_bit_cast(v4u, acc[i]), rp, stp + i * 1024, s * 524288);
            FENCE();
            PT(4);
        }
        if (w8 == 5) {
            flags_wait<4>(a.ctl + C_Y + j * 32 + 1, 1, 1, l, err);
            const unsigned tok = 4095u - (lp<unsigned>(L_KEY)[l & 7] & 4095u); const int xo = tok * DM + j * 32 + (l >> 3) * 4;
            const frag_cd y = __builtin_bit_cast(frag_cd, __builtin_amdgcn_raw_buffer_load_b128(mkrs(a.ypart + (32 * 28 + j) * 1024, 1024), l * 16, 0, 16));
            const v2u xv = *(const v2u*)(a.x + xo); const float gt = __uint_as_float(gld(a.gate + tok)), omg = rbf(1.0f - gt);
            const float b[4] = {rbf(lo16(xv.x) + y[0]), rbf(hi16(xv.x) + y[1]), rbf(lo16(xv.y) + y[2]), rbf(hi16(xv.y) + y[3])};
            asm volatile("" :: "v"(b[0]), "v"(b[1]), "v"(b[2]), "v"(b[3]), "v"(omg));
            BAR();
            const frag_cd t = *lp<frag_cd>(L_Y + l * 16);
            *(v2u*)(a.out + xo) = v2u{mix(gt, omg, t[0], t[1], b[0], b[1]), mix(gt, omg, t[2], t[3], b[2], b[3])};
        } else BAR();
    } else {
        const Finisher fz(a, j, w8, l, L_Y);
        int fin = 0, have = 0, pc = 0;
        for (int s = 0; s <= NSEG; ++s) {
            if (!have) {
                for (int i = 0; fin < s; ++i) {
                    if (s < NSEG && RFL(ldsr(L_DEC + 12)) > s) break;
                    pc = RFL(ldsr(L_DEC + 8));
                    if (fin < pc) { FENCE(); fz.finish(fin, fz.issue(fin, true), [](int) {}); ++fin; }
                    else { __builtin_amdgcn_s_sleep(1); if (i > LCAP) endpgm(); }
                }
                if (s == NSEG) break;
            }
            FENCE();
            const bool fi = fin < pc;
            const Finisher::Fin f = fz.issue(fin, fi);
            WAITL(); WAITV(15);
            PT(0);
            BAR();
            PT(2);
            PRIO(2);
            frag_cd acc[4];
            v4u dec;
            seg_gemm<1, false>(wf, rb + (s & 1) * 65536, acc, [&](int i) {
                if (i == BARI) { PRIO(3); PT(1); WAITV(6); BAR(); PT(6); }
                if (i == 26) asm volatile("ds_read_b128 %0, %1" : "=v"(dec) : "v"(L_DEC) : "memory"); });
            PRIO(1);
            PT(3);
            asm volatile("s_waitcnt lgkmcnt(0)" : "+v"(dec));
            have = RFL(s & 1 ? dec.x : dec.y) & 5; pc = RFL(dec.z);
            half_put(L_EX, w8, l, acc);
            WAITV(0);
            if (have & 4) {
#pragma unroll
                for (int i = 0; i < 4; ++i) oissue(s + 1, w8, i); }
            if (have) { oissue(s + 1, w8 ^ 4, 0); oissue(s + 1, w8 ^ 4, 1); }
            PT(4);
            if (!have) { WAITL(); BAR(); }
            fz.finish(fin, f, [&](int i) { if (have) oissue(s + 1, w8 ^ 4, 2 + i); });
            fin += fi;
            MARK(1 + s);
            PT(5);
        }
        WAITL(); BAR();
    }
    PFLUSH();
    MARK(65);
}

__global__ void __launch_bounds__(512, 2) k_mega(Args a) {
    __shared__ __attribute__((aligned(1024))) unsigned char smem[163840];
    const unsigned L = (unsigned)(size_t)(LAS unsigned char*)smem;
    const int b = blockIdx.x, u = (b & 3) + 4 * (b >> 3);
    MARK(255); MARKC(254);
    const unsigned hb = __builtin_amdgcn_raw_buffer_load_b32(mkrs(a.ctl + C_HB, 4), threadIdx.x >> 6 == (b & 4 ? (u < 96 ? 4 : 0) : 3) ? 0 : OOB, 0, 16);
    FENCE();
    if (!(b & 4)) chain(a, u, L, hb);
    else if (u < 96) gwork(a, (u >> 2) % 3, (u & 3) * 8 + (u >> 2) / 3, L, hb);
    else owork(a, (u >> 2) - 24, u & 3, L, hb);
}

static constexpr int g_coff = 0, g_force = 0; static constexpr int g_d1 = 620, g_d2 = 1340, g_per = 1800, g_dinit = 250; static constexpr unsigned* g_dbg = nullptr;
extern "C" void kernel_launch(void* const* d_in, const int* in_sizes, int n_in, void* d_out, int out_size, void* d_ws, size_t ws_size, hipStream_t stream) {
    auto in = [&](int i) { return (const bf16_t*)d_in[i]; };
    unsigned char* ws = (unsigned char*)d_ws; size_t off = g_coff;
    auto alloc = [&](size_t bytes) { unsigned char* p = ws + off; off += (bytes + 255) & ~(size_t)255; return p; };
    unsigned* ctl = (unsigned*)alloc(C_WORDS * 4);
    unsigned* gate = (unsigned*)alloc(2 * S_TOK * 4); unsigned char* ypart = alloc(32 * 29 * 1024);
    off += (size_t)(-(uintptr_t)(ws + off)) & ((2u << 20) - 1);
    unsigned char* ring = alloc(RING_BYTES);
    unsigned char* qkv = alloc((size_t)NSEG * NH * 24576);
    unsigned char* ox = alloc((size_t)NSEG * 262144); unsigned char* part = alloc((size_t)NSEG * 524288);
    Args a{in(0), in(1), in(2), in(3), in(4), in(5), (bf16_t*)d_out, ctl, ring, qkv, ox, part, in(8), in(9), in(10), in(11), in(12), gate, ypart, g_dbg,
           g_d1 * 16 / 10, g_d2 * 16 / 10, g_per * 16 / 10, g_dinit * 16 / 10 + (g_force << 16)};
    k_mega<<<256, 512, 0, stream>>>(a);
}
```

```cpp
#include <hip/hip_runtime.h>
#include <stdint.h>
#include <type_traits>

typedef unsigned short bf16_t;
typedef unsigned long long u64;
typedef float frag_cd __attribute__((ext_vector_type(4)));
typedef unsigned v4u __attribute__((ext_vector_type(4)));
typedef unsigned v8u __attribute__((ext_vector_type(8)));
typedef unsigned v2u __attribute__((ext_vector_type(2)));
typedef float v2f __attribute__((ext_vector_type(2)));
typedef __bf16 bf16x2_t __attribute__((ext_vector_type(2)));
typedef __bf16 v8bf __attribute__((ext_vector_type(8)));
typedef __bf16 v16bf __attribute__((ext_vector_type(16)));
typedef short v4s_t __attribute__((ext_vector_type(4)));
typedef __amdgpu_buffer_rsrc_t rsrc_t;
#define LAS __attribute__((address_space(3)))
#define GAS __attribute__((address_space(1)))

static constexpr int S_TOK = 2048, DM = 1024, NH = 32, DH = 128, MM = 8, SEG = 32, NSEG = 64, HD = NH * DH, FF = 3584, TOPC = 8;
static constexpr int C_ERR = 0, C_READY = 32, C_CAL = 64  , C_ACK = 96, C_G = 128  , C_M = C_G + 4096  ,
                     C_P = C_M + 4096  , C_R = C_P + 4096, C_Y = C_R + 1024  , C_HB = C_Y + 1024, C_WORDS = C_HB + 32;
static constexpr int RING_BYTES = 4 << 20, B_H1 = 64, B_H3 = 96, B_TS = 112;
static constexpr unsigned SPIN_CAP = 1u << 18;
static constexpr int OOB = 0x7ffffff0, LCAP = 1 << 22, GCAP = 1 << 20;
static constexpr float SCALE = 0.08838834764831845f, LOG2E = 1.4426950408889634f;

struct Args {
    const bf16_t* x; const bf16_t* mem0; const bf16_t* Wq; const bf16_t* Wk; const bf16_t* Wv; const bf16_t* Wo;
    bf16_t* out; unsigned* ctl; unsigned char* ring; unsigned char* qkv; unsigned char* ox; unsigned char* part;
    const bf16_t* wr; const bf16_t* wg; const bf16_t* wu; const bf16_t* wd; const bf16_t* gw;
    unsigned* gate  ; unsigned char* ypart; unsigned* dbg;
    int d1, d2, per, dinit;
};

__device__ __forceinline__ unsigned cvt_pk(float lo, float hi) { v2f v = {lo, hi}; return __builtin_bit_cast(unsigned, __builtin_convertvector(v, bf16x2_t)); }
__device__ __forceinline__ float lo16(unsigned p) { return __uint_as_float(p << 16); }
__device__ __forceinline__ float hi16(unsigned p) { return __uint_as_float(p & 0xffff0000u); }
__device__ __forceinline__ float rbf(float f) { return lo16(cvt_pk(f, 0.f)); }
template <int C> __device__ __forceinline__ unsigned dppu(unsigned v) { return (unsigned)__builtin_amdgcn_update_dpp(0, (int)v, C, 0xf, 0xf, true); }
template <int C> __device__ __forceinline__ float dppf(float v) { return __uint_as_float(dppu<C>(__float_as_uint(v))); }
#define ROR8 0x128
#define HMIR 0x141
__device__ __forceinline__ v2u sw16(unsigned a, unsigned b) { auto r = __builtin_amdgcn_permlane16_swap(a, b, false, false); return v2u{r[0], r[1]}; }
__device__ __forceinline__ v2u sw32(unsigned a, unsigned b) { auto r = __builtin_amdgcn_permlane32_swap(a, b, false, false); return v2u{r[0], r[1]}; }
__device__ __forceinline__ float tadd16(float a, float b) { v2u r = sw16(__float_as_uint(a), __float_as_uint(b)); return __uint_as_float(r.x) + __uint_as_float(r.y); }
__device__ __forceinline__ float tadd32(float a, float b) { v2u r = sw32(__float_as_uint(a), __float_as_uint(b)); return __uint_as_float(r.x) + __uint_as_float(r.y); }
__device__ __forceinline__ float tmax16(float a) { v2u r = sw16(__float_as_uint(a), __float_as_uint(a)); return fmaxf(__uint_as_float(r.x), __uint_as_float(r.y)); }
__device__ __forceinline__ float tmax32(float a) { v2u r = sw32(__float_as_uint(a), __float_as_uint(a)); return fmaxf(__uint_as_float(r.x), __uint_as_float(r.y)); }
__device__ __forceinline__ unsigned umax3(unsigned a, unsigned b, unsigned c) { return max(max(a, b), c); }
__device__ __forceinline__ v4u pack_swap(frag_cd a, frag_cd b) { v2u x0 = sw16(cvt_pk(a[0], a[1]), cvt_pk(b[0], b[1])), x1 = sw16(cvt_pk(a[2], a[3]), cvt_pk(b[2], b[3])); return v4u{x0.x, x1.x, x0.y, x1.y}; }
#define MFMA(a, b, c) __builtin_amdgcn_mfma_f32_16x16x32_bf16(__builtin_bit_cast(v8bf, a), __builtin_bit_cast(v8bf, b), c, 0, 0, 0)
#define SMF(a, b, c, ix) __builtin_amdgcn_smfmac_f32_16x16x64_bf16(__builtin_bit_cast(v8bf, a), __builtin_bit_cast(v16bf, b), c, ix, 0, 0)
#define FENCE() asm volatile("" ::: "memory")
#define SB() __builtin_amdgcn_sched_barrier(0)
#define RFL(x) __builtin_amdgcn_readfirstlane(x)
#define BAR() do { FENCE(); __builtin_amdgcn_s_barrier(); FENCE(); } while (0)
#define WAITV(n) do { FENCE(); __builtin_amdgcn_s_waitcnt(0x0F70 | (n)); FENCE(); } while (0)
#define WAITL() asm volatile("s_waitcnt lgkmcnt(0)" ::: "memory")
__device__ __forceinline__ void STB(v4u v, rsrc_t rs, int off, int soff) { __builtin_amdgcn_raw_buffer_store_b128(v, rs, off, soff, 16); asm volatile("s_nop 1" :: "v"(v) : "memory"); }
template <class T> __device__ __forceinline__ LAS T* lp(unsigned a) { return (LAS T*)(size_t)a; }
__device__ __forceinline__ void glds(const void* g, unsigned voff, unsigned lds) { asm volatile("s_mov_b32 m0, %2\n\ts_nop 0\n\tglobal_load_lds_dwordx4 %0, %1" :: "v"(voff), "s"(g), "s"(lds) : "memory"); }
#define NTC " nt"
#define NTW " nt"
__device__ __forceinline__ void gldsc(const void* g, unsigned voff, unsigned lds) { asm volatile("s_mov_b32 m0, %2\n\ts_nop 0\n\tglobal_load_lds_dwordx4 %0, %1" NTC :: "v"(voff), "s"(g), "s"(lds) : "memory"); }
__device__ __forceinline__ void gldsw(const void* g, unsigned voff, unsigned lds) { asm volatile("s_mov_b32 m0, %2\n\ts_nop 0\n\tglobal_load_lds_dwordx4 %0, %1" NTW :: "v"(voff), "s"(g), "s"(lds) : "memory"); }
template <int N> __device__ __forceinline__ void glds1(const void* g, unsigned voff, unsigned lds) {
#define GL(o) "\n\tglobal_load_lds_dwordx4 %0, %1 offset:" #o " sc1"
    if constexpr (N == 1) asm volatile("s_mov_b32 m0, %2\n\ts_nop 0" GL(0) :: "v"(voff), "s"(g), "s"(lds) : "memory");
    if constexpr (N == 2) asm volatile("s_mov_b32 m0, %2\n\ts_nop 0" GL(0) GL(1024) :: "v"(voff), "s"(g), "s"(lds) : "memory");
    if constexpr (N == 4) asm volatile("s_mov_b32 m0, %2\n\ts_nop 0" GL(0) GL(1024) GL(2048) GL(3072) :: "v"(voff), "s"(g), "s"(lds) : "memory");
}
__device__ __forceinline__ rsrc_t mkrs(const void* p, int bytes) { return __builtin_amdgcn_make_buffer_rsrc((void*)p, 0, bytes, 0x00020000); }
__device__ __forceinline__ unsigned gld(const unsigned* p) { return __hip_atomic_load(p, __ATOMIC_RELAXED, __HIP_MEMORY_SCOPE_AGENT); }
__device__ __forceinline__ void gst(unsigned* p, unsigned v) { __hip_atomic_store(p, v, __ATOMIC_RELAXED, __HIP_MEMORY_SCOPE_AGENT); }
__device__ __forceinline__ void gst1(unsigned* p, unsigned v) { asm volatile("s_mov_b64 exec, 1\n\tglobal_store_dword %0, %1, %2 sc1\n\ts_mov_b64 exec, -1" :: "v"(0u), "v"(v), "s"(p) : "memory"); }
__device__ __forceinline__ unsigned rt16() { return (unsigned)__builtin_amdgcn_s_memrealtime() << 4; }

#define NSLOT 12
#define PT(k) do {} while (0)
#define PFLUSH() do {} while (0)
#define MARK(i) do {} while (0)
#define MARKC(i) do {} while (0)

#define H3X 0
#define EW 30
#define E0 32
#define E2 60
#define E3 120
#define AT 0
#define XH 0
#define XP 0
#define VR 0
#define DWM 4
#define NQ 1130
#ifndef NV
#define NV 960
#endif
#define LXC 200
#define TSL 1
#define RSL 1
#define VDEL1 15
#define AEARLY 1
#define XC 4
#define ODEL 1
#define SMQ 0x53b
#define SMV 0x79b
#define AL1 1
#define AL2 0
#define AL3 0
#define AL4 1
#define AL5 7
#define AL6 0
#define HB 0
#define HP 0
#define H3 0
#define HSM 0x2d
#define PF 22
#define US 1
#define ROT 0
#define VL 1
#define VP 0x3911
#define VE 0
#define VN3 36
#define CV 0
#define CD 60
#define XS1 0
#define XS2 6
#define PLO 0
#define MSP 1
#define VD 260
#define LK 300
#define LT 400
#define NS 16
#define GS 6
#define KC 560
#define GM 200
#define SGAP 60
#define HSL 6
#define CSL 3
#define WK 40
#define WKO 100
#define WB 200
#define WC1 0
#define WC2 64
#define WTHR 340
#define SLIP_THR 400
#define CKA 0.75
#define CKF 22.5
#define TG_THR 480
#define SLIP_ADD 640
#define SLIP_INC 10
__device__ __forceinline__ void endpgm() { asm volatile("s_endpgm" ::: "memory"); }
__device__ __forceinline__ void fatal(unsigned* err) { asm volatile("global_store_dword %0, %1, off sc1\n\ts_waitcnt vmcnt(0)\n\ts_endpgm" :: "v"(err), "v"(1u) : "memory"); }
__device__ __forceinline__ unsigned ldsr(unsigned p) { return __hip_atomic_load(lp<unsigned>(p), __ATOMIC_RELAXED, __HIP_MEMORY_SCOPE_WORKGROUP); }
__device__ __forceinline__ v2u trd(unsigned p) { return __builtin_bit_cast(v2u, __builtin_amdgcn_ds_read_tr16_b64_v4i16(lp<v4s_t>(p))); }
__device__ __forceinline__ v8u cat(v2u a, v2u b, v2u c, v2u d) { return v8u{a.x, a.y, b.x, b.y, c.x, c.y, d.x, d.y}; }
__device__ __forceinline__ v4u cat2(v2u a, v2u b) { return v4u{a.x, a.y, b.x, b.y}; }
template <int SL, int FAR = 0> __device__ __forceinline__ void flags_wait(unsigned& v, const unsigned* p, unsigned need, unsigned* err) {
    for (int i = 0; __builtin_amdgcn_ballot_w64(v < need) != 0; ++i) {
        if (i) { if (FAR && __builtin_amdgcn_ballot_w64(v + 1 < need) != 0) __builtin_amdgcn_s_sleep(FAR); __builtin_amdgcn_s_sleep(SL); }
        if (v < need) v = gld(p);
        if (i > GCAP) fatal(err); }
    FENCE();
}
template <int SL> __device__ __forceinline__ void flags_wait(const unsigned* p, int nw, unsigned need, int l, unsigned* err) { unsigned v = need - 1; flags_wait<SL>(v, p + min(l, nw - 1) * 32, need, err); }

__device__ __forceinline__ void gadd1(unsigned* p) { asm volatile("s_mov_b64 exec, 1\n\tglobal_atomic_add %0, %1, off sc1\n\ts_mov_b64 exec, -1" :: "v"(p), "v"(1u) : "memory"); }
__device__ __forceinline__ void gld_req(unsigned& v, const unsigned* p) { asm volatile("global_load_dword %0, %1, off sc1" : "+v"(v) : "v"(p) : "memory"); }
template <int N> __device__ __forceinline__ void gld_wait(unsigned& v) { asm volatile("s_waitcnt vmcnt(%1)" : "+v"(v) : "n"(N) : "memory"); }
__device__ __forceinline__ void ctl_wait(const Args& a, unsigned hb) {
    for (int i = 0; RFL(gld(a.ctl + C_HB)) == RFL(hb); ++i) { __builtin_amdgcn_s_sleep(8); if (i > GCAP) fatal(a.ctl + C_ERR); }
    FENCE();
}

typedef unsigned v16u __attribute__((ext_vector_type(16)));
typedef unsigned v32u __attribute__((ext_vector_type(32)));
#define STR_(x) #x
#define STR(x) STR_(x)
#define SET(x) "\n.set " #x ", " STR(x)
#define A_STAMPS "0"
#define A_BENCH "0"
static_assert(NH * 24576 == 0xC0000, "");
#define A_CHAIN ".set STAMPS, " A_STAMPS "\n.set BENCH, " A_BENCH "\n.set WB, " STR(WB) "\n.set WC1, " STR(WC1) "\n.set WC2, " STR(WC2) SET(SMQ) SET(SMV) SET(AL1) SET(AL2) SET(AL3) SET(AL4) SET(AL5) SET(AL6) SET(HB) SET(US) SET(PF) SET(HP) SET(H3) "\n.set HS, " STR(HSM) SET(VL) SET(VP) SET(VE) SET(VN3) SET(CV) SET(CD) SET(XS1) SET(XS2) SET(PLO) SET(CSL) SET(MSP) SET(VD) SET(LK) SET(LT) SET(NS) SET(GS) SET(GM) SET(SGAP) SET(NQ) SET(NV) SET(LXC) SET(TSL) SET(RSL) SET(VR) SET(DWM) SET(AT) SET(EW) SET(E0) SET(E2) SET(E3) SET(HSL) "\n.set WK, " STR(WK) "\n.set WN, 64\n.set WZ, WN+5\n.set WTHR, " STR(WTHR) "\n" R"(
.macro SLED n
  s_getpc_b64 vcc
4: s_sub_u32 s77, (3f-4b)/4, s77
  s_lshl2_add_u32 vcc_lo, s77, vcc_lo
  s_addc_u32 vcc_hi, vcc_hi, 0
  s_setpc_b64 vcc
.rept \n
  s_nop 1
.endr
3: s_waitcnt lgkmcnt(0)
.endm
.macro WAITC T, thr
  s_movk_i32 s76, 0x1000
1: s_memrealtime vcc
  s_memtime s[6:7]
  s_sub_u32 s76, s76, 1
  s_cbranch_scc1 3f
  s_waitcnt lgkmcnt(0)
  s_lshl_b32 s77, vcc_lo, 4
  s_sub_i32 s77, \T, s77
  s_cmp_ge_i32 s77, \thr
  s_cbranch_scc1 1b
.endm
.macro WAITT T
  WAITC \T, 64
3: s_waitcnt lgkmcnt(0)
.endm
.macro WAITP T
  WAITC \T, WTHR
  s_sub_i32 s77, s77, WC1
  s_cmp_lt_i32 s77, WB
  s_cbranch_scc1 5f
.irp r,30,32,34,36,38,94,96,98
  s_memrealtime s[\r:\r+1]
  s_nop 1
.endr
  s_memtime s[6:7]
  s_sub_i32 s77, \T, WC2
  s_sub_i32 s4, s4, EW
  s_waitcnt lgkmcnt(0)
  s_add_u32 s30, s30, s32
  s_add_u32 s34, s34, s36
  s_add_u32 s38, s38, s94
  s_add_u32 s96, s96, s98
  s_add_u32 s30, s30, s34
  s_add_u32 s38, s38, s96
  s_add_u32 s30, s30, s38
  s_lshl_b32 s30, s30, 1
  s_sub_i32 s77, s77, s30
5: s_max_i32 s77, s77, 0
  s_mulk_i32 s77, 45
  s_lshr_b32 s77, s77, 8
  s_sub_i32 s6, s4, s6
  s_ashr_i32 s6, s6, 3
  s_max_i32 s77, s77, s6
  s_min_u32 s77, s77, WN
  SLED WN
.endm
.macro WAITK x, nom
  s_add_i32 s5, s59, \x-\nom
  s_add_i32 s75, s92, s59
  s_movk_i32 s76, 0x1000
  s_add_i32 s75, s75, \x
1: s_memtime vcc
  s_sub_u32 s76, s76, 1
  s_cbranch_scc1 3f
  s_waitcnt lgkmcnt(0)
  s_sub_i32 s77, s75, vcc_lo
  s_sub_i32 s7, s6, vcc_lo
  s_add_i32 s7, s7, s5
  s_max_i32 s77, s77, s7
  s_cmp_ge_i32 s77, 8*WK
  s_cbranch_scc1 1b
  s_max_i32 s77, s77, 0
  s_lshr_b32 s77, s77, 3
  SLED WK
.endm
.macro H1ST
.if AT == 1
  buffer_atomic_swap v[RES], v[ST1], s[40:43], s66 offen sc1
.elseif AT == 2
  buffer_atomic_and v[RES], v[ST1], s[40:43], s66 offen sc1
.elseif AT == 3
  buffer_store_dword v[RES], v[ST1], s[40:43], s66 offen sc0 sc1
.elseif AT == 4
  buffer_store_dword v[RES], v[ST1], s[40:43], s66 offen sc1 nt
.else
  buffer_store_dword v[RES], v[ST1], s[40:43], s66 offen sc1
.endif
.endm
.macro ALN k
.p2align 6
.rept \k
  s_nop 0
.endr
.endm
.macro HT n, r
.rept \n
  v_mov_b32 v[\r], 0
  s_nop 1
.endr
.endm
.macro TMAX a, t
  v_readlane_b32 \t, v[\a], 0
.irp r,1,2,3
  v_readlane_b32 s77, v[\a], \r
  s_sub_i32 s77, s77, \t
  s_max_i32 s77, s77, 0
  s_add_i32 \t, \t, s77
.endr
.endm
.macro TGET a
  v_mbcnt_lo_u32_b32 v[\a+1], 7, 0
  v_lshlrev_b32 v[\a+1], 7, v[\a+1]
  s_add_i32 s75, s88, s68
8: buffer_load_dword v[\a], v[\a+1], s[40:43], s75 offen sc1
  s_sub_u32 s71, s71, 1
  s_cbranch_scc1 L_fatal
  s_waitcnt vmcnt(0)
  v_cmp_eq_u32 vcc, -1, v[\a]
  s_cbranch_vccz 9f
  s_sleep TSL
  s_branch 8b
9:
  TMAX \a, s75
  s_mov_b32 s4, s92
  WAITP s75
  s_mov_b32 s76, 0x400000
.endm
.macro PLAN
  s_mulk_i32 s74, PF
  s_ashr_i32 s74, s74, 3
  s_add_i32 s73, s73, s58
  s_add_i32 s73, s73, s74
.endm
.macro STAMP k
.if STAMPS && ((SMK >> \k) & 1)
  s_memtime s[8+2*\k:9+2*\k]
.endif
.endm
.macro NEXT t
.if STAMPS
  s_waitcnt lgkmcnt(0)
.irp k,0,1,2,3,4,5,6,7,8,9,10
  v_writelane_b32 v[\t], s[8+2*\k], \k
.endr
  s_lshl_b32 s76, s64, 3
  s_add_i32 s76, s76, s63
  s_mul_i32 s76, s76, 48
  v_mbcnt_lo_u32_b32 v[\t+1], -1, 0
  v_lshl_add_u32 v[\t+1], v[\t+1], 2, s76
  s_mov_b64 exec, 0x7ff
  ds_write_b32 v[\t+1], v[\t]
  s_mov_b64 exec, -1
.endif
  s_lshl_b32 s72, s64, 18
  s_mov_b32 s64, s65
  s_add_i32 s65, s65, 1
  s_xor_b32 s66, s66, 0x8000
  s_xor_b32 s67, s67, 0x8000
  s_mov_b32 s76, s68
  s_mov_b32 s68, s69
  s_mov_b32 s69, s70
  s_mov_b32 s70, s76
.endm
.macro RETRY c
.if BENCH
  s_bitset1_b64 s[80+2*\c:81+2*\c], s64
.endif
  s_sub_u32 s71, s71, 1
  s_cbranch_scc1 L_fatal
  s_sleep RSL
.endm
.macro LD d, a, so, o
  buffer_load_dwordx4 v[\d:\d+3], v[\a], s[40:43], \so offen offset:\o sc1
.endm
.macro SM d, a, b
  v_smfmac_f32_16x16x64_bf16 v[\d:\d+3], v[\a:\a+3], v[\b:\b+7], v[SIX]
.endm
.macro MX0 a
  v_max_u32 v[MX], v[\a], v[\a+1]
  v_max3_u32 v[MX], v[MX], v[\a+2], v[\a+3]
.endm
.macro MX1 a
  v_max3_u32 v[MX], v[MX], v[\a], v[\a+1]
  v_max3_u32 v[MX], v[MX], v[\a+2], v[\a+3]
.endm
.macro AW r, k
  ds_write_addtid_b32 v[\r] offset:63744+256*\k
.endm
.macro HS a
  v_pk_add_f32 v[\a:\a+1], v[\a:\a+1], v[\a+2:\a+3]
.endm
.macro ZR a, n
.set zi, 0
.rept \n
  v_mov_b64 v[\a+zi:\a+zi+1], 0
.set zi, zi+2
.endr
.endm
.macro PRE
  s_mov_b32 s64, 63
  s_mov_b32 s65, 0
  s_mov_b32 s66, 0x8000
  s_mov_b32 s67, 0
  s_mov_b32 s68, 0x8000
  s_mov_b32 s69, 0x10000
  s_mov_b32 s70, 0
.endm
.macro XSL r
  s_and_b32 s75, \r, 3
  s_mulk_i32 s75, 20480
.endm
.macro CAP
  s_sub_u32 s76, s76, 1
  s_cbranch_scc1 L_fatal
.endm
  s_mov_b32 s71, 0x40000
  s_mov_b32 s88, s64
  s_mov_b32 s90, s65
  s_mov_b32 s91, s66
  s_mov_b64 s[80:81], 0
  s_mov_b64 s[82:83], 0
  s_mov_b64 s[84:85], 0
  s_mov_b64 s[86:87], 0
  s_mov_b32 s100, s67
  s_mov_b32 s101, s68
  s_cmp_gt_u32 s63, 3
  s_cbranch_scc1 LV_init
; ======== waves 0..3
.set SMK, SMQ
.set O3,168
.set ST1,169
.set RW1,170
.set RW2,171
.set RW3,172
.set XA,173
.set FW,174
.set FR,175
.set SIX,176
.set STK,177
.set FL,183
.set MB,128
.set AC,144
.set TAG,160
.set MX,161
.set RES,162
.set FA,163
.set FB,164
.set VA,164
.set VB,184
.set WV,188
.set RA,134
.set KX,196
.set XF,177
.set XC,)" STR(XC) R"(
.macro MEMQ
  LD MB, O3, s68, 0
  LD MB+4, O3, s68, 1024
  LD MB+8, O3, s68, 2048
  LD MB+12, O3, s68, 3072
.endm
.macro XCHK R
  s_add_i32 s75, s64, 2
  s_min_u32 s75, s75, 63
  v_cmp_ge_u32 vcc, s75, v[FL]
  s_cbranch_vccnz LX_w\R
LX_g\R:
  s_add_i32 s75, s64, 2
  XSL s75
  s_add_i32 m0, s75, s61
.endm
.macro XL4 a
.irp o,0,1024,2048,3072
  global_load_lds_dwordx4 v[\a], s[52:53] offset:\o sc1
.endr
  s_add_i32 m0, m0, 0x1000
.endm
.macro XREF R
  s_add_i32 s75, s64, 3
  s_min_u32 s75, s75, 63
  v_cmp_ge_u32 vcc, s75, v[FL]
  s_cbranch_vccz LX_n\R
  global_load_dword v[FL], v[XF], s[54:55] sc1
LX_n\R:
.endm
.macro XADV
  s_cmp_lt_u32 s64, 61
  s_cselect_b32 s75, 0xC0000, 0
  s_add_u32 s52, s52, s75
  s_addc_u32 s53, s53, 0
.endm
.macro XWAIT R
LX_w\R:
  s_mov_b32 s76, 0x100000
1: global_load_dword v[FL], v[XF], s[54:55] sc1
  s_waitcnt vmcnt(0)
  v_cmp_ge_u32 vcc, s75, v[FL]
  s_cbranch_vccz LX_g\R
  s_sleep 4
  CAP
  s_branch 1b
.endm
.macro OXST
  buffer_store_dwordx4 v[KX:KX+3], v[STK], s[44:47], s72 offen sc1
  buffer_store_dwordx4 v[KX+4:KX+7], v[STK], s[44:47], s72 offen offset:1024 sc1
.endm
.macro FLAGQ val
  v_mov_b32 v[MX], 0
  v_mov_b32 v[FA], \val
  s_mov_b64 exec, 1
  global_store_dword v[MX], v[FA], s[50:51] sc1
  s_mov_b64 exec, -1
.endm
.macro SMW i
  SM VA, MB+4*\i, WV+8*\i
  SM VB, MB+4*\i, WV+32+8*\i
.endm
.macro SMN k
.if (VP >> \k) & 1
  SM VA+(VB-VA)*(vn&1), MB+4*(vn>>1), WV+32*(vn&1)+8*(vn>>1)
.set vn, vn+1
.endif
.endm
.macro VOUT
  v_add_f32 v[VA], v[VA], v[VA+2]
  v_add_f32 v[VA+1], v[VA+1], v[VA+3]
  v_add_f32 v[VB], v[VB], v[VB+2]
  v_add_f32 v[VB+1], v[VB+1], v[VB+3]
  AW VA, 0
  AW VA+1, 2
  AW VB, 1
  AW VB+1, 3
  AW TAG, 6
.endm
.macro DM k
.if (HS >> \k) & 1
  v_mfma_f32_16x16x32_bf16 v[VA:VA+3], v[MB:MB+3], v[MB:MB+3], 0
.endif
.endm
.macro ZA
  ZR AC, 8
  ZR VA, 2
  ZR VB, 2
.endm
.macro PHASEA R
LQ_loop\R:
  STAMP 0
  ZA
  v_mov_b32 v[TAG], s65
LQ_bar\R:
  s_barrier
  STAMP 1
.if MSP
  LD MB, O3, s68, 0
  s_memtime s[92:93]
  s_nop MSP-1
  LD MB+4, O3, s68, 1024
  s_mov_b32 m0, s100
  s_nop MSP-1
  LD MB+8, O3, s68, 2048
  s_nop MSP
  LD MB+12, O3, s68, 3072
.else
  s_memtime s[92:93]
  MEMQ
  s_mov_b32 m0, s100
.endif
.set XS, 0
.if \R == 2
.set XS, 2
.irp i,0,1,4,5,6,7,8,9,10,11,12,13,14,15
  ds_read_b128 v[WV+4*\i:WV+4*\i+3], v[XA] offset:4096+1024*\i
.if \i == 9
  s_waitcnt lgkmcnt(6)
.endif
.endr
.rept )" STR(ODEL) R"(
  s_nop 7
.endr
  OXST
  s_nop 1
.irp i,2,3
  ds_read_b128 v[WV+4*\i:WV+4*\i+3], v[XA] offset:4096+1024*\i
.endr
  s_waitcnt lgkmcnt(0)
.endif
  s_mov_b32 s76, 0x400000
  ALN AL1
LQ_a\R:
  s_waitcnt vmcnt(3+XS)
  SM AC+8, MB, 64
  v_max_u32 v[MX], v[MB], v[MB+1]
  SM AC+12, MB, 96
  v_max3_u32 v[MX], v[MX], v[MB+2], v[MB+3]
  SM AC, MB, 0
  SM AC+4, MB, 32
  SMW 0
.irp i,1,2,3
  s_waitcnt vmcnt(3+XS-\i)
  SM AC+8, MB+4*\i, 64+8*\i
  v_max3_u32 v[MX], v[MX], v[MB+4*\i], v[MB+4*\i+1]
  SM AC+12, MB+4*\i, 96+8*\i
  v_max3_u32 v[MX], v[MX], v[MB+4*\i+2], v[MB+4*\i+3]
  SM AC, MB+4*\i, 8*\i
.if \i == 3
  v_cmp_eq_u32 vcc, -1, v[MX]
.endif
  SM AC+4, MB+4*\i, 32+8*\i
.if \i < VL
  SMW \i
.endif
.endr
  STAMP 2
  v_add_f32 v[AC+8], v[AC+8], v[AC+10]
  v_add_f32 v[AC+12], v[AC+12], v[AC+14]
  v_add_f32 v[AC+9], v[AC+9], v[AC+11]
  v_add_f32 v[AC+13], v[AC+13], v[AC+15]
  v_add_f32 v[AC+3], v[AC+1], v[AC+3]
  v_add_f32 v[AC], v[AC], v[AC+2]
  v_add_f32 v[AC+5], v[AC+5], v[AC+7]
  s_cbranch_vccnz LQ_ra\R
.set vn, 2*VL
  AW AC+8, 0
  SMN 0
  AW AC+12, 1
  SMN 1
  AW AC+9, 2
  SMN 2
  AW AC+13, 3
  SMN 3
  AW AC+3, 4
  SMN 4
  AW AC+5, 5
  SMN 5
  AW TAG, 6
  SMN 6
  STAMP 6
  s_barrier
  SMN 7
  ds_read_b32 v[FA], v[FR]
  SMN 8
  ds_read2st64_b32 v[AC+8:AC+9], v[RW1] offset1:1
  SMN 9
  ds_read2st64_b32 v[AC+10:AC+11], v[RW2] offset1:1
  SMN 10
  ds_read2st64_b32 v[AC+12:AC+13], v[RW3] offset1:1
  SMN 11
  s_add_i32 m0, s100, 0x2000
  v_add_f32 v[AC+1], v[AC+4], v[AC+6]
  SMN 12
  SMN 13
  SMN 14
.if vn != 8
.error "v projection incomplete"
.endif
.if VE
  VOUT
.endif
  s_waitcnt lgkmcnt(5*VE)
LQ_p\R:
  v_cmp_gt_u32 vcc, s65, v[FA]
  v_add_f32 v[AC+8], v[AC+8], v[AC]
  v_add_f32 v[AC+9], v[AC+9], v[AC+1]
  v_add_f32 v[AC+8], v[AC+8], v[AC+10]
  v_add_f32 v[AC+9], v[AC+9], v[AC+11]
  v_add_f32 v[AC+8], v[AC+8], v[AC+12]
  v_add_f32 v[AC+9], v[AC+9], v[AC+13]
  v_cvt_pk_bf16_f32 v[RES], v[AC+8], v[AC+9]
  s_cbranch_vccnz LQ_pb\R
  H1ST
  s_memtime s[6:7]
  STAMP 3
.if !VE
  VOUT
.endif
.if \R != 2
  s_cmp_eq_u32 s64, 63
  s_cbranch_scc1 L_last
.endif
.if \R != 3
  WAITK 0, NQ
.endif
  STAMP 4
.endm
.macro COLDA R
LQ_pb\R:
.if BENCH && \R != 3
  s_bitset1_b64 s[86:87], s64
.endif
  CAP
  ds_read_b32 v[FA], v[FR]
  ds_read2st64_b32 v[AC+8:AC+9], v[RW1] offset1:1
  ds_read2st64_b32 v[AC+10:AC+11], v[RW2] offset1:1
  ds_read2st64_b32 v[AC+12:AC+13], v[RW3] offset1:1
  s_waitcnt lgkmcnt(0)
  s_branch LQ_p\R
LQ_ra\R:
  RETRY 2
  MEMQ
  s_add_i32 s59, s59, LXC
  TGET RES
  s_cmp_eq_u32 s77, WZ
  s_cbranch_scc1 7f
  MEMQ
7:
  ZA
  s_waitcnt vmcnt(0)
  s_branch LQ_a\R
.endm
  s_mov_b32 s64, 0
  s_mov_b32 s65, 1
  s_mov_b32 s66, 0
  s_mov_b32 s67, 0x8000
  s_mov_b32 s68, 0x10000
  s_mov_b32 s69, 0
  s_mov_b32 s70, 0x8000
  s_mov_b32 s72, 0xFC0000
  s_setprio 3
  s_cmp_eq_u32 s60, 2
  s_cbranch_scc1 LQ_init2
  s_cmp_eq_u32 s60, 3
  s_cbranch_scc1 LQ_init3
; ---- waves 0,1: scores against the x rows
  PHASEA 0
  LD MB, 178, s66, 0
  LD MB+4, 178, s66, 1024
  XSL s64
  v_add_u32 v[FA], s75, v[179]
  ds_read_b128 v[MB+8:MB+11], v[FA]
  ds_read_b128 v[MB+12:MB+15], v[FA] offset:1024
  ds_read_b128 v[AC+8:AC+11], v[FA] offset:2048
  ds_read_b128 v[AC+12:AC+15], v[FA] offset:3072
  s_waitcnt lgkmcnt(0)
  ALN AL2
LQ_b0:
  s_waitcnt vmcnt(1)
  v_mfma_f32_16x16x32_bf16 v[AC+4:AC+7], v[MB+12:MB+15], v[MB:MB+3], 0
  MX0 MB
  v_mfma_f32_16x16x32_bf16 v[AC:AC+3], v[MB+8:MB+11], v[MB:MB+3], 0
  s_waitcnt vmcnt(0)
  v_mfma_f32_16x16x32_bf16 v[AC+4:AC+7], v[AC+12:AC+15], v[MB+4:MB+7], v[AC+4:AC+7]
  MX1 MB+4
  v_mfma_f32_16x16x32_bf16 v[AC:AC+3], v[AC+8:AC+11], v[MB+4:MB+7], v[AC:AC+3]
  v_cmp_eq_u32 vcc, -1, v[MX]
  s_nop 5
  s_cbranch_vccnz LQ_rb0
.irp e,0,1,2,3
  v_add_f32_dpp v[AC+\e], v[AC+4+\e], v[AC+\e] quad_perm:[2,3,0,1] row_mask:0xf bank_mask:0xf
.endr
  DM 0
  ds_write_b128 v[180], v[AC:AC+3]
  DM 1
  ds_write_b32 v[FW], v[TAG] offset:2048
  DM 2
  s_barrier
  STAMP 5
  s_setprio PLO
  HT HP, VB
.if XS1
  s_sleep XS1
.endif
  XCHK 0
  s_nop 0
  XL4 181
.if XC == 4
  s_nop 0
  XL4 182
.endif
  XREF 0
.if XS2
  s_sleep XS2
.endif
  XADV
  v_mov_b32 v[FA], s62
  ds_read_b32 v[RA], v[FA] offset:8
  s_waitcnt lgkmcnt(0)
  v_readfirstlane_b32 s59, v[RA]
  NEXT MB
  s_setprio 3
  s_branch LQ_loop0
  COLDA 0
LQ_rb0:
  RETRY 0
  LD MB, 178, s66, 0
  LD MB+4, 178, s66, 1024
  s_branch LQ_b0
  XWAIT 0
; ---- wave 2: scores against the memory rows; attention of 16 x rows (64 columns)
.set AQ,181
.set AVF,182
.set AVM,183
.set RA,184
.set QX,196
.set OA,212
.set MSK,228
.set LX,237
.set PB,238
.set S1,240
.set K7,244
.set P4,244
.macro MF d, a, b, c
  v_mfma_f32_16x16x32_bf16 v[\d:\d+3], v[\a:\a+3], v[\b:\b+3], \c
.endm
.macro XQ op, r
  v_mov_b32 v[FB], v[\r]
  s_nop 1
  v_permlane16_swap_b32 v[\r], v[FB]
  \op v[\r], v[\r], v[FB]
  v_mov_b32 v[FB], v[\r]
  s_nop 1
  v_permlane32_swap_b32 v[\r], v[FB]
  \op v[\r], v[\r], v[FB]
.endm
.macro QKQ
  LD MB, 178, s66, 0
  LD MB+4, 179, s66, 0
  LD MB+8, 178, s66, 1024
  LD MB+12, 179, s66, 1024
.endm
.macro SOFTX
  v_add_f32 v[AC+6], v[AC+4], v[S1+2]
  v_add_f32 v[AC+7], v[AC+5], v[S1+3]
  v_max3_f32 v[MX], v[LX], v[AC+6], v[AC+7]
  XQ v_max_f32, MX
  v_mul_f32 v[MX], s74, v[MX]
.irp i,6,7,8,9,10,11,12,13,14,15
  v_fma_f32 v[AC+\i], v[AC+\i], s73, v[MX]
.endr
.irp i,6,7,8,9,10,11,12,13,14,15
  v_exp_f32 v[AC+\i], v[AC+\i]
.endr
  v_add_f32 v[LX], v[AC+6], v[AC+7]
.irp i,8,9,10,11,12,13,14,15
  v_add_f32 v[LX], v[LX], v[AC+\i]
.endr
  XQ v_add_f32, LX
  v_rcp_f32 v[LX], v[LX]
  s_nop 0
.irp i,6,7,8,9,10,11,12,13,14,15
  v_mul_f32 v[AC+\i], v[AC+\i], v[LX]
.endr
.irp i,0,1,2,3
  v_cvt_pk_bf16_f32 v[P4+\i], v[AC+8+2*\i], v[AC+9+2*\i]
.endr
  v_cvt_pk_bf16_f32 v[PB], v[AC+6], v[AC+7]
.endm
LQ_init2:
  s_mov_b32 s73, 0x3e0293ee
  s_mov_b32 s74, 0xbe0293ee
  PHASEA 2
  QKQ
  XSL s64
  ds_read_b128 v[MSK:MSK+3], v[XA]
  ds_read_b128 v[MSK+4:MSK+7], v[XA] offset:1024
  v_add_u32 v[FB], s75, v[AQ]
  v_add_u32 v[RES], s75, v[AVF]
  v_mov_b32 v[PB+1], 0
.irp i,0,1,2,3
  ds_read_b128 v[QX+4*\i:QX+4*\i+3], v[FB] offset:16384+1024*\i
  ds_read_b128 v[OA+4*\i:OA+4*\i+3], v[FB] offset:1024*\i
.endr
.irp i,0,1,2
  ds_read_b128 v[RA+4*\i:RA+4*\i+3], v[FB] offset:4096+1024*\i
.endr
  ds_read_b128 v[K7:K7+3], v[FB] offset:7168
  s_waitcnt lgkmcnt(10)
  MF AC+8, OA, QX, v[MSK:MSK+3]
.irp i,4,8,12
  s_waitcnt lgkmcnt(10-\i/2)
  MF AC+8, OA+\i, QX+\i, v[AC+8:AC+11]
.endr
  s_waitcnt lgkmcnt(0)
  MF AC+12, RA, QX, v[MSK+4:MSK+7]
  MF AC+12, RA+4, QX+4, v[AC+12:AC+15]
  MF AC+12, RA+8, QX+8, v[AC+12:AC+15]
  MF AC+12, K7, QX+12, v[AC+12:AC+15]
  v_max3_f32 v[LX], v[AC+8], v[AC+9], v[AC+10]
  s_nop 6
  v_max3_f32 v[LX], v[LX], v[AC+11], v[AC+12]
  v_max3_f32 v[LX], v[LX], v[AC+13], v[AC+14]
  v_max_f32 v[LX], v[LX], v[AC+15]
  STAMP 10
  ALN AL3
LQ_b2:
  s_waitcnt vmcnt(3)
  MX0 MB
  s_waitcnt vmcnt(2)
  v_mfma_f32_16x16x32_bf16 v[AC:AC+3], v[MB+4:MB+7], v[MB:MB+3], 0
  MX1 MB+4
  s_waitcnt vmcnt(1)
  MX1 MB+8
  s_waitcnt vmcnt(0)
  v_mfma_f32_16x16x32_bf16 v[AC:AC+3], v[MB+12:MB+15], v[MB+8:MB+11], v[AC:AC+3]
  MX1 MB+12
  v_cmp_eq_u32 vcc, -1, v[MX]
  s_nop 3
  s_cbranch_vccnz LQ_rb2
  v_add_f32_dpp v[AC], v[AC+2], v[AC] quad_perm:[2,3,0,1] row_mask:0xf bank_mask:0xf
  v_add_f32_dpp v[AC+1], v[AC+3], v[AC+1] quad_perm:[2,3,0,1] row_mask:0xf bank_mask:0xf
  DM 3
  ds_write_b64 v[180], v[AC:AC+1]
  DM 4
  ds_write_b32 v[FW], v[TAG] offset:2048
  DM 5
  s_barrier
  STAMP 5
  s_setprio 0
  MF AC+4, MB+4, QX, 0
  MF S1, MB+4, QX+4, 0
  MF AC+4, MB+12, QX+8, v[AC+4:AC+7]
  MF S1, MB+12, QX+12, v[S1:S1+3]
  FLAGQ s64
.irp e,0,1,2,3
  ds_read_b64_tr_b16 v[MB+4*\e:MB+4*\e+1], v[RES] offset:512*\e
  ds_read_b64_tr_b16 v[MB+4*\e+2:MB+4*\e+3], v[RES] offset:512*\e+4096
.endr
.if )" STR(AEARLY) R"(
  SOFTX
.endif
  v_mov_b32 v[FA], s62
  ds_read_b32 v[FB], v[FA] offset:8
  s_mov_b32 s76, 0x400000
LA_w:
  ds_read_b32 v[FA], v[FW] offset:2304
  CAP
  s_waitcnt lgkmcnt(0)
  v_cmp_gt_u32 vcc, s65, v[FA]
  s_cbranch_vccz LA_g
  s_sleep 1
  s_branch LA_w
LA_g:
  STAMP 8
  v_readfirstlane_b32 s59, v[FB]
  s_setprio 2
.irp e,0,1,2,3
  ds_read_b64_tr_b16 v[RA+2*\e:RA+2*\e+1], v[AVM] offset:256*\e
.endr
.if !)" STR(AEARLY) R"(
  SOFTX
  s_nop 0
.endif
.irp e,0,4,8,12
  MF OA+\e, MB+\e, P4, 0
.endr
  s_waitcnt lgkmcnt(0)
.irp e,0,1,2,3
  v_mfma_f32_16x16x16_bf16 v[OA+4*\e:OA+4*\e+3], v[RA+2*\e:RA+2*\e+1], v[PB:PB+1], v[OA+4*\e:OA+4*\e+3]
.endr
  s_nop 1
.irp i,0,1,2,3,4,5,6,7
  v_cvt_pk_bf16_f32 v[KX+\i], v[OA+2*\i], v[OA+2*\i+1]
.endr
.irp i,0,1,4,5
  v_permlane16_swap_b32 v[KX+\i], v[KX+\i+2]
.endr
  STAMP 9
  s_setprio 3
  NEXT MB
  s_cmp_lg_u32 s64, 64
  s_cbranch_scc1 LQ_loop2
  OXST
  s_waitcnt vmcnt(0)
  FLAGQ 64
  s_branch L_end
  COLDA 2
LQ_rb2:
  RETRY 0
  QKQ
  s_branch LQ_b2
; ---- wave 3: pacing, clearing, sum over the heads
.set CK,144
.set RA,162
.set RC,184
.macro PARTQ
  LD MB, 178, s66, 0
  LD MB+4, 178, s66, 1024
  LD MB+8, 178, s66, 2048
  LD MB+12, 178, s66, 3072
.endm
LQ_init3:
  s_memrealtime vcc
  s_mov_b64 s[44:45], 0
  s_mov_b32 s46, s62
  s_mov_b32 s79, NS*GS
  s_mov_b32 s62, s101
  s_waitcnt lgkmcnt(0)
  s_lshl_b32 s73, vcc_lo, 4
  PHASEA 3
  s_setprio PLO
  v_mov_b32 v[MB+4], s46
  s_memtime s[96:97]
  s_memrealtime s[94:95]
  ds_read_b128 v[VA:VA+3], v[XA] offset:8192
  v_mov_b64 v[MB:MB+1], -1
  v_mov_b64 v[MB+2:MB+3], -1
  v_rcp_f32 v[CK+6], s45
  v_mov_b64 v[CK:CK+1], s[44:45]
  v_cvt_f32_u32 v[CK+4], s57
  v_mul_f32 v[CK+6], s44, v[CK+6]
  s_waitcnt lgkmcnt(0)
  s_sub_u32 s76, s96, s90
  s_sub_u32 s77, s94, s91
  s_mov_b32 s90, s96
  s_mov_b32 s91, s94
  v_mul_f32 v[CK+6], 256.0, v[CK+6]
  v_cvt_f32_u32 v[CK+2], s76
  v_cvt_f32_u32 v[CK+3], s77
  v_cvt_i32_f32 v[CK+6], v[CK+6]
  v_fmac_f32 v[CK+2], )" STR(CKA) R"(, v[CK]
  v_fmac_f32 v[CK+3], )" STR(CKA) R"(, v[CK+1]
  v_rcp_f32 v[CK+5], v[CK+2]
  v_readfirstlane_b32 s51, v[CK+6]
  v_readfirstlane_b32 s44, v[CK+2]
  v_readfirstlane_b32 s45, v[CK+3]
  v_mul_f32 v[CK+3], )" STR(CKF) R"(, v[CK+3]
  v_fma_f32 v[CK+3], v[CK+3], v[CK+5], -1.0
  v_med3_f32 v[CK+3], v[CK+3], -0.5, 1.0
  v_mul_f32 v[CK+3], v[CK+3], v[CK+4]
  v_cvt_i32_f32 v[CK+3], v[CK+3]
  STAMP 7
  s_nop 0
  v_readfirstlane_b32 s47, v[CK+3]
  WAITK VD+CD, NQ
  buffer_store_dwordx4 v[MB:MB+3], v[179], s[40:43], s67 offen sc1
  buffer_store_dwordx2 v[MB:MB+1], v[182], s[40:43], s67 offen sc1
  buffer_store_dwordx2 v[MB:MB+1], v[180], s[40:43], s70 offen sc1
  buffer_store_dwordx2 v[MB:MB+1], v[181], s[40:43], s70 offen sc1
  ds_read_b128 v[RC:RC+3], v[MB+4]
  ds_read_b32 v[RA], v[MB+4] offset:24
  XCHK 3
  XREF 3
  STAMP 5
  s_barrier
  s_memrealtime s[94:95]
  s_mov_b32 s76, 0x400000
  s_mov_b32 s78, s73
LQ_t3:
  s_waitcnt lgkmcnt(0)
  v_cmp_gt_u32 vcc, s65, v[RC+3]
  s_cbranch_vccnz LQ_t4
  v_readfirstlane_b32 s73, v[RC]
  v_readfirstlane_b32 s74, v[RC+1]
  v_readfirstlane_b32 s59, v[RC+2]
  v_readfirstlane_b32 s92, v[RA]
  s_sub_i32 s78, s73, s78
  s_cmp_gt_i32 s78, 512
  s_cbranch_scc1 LQ_slip
LQ_b5:
  s_lshl1_add_u32 s4, s74, s57
  s_add_i32 s50, s4, s73
  PLAN
  s_sub_i32 s5, s73, s50
  s_mul_i32 s4, s4, s51
  s_mul_i32 s5, s5, s51
  s_lshr_b32 s4, s4, 12
  s_lshr_b32 s5, s5, 12
  s_add_i32 s4, s4, s92
  s_sub_i32 s4, s4, E2
  s_sub_i32 s5, s5, E3
  s_cmp_lt_u32 s64, NS
  s_cselect_b32 s78, GS, 0
  s_sub_u32 s79, s79, s78
  s_ashr_i32 s47, s47, 1
  s_add_i32 s47, s47, s79
.if CSL
  s_sleep CSL
.endif
  s_lshl4_add_u32 s78, s94, s62
  s_sub_i32 s78, s78, s50
  s_cmp_gt_i32 s78, LT
  s_cselect_b32 s78, s78, -GM
  s_add_i32 s78, s78, s50
  s_add_i32 s78, s78, GM
  s_setprio 3
  WAITP s78
  STAMP 8
  PARTQ
  s_memrealtime vcc
  s_memtime s[6:7]
  v_mov_b32 v[RA], s46
  XL4 XA
  XADV
  ZR AC, 8
  v_add_u32 v[TAG], 1, v[TAG]
  s_waitcnt lgkmcnt(0)
  s_add_i32 s6, s6, s5
  v_mov_b32 v[RA+1], s6
  ds_write_b32 v[RA], v[RA+1] offset:20
LQ_b3:
  s_waitcnt lgkmcnt(0)
.if BENCH
  s_mov_b32 m0, s64
  s_mov_b32 s77, vcc_lo
  v_writelane_b32 v[FW], s77, m0
.endif
  s_lshl_b32 s89, vcc_lo, 4
  s_sub_i32 s89, s89, s50
  s_add_i32 s78, s89, )" STR(SLIP_ADD) R"(
  s_sub_i32 s77, s89, E0
  s_max_i32 s77, s77, 0
  s_cmp_gt_i32 s89, )" STR(SLIP_THR) R"(
  s_cselect_b32 s78, s78, s77
  s_cselect_b32 s77, SGAP, 0
  s_add_i32 s78, s78, s73
  s_and_b32 s78, s78, -2
  s_add_i32 s77, s77, s47
  v_mov_b32 v[RA], s78
  v_mov_b32 v[RA+1], s77
  ALN AL4
  s_waitcnt vmcnt(3+XC)
  v_mfma_f32_16x16x32_bf16 v[RC:RC+3], v[VA:VA+3], v[MB:MB+3], 0
  MX0 MB
.irp i,1,2,3
  s_waitcnt vmcnt(3+XC-\i)
  v_mfma_f32_16x16x32_bf16 v[RC:RC+3], v[VA:VA+3], v[MB+4*\i:MB+4*\i+3], v[RC:RC+3]
  MX1 MB+4*\i
.endr
  v_cmp_eq_u32 vcc, -1, v[MX]
  STAMP 9
  s_nop 3
.irp e,0,1,2,3
  v_add_f32_dpp v[RC+\e], v[RC+\e], v[RC+\e] row_ror:8 row_mask:0xf bank_mask:0xf
.endr
  v_cvt_pk_bf16_f32 v[RC], v[RC], v[RC+1]
  v_cvt_pk_bf16_f32 v[RC+1], v[RC+2], v[RC+3]
  s_cbranch_vccnz LQ_rb3
  buffer_store_dwordx2 v[RC:RC+1], v[180], s[40:43], s69 offen sc1
  buffer_store_dwordx2 v[RA:RA+1], v[181], s[40:43], s69 offen sc1
  STAMP 10
  ZR VA, 2
  ZR VB, 2
  NEXT MB
  STAMP 0
  s_cmp_gt_i32 s89, )" STR(TG_THR) R"(
  s_cbranch_scc0 LQ_bar3
  TGET RA
  s_branch LQ_bar3
  COLDA 3
  XWAIT 3
LQ_rb3:
  RETRY 1
  PARTQ
  s_memrealtime vcc
  s_waitcnt vmcnt(0)
  s_branch LQ_b3
LQ_t4:
  ds_read_b128 v[RC:RC+3], v[MB+4]
  CAP
  s_branch LQ_t3
LQ_slip:
.if BENCH
  s_bitset1_b64 s[86:87], s64
.endif
  s_cmp_lt_u32 s64, NS
  s_cbranch_scc1 LQ_b5
  s_add_i32 s79, s79, )" STR(SLIP_INC) R"(
  s_min_u32 s79, s79, 4000
  s_branch LQ_b5
; ======== waves 4..7
.set SMK, SMV
.set UB,128
.set B1,160
.set O3,168
.set O1,169
.set OT,170
.set ST1,171
.set ST2,172
.set AIM,173
.set RR,175
.set FW,176
.set FR,177
.set FS,178
.set ASC,179
.set ASM,180
.set AVX,181
.set SIX,182
.set RES,184
.set TAG,185
.set MX,186
.set TM,187
.set MB,188
.set AC,204
.set TV,212
.set FA,214
.set RA,216
.set DD,188
.set SX,208
.set SMM,212
.set ZZ,224
.set VM,240
.set PA,248
.set VX,188
.set UU,220
.macro SCV
  ds_read_b32 v[FA], v[FS]
  ds_read_b128 v[SX:SX+3], v[ASC]
  ds_read_b64 v[SMM:SMM+1], v[ASM]
.endm
.macro CZV t
  v_cvt_pk_bf16_f32 v[UB+8*\t+2], v[ZZ+4*\t], v[ZZ+4*\t+2]
  v_cvt_pk_bf16_f32 v[UB+8*\t+3], v[ZZ+4*\t+1], v[ZZ+4*\t+3]
.endm
.macro URD h, r
.irp k,0,1,2,3
  ds_read_b128 v[VX+\r+4*\k:VX+\r+4*\k+3], v[TM] offset:1024*(\h+\k)
.endr
.endm
.macro UMM h, r, n
.irp k,0,1,2,3
  s_waitcnt lgkmcnt(\n+3-\k)
.irp t,0,1,2,3
.if \k == 0
  v_mfma_f32_16x16x32_bf16 v[UU+\r+4*\t:UU+\r+4*\t+3], v[VX+\r:VX+\r+3], v[16*\t:16*\t+3], 0
.else
  v_mfma_f32_16x16x32_bf16 v[UU+\r+4*\t:UU+\r+4*\t+3], v[VX+\r+4*\k:VX+\r+4*\k+3], v[16*\t+4*\k:16*\t+4*\k+3], v[UU+\r+4*\t:UU+\r+4*\t+3]
.endif
.endr
.endr
  s_nop 1
.irp t,0,1,2,3
  v_cvt_pk_bf16_f32 v[UB+8*\t+\h], v[UU+\r+4*\t], v[UU+\r+4*\t+1]
  v_cvt_pk_bf16_f32 v[UB+8*\t+\h+1], v[UU+\r+4*\t+2], v[UU+\r+4*\t+3]
.endr
.endm
.macro UREST
.if US
  UMM 4, 0, 0
.endif
  ZR ZZ, 8
  ZR DD, 10
  v_mov_b32 v[TAG], s65
.endm
.macro STEPV R
LV_loop\R:
  STAMP 0
.if US
  URD 4, 0
.if \R == 3
  ds_read_b32 v[MX], v[183] offset:20
.endif
  s_waitcnt lgkmcnt(0)
.endif
.if \R == 3
  v_readfirstlane_b32 s4, v[MX]
  s_setprio 3
  WAITP s73
  s_memtime s[6:7]
.endif
  s_barrier
  s_memtime s[92:93]
  s_setprio 1
  s_nop )" STR(VDEL1) R"(
  STAMP 1
  UREST
.if \R == 3
  v_mov_b32 v[TV+2], s6
  buffer_load_dwordx2 v[TV:TV+1], v[OT], s[40:43], s68 offen sc1
  ds_write_b32 v[183], v[TV+2] offset:24
.endif
.if \R == 2
  v_mov_b64 v[PA:PA+1], -1
  v_mov_b64 v[PA+2:PA+3], -1
.rept CV
  s_nop 3
.endr
.irp o,0,1024,2048,3072
  buffer_store_dwordx4 v[PA:PA+3], v[ST1], s[40:43], s67 offen offset:\o sc1
.endr
.endif
  s_mov_b32 s76, 0x400000
  s_setprio 2
  STAMP 9
  ALN AL6
  s_barrier
  s_memtime s[6:7]
  STAMP 10
.if \R == 3
  s_cmp_eq_u32 s64, 0
  s_cbranch_scc1 LV_t0
LV_t1:
  s_waitcnt vmcnt(0)
  v_cmp_eq_u32 vcc, -1, v[TV]
  v_subrev_u32 v[TV], s73, v[TV]
  v_max_i32_dpp v[TV+1], v[TV+1], v[TV+1] quad_perm:[1,0,3,2] row_mask:0xf bank_mask:0xf
  s_cbranch_vccnz LV_rt
  v_max_i32_dpp v[TV], v[TV], v[TV] quad_perm:[1,0,3,2] row_mask:0xf bank_mask:0xf
  v_max_i32_dpp v[TV+1], v[TV+1], v[TV+1] quad_perm:[2,3,0,1] row_mask:0xf bank_mask:0xf
  s_nop 0
  v_max_i32_dpp v[TV], v[TV], v[TV] quad_perm:[2,3,0,1] row_mask:0xf bank_mask:0xf
  s_nop 0
  v_readfirstlane_b32 s75, v[TV]
  v_add_u32 v[TV], s73, v[TV]
  v_readfirstlane_b32 s74, v[TV+1]
  s_add_i32 s73, s73, s75
  s_cmp_gt_i32 s75, 512
  s_cselect_b32 s75, )" STR(SLIP_INC) R"(*45/32, 0
  s_cmp_lt_u32 s64, NS
  s_cselect_b32 s75, -(GS*45/32), s75
  s_add_i32 s75, s75, s59
  v_mov_b32 v[TV+2], s75
  v_mov_b32 v[TV+3], s65
  ds_write_b128 v[183], v[TV:TV+3]
  PLAN
.endif
.if \R == 2
  HT HB, FA
.endif
.if \R == 0
.rept VN3
  s_nop 1
.endr
LV_p:
  ds_read_b32 v[FA], v[FR]
.irp k,0,1,2,3
  ds_read2st64_b32 v[RA+2*\k:RA+2*\k+1], v[RR-(\k&1)] offset0:8*\k offset1:8*\k+1
.endr
  s_waitcnt lgkmcnt(4)
  v_cmp_gt_u32 vcc, s65, v[FA]
  s_waitcnt lgkmcnt(2)
  v_add_f32 v[RA], v[RA], v[RA+2]
  v_add_f32 v[RA+1], v[RA+1], v[RA+3]
  s_waitcnt lgkmcnt(0)
  v_add_f32 v[RA+4], v[RA+4], v[RA+6]
  v_add_f32 v[RA+5], v[RA+5], v[RA+7]
  v_add_f32 v[RA], v[RA], v[RA+4]
  v_add_f32 v[RA+1], v[RA+1], v[RA+5]
  v_cvt_pk_bf16_f32 v[RES], v[RA], v[RA+1]
  s_cbranch_vccnz LV_pb
  H1ST
.endif
  STAMP 3
.if \R != 2
  s_cmp_eq_u32 s64, 63
  s_cbranch_scc1 L_last
.endif
  s_waitcnt lgkmcnt(0)
.if \R == 2
  s_waitcnt vmcnt(0)
.endif
  s_sub_i32 s77, s6, s92
  s_cmp_gt_i32 s77, NV+200
  s_cselect_b32 s77, LXC, 0
  s_add_i32 s6, s6, s77
  s_setprio 3
  WAITK VD, NV
  STAMP 4
  LD VM, O1, s66, 0
  LD VM+4, O1, s66, 1024
  s_mov_b32 s76, 0x400000
.if \R == 2
  s_cmp_eq_u32 s64, 63
  s_cbranch_scc1 LV_lst
.endif
  ALN AL5
  s_barrier
  SCV
  STAMP 5
.if VR == 0
  s_waitcnt vmcnt(1)
  SM ZZ, VM, 0
  v_max_u32 v[RES], v[VM], v[VM+1]
  v_max3_u32 v[RES], v[RES], v[VM+2], v[VM+3]
  SM ZZ+4, VM, 16
  SM ZZ+8, VM, 32
  SM ZZ+12, VM, 48
LV_s\R:
  s_waitcnt lgkmcnt(0)
  STAMP 6
  v_cmp_gt_u32 vcc, s65, v[FA]
  v_max3_f32 v[MX], v[SX], v[SX+1], v[SX+2]
  v_max3_f32 v[TM], v[SX+3], v[SMM], v[SMM+1]
  v_max_f32 v[MX], v[MX], v[TM]
  s_cbranch_vccnz LV_rs\R
  s_nop 0
  v_max_f32_dpp v[MX], v[MX], v[MX] quad_perm:[2,3,0,1] row_mask:0xf bank_mask:0xf
  v_mov_b32 v[TM], v[MX]
  s_nop 1
  v_permlane16_swap_b32 v[MX], v[TM]
  v_max_f32 v[MX], v[MX], v[TM]
  v_mov_b32 v[TM], v[MX]
  s_waitcnt vmcnt(0)
  SM ZZ, VM+4, 8
  v_permlane32_swap_b32 v[MX], v[TM]
  v_max_f32 v[MX], v[MX], v[TM]
  v_mul_f32 v[MX], s79, v[MX]
  SM ZZ+4, VM+4, 24
  v_fma_f32 v[SX], v[SX], s78, v[MX]
  v_fma_f32 v[SX+1], v[SX+1], s78, v[MX]
  v_fma_f32 v[SMM], v[SMM], s78, v[MX]
  SM ZZ+8, VM+4, 40
  v_fma_f32 v[SMM+1], v[SMM+1], s78, v[MX]
  v_fma_f32 v[SX+2], v[SX+2], s78, v[MX]
  v_fma_f32 v[SX+3], v[SX+3], s78, v[MX]
  SM ZZ+12, VM+4, 56
  v_exp_f32 v[SX], v[SX]
  v_exp_f32 v[SX+1], v[SX+1]
  v_exp_f32 v[SMM], v[SMM]
  v_cvt_pk_bf16_f32 v[PA], v[SX], v[SX+1]
  v_exp_f32 v[SMM+1], v[SMM+1]
  v_cvt_pk_bf16_f32 v[PA+1], v[SMM], v[SMM]
  v_exp_f32 v[SX+2], v[SX+2]
  v_exp_f32 v[SX+3], v[SX+3]
  v_cvt_pk_bf16_f32 v[PA+3], v[SMM+1], v[SMM+1]
  v_cvt_pk_bf16_f32 v[PA+2], v[SX+2], v[SX+3]
.else
.rept DWM
  v_mfma_f32_16x16x32_bf16 v[PA:PA+3], v[B1:B1+3], v[B1:B1+3], 0
.endr
LV_s\R:
  s_waitcnt lgkmcnt(0)
  STAMP 6
  v_cmp_gt_u32 vcc, s65, v[FA]
  v_max3_f32 v[MX], v[SX], v[SX+1], v[SX+2]
  v_max3_f32 v[TM], v[SX+3], v[SMM], v[SMM+1]
  v_max_f32 v[MX], v[MX], v[TM]
  s_cbranch_vccnz LV_rs\R
.if VR == 2
  s_waitcnt vmcnt(1)
  SM ZZ, VM, 0
.else
  s_nop 0
.endif
  v_max_f32_dpp v[MX], v[MX], v[MX] quad_perm:[2,3,0,1] row_mask:0xf bank_mask:0xf
  v_mov_b32 v[TM], v[MX]
.if VR == 2
  SM ZZ+4, VM, 16
  s_nop 0
.else
  s_waitcnt vmcnt(1)
  SM ZZ, VM, 0
.endif
  v_permlane16_swap_b32 v[MX], v[TM]
  v_max_f32 v[MX], v[MX], v[TM]
  v_mov_b32 v[TM], v[MX]
  v_max_u32 v[RES], v[VM], v[VM+1]
.if VR == 2
  SM ZZ+8, VM, 32
.else
  SM ZZ+4, VM, 16
.endif
  v_permlane32_swap_b32 v[MX], v[TM]
  v_max_f32 v[MX], v[MX], v[TM]
  v_mul_f32 v[MX], s79, v[MX]
.if VR == 2
  SM ZZ+12, VM, 48
.else
  SM ZZ+8, VM, 32
.endif
  v_fma_f32 v[SX], v[SX], s78, v[MX]
  v_fma_f32 v[SX+1], v[SX+1], s78, v[MX]
  v_fma_f32 v[SMM], v[SMM], s78, v[MX]
.if VR == 2
  s_waitcnt vmcnt(0)
  SM ZZ, VM+4, 8
.else
  SM ZZ+12, VM, 48
.endif
  v_fma_f32 v[SMM+1], v[SMM+1], s78, v[MX]
  v_fma_f32 v[SX+2], v[SX+2], s78, v[MX]
  v_fma_f32 v[SX+3], v[SX+3], s78, v[MX]
.if VR == 2
  SM ZZ+4, VM+4, 24
.else
  s_waitcnt vmcnt(0)
  SM ZZ, VM+4, 8
.endif
  v_exp_f32 v[SX], v[SX]
.if VR == 2
  SM ZZ+8, VM+4, 40
.else
  SM ZZ+4, VM+4, 24
.endif
  v_exp_f32 v[SX+1], v[SX+1]
.if VR == 2
  SM ZZ+12, VM+4, 56
.else
  SM ZZ+8, VM+4, 40
.endif
  v_exp_f32 v[SMM], v[SMM]
.if VR == 1
  SM ZZ+12, VM+4, 56
.endif
  v_cvt_pk_bf16_f32 v[PA], v[SX], v[SX+1]
  v_exp_f32 v[SMM+1], v[SMM+1]
  v_cvt_pk_bf16_f32 v[PA+1], v[SMM], v[SMM]
  v_exp_f32 v[SX+2], v[SX+2]
  v_exp_f32 v[SX+3], v[SX+3]
  v_cvt_pk_bf16_f32 v[PA+3], v[SMM+1], v[SMM+1]
  v_cvt_pk_bf16_f32 v[PA+2], v[SX+2], v[SX+3]
  v_max3_u32 v[RES], v[RES], v[VM+2], v[VM+3]
.endif
LV_z\R:
  CZV 0
  SM DD+16, PA, B1
  CZV 1
  SM DD, PA, UB
  CZV 2
  v_max3_u32 v[RES], v[RES], v[VM+4], v[VM+5]
  SM DD+4, PA, UB+8
  CZV 3
  v_max3_u32 v[RES], v[RES], v[VM+6], v[VM+7]
  SM DD+8, PA, UB+16
  v_add_f32 v[DD+16], v[DD+16], v[DD+18]
  v_add_f32 v[DD+17], v[DD+17], v[DD+19]
  v_cmp_eq_u32 vcc, -1, v[RES]
  SM DD+12, PA, UB+24
  v_rcp_f32 v[DD+16], v[DD+16]
  v_rcp_f32 v[DD+17], v[DD+17]
  s_cbranch_vccnz LV_rb\R
  HS DD
  HS DD+4
  HS DD+8
  v_pk_mul_f32 v[DD:DD+1], v[DD:DD+1], v[DD+16:DD+17]
  v_pk_mul_f32 v[DD+4:DD+5], v[DD+4:DD+5], v[DD+16:DD+17]
  HS DD+12
  v_pk_mul_f32 v[DD+8:DD+9], v[DD+8:DD+9], v[DD+16:DD+17]
  v_cvt_pk_bf16_f32 v[SX], v[DD], v[DD+4]
  v_pk_mul_f32 v[DD+12:DD+13], v[DD+12:DD+13], v[DD+16:DD+17]
  v_cvt_pk_bf16_f32 v[SX+2], v[DD+1], v[DD+5]
  v_cvt_pk_bf16_f32 v[SX+1], v[DD+8], v[DD+12]
  v_cvt_pk_bf16_f32 v[SX+3], v[DD+9], v[DD+13]
  buffer_store_dwordx4 v[SX:SX+3], v[ST2], s[40:43], s66 offen sc1
  STAMP 7
  s_setprio 1
.if \R == 2
LV_img:
  v_add_u32 v[TM], 1024, v[AIM]
  ds_write2_b32 v[AIM], v[VM], v[VM+1] offset1:2
  ds_write2_b32 v[AIM], v[VM+2], v[VM+3] offset0:32 offset1:34
  ds_write2_b32 v[TM], v[VM+4], v[VM+5] offset1:2
  ds_write2_b32 v[TM], v[VM+6], v[VM+7] offset0:32 offset1:34
  ds_write_b32 v[FW], v[TAG] offset:2304
  s_cmp_eq_u32 s64, 63
  s_cbranch_scc1 L_end
.endif
LV_u\R:
  XSL s65
  v_add_u32 v[TM], s75, v[AVX]
  ds_read_b32 v[MX], v[183] offset:8
  URD 0, 0
.if US
  UMM 0, 0, 0
.else
  URD 4, 16
  UMM 0, 0, 4
  UMM 4, 16, 0
.endif
  v_readfirstlane_b32 s59, v[MX]
  STAMP 8
  NEXT MB
  s_branch LV_loop\R
.if \R == 0
LV_pb:
.if BENCH
  s_bitset1_b64 s[86:87], s64
.endif
  CAP
  s_branch LV_p
.endif
LV_rb\R:
  RETRY 0
  LD VM, O1, s66, 0
  LD VM+4, O1, s66, 1024
  ZR ZZ, 8
  ZR DD, 10
  s_waitcnt vmcnt(0)
.irp t,0,1,2,3
  SM ZZ+4*\t, VM, 16*\t
  s_nop 7
  SM ZZ+4*\t, VM+4, 16*\t+8
.endr
  v_max_u32 v[RES], v[VM], v[VM+1]
  v_max3_u32 v[RES], v[RES], v[VM+2], v[VM+3]
  s_nop 7
  s_branch LV_z\R
.if \R == 2
LV_lst:
  s_barrier
LV_63:
  s_waitcnt vmcnt(0)
  MX0 VM
  MX1 VM+4
  v_cmp_eq_u32 vcc, -1, v[MX]
  s_cbranch_vccz LV_img
  RETRY 0
  LD VM, O1, s66, 0
  LD VM+4, O1, s66, 1024
  s_branch LV_63
.endif
LV_rs\R:
.if BENCH
  s_bitset1_b64 s[86:87], s64
.endif
  SCV
  CAP
  s_branch LV_s\R
.if \R == 3
LV_t0:
  s_memrealtime vcc
  s_waitcnt vmcnt(0) lgkmcnt(0)
  s_lshl_b32 s73, vcc_lo, 4
  s_sub_i32 s73, s73, 0x2000
  v_mov_b32 v[TV], s73
  v_mov_b32 v[TV+1], 0
  s_branch LV_t1
LV_rt:
  RETRY 2
  buffer_load_dwordx2 v[TV:TV+1], v[OT], s[40:43], s68 offen sc1
  s_branch LV_t1
.endif
.endm
LV_init:
  s_memrealtime vcc
  PRE
  s_waitcnt lgkmcnt(0)
  s_lshl_b32 s73, vcc_lo, 4
  s_mov_b32 s78, 0x3e0293ee
  s_mov_b32 s79, 0xbe0293ee
  ZR UB, 16
  v_mov_b32 v[B1], 0x3f803f80
  v_mov_b32 v[B1+1], 0x3f803f80
  v_mov_b32 v[B1+2], 0x3f80
  v_mov_b32 v[B1+3], 0x3f80
  v_mov_b32 v[B1+4], 0x3f803f80
  v_mov_b32 v[B1+5], 0x3f803f80
  v_mov_b64 v[B1+6:B1+7], 0
  s_cmp_eq_u32 s60, 2
  s_cbranch_scc1 LV_u2
  s_cmp_eq_u32 s60, 3
  s_cbranch_scc1 LV_u3
  s_branch LV_u0
  STEPV 0
  STEPV 2
  STEPV 3
L_fatal:
  v_mov_b32 v[MX], 0
  v_mov_b32 v[TAG], 1
  global_store_dword v[MX], v[TAG], s[48:49] sc1
  s_waitcnt vmcnt(0)
  s_endpgm
L_last:
  s_barrier
L_end:
  s_waitcnt vmcnt(0) lgkmcnt(0)
)"
static_assert(__builtin_bit_cast(unsigned, SCALE * LOG2E) == 0x3e0293eeu, "");
#define CL10(p) p "0", p "1", p "2", p "3", p "4", p "5", p "6", p "7", p "8", p "9"
#define CL_ST
#define CL_S CL_ST "s4", "s5", "s6", "s7", "s72", "s73", "s74", "s75", "s76", "s77", "s78", "s79", "s88", "s89", "s90", "s91", "s92", "s93", "s94", "s95", "s96", "s97", "s98", "s99", "s100", "s101", CL10("s3"), "vcc", "scc", "memory"

__device__ __forceinline__ void chain(const Args& a, const int c, const unsigned L, const unsigned hb) {
    const int tid = threadIdx.x, w8 = RFL(tid >> 6), w = w8 & 3, l = tid & 63;
    const int g = c & 3, aa = c >> 2, h = g * 8 + (aa >> 2), hp = h ^ XH * 8, jj = aa & 3;
    const int n = l & 15, q = l >> 4, hf = (n >> 1) & 1, n2 = (n >> 2) & 1, n3 = n >> 3;
    const unsigned L_EX = L + 114688, L_SC = L + 131072, L_SM = L_SC + 2048, L_FL = L_SM + 512, L_TB = L_FL + 3072, L_VM = L_TB + 256, L_W2 = L + 143360, L_KX = L + 32768, L_VX = L_KX + 8192;
    const rsrc_t rs = mkrs(a.ring, RING_BYTES);
    unsigned* const err = a.ctl + C_ERR;
    const v4u ones = {~0u, ~0u, ~0u, ~0u};
    const unsigned cyc0 = RFL((unsigned)__builtin_amdgcn_s_memtime()), rt0 = RFL((unsigned)__builtin_amdgcn_s_memrealtime());
    if (tid < 384) lp<v2u>(L_FL)[tid] = v2u{0u, 0u};
    if (c < B_H3 || (c < B_TS + 2 && !(c & 2))) {
#pragma unroll
        for (int i = 0; i < 4; ++i) STB(ones, rs, c * 32768 + i * 8192 + tid * 16, 0); }
    const int lg3 = (c & 31) * 128 + n2 * 64 + hf * 32 + (q & 1) * 16 + (n & 1) * 8;
    if (w8 == 3) { const bf16_t* p = a.mem0 + ((c & 1) * 4 + n2 * 2 + (q & 1)) * DM + 16 * (c >> 1) + (n & 3);
        const v2u m = {min(p[0] | (unsigned)p[4] << 16, 0xFFFFFFFEu), min(p[8] | (unsigned)p[12] << 16, 0xFFFFFFFEu)};
#pragma unroll
        for (int i = 0; i < 2; ++i) __builtin_amdgcn_raw_buffer_store_b64(m, rs, (B_H3 + 4 * (c >> 5) + 2) * 32768 + ((q >> 1) * 2 + n3) * 4096 + lg3, i * 16384, 16); }
    const unsigned stg = L + w8 * 16384;
    const int rot = 0;
    const int prr = 16 * (q >> 1) + (q & 1) + 4 * n2 + 2 * n3;
    v32u W[6];
    auto trd4 = [&](int k, unsigned p, int o1, int o2) { const v2u t[4] = {trd(p), trd(p + o1), trd(p + o2), trd(p + o2 + o1)};
#pragma unroll
        for (int i = 0; i < 8; ++i) W[k >> 2][(k & 3) * 8 + i] = t[i >> 1][i & 1]; };
    unsigned cal;
    const unsigned d1c = (a.d1 * 45 >> 5) + NS * (GS * 45 / 32) - WKO;
    auto pg = [&](int blk, int x) { return blk * 32768 + (int)((((cal >> (blk >> 6) * 3) ^ ((x ^ blk >> 4) & 1) * 4 ^ ((x >> 1 ^ blk >> 5) & 1)) & 7) << 12); };
    auto h1o = [&](int inner, int x = 0) { return pg(B_H1 + (aa & 28) + (inner >> 12) * 2, g ^ x ^ XP) + (inner & 4095); };
    auto stream = [&](auto issue, auto read) {
        issue(0); issue(1);
        WAITV(8); BAR();
        unsigned h2 = hb;
        auto arrive = [&]() { gadd1(a.ctl + C_READY); if (c == 0) gst1(a.ctl + C_ACK, 1u); };
        if (w8 == 3) gld_req(h2, a.ctl + C_HB);
        read(std::integral_constant<int, 0>{}); WAITL(); issue(2);
        gld_wait<8>(h2);
        const bool seen = RFL(h2) != RFL(hb);
        if (seen) arrive();
        read(std::integral_constant<int, 1>{}); WAITL(); issue(3);
        WAITV(8); read(std::integral_constant<int, 2>{});
        WAITV(0); read(std::integral_constant<int, 3>{}); WAITL();
        if (w8 == 3 && !seen) { ctl_wait(a, hb); arrive(); }
    };
    auto start = [&]() {
        if (w8 == 3) {
            for (int i = 0; RFL(gld(a.ctl + C_READY)) < 256; ++i) { if (c == 0) gadd1(a.ctl + C_HB); __builtin_amdgcn_s_sleep(4); if (i > GCAP) fatal(err); }
            FENCE();
            const unsigned v = ~gld(a.ctl + C_CAL + (l & 15)) & 0xffffu;
            unsigned u = (2 * v + dppu<0xB1>(v) + dppu<0x1B>(dppu<HMIR>(v))) * 8 + (l & 7);
            u = min(u, dppu<0xB1>(u)); u = min(u, dppu<0x4E>(u)); u = min(u, dppu<HMIR>(u));
            unsigned t = ((__builtin_amdgcn_readlane(u, 0) & 7) | (__builtin_amdgcn_readlane(u, 8) & 7) << 3) ^ 022;
            *lp<v2u>(L_TB + 16) = v2u{t, cyc0}; *lp<v2u>(L_TB + 8) = v2u{d1c, 0u}; WAITL();
        }
        lp<unsigned>(L_EX + w8 * 2048 + 1536)[l] = 0; WAITL();
        BAR();
        cal = RFL(ldsr(L_TB + 16));
    };
    const unsigned char* xp = a.qkv + h * 24576 + w * 8192 + (w == 2 ? (jj & 1) * 4096 : 0);
    const unsigned char* xq = a.qkv + h * 24576 + 2 * NH * 24576;
    const unsigned xw = w == 3 ? 20480 - XC * 1024 : w * (XC == 4 ? 8192 : 4096);
    const unsigned* fG = a.ctl + C_G + h * 128 + w * 32;
    auto lo = [](const void* p) { return (unsigned)(u64)p; };
    auto hi = [](const void* p) { return (unsigned)((u64)p >> 32); };
    v16u S0 = {lo(a.ring), hi(a.ring), RING_BYTES, 0x20000u, lo(a.ox), hi(a.ox), NSEG * 262144, 0x20000u, lo(err), hi(err), lo(a.ctl + C_M + h * 128 + jj * 32), hi(a.ctl + C_M + h * 128 + jj * 32),
               lo(xq), hi(xq), lo(fG - w * 32), hi(fG - w * 32)};
    v16u S1 = {(unsigned)rot << 10, (unsigned)a.d2, (unsigned)a.per, d1c, (unsigned)w, L_KX + xw, L_TB, (unsigned)w8};
    v8u cnts;
    v16u A;
    const unsigned six = hf ? 0xEEEE : 0x4444;

    if (w8 < 4) {
        { const bf16_t* Wm = (jj >> 1 ? a.Wk : a.Wq) + (size_t)256 * w * HD + hp * DH + 64 * (jj & 1);
          const unsigned p = stg + prr * 128 + (((hf ^ (4 * n2 + 2 * n3)) * 16 + (n & 1) * 8) ^ ((w >> 1) * 64));
          stream([&](int i) { _Pragma("unroll 1") for (int j = 0; j < 8; ++j) gldsc(Wm + (size_t)(64 * (i ^ rot) + 8 * j) * HD, (l >> 3) * (HD * 2) + ((l & 7) ^ (2 * q)) * 16, stg + (i & 1) * 8192 + j * 1024); },
                 [&](auto i) { _Pragma("unroll") for (int t = 0; t < 4; ++t) trd4(4 * t + i, (p ^ (t * 32)) + (i & 1) * 8192, 1024, 4096); }); }
        MARK(2);
        BAR();
        { const unsigned pv = stg + 65536 + prr * 64 + (hf ^ (2 * n2)) * 16 + (n & 1) * 8;
#pragma unroll
          for (int k = 0; k < 8; ++k) trd4(16 + k, (pv ^ ((k >> 2) * 32)) + ((k & 3) ^ rot) * 4096, 512, 2048); }
        WAITL();
        BAR();
        if (w < 3) {
            flags_wait<4>(fG, 1, 2, l, err);
#pragma unroll
            for (int i = 0; i < 4; ++i) if (w < 2 || !(i & 1)) glds1<4>(xp + (i >> 1) * (NH * 24576) + (i & 1) * 4096, l * 16, L_KX + (i >> 1) * 20480 + w * 8192 + (i & 1) * 4096);
            WAITV(0);
        }
        A[5] = w == 2 ? L_W2 + l * 16 : 16384 + (jj & 1) * 4096 + l * 16;
        A[13] = w < 3 ? xw + l * 16 : 0; A[14] = xw + 4096 + l * 16; A[15] = 0;
        if (w == 2) { const int ck = 8 * (jj >> 1) + hf;
#pragma unroll
            for (int i = 0; i < 8; ++i) lp<unsigned>(A[5] + (i >> 2) * 1024)[i & 3] = 16 * (i >> 2) + 4 * q + (i & 3) > 16 * (jj & 1) + n ? 0xFF800000u : 0u;
#pragma unroll
            for (int i = 0; i < 16; ++i) *lp<v4u>(A[5] + 4096 + i * 1024) = v4u{W[4 + i / 8][i % 8 * 4], W[4 + i / 8][i % 8 * 4 + 1], W[4 + i / 8][i % 8 * 4 + 2], W[4 + i / 8][i % 8 * 4 + 3]};
            A[13] = L_KX + l * 16; A[14] = L_VX + (ck * 16 + 4 * q + (n >> 2)) * 16 + (l & 1) * 8; A[15] = L_VM + ck * 128 + (2 * q + n2) * 16 + (l & 1) * 8; }
        if (w == 3) {
#pragma unroll
            for (int i = 0; i < 4; ++i) lp<unsigned>(A[5] + 8192)[i] = ((n & 7) >> 1) == i ? 0x3F80u << (16 * (n & 1)) : 0u; }
        start();
        MARK(0); MARKC(4);
        const int sl = 2 * (jj & 1) + (w >> 1);
        A[0] = pg(B_H3 + 4 * w, g ^ (H3X)) + (l ^ (w & 1)) * 16;
        A[1] = h1o((jj >> 1) * 2048 + ((sl * 2 + n3) * 2 + (w & 1)) * 128 + (q >> 1) * 64 + n2 * 32 + (q & 1) * 16 + (n & 3) * 4, XH);
        A[2] = L_EX + (w ^ 1) * 2048 + 1024 + l * 4; A[3] = L_EX + (w ^ 2) * 2048 + l * 4; A[4] = L_EX + (w ^ 3) * 2048 + 512 + l * 4;
        A[6] = L_FL + w * 256 + l * 4; A[7] = L_EX + (l & 3) * 2048 + 1536 + l * 4; A[8] = six;
        A[9] = w == 2 ? h * 8192 + jj * 2048 + ((2 * (q & 1) + (q >> 1)) * 16 + n) * 16 : (XC == 4 ? min(w, 2) * 128 : (w == 3 ? 128 + (l & 1) * 128 : 0));
        A[10] = w < 3 ? h1o(l * 16) : pg(2 * aa, g) + l * 16;
        A[11] = w < 2 ? L_KX + w * 4096 + l * 16 : h1o((w == 2 ? 2048 : jj * 1024) + l * 16);
        A[12] = w < 2 ? L_SC + w * 1024 + ((l * 16) ^ ((q & 1) * 32)) : (w == 2 ? L_SM + ((l * 8) ^ ((q & 1) * 16)) : pg(B_H3 + 4 * (c >> 5), (q >> 1) * 2 + n3) + lg3);
        if (w == 3) { A[13] = c % 33 == 0 && l < 4 ? pg(B_TS, l) + (c >> 5) * 128 : OOB; A[14] = h1o(4096 + jj * 512 + l * 8); }
    } else {
        { const bf16_t* Wo = a.Wo + (size_t)h * DH * DM + 256 * jj + 64 * w; const bf16_t* Wv = a.Wv + (size_t)256 * w * HD + hp * DH + 32 * jj;
          const unsigned po = stg + (4 * q + n3 + 16 * n2) * 128 + (((n & 3) * 2) ^ (q & 1)) * 16;
          stream([&](int i) {
                     if (i < 2) { _Pragma("unroll 1") for (int j = 0; j < 8; ++j) gldsc(Wo + (size_t)(64 * i + 8 * j) * DM, (l >> 3) * (DM * 2) + ((l & 7) ^ (l >> 5)) * 16, stg + i * 8192 + j * 1024); }
                     else { _Pragma("unroll 1") for (int j = 0; j < 8; ++j) glds(Wv + (size_t)(128 * (i - 2) + 16 * j) * HD, (l >> 2) * (HD * 2) + ((l & 3) ^ (2 * (q & 1))) * 16, stg + (i & 1) * 8192 + j * 1024); } },
                 [&](auto i) { _Pragma("unroll") for (int t = 0; t < 4; ++t) if constexpr (i < 2) trd4(2 * t + i, (po ^ ((t >> 1) * 16)) + (t & 1) * 8 + i * 8192, 256, 4096); }); }
        BAR(); BAR();
        start();
        A[0] = 0; A[1] = h1o(4096 + l * 16); A[2] = pg(B_TS, g) + (l & 3) * 128;
        A[3] = w >= 2 ? pg(2 * aa, g) + l * 16 : h1o(4096 + ((jj * 2 + n3) * 2 + (w & 1)) * 128 + (q & 1) * 64 + (n & 1) * 32 + (q >> 1) * 16 + ((n >> 1) & 3) * 4, XH);
        A[4] = pg(jj * 16 + w * 4 + n3 * 2, n2 * 2 + (q >> 1)) + h * 128 + ((q & 1) * 4 + (n & 3)) * 16;
        A[5] = L_VM + q * 256 + (4 * n3 + 2 * (n & 1) + n2) * 16 + hf * 4;
        A[7] = L_EX + 8192 + (w & 1) * 512 + l * 4; A[6] = A[7] ^ 512;
        A[8] = L_FL + w * 256 + l * 4; A[9] = L_EX + 8192 + (l & 3) * 2048 + 1536 + l * 4; A[10] = L_FL + 2048 + min(l & 3, 2) * 256 + l * 4;
        A[11] = L_SC + q * 512 + hf * 256 + n * 16; A[12] = L_SM + (q & 1) * 256 + hf * 128 + n * 8;
        A[13] = L_VX + q * 256 + (8 * n3 + 4 * hf + 2 * n2 + (n & 1)) * 16; A[14] = six; A[15] = L_TB;
    }
    S1[12] = a.d2 - a.d1 - KC; S1[8] = RFL(pg(B_TS, g)); S1[9] = cyc0; S1[10] = rt0; S1[11] = RFL(L_EX - 63744 + w8 * 2048);
    asm volatile(A_CHAIN : "+{s[40:55]}"(S0), "+{s[56:71]}"(S1), "={s[80:87]}"(cnts), "+{v[168:183]}"(A), "+{v[188:219]}"(W[4]), "+{v[220:251]}"(W[5]) : "{v[0:31]}"(W[0]), "{v[32:63]}"(W[1]), "{v[64:95]}"(W[2]), "{v[96:127]}"(W[3])
                 : "v128", "v129", CL10("v13"), CL10("v14"), CL10("v15"), "v160", "v161", "v162", "v163", "v164", "v165", "v166", "v167", "v184", "v185", "v186", "v187", CL_S);
}

struct Tile { const bf16_t* W; int ld, wide, perm; };
__device__ __forceinline__ void tile_issue(const Tile& T, int t, unsigned L, int w8, int l) {
    const bf16_t* p = T.W + (T.wide ? (w8 & 3) * 32 * T.ld + (2 * t + (w8 >> 2)) * 128 : (t * 256 + w8 * 32) * T.ld);
    const unsigned vo = (T.perm ? (l >> 5) + (l & 16) : l >> 4) * T.ld * 2 + ((l & 15) ^ (2 * (l >> 4))) * 16;
#pragma unroll 1
    for (int i = 0; i < 8; ++i) { const bf16_t* g = p + i * (T.perm ? 2 : 4) * T.ld; const unsigned v = vo ^ ((i & 2) << 6), d = L + (t & 1) * 65536 + (w8 * 8 + i) * 1024;
        gldsw(g, v, d); }
}
template <class F>
__device__ __forceinline__ void tile_load(const Tile& T, v4u (&wf)[32], unsigned L, int w8, int l, F next) {
    const int n = l & 15, q = l >> 4;
    const unsigned p = L + (8 * q + (n >> 2)) * 256 + ((4 * (w8 & 3) + ((l >> 1) & 1)) ^ (2 * ((n >> 2) | ((q & 1) << 2)))) * 16 + (l & 1) * 8;
    bool more = true;
#pragma unroll 1
    for (int hh = 0; hh < 2; ++hh)
#pragma unroll
    for (int u = 0; u < 2; ++u) {
        if (!more) WAITV(0);
        WAITV(8); BAR();
        if ((w8 >> 2) == hh) {
#pragma unroll
            for (int i = 0; i < 16; ++i) { const unsigned pp = (p ^ ((i & 1) * 32)) + u * 65536 + (i >> 1) * 8192; wf[u * 16 + i] = cat2(trd(pp), trd(pp + 1024)); }
            WAITL(); }
        BAR();
        if (hh == 0) tile_issue(T, u + 2, L, w8, l); else more = next(u);
    }
}
#define NB 8
#define GD 4
template <int XR, bool ROWS, class F>
__device__ __forceinline__ void seg_gemm(const v4u (&wf)[32], unsigned b0, frag_cd (&acc)[4], F side) {
    v4u f[2 * GD];
    const unsigned bb[4] = {b0, ROWS ? b0 ^ 64 : b0, ROWS ? b0 ^ 128 : b0, ROWS ? b0 ^ 192 : b0};
#define RD(ks, mt) asm volatile("ds_read_b128 %0, %1 offset:%2" : "=v"(f[((ks) % GD) * 2 + mt]) : "v"(bb[(ks) & 3]), "n"(ROWS ? ((ks) >> 2) * 256 + mt * 16384 : ((ks) >> 1) * 4096 + ((ks) & 1) * 1024 + mt * 2048) : "memory")
#define MM(ct, mt) do { if (ks == 0) asm volatile("v_mfma_f32_16x16x32_bf16 %0, %1, %2, 0" : "=&v"(acc[2 * ct + mt]) : "v"(wf[ct]), "v"(f[mt])); \
        else asm volatile("v_mfma_f32_16x16x32_bf16 %0, %1, %2, %0" : "+v"(acc[2 * ct + mt]) : "v"(wf[2 * ks + ct]), "v"(f[(ks % GD) * 2 + mt])); } while (0)
#pragma unroll
    for (int ks = 0; ks < GD - 1; ++ks) { RD(ks, 0); RD(ks, 1); }
#pragma unroll
    for (int ks = 0; ks < 16; ++ks) {
        side(2 * ks);
        asm volatile("s_waitcnt lgkmcnt(%0)" :: "n"(2 * (ks + GD - 1 <= 16 ? GD - 2 : 15 - ks) + (ks >= 13 ? XR : 0)));
        MM(0, 0);
        if (ks + GD - 1 < 16) RD(ks + GD - 1, 0);
        MM(0, 1);
        side(2 * ks + 1);
        MM(1, 0);
        if (ks + GD - 1 < 16) RD(ks + GD - 1, 1);
        MM(1, 1);
    }
    asm volatile("s_nop 7" : "+v"(acc[0]), "+v"(acc[1]), "+v"(acc[2]), "+v"(acc[3]));
#undef RD
#undef MM
}
static_assert(GD == 4, "");
__device__ __forceinline__ float fadd(float a, float b) { float r; asm("v_add_f32 %0, %1, %2" : "=v"(r) : "v"(a), "v"(b)); return r; }
__device__ __forceinline__ float fmul(float a, float b) { float r; asm("v_mul_f32 %0, %1, %2" : "=v"(r) : "v"(a), "v"(b)); return r; }
__device__ __forceinline__ void half_put(unsigned ex, int w8, int l, const frag_cd (&acc)[4]) {
#pragma unroll
    for (int i = 0; i < 4; ++i) *lp<frag_cd>(ex + w8 * 4096 + i * 1024 + l * 16) = acc[i];
    FENCE();
}
struct Half { frag_cd t[4]; unsigned dec; };
__device__ __forceinline__ void half_req(unsigned ex, unsigned dec, Half& x) {
    asm volatile("ds_read_b32 %0, %1" : "=v"(x.dec) : "v"(dec) : "memory");
#pragma unroll
    for (int i = 0; i < 4; ++i) asm volatile("ds_read_b128 %0, %1 offset:%2" : "=v"(x.t[i]) : "v"(ex), "n"(i * 1024) : "memory");
}
template <class F>
__device__ __forceinline__ void half_get(Half& x, frag_cd (&acc)[4], F side) {
    asm volatile("s_waitcnt lgkmcnt(0)" : "+v"(x.t[0]), "+v"(x.t[1]), "+v"(x.t[2]), "+v"(x.t[3]), "+v"(x.dec) :: "memory");
#pragma unroll
    for (int i = 0; i < 4; ++i) { side(i);
#pragma unroll
        for (int e = 0; e < 4; ++e) acc[i][e] = fadd(acc[i][e], x.t[i][e]);
        asm volatile("" : "+v"(acc[i])); }
}
#define GEARLY 2
#define BARI 25
#define PRIO(n) __builtin_amdgcn_s_setprio(n)

__device__ __forceinline__ unsigned topk(const Args& a, int l) {
    const rsrc_t rg = mkrs(a.gate, 2 * S_TOK * 4);
    flags_wait<16>(a.ctl + C_R, 32, 1, l, a.ctl + C_ERR);
    v4u kv[8]; unsigned prev = ~0u, mine = 0;
#pragma unroll
    for (int i = 0; i < 8; ++i) kv[i] = __builtin_amdgcn_raw_buffer_load_b128(rg, S_TOK * 4 + l * 16, i * 1024, 16);
#pragma unroll 1
    for (int c = 0; c < TOPC; ++c) { unsigned mx = 0;
        FENCE();
#pragma unroll
        for (int i = 0; i < 8; ++i)
#pragma unroll
            for (int e = 0; e < 4; ++e) mx = max(mx, kv[i][e] < prev ? kv[i][e] : 0u);
        mx = max(mx, dppu<0x128>(mx)); mx = max(mx, dppu<0x124>(mx)); mx = max(mx, dppu<0x122>(mx)); mx = max(mx, dppu<0x121>(mx));
        { v2u r = sw16(mx, mx); mx = max(r.x, r.y); r = sw32(mx, mx); mx = max(r.x, r.y); }
        prev = RFL(mx); if ((l & 7) == c) mine = prev; }
    return mine;
}
__device__ __forceinline__ unsigned mix(float gt, float omg, float t0, float t1, float b0, float b1) {
    unsigned av = cvt_pk(t0, t1); av = cvt_pk(fmul(gt, lo16(av)), fmul(gt, hi16(av))); const unsigned bv = cvt_pk(fmul(omg, b0), fmul(omg, b1));
    return cvt_pk(fadd(lo16(av), lo16(bv)), fadd(hi16(av), hi16(bv)));
}
struct Finisher {
    rsrc_t rp, rx, rg, ro; int po, frow, xo, w8; unsigned seltok, ly;
    struct Fin { v4u p[4]; v2u xv; float gt; int k; };
    __device__ __forceinline__ Finisher(const Args& a, int j, int w8_, int l, unsigned L_Y) {
        rp = mkrs(a.part, NSEG * 524288); rx = mkrs(a.x, S_TOK * DM * 2); rg = mkrs(a.gate, S_TOK * 4); ro = mkrs(a.out, S_TOK * DM * 2);
        w8 = w8_; po = j * 16384 + w8 * 1024 + l * 16; frow = 16 * (w8 & 1) + (l & 15); xo = (frow * DM + j * 32 + (w8 >> 1) * 16 + 4 * (l >> 4)) * 2;
        seltok = 4095u - (lp<unsigned>(L_Y + 1024)[l & 7] & 4095u); ly = L_Y + ((w8 >> 1) * 32 + (l >> 4) * 8) * 16;
    }
    __device__ __forceinline__ Fin issue(int s, bool on) const { Fin f; int k = on ? 0 : 0x40000000; asm("" : "+s"(k));
#pragma unroll
        for (int i = 0; i < 4; ++i) f.p[i] = __builtin_amdgcn_raw_buffer_load_b128(rp, k + po, s * 524288 + i * 4096, 16);
        f.k = k + xo;
        f.xv = __builtin_amdgcn_raw_buffer_load_b64(rx, f.k, s * 65536, 0);
        f.gt = __uint_as_float(__builtin_amdgcn_raw_buffer_load_b32(rg, k + frow * 4, s * 128, 16));
        return f;
    }
    template <class F> __device__ __forceinline__ void finish(int s, const Fin& f, F side) const {
        const int tok = 32 * s + frow; const float gt = f.gt, omg = rbf(1.0f - gt);
        frag_cd t; v2u o; int k = f.k;
#pragma unroll
        for (int e = 0; e < 4; e += 2) {
            side(e >> 1);
#pragma unroll
            for (int u = e; u < e + 2; ++u) t[u] = fadd(fadd(__uint_as_float(f.p[0][u]), __uint_as_float(f.p[1][u])), fadd(__uint_as_float(f.p[2][u]), __uint_as_float(f.p[3][u])));
            o[e >> 1] = mix(gt, omg, t[e], t[e + 1], lo16(f.xv[e >> 1]), hi16(f.xv[e >> 1])); asm volatile("" : "+v"(o[e >> 1])); }
        for (unsigned mk = (unsigned)__builtin_amdgcn_ballot_w64((seltok >> 4) == (unsigned)(2 * s + (w8 & 1))) & 255u; mk; mk &= mk - 1) {
            const int slot = __builtin_ctz(mk);
            if (__builtin_amdgcn_readlane(seltok, slot) == (unsigned)tok) { *lp<frag_cd>(ly + slot * 16) = t; k = 0x40000000; }
        }
        __builtin_amdgcn_raw_buffer_store_b64(o, ro, k, s * 65536, 0);
    }
};

__device__ __forceinline__ void gwork(const Args& a, const int m, const int h, const unsigned L, const unsigned hb) {
    const int w8 = RFL(threadIdx.x >> 6); int l = threadIdx.x & 63, n = l & 15, q = l >> 4;
    unsigned* const err = a.ctl + C_ERR;
    const unsigned L_EX = L + 131072, L_XS = L_EX, L_HS = L_XS + 16384, L_GU = L_HS + 2048;
    unsigned* const fG = a.ctl + C_G + h * 128 + m * 32;
    const unsigned xo = (l ^ (8 * (w8 & 1))) * 16;
    auto xissue = [&](int s, int i0, int i1) {
        const bf16_t* p = a.x + (size_t)(32 * s + 8 * (w8 & 3)) * DM + (~w8 & 4) * 128; const unsigned d = L + (s & 1) * 65536 + (w8 ^ 4) * 8192;
#pragma unroll
        for (int i = i0; i < i1; ++i) {
            if (i == i0 || i == 4) asm volatile("s_mov_b32 m0, %0" :: "s"(d + (i & 4) * 1024));
            unsigned t; asm volatile("v_xor_b32 %0, %4, %1\n\tglobal_load_lds_dwordx4 %0, %2 offset:%3" : "=&v"(t) : "v"(xo), "s"(p), "n"((i & 3) * 1024), "n"(i * 1040 + (i & 4) * 1024) : "memory"); }
    };
    v4u wf[32];
    { const Tile T{(m == 0 ? a.Wk : (m == 1 ? a.Wv : a.Wq)) + h * DH, HD, 0, 0};
      tile_issue(T, 0, L, w8, l); tile_issue(T, 1, L, w8, l);
      tile_load(T, wf, L, w8, l, [&](int b) { if (b == 0 || w8 >= 4) xissue(b, 0, 8); return true; }); }
    MARK(0);
    const unsigned rb0 = L + (w8 >> 2) * 32768 + n * 1024 + (q ^ n) * 16;
    unsigned pacc[8] = {}, plast = 0; (void)pacc; (void)plast;
    PT(7);
    if (w8 >= 4) {
        const rsrc_t rq = mkrs(a.qkv, NSEG * NH * 24576), rf = mkrs(fG, w8 == 4 ? 4 : 0);
        const int sto = h * 24576 + m * 8192 + (m == 1 ? (w8 & 3) * 64 + l : ((w8 & 2) + (q & 1)) * 64 + (2 * (w8 & 1) + (q >> 1)) * 16 + n) * 16;
        WAITV(8); BAR(); WAITV(0);
        if (w8 == 4) { ctl_wait(a, hb); gadd1(a.ctl + C_READY); }
        for (int sg = 0; sg < NSEG; ++sg) {
            PT(0);
            BAR();
            PT(1);
            PRIO(2);
            if (sg <= GEARLY) __builtin_amdgcn_raw_buffer_store_b32(sg, rf, l * 4, 0, 16);
            frag_cd acc[4]; Half hx;
            seg_gemm<5, true>(wf, rb0 + (sg & 1) * 65536, acc, [&](int i) {
                if (i == BARI) { PRIO(3); PT(5); WAITV(3); BAR(); PT(6); }
                if (i == 26) half_req(L_EX + (w8 & 3) * 4096 + l * 16, L_EX, hx);
                if (i >= 26) xissue((sg + 2) & 63, i - 26, i - 25); });
            PRIO(0);
            PT(2);
            half_get(hx, acc, [&](int i) { if (i & 1) xissue((sg + 2) & 63, 6 + i / 2, 7 + i / 2); });
            v4u r[2];
#pragma unroll
            for (int mt = 0; mt < 2; ++mt) r[mt] = v4u{cvt_pk(acc[mt][0], acc[2 + mt][0]), cvt_pk(acc[mt][1], acc[2 + mt][1]), cvt_pk(acc[mt][2], acc[2 + mt][2]), cvt_pk(acc[mt][3], acc[2 + mt][3])};
            PT(3);
#pragma unroll
            for (int mt = 0; mt < 2; ++mt) STB(r[mt], rq, sto + mt * 4096, sg * (NH * 24576));
            __builtin_amdgcn_raw_buffer_store_b32(sg <= GEARLY ? sg : sg - 1, rf, l * 4, 0, 16);
            FENCE();
            if (sg < GEARLY) WAITV(0);
            PT(4);
        }
        PFLUSH();
        WAITV(0); BAR();
        if (w8 == 4) gst1(fG, NSEG);
    } else {
        for (int sg = 0; sg < NSEG; ++sg) {
            frag_cd acc[4];
            WAITL();
            PT(0);
            BAR();
            PT(1);
            PRIO(2);
            seg_gemm<0, true>(wf, rb0 + (sg & 1) * 65536, acc, [&](int i) { if (i == BARI) { PRIO(3); PT(5); WAITV(0); BAR(); PT(6); } });
            PRIO(1);
            PT(2);
            half_put(L_EX, w8, l, acc);
            xissue((sg + 1) & 63, 0, 8);
            PT(3);
            MARK(sg + 1);
        }
        PFLUSH();
        WAITL(); BAR();
        WAITV(0); BAR();
    }
    if (m != 2) return;
    asm volatile("v_mbcnt_lo_u32_b32 %0, -1, 0\n\tv_mbcnt_hi_u32_b32 %0, -1, %0" : "=v"(l));
    n = l & 15; q = l >> 4;
    const rsrc_t ry = mkrs(a.ypart, 32 * 29 * 1024);
    auto tile = [&](int t) { return t < 2 ? Tile{(t ? a.wu : a.wg) + h * 128, FF, 0, 0} : Tile{a.wd + (size_t)h * 128 * DM, DM, 1, 0}; };
    unsigned mykey = 0;
    if (w8 == 0) {
        mykey = topk(a, l);
#pragma unroll 1
        for (int i = 0; i < 16; ++i) glds(a.x + i * 64, (4095u - (mykey & 4095u)) * (DM * 2) + (l >> 3) * 16, L_XS + i * 1024);
    }
    if (h < FF / 128) {
      Tile T = tile(0);
      tile_issue(T, 0, L, w8, l); tile_issue(T, 1, L, w8, l);
      frag_cd gv[2] = {};
#pragma unroll 1
      for (int ti = 0; ti < 3; ++ti) {
        const Tile N = tile(ti + 1);
        tile_load(T, wf, L, w8, l, [&](int b) { if (ti < 2) tile_issue(N, b, L, w8, l); return ti < 2; });
        T = N;
        if (ti < 2) {
            frag_cd acc[2] = {};
#pragma unroll
            for (int ks = 0; ks < 16; ++ks) { const v4u xf = *lp<v4u>(L_XS + ((w8 >> 2) * 16 + ks) * 512 + q * 128 + (n & 7) * 16); acc[0] = MFMA(wf[2 * ks], xf, acc[0]); acc[1] = MFMA(wf[2 * ks + 1], xf, acc[1]); }
            if (ti == 1) {
                frag_cd g = n < 8 ? gv[0] : gv[1], u = n < 8 ? acc[0] : acc[1];
                if (w8 >= 4) { *lp<frag_cd>(L_GU + (w8 & 3) * 2048 + l * 16) = g; *lp<frag_cd>(L_GU + (w8 & 3) * 2048 + 1024 + l * 16) = u; WAITL(); }
                BAR();
                if (w8 < 4) { g += *lp<frag_cd>(L_GU + w8 * 2048 + l * 16); u += *lp<frag_cd>(L_GU + w8 * 2048 + 1024 + l * 16);
                    float hv[4];
#pragma unroll
                    for (int e = 0; e < 4; ++e) { const float gg = rbf(g[e]); hv[e] = rbf(gg / (1.0f + __expf(-gg))) * rbf(u[e]); }
                    *lp<v2u>(L_HS + w8 * 512 + (2 * (n >> 3) + (q >> 1)) * 128 + (n & 7) * 16 + (q & 1) * 8) = v2u{cvt_pk(hv[0], hv[1]), cvt_pk(hv[2], hv[3])};
                    WAITL(); } }
            gv[0] = acc[0]; gv[1] = acc[1];
        } else {
#pragma unroll
            for (int cb = 0; cb < 4; ++cb) { frag_cd acc[2] = {};
#pragma unroll
                for (int i = 0; i < 4; ++i) { const v4u hf = *lp<v4u>(L_HS + i * 512 + q * 128 + (n & 7) * 16); acc[0] = MFMA(wf[8 * cb + 2 * i], hf, acc[0]); acc[1] = MFMA(wf[8 * cb + 2 * i + 1], hf, acc[1]); }
                STB(__builtin_bit_cast(v4u, n < 8 ? acc[0] : acc[1]), ry, ((w8 & 3) * 28 + h) * 1024 + ((n >> 3) * 32 + q * 8 + (n & 7)) * 16, ((w8 >> 2) * 4 + cb) * (4 * 28 * 1024)); }
        }
        MARK(67 + ti);
      }
      WAITV(0); BAR();
    }
    if (w8 == 0) {
        gst1(a.ctl + C_Y + h * 32, 1u);
        flags_wait<16>(a.ctl + C_Y, FF / 128, 1, l, err);
        frag_cd y = {};
#pragma unroll 1
        for (int i = 0; i < 28; i += 7) { v4u t[7];
#pragma unroll
            for (int u = 0; u < 7; ++u) t[u] = __builtin_amdgcn_raw_buffer_load_b128(ry, l * 16, (h * 28 + i + u) * 1024, 16);
#pragma unroll
            for (int u = 0; u < 7; ++u) y += __builtin_bit_cast(frag_cd, t[u]); }
        const float r = hi16((mykey & 0x80000000u) ? mykey & 0x7fffffffu : ~mykey), rs = rbf(1.0f / (1.0f + __expf(-r)));
#pragma unroll
        for (int e = 0; e < 4; ++e) y[e] = rbf(rbf(y[e]) * rs);
        STB(__builtin_bit_cast(v4u, y), ry, l * 16, (32 * 28 + h) * 1024);
        WAITV(0);
        gst1(a.ctl + C_Y + h * 32 + 1, 1u);
        MARK(120);
    }
}

__device__ __forceinline__ void owork(const Args& a, const int nt, const int kq, const unsigned L, const unsigned hb) {
    const int tid = threadIdx.x, w8 = RFL(tid >> 6), l = tid & 63, j = nt * 4 + kq;
    unsigned* const err = a.ctl + C_ERR;
    const unsigned L_EX = L + 131072, L_DEC = L_EX + 16384, L_Y = L_DEC + 64, L_KEY = L_Y + 1024;
    if (tid < 4) lp<unsigned>(L_DEC)[tid] = 0;
    {
        const bool lead = j == 0 && w8 == 1;
        if (lead) { const rsrc_t rc = mkrs(a.ctl, C_HB * 4);
#pragma unroll
            for (int i = 0; i < 8; ++i) STB(v4u{}, rc, l * 128, i * 8192);
            STB(v4u{}, rc, l * 16, 0); }
        unsigned char* p = a.ring + ((l >> 3 & 1) * 64 + (j >> 1)) * 32768 + ((l ^ (kq & 1) * 4 ^ kq >> 1) & 7) * 4096 + (w8 * 2 + (nt & 1)) * 256 + (kq & 1) * 128;
        unsigned best = 0;
        if (l < 16) gst((unsigned*)p, ~0u);
        WAITV(0);
#pragma unroll 1
        for (int i = 0; i < 16; ++i) { u64 t1, t2; unsigned v;
            if (lead && !(i & 3)) gadd1(a.ctl + C_HB);
            const u64 pi = ((u64)__builtin_amdgcn_readlane((unsigned)((u64)p >> 32), i) << 32) | (unsigned)__builtin_amdgcn_readlane((unsigned)(u64)p, i);
            asm volatile("s_nop 4\n\ts_memtime %0\n\ts_waitcnt lgkmcnt(0)\n\tglobal_load_dword %2, %3, %4 sc1\n\ts_waitcnt vmcnt(0)\n\ts_memtime %1\n\ts_waitcnt lgkmcnt(0)"
                         : "=&s"(t1), "=&s"(t2), "=&v"(v) : "v"(0u), "s"(pi) : "memory");
            if (l == i) best = ~(unsigned)(t2 - t1); }
        if (l < 16) { gst((unsigned*)p, ~0u); lp<unsigned>(L)[w8 * 16 + l] = best; }
        WAITV(0); WAITL(); BAR();
        if (w8 == 0) {
#pragma unroll
            for (int i = 1; i < 8; ++i) best = max(best, lp<unsigned>(L)[i * 16 + (l & 15)]);
            if (j) ctl_wait(a, hb);
            if (l < 16) __hip_atomic_fetch_max(a.ctl + C_CAL + l, best, __ATOMIC_RELAXED, __HIP_MEMORY_SCOPE_AGENT);
            WAITV(0);
            gadd1(a.ctl + C_READY);
            MARK(66);
        }
        if (lead) for (int i = 0; !RFL(gld(a.ctl + C_ACK)); ++i) { gadd1(a.ctl + C_HB); __builtin_amdgcn_s_sleep(8); if (i > GCAP) fatal(err); }
    }
    BAR();
    {
#pragma unroll 1
        for (int i = 0; i < 16; ++i) glds(a.x + (size_t)(64 * j + 8 * w8) * DM + i * 512, l * 16, L + (w8 * 16 + i) * 1024);
        v4u gw[2], wr[2];
#pragma unroll
        for (int i = 0; i < 2; ++i) { gw[i] = *(const v4u*)(a.gw + 512 * i + 8 * l); wr[i] = *(const v4u*)(a.wr + 512 * i + 8 * l); }
        WAITV(0);
        float res = 0.f;
#pragma unroll 1
        for (int t = 0; t < 8; ++t) { float sg = 0.f, sr = 0.f;
#pragma unroll
            for (int i = 0; i < 2; ++i) { const v4u xv = *lp<v4u>(L + (w8 * 16 + 2 * t + i) * 1024 + l * 16);
#pragma unroll
                for (int e = 0; e < 4; ++e) { const float x0 = lo16(xv[e]), x1 = hi16(xv[e]); const unsigned p = cvt_pk(x0 * lo16(gw[i][e]), x1 * hi16(gw[i][e]));
                    sg += lo16(p) + hi16(p); sr = __builtin_fmaf(x0, lo16(wr[i][e]), __builtin_fmaf(x1, hi16(wr[i][e]), sr)); } }
            float v = tadd32(sg, sr);
            v = tadd16(v, v); v += dppf<ROR8>(v); v += dppf<0x124>(v); v += dppf<0x122>(v); v += dppf<0x121>(v);
            if ((l & 7) == t) res = v; }
        const unsigned rb = cvt_pk(res, 0.f), tok = 64 * j + 8 * w8 + (l & 7);
        const unsigned val = l < 32 ? __float_as_uint(rbf(1.0f / (1.0f + __expf(-lo16(rb))))) : (((rb & 0x8000u) ? ~rb : rb | 0x8000u) << 16) | (4095u - tok);
        if ((l & 31) < 8) gst(a.gate + (l >> 5) * S_TOK + tok, val);
        WAITV(0); BAR();
        if (w8 == 0) gst1(a.ctl + C_R + j * 32, 1u);
        MARK(67);
    }
    v4u wf[32];
    { const Tile T{a.Wo + (size_t)kq * 1024 * DM + nt * 128, DM, 0, 1};
      tile_issue(T, 0, L, w8, l); tile_issue(T, 1, L, w8, l);
      tile_load(T, wf, L, w8, l, [](int) { return false; }); }
    MARK(0);
    if (w8 == 1) { const unsigned mine = topk(a, l); if (l < 8) lp<unsigned>(L_KEY)[l] = mine; WAITL(); }
    BAR();
    MARK(68);
    unsigned* const fP = a.ctl + C_P + nt * 512;
    const unsigned rb = L + (w8 >> 2) * 32768 + l * 16;
    unsigned pacc[8] = {}, plast = 0; (void)pacc; (void)plast;
    PT(7);
    auto oissue = [&](int s, int wv, int i) { glds1<2>(a.ox + (size_t)s * 262144 + kq * 65536 + wv * 8192 + i * 2048, l * 16, L + (s & 1) * 65536 + wv * 8192 + i * 2048); };
    if (w8 >= 4) {
        const rsrc_t rf = mkrs(fP + (kq * 4 + (w8 & 3)) * 32, 4), rp = mkrs(a.part, NSEG * 524288);
        const int stp = ((nt * 4 + (w8 & 3)) * 4 + kq) * 4096 + l * 16;
        const unsigned* fW = l < 32 ? a.ctl + C_M + (kq * 32 + l) * 32 : fP + ((l & 3) * 4 + kq) * 32;
        unsigned fv = 0; int pc = 0;
        auto a_ok = [&](int s) { return s < NSEG && (unsigned)__builtin_amdgcn_ballot_w64(fv <= (unsigned)s) == 0; };
        auto p_upd = [&]() { if ((__builtin_amdgcn_ballot_w64(fv <= (unsigned)pc) >> 32) == 0) ++pc; if ((__builtin_amdgcn_ballot_w64(fv <= (unsigned)pc) >> 32) == 0) ++pc; lp<unsigned>(L_DEC)[2] = pc; };
        int have = 0, nxt = 0, late = 0;
        for (int s = 0; s <= NSEG; ++s) {
            if (!have) {
                if (!late) {
                    gld_wait<0>(fv);
                    __builtin_amdgcn_raw_buffer_store_b32(s, rf, l * 4, 0, 16);
                    if (w8 == 4) {
                        for (int i = 0;; ++i) { if (fv <= (unsigned)(l < 32 ? s + 1 : s - 1)) fv = gld(fW); WAITV(0); p_upd(); if (a_ok(s) || pc == NSEG) break; __builtin_amdgcn_s_sleep(2); if (i > GCAP) fatal(err); }
                        lp<unsigned>(L_DEC)[3] = s + 1; lp<unsigned>(L_DEC)[~s & 1] = a_ok(s + 1); FENCE(); }
                    if (s == NSEG) break;
                    for (int i = 0; ldsr(L_DEC + 12) <= (unsigned)s; ++i) { __builtin_amdgcn_s_sleep(1); if (i > LCAP) endpgm(); }
                    FENCE();
                    PT(0);
#pragma unroll
                    for (int i = 0; i < 8; ++i) oissue(s, w8 ^ (~i & 4), i & 3);
                    WAITV(8);
                }
                WAITV(12); BAR();
                nxt = RFL(ldsr(L_DEC + 4 * (~s & 1))) & 1;
                if (late) WAITV(4); else WAITV(0);
                if (nxt) {
#pragma unroll
                    for (int i = 0; i < 4; ++i) oissue(s + 1, w8 ^ 4, i); }
                PT(1);
            }
            if (w8 == 4 && !nxt) {
                if (!have) { if (fv <= (unsigned)(s + 1)) gld_req(fv, fW); WAITV(0); }
                gld_wait<4>(fv);
                p_upd(); lp<unsigned>(L_DEC)[~s & 1] = 4 * a_ok(s + 1); WAITL();
                if (fv <= (unsigned)(s + 2)) gld_req(fv, fW); }
            BAR();
            PT(2);
            PRIO(2);
            frag_cd acc[4]; Half hx; unsigned mid;
            seg_gemm<5, false>(wf, rb + (s & 1) * 65536, acc, [&](int i) {
                if (i == BARI - 5) { if (!(have & nxt)) WAITV(0);
                    gld_wait<4>(fv);
                    if (w8 == 4) { p_upd(); const bool k = a_ok(s + 1); if (k) lp<unsigned>(L_DEC)[3] = s + 2; lp<unsigned>(L_DEC)[s & 1] = a_ok(s + 2) + 2 * k; }
                    __builtin_amdgcn_raw_buffer_store_b32(s - (have & nxt), rf, l * 4, 0, 16); FENCE(); }
                if (i == BARI) { PRIO(3); PT(5); BAR(); PT(6); }
                if (i == 3) asm volatile("ds_read_b32 %0, %1" : "=v"(mid) : "v"(L_DEC + 4 * (~s & 1)) : "memory");
                if (i == 26) half_req(L_EX + (w8 & 3) * 4096 + l * 16, L_DEC + 4 * (s & 1), hx);
                if (i == 28) { asm volatile("s_waitcnt lgkmcnt(4)" : "+v"(hx.dec), "+v"(mid)); const int d = RFL(hx.dec); have = nxt | RFL(mid) >> 2; nxt = have & d; late = ~have & d >> 1 & 1; }
                if (i >= 28 && (nxt | late)) oissue(s + 1 + nxt, w8 ^ 4, i - 28); });
            PRIO(0);
            PT(3);
            half_get(hx, acc, [&](int i) { if (late) oissue(s + 1, w8, i); });
            if (w8 == 4 && fv <= (unsigned)(s + 3)) gld_req(fv, fW);
#pragma unroll
            for (int i = 0; i < 4; ++i) STB(__builtin_bit_cast(v4u, acc[i]), rp, stp + i * 1024, s * 524288);
            FENCE();
            PT(4);
        }
        if (w8 == 5) {
            flags_wait<4>(a.ctl + C_Y + j * 32 + 1, 1, 1, l, err);
            const unsigned tok = 4095u - (lp<unsigned>(L_KEY)[l & 7] & 4095u); const int xo = tok * DM + j * 32 + (l >> 3) * 4;
            const frag_cd y = __builtin_bit_cast(frag_cd, __builtin_amdgcn_raw_buffer_load_b128(mkrs(a.ypart + (32 * 28 + j) * 1024, 1024), l * 16, 0, 16));
            const v2u xv = *(const v2u*)(a.x + xo); const float gt = __uint_as_float(gld(a.gate + tok)), omg = rbf(1.0f - gt);
            const float b[4] = {rbf(lo16(xv.x) + y[0]), rbf(hi16(xv.x) + y[1]), rbf(lo16(xv.y) + y[2]), rbf(hi16(xv.y) + y[3])};
            asm volatile("" :: "v"(b[0]), "v"(b[1]), "v"(b[2]), "v"(b[3]), "v"(omg));
            BAR();
            const frag_cd t = *lp<frag_cd>(L_Y + l * 16);
            *(v2u*)(a.out + xo) = v2u{mix(gt, omg, t[0], t[1], b[0], b[1]), mix(gt, omg, t[2], t[3], b[2], b[3])};
        } else BAR();
    } else {
        const Finisher fz(a, j, w8, l, L_Y);
        int fin = 0, have = 0, pc = 0;
        for (int s = 0; s <= NSEG; ++s) {
            if (!have) {
                for (int i = 0; fin < s; ++i) {
                    if (s < NSEG && RFL(ldsr(L_DEC + 12)) > s) break;
                    pc = RFL(ldsr(L_DEC + 8));
                    if (fin < pc) { FENCE(); fz.finish(fin, fz.issue(fin, true), [](int) {}); ++fin; }
                    else { __builtin_amdgcn_s_sleep(1); if (i > LCAP) endpgm(); }
                }
                if (s == NSEG) break;
            }
            FENCE();
            const bool fi = fin < pc;
            const Finisher::Fin f = fz.issue(fin, fi);
            WAITL(); WAITV(15);
            PT(0);
            BAR();
            PT(2);
            PRIO(2);
            frag_cd acc[4];
            v4u dec;
            seg_gemm<1, false>(wf, rb + (s & 1) * 65536, acc, [&](int i) {
                if (i == BARI) { PRIO(3); PT(1); WAITV(6); BAR(); PT(6); }
                if (i == 26) asm volatile("ds_read_b128 %0, %1" : "=v"(dec) : "v"(L_DEC) : "memory"); });
            PRIO(1);
            PT(3);
            asm volatile("s_waitcnt lgkmcnt(0)" : "+v"(dec));
            have = RFL(s & 1 ? dec.x : dec.y) & 5; pc = RFL(dec.z);
            half_put(L_EX, w8, l, acc);
            WAITV(0);
            if (have & 4) {
#pragma unroll
                for (int i = 0; i < 4; ++i) oissue(s + 1, w8, i); }
            if (have) { oissue(s + 1, w8 ^ 4, 0); oissue(s + 1, w8 ^ 4, 1); }
            PT(4);
            if (!have) { WAITL(); BAR(); }
            fz.finish(fin, f, [&](int i) { if (have) oissue(s + 1, w8 ^ 4, 2 + i); });
            fin += fi;
            MARK(1 + s);
            PT(5);
        }
        WAITL(); BAR();
    }
    PFLUSH();
    MARK(65);
}

__global__ void __launch_bounds__(512, 2) k_mega(Args a) {
    __shared__ __attribute__((aligned(1024))) unsigned char smem[163840];
    const unsigned L = (unsigned)(size_t)(LAS unsigned char*)smem;
    const int b = blockIdx.x, u = (b & 3) + 4 * (b >> 3);
    MARK(255); MARKC(254);
    const unsigned hb = __builtin_amdgcn_raw_buffer_load_b32(mkrs(a.ctl + C_HB, 4), threadIdx.x >> 6 == (b & 4 ? (u < 96 ? 4 : 0) : 3) ? 0 : OOB, 0, 16);
    FENCE();
    if (!(b & 4)) chain(a, u, L, hb);
    else if (u < 96) gwork(a, (u >> 2) % 3, (u & 3) * 8 + (u >> 2) / 3, L, hb);
    else owork(a, (u >> 2) - 24, u & 3, L, hb);
}

static constexpr int g_coff = 0, g_force = 0; static constexpr int g_d1 = 620, g_d2 = 1340, g_per = 1800, g_dinit = 250; static constexpr unsigned* g_dbg = nullptr;
extern "C" void kernel_launch(void* const* d_in, const int* in_sizes, int n_in, void* d_out, int out_size, void* d_ws, size_t ws_size, hipStream_t stream) {
    auto in = [&](int i) { return (const bf16_t*)d_in[i]; };
    unsigned char* ws = (unsigned char*)d_ws; size_t off = g_coff;
    auto alloc = [&](size_t bytes) { unsigned char* p = ws + off; off += (bytes + 255) & ~(size_t)255; return p; };
    unsigned* ctl = (unsigned*)alloc(C_WORDS * 4);
    unsigned* gate = (unsigned*)alloc(2 * S_TOK * 4); unsigned char* ypart = alloc(32 * 29 * 1024);
    off += (size_t)(-(uintptr_t)(ws + off)) & ((2u << 20) - 1);
    unsigned char* ring = alloc(RING_BYTES);
    unsigned char* qkv = alloc((size_t)NSEG * NH * 24576);
    unsigned char* ox = alloc((size_t)NSEG * 262144); unsigned char* part = alloc((size_t)NSEG * 524288);
    Args a{in(0), in(1), in(2), in(3), in(4), in(5), (bf16_t*)d_out, ctl, ring, qkv, ox, part, in(8), in(9), in(10), in(11), in(12), gate, ypart, g_dbg,
           g_d1 * 16 / 10, g_d2 * 16 / 10, g_per * 16 / 10, g_dinit * 16 / 10 + (g_force << 16)};
    k_mega<<<256, 512, 0, stream>>>(a);
}
```

```cpp
#include <hip/hip_runtime.h>
#include <stdint.h>
#include <type_traits>

typedef unsigned short bf16_t;
typedef unsigned long long u64;
typedef float frag_cd __attribute__((ext_vector_type(4)));
typedef unsigned v4u __attribute__((ext_vector_type(4)));
typedef unsigned v8u __attribute__((ext_vector_type(8)));
typedef unsigned v2u __attribute__((ext_vector_type(2)));
typedef float v2f __attribute__((ext_vector_type(2)));
typedef __bf16 bf16x2_t __attribute__((ext_vector_type(2)));
typedef __bf16 v8bf __attribute__((ext_vector_type(8)));
typedef __bf16 v16bf __attribute__((ext_vector_type(16)));
typedef short v4s_t __attribute__((ext_vector_type(4)));
typedef __amdgpu_buffer_rsrc_t rsrc_t;
#define LAS __attribute__((address_space(3)))
#define GAS __attribute__((address_space(1)))

static constexpr int S_TOK = 2048, DM = 1024, NH = 32, DH = 128, MM = 8, SEG = 32, NSEG = 64, HD = NH * DH, FF = 3584, TOPC = 8;
static constexpr int C_ERR = 0, C_READY = 32, C_CAL = 64  , C_ACK = 96, C_G = 128  , C_M = C_G + 4096  ,
                     C_P = C_M + 4096  , C_R = C_P + 4096, C_Y = C_R + 1024  , C_HB = C_Y + 1024, C_WORDS = C_HB + 32;
static constexpr int RING_BYTES = 4 << 20, B_H1 = 64, B_H3 = 96, B_TS = 112;
static constexpr unsigned SPIN_CAP = 1u << 18;
static constexpr int OOB = 0x7ffffff0, LCAP = 1 << 22, GCAP = 1 << 20;
static constexpr float SCALE = 0.08838834764831845f, LOG2E = 1.4426950408889634f;

struct Args {
    const bf16_t* x; const bf16_t* mem0; const bf16_t* Wq; const bf16_t* Wk; const bf16_t* Wv; const bf16_t* Wo;
    bf16_t* out; unsigned* ctl; unsigned char* ring; unsigned char* qkv; unsigned char* ox; unsigned char* part;
    const bf16_t* wr; const bf16_t* wg; const bf16_t* wu; const bf16_t* wd; const bf16_t* gw;
    unsigned* gate  ; unsigned char* ypart; unsigned* dbg;
    int d1, d2, per, dinit;
};

__device__ __forceinline__ unsigned cvt_pk(float lo, float hi) { v2f v = {lo, hi}; return __builtin_bit_cast(unsigned, __builtin_convertvector(v, bf16x2_t)); }
__device__ __forceinline__ float lo16(unsigned p) { return __uint_as_float(p << 16); }
__device__ __forceinline__ float hi16(unsigned p) { return __uint_as_float(p & 0xffff0000u); }
__device__ __forceinline__ float rbf(float f) { return lo16(cvt_pk(f, 0.f)); }
template <int C> __device__ __forceinline__ unsigned dppu(unsigned v) { return (unsigned)__builtin_amdgcn_update_dpp(0, (int)v, C, 0xf, 0xf, true); }
template <int C> __device__ __forceinline__ float dppf(float v) { return __uint_as_float(dppu<C>(__float_as_uint(v))); }
#define ROR8 0x128
#define HMIR 0x141
__device__ __forceinline__ v2u sw16(unsigned a, unsigned b) { auto r = __builtin_amdgcn_permlane16_swap(a, b, false, false); return v2u{r[0], r[1]}; }
__device__ __forceinline__ v2u sw32(unsigned a, unsigned b) { auto r = __builtin_amdgcn_permlane32_swap(a, b, false, false); return v2u{r[0], r[1]}; }
__device__ __forceinline__ float tadd16(float a, float b) { v2u r = sw16(__float_as_uint(a), __float_as_uint(b)); return __uint_as_float(r.x) + __uint_as_float(r.y); }
__device__ __forceinline__ float tadd32(float a, float b) { v2u r = sw32(__float_as_uint(a), __float_as_uint(b)); return __uint_as_float(r.x) + __uint_as_float(r.y); }
__device__ __forceinline__ float tmax16(float a) { v2u r = sw16(__float_as_uint(a), __float_as_uint(a)); return fmaxf(__uint_as_float(r.x), __uint_as_float(r.y)); }
__device__ __forceinline__ float tmax32(float a) { v2u r = sw32(__float_as_uint(a), __float_as_uint(a)); return fmaxf(__uint_as_float(r.x), __uint_as_float(r.y)); }
__device__ __forceinline__ unsigned umax3(unsigned a, unsigned b, unsigned c) { return max(max(a, b), c); }
__device__ __forceinline__ v4u pack_swap(frag_cd a, frag_cd b) { v2u x0 = sw16(cvt_pk(a[0], a[1]), cvt_pk(b[0], b[1])), x1 = sw16(cvt_pk(a[2], a[3]), cvt_pk(b[2], b[3])); return v4u{x0.x, x1.x, x0.y, x1.y}; }
#define MFMA(a, b, c) __builtin_amdgcn_mfma_f32_16x16x32_bf16(__builtin_bit_cast(v8bf, a), __builtin_bit_cast(v8bf, b), c, 0, 0, 0)
#define SMF(a, b, c, ix) __builtin_amdgcn_smfmac_f32_16x16x64_bf16(__builtin_bit_cast(v8bf, a), __builtin_bit_cast(v16bf, b), c, ix, 0, 0)
#define FENCE() asm volatile("" ::: "memory")
#define SB() __builtin_amdgcn_sched_barrier(0)
#define RFL(x) __builtin_amdgcn_readfirstlane(x)
#define BAR() do { FENCE(); __builtin_amdgcn_s_barrier(); FENCE(); } while (0)
#define WAITV(n) do { FENCE(); __builtin_amdgcn_s_waitcnt(0x0F70 | (n)); FENCE(); } while (0)
#define WAITL() asm volatile("s_waitcnt lgkmcnt(0)" ::: "memory")
__device__ __forceinline__ void STB(v4u v, rsrc_t rs, int off, int soff) { __builtin_amdgcn_raw_buffer_store_b128(v, rs, off, soff, 16); asm volatile("s_nop 1" :: "v"(v) : "memory"); }
template <class T> __device__ __forceinline__ LAS T* lp(unsigned a) { return (LAS T*)(size_t)a; }
__device__ __forceinline__ void glds(const void* g, unsigned voff, unsigned lds) { asm volatile("s_mov_b32 m0, %2\n\ts_nop 0\n\tglobal_load_lds_dwordx4 %0, %1" :: "v"(voff), "s"(g), "s"(lds) : "memory"); }
#define NTC " nt"
#define NTW " nt"
__device__ __forceinline__ void gldsc(const void* g, unsigned voff, unsigned lds) { asm volatile("s_mov_b32 m0, %2\n\ts_nop 0\n\tglobal_load_lds_dwordx4 %0, %1" NTC :: "v"(voff), "s"(g), "s"(lds) : "memory"); }
__device__ __forceinline__ void gldsw(const void* g, unsigned voff, unsigned lds) { asm volatile("s_mov_b32 m0, %2\n\ts_nop 0\n\tglobal_load_lds_dwordx4 %0, %1" NTW :: "v"(voff), "s"(g), "s"(lds) : "memory"); }
template <int N> __device__ __forceinline__ void glds1(const void* g, unsigned voff, unsigned lds) {
#define GL(o) "\n\tglobal_load_lds_dwordx4 %0, %1 offset:" #o " sc1"
    if constexpr (N == 1) asm volatile("s_mov_b32 m0, %2\n\ts_nop 0" GL(0) :: "v"(voff), "s"(g), "s"(lds) : "memory");
    if constexpr (N == 2) asm volatile("s_mov_b32 m0, %2\n\ts_nop 0" GL(0) GL(1024) :: "v"(voff), "s"(g), "s"(lds) : "memory");
    if constexpr (N == 4) asm volatile("s_mov_b32 m0, %2\n\ts_nop 0" GL(0) GL(1024) GL(2048) GL(3072) :: "v"(voff), "s"(g), "s"(lds) : "memory");
}
__device__ __forceinline__ rsrc_t mkrs(const void* p, int bytes) { return __builtin_amdgcn_make_buffer_rsrc((void*)p, 0, bytes, 0x00020000); }
__device__ __forceinline__ unsigned gld(const unsigned* p) { return __hip_atomic_load(p, __ATOMIC_RELAXED, __HIP_MEMORY_SCOPE_AGENT); }
__device__ __forceinline__ void gst(unsigned* p, unsigned v) { __hip_atomic_store(p, v, __ATOMIC_RELAXED, __HIP_MEMORY_SCOPE_AGENT); }
__device__ __forceinline__ void gst1(unsigned* p, unsigned v) { asm volatile("s_mov_b64 exec, 1\n\tglobal_store_dword %0, %1, %2 sc1\n\ts_mov_b64 exec, -1" :: "v"(0u), "v"(v), "s"(p) : "memory"); }
__device__ __forceinline__ unsigned rt16() { return (unsigned)__builtin_amdgcn_s_memrealtime() << 4; }

#define NSLOT 12
#define PT(k) do {} while (0)
#define PFLUSH() do {} while (0)
#define MARK(i) do {} while (0)
#define MARKC(i) do {} while (0)

#define H3X 0
#define EW 30
#define E0 32
#define E2 160
#define E3 120
#define AT 0
#define XH 0
#define XP 0
#define VR 0
#define DWM 4
#define NQ 1130
#ifndef NV
#define NV 960
#endif
#define LXC 200
#define TSL 1
#define RSL 1
#define SO 58
#define TS1 2
#define TS2 16
#define TMAP 1
#define TIF 0
#define TSP 0
#define OPL 1
#define CPL 0
#define TNT 0
#define EF 0
#define TFK 8
#define TS3 0
#define TSPEC 0
#define TD 10
#define VDEL1 15
#define AEARLY 1
#define XC 4
#define ODEL 1
#define SMQ 0x53b
#define SMV 0x79b
#define AL1 1
#define AL2 0
#define AL3 0
#define AL4 1
#define AL5 7
#define AL6 0
#define HB 0
#define HP 0
#define H3 0
#define HSM 0x2d
#define PF 22
#define US 1
#define ROT 0
#define VL 1
#define VP 0x3911
#define VE 0
#define VN3 36
#define CV 0
#define CD 60
#define XS1 0
#define XS2 6
#define PLO 0
#define MSP 1
#define VD 260
#define LK 300
#define LT 400
#define NS 16
#define GS 6
#define KC 560
#define GM 200
#define SGAP 60
#define HSL 6
#define CSL 3
#define WK 40
#define WKO 100
#define WB 200
#define WC1 0
#define WC2 64
#define WTHR 340
#define SLIP_THR 400
#define CKA 0.75
#define CKF 22.5
#define TG_THR 480
#define SLIP_ADD 640
#define SLIP_INC 10
__device__ __forceinline__ void endpgm() { asm volatile("s_endpgm" ::: "memory"); }
__device__ __forceinline__ void fatal(unsigned* err) { asm volatile("global_store_dword %0, %1, off sc1\n\ts_waitcnt vmcnt(0)\n\ts_endpgm" :: "v"(err), "v"(1u) : "memory"); }
__device__ __forceinline__ unsigned ldsr(unsigned p) { return __hip_atomic_load(lp<unsigned>(p), __ATOMIC_RELAXED, __HIP_MEMORY_SCOPE_WORKGROUP); }
__device__ __forceinline__ v2u trd(unsigned p) { return __builtin_bit_cast(v2u, __builtin_amdgcn_ds_read_tr16_b64_v4i16(lp<v4s_t>(p))); }
__device__ __forceinline__ v8u cat(v2u a, v2u b, v2u c, v2u d) { return v8u{a.x, a.y, b.x, b.y, c.x, c.y, d.x, d.y}; }
__device__ __forceinline__ v4u cat2(v2u a, v2u b) { return v4u{a.x, a.y, b.x, b.y}; }
template <int SL, int FAR = 0> __device__ __forceinline__ void flags_wait(unsigned& v, const unsigned* p, unsigned need, unsigned* err) {
    for (int i = 0; __builtin_amdgcn_ballot_w64(v < need) != 0; ++i) {
        if (i) { if (FAR && __builtin_amdgcn_ballot_w64(v + 1 < need) != 0) __builtin_amdgcn_s_sleep(FAR); __builtin_amdgcn_s_sleep(SL); }
        if (v < need) v = gld(p);
        if (i > GCAP) fatal(err); }
    FENCE();
}
template <int SL> __device__ __forceinline__ void flags_wait(const unsigned* p, int nw, unsigned need, int l, unsigned* err) { unsigned v = need - 1; flags_wait<SL>(v, p + min(l, nw - 1) * 32, need, err); }

__device__ __forceinline__ void gadd1(unsigned* p) { asm volatile("s_mov_b64 exec, 1\n\tglobal_atomic_add %0, %1, off sc1\n\ts_mov_b64 exec, -1" :: "v"(p), "v"(1u) : "memory"); }
__device__ __forceinline__ void gld_req(unsigned& v, const unsigned* p) { asm volatile("global_load_dword %0, %1, off sc1" : "+v"(v) : "v"(p) : "memory"); }
template <int N> __device__ __forceinline__ void gld_wait(unsigned& v) { asm volatile("s_waitcnt vmcnt(%1)" : "+v"(v) : "n"(N) : "memory"); }
__device__ __forceinline__ void ctl_wait(const Args& a, unsigned hb) {
    for (int i = 0; RFL(gld(a.ctl + C_HB)) == RFL(hb); ++i) { __builtin_amdgcn_s_sleep(8); if (i > GCAP) fatal(a.ctl + C_ERR); }
    FENCE();
}

typedef unsigned v16u __attribute__((ext_vector_type(16)));
typedef unsigned v32u __attribute__((ext_vector_type(32)));
#define STR_(x) #x
#define STR(x) STR_(x)
#define SET(x) "\n.set " #x ", " STR(x)
#define A_STAMPS "0"
#define A_BENCH "0"
static_assert(NH * 24576 == 0xC0000, "");
#define A_CHAIN ".set STAMPS, " A_STAMPS "\n.set BENCH, " A_BENCH "\n.set WB, " STR(WB) "\n.set WC1, " STR(WC1) "\n.set WC2, " STR(WC2) SET(SMQ) SET(SMV) SET(AL1) SET(AL2) SET(AL3) SET(AL4) SET(AL5) SET(AL6) SET(HB) SET(US) SET(PF) SET(HP) SET(H3) "\n.set HS, " STR(HSM) SET(VL) SET(VP) SET(VE) SET(VN3) SET(CV) SET(CD) SET(XS1) SET(XS2) SET(PLO) SET(CSL) SET(MSP) SET(VD) SET(LK) SET(LT) SET(NS) SET(GS) SET(GM) SET(SGAP) SET(EF) SET(NQ) SET(NV) SET(LXC) SET(TSL) SET(RSL) SET(VR) SET(DWM) SET(AT) SET(EW) SET(E0) SET(E2) SET(E3) SET(HSL) "\n.set WK, " STR(WK) "\n.set WN, 64\n.set WZ, WN+5\n.set WTHR, " STR(WTHR) "\n" R"(
.macro SLED n
  s_getpc_b64 vcc
4: s_sub_u32 s77, (3f-4b)/4, s77
  s_lshl2_add_u32 vcc_lo, s77, vcc_lo
  s_addc_u32 vcc_hi, vcc_hi, 0
  s_setpc_b64 vcc
.rept \n
  s_nop 1
.endr
3: s_waitcnt lgkmcnt(0)
.endm
.macro WAITC T, thr
  s_movk_i32 s76, 0x1000
1: s_memrealtime vcc
  s_memtime s[6:7]
  s_sub_u32 s76, s76, 1
  s_cbranch_scc1 3f
  s_waitcnt lgkmcnt(0)
  s_lshl_b32 s77, vcc_lo, 4
  s_sub_i32 s77, \T, s77
  s_cmp_ge_i32 s77, \thr
  s_cbranch_scc1 1b
.endm
.macro WAITT T
  WAITC \T, 64
3: s_waitcnt lgkmcnt(0)
.endm
.macro WAITP T
  WAITC \T, WTHR
  s_sub_i32 s77, s77, WC1
  s_cmp_lt_i32 s77, WB
  s_cbranch_scc1 5f
.irp r,30,32,34,36,38,94,96,98
  s_memrealtime s[\r:\r+1]
  s_nop 1
.endr
  s_memtime s[6:7]
  s_sub_i32 s77, \T, WC2
  s_sub_i32 s4, s4, EW
  s_waitcnt lgkmcnt(0)
  s_add_u32 s30, s30, s32
  s_add_u32 s34, s34, s36
  s_add_u32 s38, s38, s94
  s_add_u32 s96, s96, s98
  s_add_u32 s30, s30, s34
  s_add_u32 s38, s38, s96
  s_add_u32 s30, s30, s38
  s_lshl_b32 s30, s30, 1
  s_sub_i32 s77, s77, s30
5: s_max_i32 s77, s77, 0
  s_mulk_i32 s77, 45
  s_lshr_b32 s77, s77, 8
  s_sub_i32 s6, s4, s6
  s_ashr_i32 s6, s6, 3
  s_max_i32 s77, s77, s6
  s_min_u32 s77, s77, WN
  SLED WN
.endm
.macro WAITK x, nom
  s_add_i32 s5, s59, \x-\nom
  s_add_i32 s75, s92, s59
  s_movk_i32 s76, 0x1000
  s_add_i32 s75, s75, \x
1: s_memtime vcc
  s_sub_u32 s76, s76, 1
  s_cbranch_scc1 3f
  s_waitcnt lgkmcnt(0)
  s_sub_i32 s77, s75, vcc_lo
  s_sub_i32 s7, s6, vcc_lo
  s_add_i32 s7, s7, s5
  s_max_i32 s77, s77, s7
  s_cmp_ge_i32 s77, 8*WK
  s_cbranch_scc1 1b
  s_max_i32 s77, s77, 0
  s_lshr_b32 s77, s77, 3
  SLED WK
.endm
.macro H1ST
.if AT == 1
  buffer_atomic_swap v[RES], v[ST1], s[40:43], s66 offen sc1
.elseif AT == 2
  buffer_atomic_and v[RES], v[ST1], s[40:43], s66 offen sc1
.elseif AT == 3
  buffer_store_dword v[RES], v[ST1], s[40:43], s66 offen sc0 sc1
.elseif AT == 4
  buffer_store_dword v[RES], v[ST1], s[40:43], s66 offen sc1 nt
.else
  buffer_store_dword v[RES], v[ST1], s[40:43], s66 offen sc1
.endif
.endm
.macro ALN k
.p2align 6
.rept \k
  s_nop 0
.endr
.endm
.macro HT n, r
.rept \n
  v_mov_b32 v[\r], 0
  s_nop 1
.endr
.endm
.macro TMAX a, t
  v_readlane_b32 \t, v[\a], 0
.irp r,1,2,3
  v_readlane_b32 s77, v[\a], \r
  s_sub_i32 s77, s77, \t
  s_max_i32 s77, s77, 0
  s_add_i32 \t, \t, s77
.endr
.endm
.macro TGET a
  v_mbcnt_lo_u32_b32 v[\a+1], 7, 0
  v_lshlrev_b32 v[\a+1], 7, v[\a+1]
  s_add_i32 s75, s88, s68
8: buffer_load_dword v[\a], v[\a+1], s[40:43], s75 offen sc1
  s_sub_u32 s71, s71, 1
  s_cbranch_scc1 L_fatal
  s_waitcnt vmcnt(0)
  v_cmp_eq_u32 vcc, -1, v[\a]
  s_cbranch_vccz 9f
  s_sleep TSL
  s_branch 8b
9:
  TMAX \a, s75
  s_mov_b32 s4, s92
  WAITP s75
  s_mov_b32 s76, 0x400000
.endm
.macro PLAN
  s_mulk_i32 s74, PF
  s_ashr_i32 s74, s74, 3
  s_add_i32 s73, s73, s58
  s_add_i32 s73, s73, s74
.endm
.macro STAMP k
.if STAMPS && ((SMK >> \k) & 1)
  s_memtime s[8+2*\k:9+2*\k]
.endif
.endm
.macro NEXT t
.if STAMPS
  s_waitcnt lgkmcnt(0)
.irp k,0,1,2,3,4,5,6,7,8,9,10
  v_writelane_b32 v[\t], s[8+2*\k], \k
.endr
  s_lshl_b32 s76, s64, 3
  s_add_i32 s76, s76, s63
  s_mul_i32 s76, s76, 48
  v_mbcnt_lo_u32_b32 v[\t+1], -1, 0
  v_lshl_add_u32 v[\t+1], v[\t+1], 2, s76
  s_mov_b64 exec, 0x7ff
  ds_write_b32 v[\t+1], v[\t]
  s_mov_b64 exec, -1
.endif
  s_lshl_b32 s72, s64, 18
  s_mov_b32 s64, s65
  s_add_i32 s65, s65, 1
  s_xor_b32 s66, s66, 0x8000
  s_xor_b32 s67, s67, 0x8000
  s_mov_b32 s76, s68
  s_mov_b32 s68, s69
  s_mov_b32 s69, s70
  s_mov_b32 s70, s76
.endm
.macro RETRY c
.if BENCH
  s_bitset1_b64 s[80+2*\c:81+2*\c], s64
.endif
  s_sub_u32 s71, s71, 1
  s_cbranch_scc1 L_fatal
  s_sleep RSL
.endm
.macro LD d, a, so, o
  buffer_load_dwordx4 v[\d:\d+3], v[\a], s[40:43], \so offen offset:\o sc1
.endm
.macro SM d, a, b
  v_smfmac_f32_16x16x64_bf16 v[\d:\d+3], v[\a:\a+3], v[\b:\b+7], v[SIX]
.endm
.macro MX0 a
  v_max_u32 v[MX], v[\a], v[\a+1]
  v_max3_u32 v[MX], v[MX], v[\a+2], v[\a+3]
.endm
.macro MX1 a
  v_max3_u32 v[MX], v[MX], v[\a], v[\a+1]
  v_max3_u32 v[MX], v[MX], v[\a+2], v[\a+3]
.endm
.macro AW r, k
  ds_write_addtid_b32 v[\r] offset:63744+256*\k
.endm
.macro HS a
  v_pk_add_f32 v[\a:\a+1], v[\a:\a+1], v[\a+2:\a+3]
.endm
.macro ZR a, n
.set zi, 0
.rept \n
  v_mov_b64 v[\a+zi:\a+zi+1], 0
.set zi, zi+2
.endr
.endm
.macro PRE
  s_mov_b32 s64, 63
  s_mov_b32 s65, 0
  s_mov_b32 s66, 0x8000
  s_mov_b32 s67, 0
  s_mov_b32 s68, 0x8000
  s_mov_b32 s69, 0x10000
  s_mov_b32 s70, 0
.endm
.macro XSL r
  s_and_b32 s75, \r, 3
  s_mulk_i32 s75, 20480
.endm
.macro CAP
  s_sub_u32 s76, s76, 1
  s_cbranch_scc1 L_fatal
.endm
  s_mov_b32 s71, 0x40000
  s_mov_b32 s88, s64
  s_mov_b32 s90, s65
  s_mov_b32 s91, s66
  s_mov_b64 s[80:81], 0
  s_mov_b64 s[82:83], 0
  s_mov_b64 s[84:85], 0
  s_mov_b64 s[86:87], 0
  s_mov_b32 s100, s67
  s_mov_b32 s101, s68
  s_cmp_gt_u32 s63, 3
  s_cbranch_scc1 LV_init
; ======== waves 0..3
.set SMK, SMQ
.set O3,168
.set ST1,169
.set RW1,170
.set RW2,171
.set RW3,172
.set XA,173
.set FW,174
.set FR,175
.set SIX,176
.set STK,177
.set FL,183
.set MB,128
.set AC,144
.set TAG,160
.set MX,161
.set RES,162
.set FA,163
.set FB,164
.set VA,164
.set VB,184
.set WV,188
.set RA,134
.set KX,196
.set XF,177
.set XC,)" STR(XC) R"(
.macro MEMQ
  LD MB, O3, s68, 0
  LD MB+4, O3, s68, 1024
  LD MB+8, O3, s68, 2048
  LD MB+12, O3, s68, 3072
.endm
.macro XCHK R
  s_add_i32 s75, s64, 2
  s_min_u32 s75, s75, 63
  v_cmp_ge_u32 vcc, s75, v[FL]
  s_cbranch_vccnz LX_w\R
LX_g\R:
  s_add_i32 s75, s64, 2
  XSL s75
  s_add_i32 m0, s75, s61
.endm
.macro XL4 a
.irp o,0,1024,2048,3072
  global_load_lds_dwordx4 v[\a], s[52:53] offset:\o sc1
.endr
  s_add_i32 m0, m0, 0x1000
.endm
.macro XREF R
  s_add_i32 s75, s64, 3
  s_min_u32 s75, s75, 63
  v_cmp_ge_u32 vcc, s75, v[FL]
  s_cbranch_vccz LX_n\R
  global_load_dword v[FL], v[XF], s[54:55] sc1
LX_n\R:
.endm
.macro XADV
  s_cmp_lt_u32 s64, 61
  s_cselect_b32 s75, 0xC0000, 0
  s_add_u32 s52, s52, s75
  s_addc_u32 s53, s53, 0
.endm
.macro XWAIT R
LX_w\R:
  s_mov_b32 s76, 0x100000
1: global_load_dword v[FL], v[XF], s[54:55] sc1
  s_waitcnt vmcnt(0)
  v_cmp_ge_u32 vcc, s75, v[FL]
  s_cbranch_vccz LX_g\R
  s_sleep 4
  CAP
  s_branch 1b
.endm
.macro OXST
  buffer_store_dwordx4 v[KX:KX+3], v[STK], s[44:47], s72 offen sc1
  buffer_store_dwordx4 v[KX+4:KX+7], v[STK], s[44:47], s72 offen offset:1024 sc1
.endm
.macro FLAGQ val
  v_mov_b32 v[MX], 0
  v_mov_b32 v[FA], \val
  s_mov_b64 exec, 1
  global_store_dword v[MX], v[FA], s[50:51] sc1
  s_mov_b64 exec, -1
.endm
.macro SMW i
  SM VA, MB+4*\i, WV+8*\i
  SM VB, MB+4*\i, WV+32+8*\i
.endm
.macro SMN k
.if (VP >> \k) & 1
  SM VA+(VB-VA)*(vn&1), MB+4*(vn>>1), WV+32*(vn&1)+8*(vn>>1)
.set vn, vn+1
.endif
.endm
.macro VOUT
  v_add_f32 v[VA], v[VA], v[VA+2]
  v_add_f32 v[VA+1], v[VA+1], v[VA+3]
  v_add_f32 v[VB], v[VB], v[VB+2]
  v_add_f32 v[VB+1], v[VB+1], v[VB+3]
  AW VA, 0
  AW VA+1, 2
  AW VB, 1
  AW VB+1, 3
  AW TAG, 6
.endm
.macro DM k
.if (HS >> \k) & 1
  v_mfma_f32_16x16x32_bf16 v[VA:VA+3], v[MB:MB+3], v[MB:MB+3], 0
.endif
.endm
.macro ZA
  ZR AC, 8
  ZR VA, 2
  ZR VB, 2
.endm
.macro PHASEA R
LQ_loop\R:
  STAMP 0
  ZA
  v_mov_b32 v[TAG], s65
LQ_bar\R:
  s_barrier
  STAMP 1
.if MSP
  LD MB, O3, s68, 0
  s_memtime s[92:93]
  s_nop MSP-1
  LD MB+4, O3, s68, 1024
  s_mov_b32 m0, s100
  s_nop MSP-1
  LD MB+8, O3, s68, 2048
  s_nop MSP
  LD MB+12, O3, s68, 3072
.else
  s_memtime s[92:93]
  MEMQ
  s_mov_b32 m0, s100
.endif
.set XS, 0
.if \R == 2
.set XS, 2
.irp i,0,1,4,5,6,7,8,9,10,11,12,13,14,15
  ds_read_b128 v[WV+4*\i:WV+4*\i+3], v[XA] offset:4096+1024*\i
.if \i == 9
  s_waitcnt lgkmcnt(6)
.endif
.endr
.rept )" STR(ODEL) R"(
  s_nop 7
.endr
  OXST
  s_nop 1
.irp i,2,3
  ds_read_b128 v[WV+4*\i:WV+4*\i+3], v[XA] offset:4096+1024*\i
.endr
  s_waitcnt lgkmcnt(0)
.endif
  s_mov_b32 s76, 0x400000
  ALN AL1
LQ_a\R:
  s_waitcnt vmcnt(3+XS)
  SM AC+8, MB, 64
  v_max_u32 v[MX], v[MB], v[MB+1]
  SM AC+12, MB, 96
  v_max3_u32 v[MX], v[MX], v[MB+2], v[MB+3]
  SM AC, MB, 0
  SM AC+4, MB, 32
  SMW 0
.irp i,1,2,3
  s_waitcnt vmcnt(3+XS-\i)
  SM AC+8, MB+4*\i, 64+8*\i
  v_max3_u32 v[MX], v[MX], v[MB+4*\i], v[MB+4*\i+1]
  SM AC+12, MB+4*\i, 96+8*\i
  v_max3_u32 v[MX], v[MX], v[MB+4*\i+2], v[MB+4*\i+3]
  SM AC, MB+4*\i, 8*\i
.if \i == 3
  v_cmp_eq_u32 vcc, -1, v[MX]
.endif
  SM AC+4, MB+4*\i, 32+8*\i
.if \i < VL
  SMW \i
.endif
.endr
  STAMP 2
  v_add_f32 v[AC+8], v[AC+8], v[AC+10]
  v_add_f32 v[AC+12], v[AC+12], v[AC+14]
  v_add_f32 v[AC+9], v[AC+9], v[AC+11]
  v_add_f32 v[AC+13], v[AC+13], v[AC+15]
  v_add_f32 v[AC+3], v[AC+1], v[AC+3]
  v_add_f32 v[AC], v[AC], v[AC+2]
  v_add_f32 v[AC+5], v[AC+5], v[AC+7]
  s_cbranch_vccnz LQ_ra\R
.set vn, 2*VL
  AW AC+8, 0
  SMN 0
  AW AC+12, 1
  SMN 1
  AW AC+9, 2
  SMN 2
  AW AC+13, 3
  SMN 3
  AW AC+3, 4
  SMN 4
  AW AC+5, 5
  SMN 5
  AW TAG, 6
  SMN 6
  STAMP 6
  s_barrier
  SMN 7
  ds_read_b32 v[FA], v[FR]
  SMN 8
  ds_read2st64_b32 v[AC+8:AC+9], v[RW1] offset1:1
  SMN 9
  ds_read2st64_b32 v[AC+10:AC+11], v[RW2] offset1:1
  SMN 10
  ds_read2st64_b32 v[AC+12:AC+13], v[RW3] offset1:1
  SMN 11
  s_add_i32 m0, s100, 0x2000
  v_add_f32 v[AC+1], v[AC+4], v[AC+6]
  SMN 12
  SMN 13
  SMN 14
.if vn != 8
.error "v projection incomplete"
.endif
.if VE
  VOUT
.endif
  s_waitcnt lgkmcnt(5*VE)
LQ_p\R:
  v_cmp_gt_u32 vcc, s65, v[FA]
  v_add_f32 v[AC+8], v[AC+8], v[AC]
  v_add_f32 v[AC+9], v[AC+9], v[AC+1]
  v_add_f32 v[AC+8], v[AC+8], v[AC+10]
  v_add_f32 v[AC+9], v[AC+9], v[AC+11]
  v_add_f32 v[AC+8], v[AC+8], v[AC+12]
  v_add_f32 v[AC+9], v[AC+9], v[AC+13]
  v_cvt_pk_bf16_f32 v[RES], v[AC+8], v[AC+9]
  s_cbranch_vccnz LQ_pb\R
  H1ST
  s_memtime s[6:7]
  STAMP 3
.if !VE
  VOUT
.endif
.if \R != 2
  s_cmp_eq_u32 s64, 63
  s_cbranch_scc1 L_last
.elseif EF
  s_cmp_eq_u32 s64, 63
  s_cbranch_scc1 LQ_ef
LQ_eg:
.endif
.if \R != 3
  WAITK 0, NQ
.endif
  STAMP 4
.endm
.macro COLDA R
LQ_pb\R:
.if BENCH && \R != 3
  s_bitset1_b64 s[86:87], s64
.endif
  CAP
  ds_read_b32 v[FA], v[FR]
  ds_read2st64_b32 v[AC+8:AC+9], v[RW1] offset1:1
  ds_read2st64_b32 v[AC+10:AC+11], v[RW2] offset1:1
  ds_read2st64_b32 v[AC+12:AC+13], v[RW3] offset1:1
  s_waitcnt lgkmcnt(0)
  s_branch LQ_p\R
LQ_ra\R:
  RETRY 2
  MEMQ
  s_add_i32 s59, s59, LXC
  TGET RES
  s_cmp_eq_u32 s77, WZ
  s_cbranch_scc1 7f
  MEMQ
7:
  ZA
  s_waitcnt vmcnt(0)
  s_branch LQ_a\R
.endm
  s_mov_b32 s64, 0
  s_mov_b32 s65, 1
  s_mov_b32 s66, 0
  s_mov_b32 s67, 0x8000
  s_mov_b32 s68, 0x10000
  s_mov_b32 s69, 0
  s_mov_b32 s70, 0x8000
  s_mov_b32 s72, 0
  s_setprio 3
  s_cmp_eq_u32 s60, 2
  s_cbranch_scc1 LQ_init2
  s_cmp_eq_u32 s60, 3
  s_cbranch_scc1 LQ_init3
; ---- waves 0,1: scores against the x rows
  PHASEA 0
  LD MB, 178, s66, 0
  LD MB+4, 178, s66, 1024
  XSL s64
  v_add_u32 v[FA], s75, v[179]
  ds_read_b128 v[MB+8:MB+11], v[FA]
  ds_read_b128 v[MB+12:MB+15], v[FA] offset:1024
  ds_read_b128 v[AC+8:AC+11], v[FA] offset:2048
  ds_read_b128 v[AC+12:AC+15], v[FA] offset:3072
  s_waitcnt lgkmcnt(0)
  ALN AL2
LQ_b0:
  s_waitcnt vmcnt(1)
  v_mfma_f32_16x16x32_bf16 v[AC+4:AC+7], v[MB+12:MB+15], v[MB:MB+3], 0
  MX0 MB
  v_mfma_f32_16x16x32_bf16 v[AC:AC+3], v[MB+8:MB+11], v[MB:MB+3], 0
  s_waitcnt vmcnt(0)
  v_mfma_f32_16x16x32_bf16 v[AC+4:AC+7], v[AC+12:AC+15], v[MB+4:MB+7], v[AC+4:AC+7]
  MX1 MB+4
  v_mfma_f32_16x16x32_bf16 v[AC:AC+3], v[AC+8:AC+11], v[MB+4:MB+7], v[AC:AC+3]
  v_cmp_eq_u32 vcc, -1, v[MX]
  s_nop 5
  s_cbranch_vccnz LQ_rb0
.irp e,0,1,2,3
  v_add_f32_dpp v[AC+\e], v[AC+4+\e], v[AC+\e] quad_perm:[2,3,0,1] row_mask:0xf bank_mask:0xf
.endr
  DM 0
  ds_write_b128 v[180], v[AC:AC+3]
  DM 1
  ds_write_b32 v[FW], v[TAG] offset:2048
  DM 2
  s_barrier
  STAMP 5
  s_setprio PLO
  HT HP, VB
.if XS1
  s_sleep XS1
.endif
  XCHK 0
  s_nop 0
  XL4 181
.if XC == 4
  s_nop 0
  XL4 182
.endif
  XREF 0
.if XS2
  s_sleep XS2
.endif
  XADV
  v_mov_b32 v[FA], s62
  ds_read_b32 v[RA], v[FA] offset:8
  s_waitcnt lgkmcnt(0)
  v_readfirstlane_b32 s59, v[RA]
  NEXT MB
  s_setprio 3
  s_branch LQ_loop0
  COLDA 0
LQ_rb0:
  RETRY 0
  LD MB, 178, s66, 0
  LD MB+4, 178, s66, 1024
  s_branch LQ_b0
  XWAIT 0
; ---- wave 2: scores against the memory rows; attention of 16 x rows (64 columns)
.set AQ,181
.set AVF,182
.set AVM,183
.set RA,184
.set QX,196
.set OA,212
.set MSK,228
.set LX,237
.set PB,238
.set S1,240
.set K7,244
.set P4,244
.macro MF d, a, b, c
  v_mfma_f32_16x16x32_bf16 v[\d:\d+3], v[\a:\a+3], v[\b:\b+3], \c
.endm
.macro XQ op, r
  v_mov_b32 v[FB], v[\r]
  s_nop 1
  v_permlane16_swap_b32 v[\r], v[FB]
  \op v[\r], v[\r], v[FB]
  v_mov_b32 v[FB], v[\r]
  s_nop 1
  v_permlane32_swap_b32 v[\r], v[FB]
  \op v[\r], v[\r], v[FB]
.endm
.macro QKQ
  LD MB, 178, s66, 0
  LD MB+4, 179, s66, 0
  LD MB+8, 178, s66, 1024
  LD MB+12, 179, s66, 1024
.endm
.macro SOFTX
  v_add_f32 v[AC+6], v[AC+4], v[S1+2]
  v_add_f32 v[AC+7], v[AC+5], v[S1+3]
  v_max3_f32 v[MX], v[LX], v[AC+6], v[AC+7]
  XQ v_max_f32, MX
  v_mul_f32 v[MX], s74, v[MX]
.irp i,6,7,8,9,10,11,12,13,14,15
  v_fma_f32 v[AC+\i], v[AC+\i], s73, v[MX]
.endr
.irp i,6,7,8,9,10,11,12,13,14,15
  v_exp_f32 v[AC+\i], v[AC+\i]
.endr
  v_add_f32 v[LX], v[AC+6], v[AC+7]
.irp i,8,9,10,11,12,13,14,15
  v_add_f32 v[LX], v[LX], v[AC+\i]
.endr
  XQ v_add_f32, LX
  v_rcp_f32 v[LX], v[LX]
  s_nop 0
.irp i,6,7,8,9,10,11,12,13,14,15
  v_mul_f32 v[AC+\i], v[AC+\i], v[LX]
.endr
.irp i,0,1,2,3
  v_cvt_pk_bf16_f32 v[P4+\i], v[AC+8+2*\i], v[AC+9+2*\i]
.endr
  v_cvt_pk_bf16_f32 v[PB], v[AC+6], v[AC+7]
.endm
LQ_init2:
  s_mov_b32 s73, 0x3e0293ee
  s_mov_b32 s74, 0xbe0293ee
  PHASEA 2
  QKQ
  XSL s64
  ds_read_b128 v[MSK:MSK+3], v[XA]
  ds_read_b128 v[MSK+4:MSK+7], v[XA] offset:1024
  v_add_u32 v[FB], s75, v[AQ]
  v_add_u32 v[RES], s75, v[AVF]
  v_mov_b32 v[PB+1], 0
.irp i,0,1,2,3
  ds_read_b128 v[QX+4*\i:QX+4*\i+3], v[FB] offset:16384+1024*\i
  ds_read_b128 v[OA+4*\i:OA+4*\i+3], v[FB] offset:1024*\i
.endr
.irp i,0,1,2
  ds_read_b128 v[RA+4*\i:RA+4*\i+3], v[FB] offset:4096+1024*\i
.endr
  ds_read_b128 v[K7:K7+3], v[FB] offset:7168
  s_waitcnt lgkmcnt(10)
  MF AC+8, OA, QX, v[MSK:MSK+3]
.irp i,4,8,12
  s_waitcnt lgkmcnt(10-\i/2)
  MF AC+8, OA+\i, QX+\i, v[AC+8:AC+11]
.endr
  s_waitcnt lgkmcnt(0)
  MF AC+12, RA, QX, v[MSK+4:MSK+7]
  MF AC+12, RA+4, QX+4, v[AC+12:AC+15]
  MF AC+12, RA+8, QX+8, v[AC+12:AC+15]
  MF AC+12, K7, QX+12, v[AC+12:AC+15]
  v_max3_f32 v[LX], v[AC+8], v[AC+9], v[AC+10]
  s_nop 6
  v_max3_f32 v[LX], v[LX], v[AC+11], v[AC+12]
  v_max3_f32 v[LX], v[LX], v[AC+13], v[AC+14]
  v_max_f32 v[LX], v[LX], v[AC+15]
  STAMP 10
  ALN AL3
LQ_b2:
  s_waitcnt vmcnt(3)
  MX0 MB
  s_waitcnt vmcnt(2)
  v_mfma_f32_16x16x32_bf16 v[AC:AC+3], v[MB+4:MB+7], v[MB:MB+3], 0
  MX1 MB+4
  s_waitcnt vmcnt(1)
  MX1 MB+8
  s_waitcnt vmcnt(0)
  v_mfma_f32_16x16x32_bf16 v[AC:AC+3], v[MB+12:MB+15], v[MB+8:MB+11], v[AC:AC+3]
  MX1 MB+12
  v_cmp_eq_u32 vcc, -1, v[MX]
  s_nop 3
  s_cbranch_vccnz LQ_rb2
  v_add_f32_dpp v[AC], v[AC+2], v[AC] quad_perm:[2,3,0,1] row_mask:0xf bank_mask:0xf
  v_add_f32_dpp v[AC+1], v[AC+3], v[AC+1] quad_perm:[2,3,0,1] row_mask:0xf bank_mask:0xf
  DM 3
  ds_write_b64 v[180], v[AC:AC+1]
  DM 4
  ds_write_b32 v[FW], v[TAG] offset:2048
  DM 5
  s_barrier
  STAMP 5
  s_setprio 0
  MF AC+4, MB+4, QX, 0
  MF S1, MB+4, QX+4, 0
  MF AC+4, MB+12, QX+8, v[AC+4:AC+7]
  MF S1, MB+12, QX+12, v[S1:S1+3]
  FLAGQ s64
.irp e,0,1,2,3
  ds_read_b64_tr_b16 v[MB+4*\e:MB+4*\e+1], v[RES] offset:512*\e
  ds_read_b64_tr_b16 v[MB+4*\e+2:MB+4*\e+3], v[RES] offset:512*\e+4096
.endr
.if )" STR(AEARLY) R"(
  SOFTX
.endif
  v_mov_b32 v[FA], s62
  ds_read_b32 v[FB], v[FA] offset:8
  s_mov_b32 s76, 0x400000
LA_w:
  ds_read_b32 v[FA], v[FW] offset:2304
  CAP
  s_waitcnt lgkmcnt(0)
  v_cmp_gt_u32 vcc, s65, v[FA]
  s_cbranch_vccz LA_g
  s_sleep 1
  s_branch LA_w
LA_g:
  STAMP 8
  v_readfirstlane_b32 s59, v[FB]
  s_setprio 2
.irp e,0,1,2,3
  ds_read_b64_tr_b16 v[RA+2*\e:RA+2*\e+1], v[AVM] offset:256*\e
.endr
.if !)" STR(AEARLY) R"(
  SOFTX
  s_nop 0
.endif
.irp e,0,4,8,12
  MF OA+\e, MB+\e, P4, 0
.endr
  s_waitcnt lgkmcnt(0)
.irp e,0,1,2,3
  v_mfma_f32_16x16x16_bf16 v[OA+4*\e:OA+4*\e+3], v[RA+2*\e:RA+2*\e+1], v[PB:PB+1], v[OA+4*\e:OA+4*\e+3]
.endr
  s_nop 1
.irp i,0,1,2,3,4,5,6,7
  v_cvt_pk_bf16_f32 v[KX+\i], v[OA+2*\i], v[OA+2*\i+1]
.endr
.irp i,0,1,4,5
  v_permlane16_swap_b32 v[KX+\i], v[KX+\i+2]
.endr
  STAMP 9
  s_setprio 3
  NEXT MB
  s_cmp_lg_u32 s64, 64
  s_cbranch_scc1 LQ_loop2
  OXST
  s_waitcnt vmcnt(0)
  FLAGQ 64
  s_branch L_end
  COLDA 2
.if EF
LQ_ef:
  s_waitcnt vmcnt(1)
  FLAGQ s64
  s_branch LQ_eg
.endif
LQ_rb2:
  RETRY 0
  QKQ
  s_branch LQ_b2
; ---- wave 3: pacing, clearing, sum over the heads
.set CK,144
.set RA,162
.set RC,184
.macro PARTQ
  LD MB, 178, s66, 0
  LD MB+4, 178, s66, 1024
  LD MB+8, 178, s66, 2048
  LD MB+12, 178, s66, 3072
.endm
LQ_init3:
  s_memrealtime vcc
  s_mov_b64 s[44:45], 0
  s_mov_b32 s46, s62
  s_mov_b32 s79, NS*GS
  s_mov_b32 s62, s101
  s_waitcnt lgkmcnt(0)
  s_lshl_b32 s73, vcc_lo, 4
  PHASEA 3
  s_setprio PLO
  v_mov_b32 v[MB+4], s46
  s_memtime s[96:97]
  s_memrealtime s[94:95]
  ds_read_b128 v[VA:VA+3], v[XA] offset:8192
  v_mov_b64 v[MB:MB+1], -1
  v_mov_b64 v[MB+2:MB+3], -1
  v_rcp_f32 v[CK+6], s45
  v_mov_b64 v[CK:CK+1], s[44:45]
  v_cvt_f32_u32 v[CK+4], s57
  v_mul_f32 v[CK+6], s44, v[CK+6]
  s_waitcnt lgkmcnt(0)
  s_sub_u32 s76, s96, s90
  s_sub_u32 s77, s94, s91
  s_mov_b32 s90, s96
  s_mov_b32 s91, s94
  v_mul_f32 v[CK+6], 256.0, v[CK+6]
  v_cvt_f32_u32 v[CK+2], s76
  v_cvt_f32_u32 v[CK+3], s77
  v_cvt_i32_f32 v[CK+6], v[CK+6]
  v_fmac_f32 v[CK+2], )" STR(CKA) R"(, v[CK]
  v_fmac_f32 v[CK+3], )" STR(CKA) R"(, v[CK+1]
  v_rcp_f32 v[CK+5], v[CK+2]
  v_readfirstlane_b32 s51, v[CK+6]
  v_readfirstlane_b32 s44, v[CK+2]
  v_readfirstlane_b32 s45, v[CK+3]
  v_mul_f32 v[CK+3], )" STR(CKF) R"(, v[CK+3]
  v_fma_f32 v[CK+3], v[CK+3], v[CK+5], -1.0
  v_med3_f32 v[CK+3], v[CK+3], -0.5, 1.0
  v_mul_f32 v[CK+3], v[CK+3], v[CK+4]
  v_cvt_i32_f32 v[CK+3], v[CK+3]
  STAMP 7
  s_nop 0
  v_readfirstlane_b32 s47, v[CK+3]
  WAITK VD+CD, NQ
  buffer_store_dwordx4 v[MB:MB+3], v[179], s[40:43], s67 offen sc1
  buffer_store_dwordx2 v[MB:MB+1], v[182], s[40:43], s67 offen sc1
  buffer_store_dwordx2 v[MB:MB+1], v[180], s[40:43], s70 offen sc1
  buffer_store_dwordx2 v[MB:MB+1], v[181], s[40:43], s70 offen sc1
  ds_read_b128 v[RC:RC+3], v[MB+4]
  ds_read_b32 v[RA], v[MB+4] offset:24
  XCHK 3
  XREF 3
  STAMP 5
  s_barrier
  s_memrealtime s[94:95]
  s_mov_b32 s76, 0x400000
  s_mov_b32 s78, s73
LQ_t3:
  s_waitcnt lgkmcnt(0)
  v_cmp_gt_u32 vcc, s65, v[RC+3]
  s_cbranch_vccnz LQ_t4
  v_readfirstlane_b32 s73, v[RC]
  v_readfirstlane_b32 s74, v[RC+1]
  v_readfirstlane_b32 s59, v[RC+2]
  v_readfirstlane_b32 s92, v[RA]
  s_sub_i32 s78, s73, s78
  s_cmp_gt_i32 s78, 512
  s_cbranch_scc1 LQ_slip
LQ_b5:
  s_lshl1_add_u32 s4, s74, s57
  s_add_i32 s50, s4, s73
  PLAN
  s_sub_i32 s5, s73, s50
  s_mul_i32 s4, s4, s51
  s_mul_i32 s5, s5, s51
  s_lshr_b32 s4, s4, 12
  s_lshr_b32 s5, s5, 12
  s_add_i32 s4, s4, s92
  s_sub_i32 s4, s4, E2
  s_sub_i32 s5, s5, E3
  s_cmp_lt_u32 s64, NS
  s_cselect_b32 s78, GS, 0
  s_sub_u32 s79, s79, s78
  s_ashr_i32 s47, s47, 1
  s_add_i32 s47, s47, s79
.if CSL
  s_sleep CSL
.endif
  s_lshl4_add_u32 s78, s94, s62
  s_sub_i32 s78, s78, s50
  s_cmp_gt_i32 s78, LT
  s_cselect_b32 s78, s78, -GM
  s_add_i32 s78, s78, s50
  s_add_i32 s78, s78, GM
  s_setprio 3
  WAITP s78
  STAMP 8
  PARTQ
  s_memrealtime vcc
  s_memtime s[6:7]
  v_mov_b32 v[RA], s46
  XL4 XA
  XADV
  ZR AC, 8
  v_add_u32 v[TAG], 1, v[TAG]
  s_waitcnt lgkmcnt(0)
  s_add_i32 s6, s6, s5
  v_mov_b32 v[RA+1], s6
  ds_write_b32 v[RA], v[RA+1] offset:20
LQ_b3:
  s_waitcnt lgkmcnt(0)
.if BENCH
  s_mov_b32 m0, s64
  s_mov_b32 s77, vcc_lo
  v_writelane_b32 v[FW], s77, m0
.endif
  s_lshl_b32 s89, vcc_lo, 4
  s_sub_i32 s89, s89, s50
  s_add_i32 s78, s89, )" STR(SLIP_ADD) R"(
  s_sub_i32 s77, s89, E0
  s_max_i32 s77, s77, 0
  s_cmp_gt_i32 s89, )" STR(SLIP_THR) R"(
  s_cselect_b32 s78, s78, s77
  s_cselect_b32 s77, SGAP, 0
  s_add_i32 s78, s78, s73
  s_and_b32 s78, s78, -2
  s_add_i32 s77, s77, s47
  v_mov_b32 v[RA], s78
  v_mov_b32 v[RA+1], s77
  ALN AL4
  s_waitcnt vmcnt(3+XC)
  v_mfma_f32_16x16x32_bf16 v[RC:RC+3], v[VA:VA+3], v[MB:MB+3], 0
  MX0 MB
.irp i,1,2,3
  s_waitcnt vmcnt(3+XC-\i)
  v_mfma_f32_16x16x32_bf16 v[RC:RC+3], v[VA:VA+3], v[MB+4*\i:MB+4*\i+3], v[RC:RC+3]
  MX1 MB+4*\i
.endr
  v_cmp_eq_u32 vcc, -1, v[MX]
  STAMP 9
  s_nop 3
.irp e,0,1,2,3
  v_add_f32_dpp v[RC+\e], v[RC+\e], v[RC+\e] row_ror:8 row_mask:0xf bank_mask:0xf
.endr
  v_cvt_pk_bf16_f32 v[RC], v[RC], v[RC+1]
  v_cvt_pk_bf16_f32 v[RC+1], v[RC+2], v[RC+3]
  s_cbranch_vccnz LQ_rb3
  buffer_store_dwordx2 v[RC:RC+1], v[180], s[40:43], s69 offen sc1
  buffer_store_dwordx2 v[RA:RA+1], v[181], s[40:43], s69 offen sc1
  STAMP 10
  ZR VA, 2
  ZR VB, 2
  NEXT MB
  STAMP 0
  s_cmp_gt_i32 s89, )" STR(TG_THR) R"(
  s_cbranch_scc0 LQ_bar3
  TGET RA
  s_branch LQ_bar3
  COLDA 3
  XWAIT 3
LQ_rb3:
  RETRY 1
  PARTQ
  s_memrealtime vcc
  s_waitcnt vmcnt(0)
  s_branch LQ_b3
LQ_t4:
  ds_read_b128 v[RC:RC+3], v[MB+4]
  CAP
  s_branch LQ_t3
LQ_slip:
.if BENCH
  s_bitset1_b64 s[86:87], s64
.endif
  s_cmp_lt_u32 s64, NS
  s_cbranch_scc1 LQ_b5
  s_add_i32 s79, s79, )" STR(SLIP_INC) R"(
  s_min_u32 s79, s79, 4000
  s_branch LQ_b5
; ======== waves 4..7
.set SMK, SMV
.set UB,128
.set B1,160
.set O3,168
.set O1,169
.set OT,170
.set ST1,171
.set ST2,172
.set AIM,173
.set RR,175
.set FW,176
.set FR,177
.set FS,178
.set ASC,179
.set ASM,180
.set AVX,181
.set SIX,182
.set RES,184
.set TAG,185
.set MX,186
.set TM,187
.set MB,188
.set AC,204
.set TV,212
.set FA,214
.set RA,216
.set DD,188
.set SX,208
.set SMM,212
.set ZZ,224
.set VM,240
.set PA,248
.set VX,188
.set UU,220
.macro SCV
  ds_read_b32 v[FA], v[FS]
  ds_read_b128 v[SX:SX+3], v[ASC]
  ds_read_b64 v[SMM:SMM+1], v[ASM]
.endm
.macro CZV t
  v_cvt_pk_bf16_f32 v[UB+8*\t+2], v[ZZ+4*\t], v[ZZ+4*\t+2]
  v_cvt_pk_bf16_f32 v[UB+8*\t+3], v[ZZ+4*\t+1], v[ZZ+4*\t+3]
.endm
.macro URD h, r
.irp k,0,1,2,3
  ds_read_b128 v[VX+\r+4*\k:VX+\r+4*\k+3], v[TM] offset:1024*(\h+\k)
.endr
.endm
.macro UMM h, r, n
.irp k,0,1,2,3
  s_waitcnt lgkmcnt(\n+3-\k)
.irp t,0,1,2,3
.if \k == 0
  v_mfma_f32_16x16x32_bf16 v[UU+\r+4*\t:UU+\r+4*\t+3], v[VX+\r:VX+\r+3], v[16*\t:16*\t+3], 0
.else
  v_mfma_f32_16x16x32_bf16 v[UU+\r+4*\t:UU+\r+4*\t+3], v[VX+\r+4*\k:VX+\r+4*\k+3], v[16*\t+4*\k:16*\t+4*\k+3], v[UU+\r+4*\t:UU+\r+4*\t+3]
.endif
.endr
.endr
  s_nop 1
.irp t,0,1,2,3
  v_cvt_pk_bf16_f32 v[UB+8*\t+\h], v[UU+\r+4*\t], v[UU+\r+4*\t+1]
  v_cvt_pk_bf16_f32 v[UB+8*\t+\h+1], v[UU+\r+4*\t+2], v[UU+\r+4*\t+3]
.endr
.endm
.macro UREST
.if US
  UMM 4, 0, 0
.endif
  ZR ZZ, 8
  ZR DD, 10
  v_mov_b32 v[TAG], s65
.endm
.macro STEPV R
LV_loop\R:
  STAMP 0
.if US
  URD 4, 0
.if \R == 3
  ds_read_b32 v[MX], v[183] offset:20
.endif
  s_waitcnt lgkmcnt(0)
.endif
.if \R == 3
  v_readfirstlane_b32 s4, v[MX]
  s_setprio 3
  WAITP s73
  s_memtime s[6:7]
.endif
  s_barrier
  s_memtime s[92:93]
  s_setprio 1
  s_nop )" STR(VDEL1) R"(
  STAMP 1
  UREST
.if \R == 3
  v_mov_b32 v[TV+2], s6
  buffer_load_dwordx2 v[TV:TV+1], v[OT], s[40:43], s68 offen sc1
  ds_write_b32 v[183], v[TV+2] offset:24
.endif
.if \R == 2
  v_mov_b64 v[PA:PA+1], -1
  v_mov_b64 v[PA+2:PA+3], -1
.rept CV
  s_nop 3
.endr
.irp o,0,1024,2048,3072
  buffer_store_dwordx4 v[PA:PA+3], v[ST1], s[40:43], s67 offen offset:\o sc1
.endr
.endif
  s_mov_b32 s76, 0x400000
  s_setprio 2
  STAMP 9
  ALN AL6
  s_barrier
  s_memtime s[6:7]
  STAMP 10
.if \R == 3
  s_cmp_eq_u32 s64, 0
  s_cbranch_scc1 LV_t0
LV_t1:
  s_waitcnt vmcnt(0)
  v_cmp_eq_u32 vcc, -1, v[TV]
  v_subrev_u32 v[TV], s73, v[TV]
  v_max_i32_dpp v[TV+1], v[TV+1], v[TV+1] quad_perm:[1,0,3,2] row_mask:0xf bank_mask:0xf
  s_cbranch_vccnz LV_rt
  v_max_i32_dpp v[TV], v[TV], v[TV] quad_perm:[1,0,3,2] row_mask:0xf bank_mask:0xf
  v_max_i32_dpp v[TV+1], v[TV+1], v[TV+1] quad_perm:[2,3,0,1] row_mask:0xf bank_mask:0xf
  s_nop 0
  v_max_i32_dpp v[TV], v[TV], v[TV] quad_perm:[2,3,0,1] row_mask:0xf bank_mask:0xf
  s_nop 0
  v_readfirstlane_b32 s75, v[TV]
  v_add_u32 v[TV], s73, v[TV]
  v_readfirstlane_b32 s74, v[TV+1]
  s_add_i32 s73, s73, s75
  s_cmp_gt_i32 s75, 512
  s_cselect_b32 s75, )" STR(SLIP_INC) R"(*45/32, 0
  s_cmp_lt_u32 s64, NS
  s_cselect_b32 s75, -(GS*45/32), s75
  s_add_i32 s75, s75, s59
  v_mov_b32 v[TV+2], s75
  v_mov_b32 v[TV+3], s65
  ds_write_b128 v[183], v[TV:TV+3]
  PLAN
.endif
.if \R == 2
  HT HB, FA
.endif
.if \R == 0
.rept VN3
  s_nop 1
.endr
LV_p:
  ds_read_b32 v[FA], v[FR]
.irp k,0,1,2,3
  ds_read2st64_b32 v[RA+2*\k:RA+2*\k+1], v[RR-(\k&1)] offset0:8*\k offset1:8*\k+1
.endr
  s_waitcnt lgkmcnt(4)
  v_cmp_gt_u32 vcc, s65, v[FA]
  s_waitcnt lgkmcnt(2)
  v_add_f32 v[RA], v[RA], v[RA+2]
  v_add_f32 v[RA+1], v[RA+1], v[RA+3]
  s_waitcnt lgkmcnt(0)
  v_add_f32 v[RA+4], v[RA+4], v[RA+6]
  v_add_f32 v[RA+5], v[RA+5], v[RA+7]
  v_add_f32 v[RA], v[RA], v[RA+4]
  v_add_f32 v[RA+1], v[RA+1], v[RA+5]
  v_cvt_pk_bf16_f32 v[RES], v[RA], v[RA+1]
  s_cbranch_vccnz LV_pb
  H1ST
.endif
  STAMP 3
.if \R != 2
  s_cmp_eq_u32 s64, 63
  s_cbranch_scc1 L_last
.endif
  s_waitcnt lgkmcnt(0)
.if \R == 2
  s_waitcnt vmcnt(0)
.endif
  s_sub_i32 s77, s6, s92
  s_cmp_gt_i32 s77, NV+200
  s_cselect_b32 s77, LXC, 0
  s_add_i32 s6, s6, s77
  s_setprio 3
  WAITK VD, NV
  STAMP 4
  LD VM, O1, s66, 0
  LD VM+4, O1, s66, 1024
  s_mov_b32 s76, 0x400000
.if \R == 2
  s_cmp_eq_u32 s64, 63
  s_cbranch_scc1 LV_lst
.endif
  ALN AL5
  s_barrier
  SCV
  STAMP 5
.if VR == 0
  s_waitcnt vmcnt(1)
  SM ZZ, VM, 0
  v_max_u32 v[RES], v[VM], v[VM+1]
  v_max3_u32 v[RES], v[RES], v[VM+2], v[VM+3]
  SM ZZ+4, VM, 16
  SM ZZ+8, VM, 32
  SM ZZ+12, VM, 48
LV_s\R:
  s_waitcnt lgkmcnt(0)
  STAMP 6
  v_cmp_gt_u32 vcc, s65, v[FA]
  v_max3_f32 v[MX], v[SX], v[SX+1], v[SX+2]
  v_max3_f32 v[TM], v[SX+3], v[SMM], v[SMM+1]
  v_max_f32 v[MX], v[MX], v[TM]
  s_cbranch_vccnz LV_rs\R
  s_nop 0
  v_max_f32_dpp v[MX], v[MX], v[MX] quad_perm:[2,3,0,1] row_mask:0xf bank_mask:0xf
  v_mov_b32 v[TM], v[MX]
  s_nop 1
  v_permlane16_swap_b32 v[MX], v[TM]
  v_max_f32 v[MX], v[MX], v[TM]
  v_mov_b32 v[TM], v[MX]
  s_waitcnt vmcnt(0)
  SM ZZ, VM+4, 8
  v_permlane32_swap_b32 v[MX], v[TM]
  v_max_f32 v[MX], v[MX], v[TM]
  v_mul_f32 v[MX], s79, v[MX]
  SM ZZ+4, VM+4, 24
  v_fma_f32 v[SX], v[SX], s78, v[MX]
  v_fma_f32 v[SX+1], v[SX+1], s78, v[MX]
  v_fma_f32 v[SMM], v[SMM], s78, v[MX]
  SM ZZ+8, VM+4, 40
  v_fma_f32 v[SMM+1], v[SMM+1], s78, v[MX]
  v_fma_f32 v[SX+2], v[SX+2], s78, v[MX]
  v_fma_f32 v[SX+3], v[SX+3], s78, v[MX]
  SM ZZ+12, VM+4, 56
  v_exp_f32 v[SX], v[SX]
  v_exp_f32 v[SX+1], v[SX+1]
  v_exp_f32 v[SMM], v[SMM]
  v_cvt_pk_bf16_f32 v[PA], v[SX], v[SX+1]
  v_exp_f32 v[SMM+1], v[SMM+1]
  v_cvt_pk_bf16_f32 v[PA+1], v[SMM], v[SMM]
  v_exp_f32 v[SX+2], v[SX+2]
  v_exp_f32 v[SX+3], v[SX+3]
  v_cvt_pk_bf16_f32 v[PA+3], v[SMM+1], v[SMM+1]
  v_cvt_pk_bf16_f32 v[PA+2], v[SX+2], v[SX+3]
.else
.rept DWM
  v_mfma_f32_16x16x32_bf16 v[PA:PA+3], v[B1:B1+3], v[B1:B1+3], 0
.endr
LV_s\R:
  s_waitcnt lgkmcnt(0)
  STAMP 6
  v_cmp_gt_u32 vcc, s65, v[FA]
  v_max3_f32 v[MX], v[SX], v[SX+1], v[SX+2]
  v_max3_f32 v[TM], v[SX+3], v[SMM], v[SMM+1]
  v_max_f32 v[MX], v[MX], v[TM]
  s_cbranch_vccnz LV_rs\R
.if VR == 2
  s_waitcnt vmcnt(1)
  SM ZZ, VM, 0
.else
  s_nop 0
.endif
  v_max_f32_dpp v[MX], v[MX], v[MX] quad_perm:[2,3,0,1] row_mask:0xf bank_mask:0xf
  v_mov_b32 v[TM], v[MX]
.if VR == 2
  SM ZZ+4, VM, 16
  s_nop 0
.else
  s_waitcnt vmcnt(1)
  SM ZZ, VM, 0
.endif
  v_permlane16_swap_b32 v[MX], v[TM]
  v_max_f32 v[MX], v[MX], v[TM]
  v_mov_b32 v[TM], v[MX]
  v_max_u32 v[RES], v[VM], v[VM+1]
.if VR == 2
  SM ZZ+8, VM, 32
.else
  SM ZZ+4, VM, 16
.endif
  v_permlane32_swap_b32 v[MX], v[TM]
  v_max_f32 v[MX], v[MX], v[TM]
  v_mul_f32 v[MX], s79, v[MX]
.if VR == 2
  SM ZZ+12, VM, 48
.else
  SM ZZ+8, VM, 32
.endif
  v_fma_f32 v[SX], v[SX], s78, v[MX]
  v_fma_f32 v[SX+1], v[SX+1], s78, v[MX]
  v_fma_f32 v[SMM], v[SMM], s78, v[MX]
.if VR == 2
  s_waitcnt vmcnt(0)
  SM ZZ, VM+4, 8
.else
  SM ZZ+12, VM, 48
.endif
  v_fma_f32 v[SMM+1], v[SMM+1], s78, v[MX]
  v_fma_f32 v[SX+2], v[SX+2], s78, v[MX]
  v_fma_f32 v[SX+3], v[SX+3], s78, v[MX]
.if VR == 2
  SM ZZ+4, VM+4, 24
.else
  s_waitcnt vmcnt(0)
  SM ZZ, VM+4, 8
.endif
  v_exp_f32 v[SX], v[SX]
.if VR == 2
  SM ZZ+8, VM+4, 40
.else
  SM ZZ+4, VM+4, 24
.endif
  v_exp_f32 v[SX+1], v[SX+1]
.if VR == 2
  SM ZZ+12, VM+4, 56
.else
  SM ZZ+8, VM+4, 40
.endif
  v_exp_f32 v[SMM], v[SMM]
.if VR == 1
  SM ZZ+12, VM+4, 56
.endif
  v_cvt_pk_bf16_f32 v[PA], v[SX], v[SX+1]
  v_exp_f32 v[SMM+1], v[SMM+1]
  v_cvt_pk_bf16_f32 v[PA+1], v[SMM], v[SMM]
  v_exp_f32 v[SX+2], v[SX+2]
  v_exp_f32 v[SX+3], v[SX+3]
  v_cvt_pk_bf16_f32 v[PA+3], v[SMM+1], v[SMM+1]
  v_cvt_pk_bf16_f32 v[PA+2], v[SX+2], v[SX+3]
  v_max3_u32 v[RES], v[RES], v[VM+2], v[VM+3]
.endif
LV_z\R:
  CZV 0
  SM DD+16, PA, B1
  CZV 1
  SM DD, PA, UB
  CZV 2
  v_max3_u32 v[RES], v[RES], v[VM+4], v[VM+5]
  SM DD+4, PA, UB+8
  CZV 3
  v_max3_u32 v[RES], v[RES], v[VM+6], v[VM+7]
  SM DD+8, PA, UB+16
  v_add_f32 v[DD+16], v[DD+16], v[DD+18]
  v_add_f32 v[DD+17], v[DD+17], v[DD+19]
  v_cmp_eq_u32 vcc, -1, v[RES]
  SM DD+12, PA, UB+24
  v_rcp_f32 v[DD+16], v[DD+16]
  v_rcp_f32 v[DD+17], v[DD+17]
  s_cbranch_vccnz LV_rb\R
  HS DD
  HS DD+4
  HS DD+8
  v_pk_mul_f32 v[DD:DD+1], v[DD:DD+1], v[DD+16:DD+17]
  v_pk_mul_f32 v[DD+4:DD+5], v[DD+4:DD+5], v[DD+16:DD+17]
  HS DD+12
  v_pk_mul_f32 v[DD+8:DD+9], v[DD+8:DD+9], v[DD+16:DD+17]
  v_cvt_pk_bf16_f32 v[SX], v[DD], v[DD+4]
  v_pk_mul_f32 v[DD+12:DD+13], v[DD+12:DD+13], v[DD+16:DD+17]
  v_cvt_pk_bf16_f32 v[SX+2], v[DD+1], v[DD+5]
  v_cvt_pk_bf16_f32 v[SX+1], v[DD+8], v[DD+12]
  v_cvt_pk_bf16_f32 v[SX+3], v[DD+9], v[DD+13]
  buffer_store_dwordx4 v[SX:SX+3], v[ST2], s[40:43], s66 offen sc1
  STAMP 7
  s_setprio 1
.if \R == 2
LV_img:
  v_add_u32 v[TM], 1024, v[AIM]
  ds_write2_b32 v[AIM], v[VM], v[VM+1] offset1:2
  ds_write2_b32 v[AIM], v[VM+2], v[VM+3] offset0:32 offset1:34
  ds_write2_b32 v[TM], v[VM+4], v[VM+5] offset1:2
  ds_write2_b32 v[TM], v[VM+6], v[VM+7] offset0:32 offset1:34
  ds_write_b32 v[FW], v[TAG] offset:2304
  s_cmp_eq_u32 s64, 63
  s_cbranch_scc1 L_end
.endif
LV_u\R:
  XSL s65
  v_add_u32 v[TM], s75, v[AVX]
  ds_read_b32 v[MX], v[183] offset:8
  URD 0, 0
.if US
  UMM 0, 0, 0
.else
  URD 4, 16
  UMM 0, 0, 4
  UMM 4, 16, 0
.endif
  v_readfirstlane_b32 s59, v[MX]
  STAMP 8
  NEXT MB
  s_branch LV_loop\R
.if \R == 0
LV_pb:
.if BENCH
  s_bitset1_b64 s[86:87], s64
.endif
  CAP
  s_branch LV_p
.endif
LV_rb\R:
  RETRY 0
  LD VM, O1, s66, 0
  LD VM+4, O1, s66, 1024
  ZR ZZ, 8
  ZR DD, 10
  s_waitcnt vmcnt(0)
.irp t,0,1,2,3
  SM ZZ+4*\t, VM, 16*\t
  s_nop 7
  SM ZZ+4*\t, VM+4, 16*\t+8
.endr
  v_max_u32 v[RES], v[VM], v[VM+1]
  v_max3_u32 v[RES], v[RES], v[VM+2], v[VM+3]
  s_nop 7
  s_branch LV_z\R
.if \R == 2
LV_lst:
  s_barrier
LV_63:
  s_waitcnt vmcnt(0)
  MX0 VM
  MX1 VM+4
  v_cmp_eq_u32 vcc, -1, v[MX]
  s_cbranch_vccz LV_img
  RETRY 0
  LD VM, O1, s66, 0
  LD VM+4, O1, s66, 1024
  s_branch LV_63
.endif
LV_rs\R:
.if BENCH
  s_bitset1_b64 s[86:87], s64
.endif
  SCV
  CAP
  s_branch LV_s\R
.if \R == 3
LV_t0:
  s_memrealtime vcc
  s_waitcnt vmcnt(0) lgkmcnt(0)
  s_lshl_b32 s73, vcc_lo, 4
  s_sub_i32 s73, s73, 0x2000
  v_mov_b32 v[TV], s73
  v_mov_b32 v[TV+1], 0
  s_branch LV_t1
LV_rt:
  RETRY 2
  buffer_load_dwordx2 v[TV:TV+1], v[OT], s[40:43], s68 offen sc1
  s_branch LV_t1
.endif
.endm
LV_init:
  s_memrealtime vcc
  PRE
  s_waitcnt lgkmcnt(0)
  s_lshl_b32 s73, vcc_lo, 4
  s_mov_b32 s78, 0x3e0293ee
  s_mov_b32 s79, 0xbe0293ee
  ZR UB, 16
  v_mov_b32 v[B1], 0x3f803f80
  v_mov_b32 v[B1+1], 0x3f803f80
  v_mov_b32 v[B1+2], 0x3f80
  v_mov_b32 v[B1+3], 0x3f80
  v_mov_b32 v[B1+4], 0x3f803f80
  v_mov_b32 v[B1+5], 0x3f803f80
  v_mov_b64 v[B1+6:B1+7], 0
  s_cmp_eq_u32 s60, 2
  s_cbranch_scc1 LV_u2
  s_cmp_eq_u32 s60, 3
  s_cbranch_scc1 LV_u3
  s_branch LV_u0
  STEPV 0
  STEPV 2
  STEPV 3
L_fatal:
  v_mov_b32 v[MX], 0
  v_mov_b32 v[TAG], 1
  global_store_dword v[MX], v[TAG], s[48:49] sc1
  s_waitcnt vmcnt(0)
  s_endpgm
L_last:
  s_barrier
L_end:
  s_waitcnt vmcnt(0) lgkmcnt(0)
)"
static_assert(__builtin_bit_cast(unsigned, SCALE * LOG2E) == 0x3e0293eeu, "");
#define CL10(p) p "0", p "1", p "2", p "3", p "4", p "5", p "6", p "7", p "8", p "9"
#define CL_ST
#define CL_S CL_ST "s4", "s5", "s6", "s7", "s72", "s73", "s74", "s75", "s76", "s77", "s78", "s79", "s88", "s89", "s90", "s91", "s92", "s93", "s94", "s95", "s96", "s97", "s98", "s99", "s100", "s101", CL10("s3"), "vcc", "scc", "memory"

__device__ __forceinline__ void chain(const Args& a, const int c, const unsigned L, const unsigned hb) {
    const int tid = threadIdx.x, w8 = RFL(tid >> 6), w = w8 & 3, l = tid & 63;
    const int g = c & 3, aa = c >> 2, h = g * 8 + (aa >> 2), hp = h ^ XH * 8, jj = aa & 3;
    const int n = l & 15, q = l >> 4, hf = (n >> 1) & 1, n2 = (n >> 2) & 1, n3 = n >> 3;
    const unsigned L_EX = L + 114688, L_SC = L + 131072, L_SM = L_SC + 2048, L_FL = L_SM + 512, L_TB = L_FL + 3072, L_VM = L_TB + 256, L_W2 = L + 143360, L_KX = L + 32768, L_VX = L_KX + 8192;
    const rsrc_t rs = mkrs(a.ring, RING_BYTES);
    unsigned* const err = a.ctl + C_ERR;
    const v4u ones = {~0u, ~0u, ~0u, ~0u};
    const unsigned cyc0 = RFL((unsigned)__builtin_amdgcn_s_memtime()), rt0 = RFL((unsigned)__builtin_amdgcn_s_memrealtime());
    if (tid < 384) lp<v2u>(L_FL)[tid] = v2u{0u, 0u};
    if (c < B_H3 || (c < B_TS + 2 && !(c & 2))) {
#pragma unroll
        for (int i = 0; i < 4; ++i) STB(ones, rs, c * 32768 + i * 8192 + tid * 16, 0); }
#pragma unroll
    for (int i = 0; i < (NSEG - SO + 3) / 4; ++i) STB(ones, mkrs(a.ox, NSEG * 262144), (SO + 4 * i + (tid >> 7)) * 262144 + (h * 4 + jj) * 2048 + (tid & 127) * 16, 0);
    const int lg3 = (c & 31) * 128 + n2 * 64 + hf * 32 + (q & 1) * 16 + (n & 1) * 8;
    if (w8 == 3) { const bf16_t* p = a.mem0 + ((c & 1) * 4 + n2 * 2 + (q & 1)) * DM + 16 * (c >> 1) + (n & 3);
        const v2u m = {min(p[0] | (unsigned)p[4] << 16, 0xFFFFFFFEu), min(p[8] | (unsigned)p[12] << 16, 0xFFFFFFFEu)};
#pragma unroll
        for (int i = 0; i < 2; ++i) __builtin_amdgcn_raw_buffer_store_b64(m, rs, (B_H3 + 4 * (c >> 5) + 2) * 32768 + ((q >> 1) * 2 + n3) * 4096 + lg3, i * 16384, 16); }
    const unsigned stg = L + w8 * 16384;
    const int rot = 0;
    const int prr = 16 * (q >> 1) + (q & 1) + 4 * n2 + 2 * n3;
    v32u W[6];
    auto trd4 = [&](int k, unsigned p, int o1, int o2) { const v2u t[4] = {trd(p), trd(p + o1), trd(p + o2), trd(p + o2 + o1)};
#pragma unroll
        for (int i = 0; i < 8; ++i) W[k >> 2][(k & 3) * 8 + i] = t[i >> 1][i & 1]; };
    unsigned cal;
    const unsigned d1c = (a.d1 * 45 >> 5) + NS * (GS * 45 / 32) - WKO;
    auto pg = [&](int blk, int x) { return blk * 32768 + (int)((((cal >> (blk >> 6) * 3) ^ ((x ^ blk >> 4) & 1) * 4 ^ ((x >> 1 ^ blk >> 5) & 1)) & 7) << 12); };
    auto h1o = [&](int inner, int x = 0) { return pg(B_H1 + (aa & 28) + (inner >> 12) * 2, g ^ x ^ XP) + (inner & 4095); };
    auto stream = [&](auto issue, auto read) {
        issue(0); issue(1);
        WAITV(8); BAR();
        unsigned h2 = hb;
        auto arrive = [&]() { gadd1(a.ctl + C_READY); if (c == 0) gst1(a.ctl + C_ACK, 1u); };
        if (w8 == 3) gld_req(h2, a.ctl + C_HB);
        read(std::integral_constant<int, 0>{}); WAITL(); issue(2);
        gld_wait<8>(h2);
        const bool seen = RFL(h2) != RFL(hb);
        if (seen) arrive();
        read(std::integral_constant<int, 1>{}); WAITL(); issue(3);
        WAITV(8); read(std::integral_constant<int, 2>{});
        WAITV(0); read(std::integral_constant<int, 3>{}); WAITL();
        if (w8 == 3 && !seen) { ctl_wait(a, hb); arrive(); }
    };
    auto start = [&]() {
        if (w8 == 3) {
            for (int i = 0; RFL(gld(a.ctl + C_READY)) < 256; ++i) { if (c == 0) gadd1(a.ctl + C_HB); __builtin_amdgcn_s_sleep(4); if (i > GCAP) fatal(err); }
            FENCE();
            const unsigned v = ~gld(a.ctl + C_CAL + (l & 15)) & 0xffffu;
            unsigned u = (2 * v + dppu<0xB1>(v) + dppu<0x1B>(dppu<HMIR>(v))) * 8 + (l & 7);
            u = min(u, dppu<0xB1>(u)); u = min(u, dppu<0x4E>(u)); u = min(u, dppu<HMIR>(u));
            unsigned t = ((__builtin_amdgcn_readlane(u, 0) & 7) | (__builtin_amdgcn_readlane(u, 8) & 7) << 3) ^ 022;
            *lp<v2u>(L_TB + 16) = v2u{t, cyc0}; *lp<v2u>(L_TB + 8) = v2u{d1c, 0u}; WAITL();
        }
        lp<unsigned>(L_EX + w8 * 2048 + 1536)[l] = 0; WAITL();
        BAR();
        cal = RFL(ldsr(L_TB + 16));
    };
    const unsigned char* xp = a.qkv + h * 24576 + w * 8192 + (w == 2 ? (jj & 1) * 4096 : 0);
    const unsigned char* xq = a.qkv + h * 24576 + 2 * NH * 24576;
    const unsigned xw = w == 3 ? 20480 - XC * 1024 : w * (XC == 4 ? 8192 : 4096);
    const unsigned* fG = a.ctl + C_G + h * 128 + w * 32;
    auto lo = [](const void* p) { return (unsigned)(u64)p; };
    auto hi = [](const void* p) { return (unsigned)((u64)p >> 32); };
    v16u S0 = {lo(a.ring), hi(a.ring), RING_BYTES, 0x20000u, lo(a.ox), hi(a.ox), NSEG * 262144, 0x20000u, lo(err), hi(err), lo(a.ctl + C_M + h * 128 + jj * 32), hi(a.ctl + C_M + h * 128 + jj * 32),
               lo(xq), hi(xq), lo(fG - w * 32), hi(fG - w * 32)};
    v16u S1 = {(unsigned)rot << 10, (unsigned)a.d2, (unsigned)a.per, d1c, (unsigned)w, L_KX + xw, L_TB, (unsigned)w8};
    v8u cnts;
    v16u A;
    const unsigned six = hf ? 0xEEEE : 0x4444;

    if (w8 < 4) {
        { const bf16_t* Wm = (jj >> 1 ? a.Wk : a.Wq) + (size_t)256 * w * HD + hp * DH + 64 * (jj & 1);
          const unsigned p = stg + prr * 128 + (((hf ^ (4 * n2 + 2 * n3)) * 16 + (n & 1) * 8) ^ ((w >> 1) * 64));
          stream([&](int i) { _Pragma("unroll 1") for (int j = 0; j < 8; ++j) gldsc(Wm + (size_t)(64 * (i ^ rot) + 8 * j) * HD, (l >> 3) * (HD * 2) + ((l & 7) ^ (2 * q)) * 16, stg + (i & 1) * 8192 + j * 1024); },
                 [&](auto i) { _Pragma("unroll") for (int t = 0; t < 4; ++t) trd4(4 * t + i, (p ^ (t * 32)) + (i & 1) * 8192, 1024, 4096); }); }
        MARK(2);
        BAR();
        { const unsigned pv = stg + 65536 + prr * 64 + (hf ^ (2 * n2)) * 16 + (n & 1) * 8;
#pragma unroll
          for (int k = 0; k < 8; ++k) trd4(16 + k, (pv ^ ((k >> 2) * 32)) + ((k & 3) ^ rot) * 4096, 512, 2048); }
        WAITL();
        BAR();
        if (w < 3) {
            flags_wait<4>(fG, 1, 2, l, err);
#pragma unroll
            for (int i = 0; i < 4; ++i) if (w < 2 || !(i & 1)) glds1<4>(xp + (i >> 1) * (NH * 24576) + (i & 1) * 4096, l * 16, L_KX + (i >> 1) * 20480 + w * 8192 + (i & 1) * 4096);
            WAITV(0);
        }
        A[5] = w == 2 ? L_W2 + l * 16 : 16384 + (jj & 1) * 4096 + l * 16;
        A[13] = w < 3 ? xw + l * 16 : 0; A[14] = xw + 4096 + l * 16; A[15] = 0;
        if (w == 2) { const int ck = 8 * (jj >> 1) + hf;
#pragma unroll
            for (int i = 0; i < 8; ++i) lp<unsigned>(A[5] + (i >> 2) * 1024)[i & 3] = 16 * (i >> 2) + 4 * q + (i & 3) > 16 * (jj & 1) + n ? 0xFF800000u : 0u;
#pragma unroll
            for (int i = 0; i < 16; ++i) *lp<v4u>(A[5] + 4096 + i * 1024) = v4u{W[4 + i / 8][i % 8 * 4], W[4 + i / 8][i % 8 * 4 + 1], W[4 + i / 8][i % 8 * 4 + 2], W[4 + i / 8][i % 8 * 4 + 3]};
            A[13] = L_KX + l * 16; A[14] = L_VX + (ck * 16 + 4 * q + (n >> 2)) * 16 + (l & 1) * 8; A[15] = L_VM + ck * 128 + (2 * q + n2) * 16 + (l & 1) * 8; }
        if (w == 3) {
#pragma unroll
            for (int i = 0; i < 4; ++i) lp<unsigned>(A[5] + 8192)[i] = ((n & 7) >> 1) == i ? 0x3F80u << (16 * (n & 1)) : 0u; }
        start();
        MARK(0); MARKC(4);
        const int sl = 2 * (jj & 1) + (w >> 1);
        A[0] = pg(B_H3 + 4 * w, g ^ (H3X)) + (l ^ (w & 1)) * 16;
        A[1] = h1o((jj >> 1) * 2048 + ((sl * 2 + n3) * 2 + (w & 1)) * 128 + (q >> 1) * 64 + n2 * 32 + (q & 1) * 16 + (n & 3) * 4, XH);
        A[2] = L_EX + (w ^ 1) * 2048 + 1024 + l * 4; A[3] = L_EX + (w ^ 2) * 2048 + l * 4; A[4] = L_EX + (w ^ 3) * 2048 + 512 + l * 4;
        A[6] = L_FL + w * 256 + l * 4; A[7] = L_EX + (l & 3) * 2048 + 1536 + l * 4; A[8] = six;
        A[9] = w == 2 ? h * 8192 + jj * 2048 + ((2 * (q & 1) + (q >> 1)) * 16 + n) * 16 : (XC == 4 ? min(w, 2) * 128 : (w == 3 ? 128 + (l & 1) * 128 : 0));
        A[10] = w < 3 ? h1o(l * 16) : pg(2 * aa, g) + l * 16;
        A[11] = w < 2 ? L_KX + w * 4096 + l * 16 : h1o((w == 2 ? 2048 : jj * 1024) + l * 16);
        A[12] = w < 2 ? L_SC + w * 1024 + ((l * 16) ^ ((q & 1) * 32)) : (w == 2 ? L_SM + ((l * 8) ^ ((q & 1) * 16)) : pg(B_H3 + 4 * (c >> 5), (q >> 1) * 2 + n3) + lg3);
        if (w == 3) { A[13] = c % 33 == 0 && l < 4 ? pg(B_TS, l) + (c >> 5) * 128 : OOB; A[14] = h1o(4096 + jj * 512 + l * 8); }
    } else {
        { const bf16_t* Wo = a.Wo + (size_t)h * DH * DM + 256 * jj + 64 * w; const bf16_t* Wv = a.Wv + (size_t)256 * w * HD + hp * DH + 32 * jj;
          const unsigned po = stg + (4 * q + n3 + 16 * n2) * 128 + (((n & 3) * 2) ^ (q & 1)) * 16;
          stream([&](int i) {
                     if (i < 2) { _Pragma("unroll 1") for (int j = 0; j < 8; ++j) (CPL ? glds : gldsc)(Wo + (size_t)(64 * i + 8 * j) * DM, (l >> 3) * (DM * 2) + ((l & 7) ^ (l >> 5)) * 16, stg + i * 8192 + j * 1024); }
                     else { _Pragma("unroll 1") for (int j = 0; j < 8; ++j) glds(Wv + (size_t)(128 * (i - 2) + 16 * j) * HD, (l >> 2) * (HD * 2) + ((l & 3) ^ (2 * (q & 1))) * 16, stg + (i & 1) * 8192 + j * 1024); } },
                 [&](auto i) { _Pragma("unroll") for (int t = 0; t < 4; ++t) if constexpr (i < 2) trd4(2 * t + i, (po ^ ((t >> 1) * 16)) + (t & 1) * 8 + i * 8192, 256, 4096); }); }
        BAR(); BAR();
        start();
        A[0] = 0; A[1] = h1o(4096 + l * 16); A[2] = pg(B_TS, g) + (l & 3) * 128;
        A[3] = w >= 2 ? pg(2 * aa, g) + l * 16 : h1o(4096 + ((jj * 2 + n3) * 2 + (w & 1)) * 128 + (q & 1) * 64 + (n & 1) * 32 + (q >> 1) * 16 + ((n >> 1) & 3) * 4, XH);
        A[4] = pg(jj * 16 + w * 4 + n3 * 2, n2 * 2 + (q >> 1)) + h * 128 + ((q & 1) * 4 + (n & 3)) * 16;
        A[5] = L_VM + q * 256 + (4 * n3 + 2 * (n & 1) + n2) * 16 + hf * 4;
        A[7] = L_EX + 8192 + (w & 1) * 512 + l * 4; A[6] = A[7] ^ 512;
        A[8] = L_FL + w * 256 + l * 4; A[9] = L_EX + 8192 + (l & 3) * 2048 + 1536 + l * 4; A[10] = L_FL + 2048 + min(l & 3, 2) * 256 + l * 4;
        A[11] = L_SC + q * 512 + hf * 256 + n * 16; A[12] = L_SM + (q & 1) * 256 + hf * 128 + n * 8;
        A[13] = L_VX + q * 256 + (8 * n3 + 4 * hf + 2 * n2 + (n & 1)) * 16; A[14] = six; A[15] = L_TB;
    }
    S1[12] = a.d2 - a.d1 - KC; S1[8] = RFL(pg(B_TS, g)); S1[9] = cyc0; S1[10] = rt0; S1[11] = RFL(L_EX - 63744 + w8 * 2048);
    asm volatile(A_CHAIN : "+{s[40:55]}"(S0), "+{s[56:71]}"(S1), "={s[80:87]}"(cnts), "+{v[168:183]}"(A), "+{v[188:219]}"(W[4]), "+{v[220:251]}"(W[5]) : "{v[0:31]}"(W[0]), "{v[32:63]}"(W[1]), "{v[64:95]}"(W[2]), "{v[96:127]}"(W[3])
                 : "v128", "v129", CL10("v13"), CL10("v14"), CL10("v15"), "v160", "v161", "v162", "v163", "v164", "v165", "v166", "v167", "v184", "v185", "v186", "v187", CL_S);
}

struct Tile { const bf16_t* W; int ld, wide, perm; };
__device__ __forceinline__ void tile_issue(const Tile& T, int t, unsigned L, int w8, int l) {
    const bf16_t* p = T.W + (T.wide ? (w8 & 3) * 32 * T.ld + (2 * t + (w8 >> 2)) * 128 : (t * 256 + w8 * 32) * T.ld);
    const unsigned vo = (T.perm ? (l >> 5) + (l & 16) : l >> 4) * T.ld * 2 + ((l & 15) ^ (2 * (l >> 4))) * 16;
#pragma unroll 1
    for (int i = 0; i < 8; ++i) { const bf16_t* g = p + i * (T.perm ? 2 : 4) * T.ld; const unsigned v = vo ^ ((i & 2) << 6), d = L + (t & 1) * 65536 + (w8 * 8 + i) * 1024;
        if (OPL && T.perm) glds(g, v, d); else gldsw(g, v, d); }
}
template <class F>
__device__ __forceinline__ void tile_load(const Tile& T, v4u (&wf)[32], unsigned L, int w8, int l, F next) {
    const int n = l & 15, q = l >> 4;
    const unsigned p = L + (8 * q + (n >> 2)) * 256 + ((4 * (w8 & 3) + ((l >> 1) & 1)) ^ (2 * ((n >> 2) | ((q & 1) << 2)))) * 16 + (l & 1) * 8;
    bool more = true;
#pragma unroll 1
    for (int hh = 0; hh < 2; ++hh)
#pragma unroll
    for (int u = 0; u < 2; ++u) {
        if (!more) WAITV(0);
        WAITV(8); BAR();
        if ((w8 >> 2) == hh) {
#pragma unroll
            for (int i = 0; i < 16; ++i) { const unsigned pp = (p ^ ((i & 1) * 32)) + u * 65536 + (i >> 1) * 8192; wf[u * 16 + i] = cat2(trd(pp), trd(pp + 1024)); }
            WAITL(); }
        BAR();
        if (hh == 0) tile_issue(T, u + 2, L, w8, l); else more = next(u);
    }
}
#define NB 8
#define GD 4
template <int XR, bool ROWS, class F>
__device__ __forceinline__ void seg_gemm(const v4u (&wf)[32], unsigned b0, frag_cd (&acc)[4], F side) {
    v4u f[2 * GD];
    const unsigned bb[4] = {b0, ROWS ? b0 ^ 64 : b0, ROWS ? b0 ^ 128 : b0, ROWS ? b0 ^ 192 : b0};
#define RD(ks, mt) asm volatile("ds_read_b128 %0, %1 offset:%2" : "=v"(f[((ks) % GD) * 2 + mt]) : "v"(bb[(ks) & 3]), "n"(ROWS ? ((ks) >> 2) * 256 + mt * 16384 : ((ks) >> 1) * 4096 + ((ks) & 1) * 1024 + mt * 2048) : "memory")
#define MM(ct, mt) do { if (ks == 0) asm volatile("v_mfma_f32_16x16x32_bf16 %0, %1, %2, 0" : "=&v"(acc[2 * ct + mt]) : "v"(wf[ct]), "v"(f[mt])); \
        else asm volatile("v_mfma_f32_16x16x32_bf16 %0, %1, %2, %0" : "+v"(acc[2 * ct + mt]) : "v"(wf[2 * ks + ct]), "v"(f[(ks % GD) * 2 + mt])); } while (0)
#pragma unroll
    for (int ks = 0; ks < GD - 1; ++ks) { RD(ks, 0); RD(ks, 1); }
#pragma unroll
    for (int ks = 0; ks < 16; ++ks) {
        side(2 * ks);
        asm volatile("s_waitcnt lgkmcnt(%0)" :: "n"(2 * (ks + GD - 1 <= 16 ? GD - 2 : 15 - ks) + (ks >= 13 ? XR : 0)));
        MM(0, 0);
        if (ks + GD - 1 < 16) RD(ks + GD - 1, 0);
        MM(0, 1);
        side(2 * ks + 1);
        MM(1, 0);
        if (ks + GD - 1 < 16) RD(ks + GD - 1, 1);
        MM(1, 1);
    }
    asm volatile("s_nop 7" : "+v"(acc[0]), "+v"(acc[1]), "+v"(acc[2]), "+v"(acc[3]));
#undef RD
#undef MM
}
static_assert(GD == 4 && SO == 58, "");
__device__ __forceinline__ float fadd(float a, float b) { float r; asm("v_add_f32 %0, %1, %2" : "=v"(r) : "v"(a), "v"(b)); return r; }
__device__ __forceinline__ float fmul(float a, float b) { float r; asm("v_mul_f32 %0, %1, %2" : "=v"(r) : "v"(a), "v"(b)); return r; }
__device__ __forceinline__ void half_put(unsigned ex, int w8, int l, const frag_cd (&acc)[4]) {
#pragma unroll
    for (int i = 0; i < 4; ++i) *lp<frag_cd>(ex + w8 * 4096 + i * 1024 + l * 16) = acc[i];
    FENCE();
}
struct Half { frag_cd t[4]; unsigned dec; };
__device__ __forceinline__ void half_req(unsigned ex, unsigned dec, Half& x) {
    asm volatile("ds_read_b32 %0, %1" : "=v"(x.dec) : "v"(dec) : "memory");
#pragma unroll
    for (int i = 0; i < 4; ++i) asm volatile("ds_read_b128 %0, %1 offset:%2" : "=v"(x.t[i]) : "v"(ex), "n"(i * 1024) : "memory");
}
template <class F>
__device__ __forceinline__ void half_get(Half& x, frag_cd (&acc)[4], F side) {
    asm volatile("s_waitcnt lgkmcnt(0)" : "+v"(x.t[0]), "+v"(x.t[1]), "+v"(x.t[2]), "+v"(x.t[3]), "+v"(x.dec) :: "memory");
#pragma unroll
    for (int i = 0; i < 4; ++i) { side(i);
#pragma unroll
        for (int e = 0; e < 4; ++e) acc[i][e] = fadd(acc[i][e], x.t[i][e]);
        asm volatile("" : "+v"(acc[i])); }
}
#define GEARLY 2
#define BARI 25
#define PRIO(n) __builtin_amdgcn_s_setprio(n)

__device__ __forceinline__ unsigned topk(const Args& a, int l) {
    const rsrc_t rg = mkrs(a.gate, 2 * S_TOK * 4);
    flags_wait<16>(a.ctl + C_R, 32, 1, l, a.ctl + C_ERR);
    v4u kv[8]; unsigned prev = ~0u, mine = 0;
#pragma unroll
    for (int i = 0; i < 8; ++i) kv[i] = __builtin_amdgcn_raw_buffer_load_b128(rg, S_TOK * 4 + l * 16, i * 1024, 16);
#pragma unroll 1
    for (int c = 0; c < TOPC; ++c) { unsigned mx = 0;
        FENCE();
#pragma unroll
        for (int i = 0; i < 8; ++i)
#pragma unroll
            for (int e = 0; e < 4; ++e) mx = max(mx, kv[i][e] < prev ? kv[i][e] : 0u);
        mx = max(mx, dppu<0x128>(mx)); mx = max(mx, dppu<0x124>(mx)); mx = max(mx, dppu<0x122>(mx)); mx = max(mx, dppu<0x121>(mx));
        { v2u r = sw16(mx, mx); mx = max(r.x, r.y); r = sw32(mx, mx); mx = max(r.x, r.y); }
        prev = RFL(mx); if ((l & 7) == c) mine = prev; }
    return mine;
}
__device__ __forceinline__ unsigned mix(float gt, float omg, float t0, float t1, float b0, float b1) {
    unsigned av = cvt_pk(t0, t1); av = cvt_pk(fmul(gt, lo16(av)), fmul(gt, hi16(av))); const unsigned bv = cvt_pk(fmul(omg, b0), fmul(omg, b1));
    return cvt_pk(fadd(lo16(av), lo16(bv)), fadd(hi16(av), hi16(bv)));
}
struct Finisher {
    rsrc_t rp, rx, rg, ro; int po, frow, xo, w8; unsigned seltok, ly;
    struct Fin { v4u p[4]; v2u xv; float gt; int k; };
    __device__ __forceinline__ Finisher(const Args& a, int j, int w8_, int l, unsigned L_Y) {
        rp = mkrs(a.part, NSEG * 524288); rx = mkrs(a.x, S_TOK * DM * 2); rg = mkrs(a.gate, S_TOK * 4); ro = mkrs(a.out, S_TOK * DM * 2);
        w8 = w8_; po = j * 16384 + w8 * 1024 + l * 16; frow = 16 * (w8 & 1) + (l & 15); xo = (frow * DM + j * 32 + (w8 >> 1) * 16 + 4 * (l >> 4)) * 2;
        seltok = 4095u - (lp<unsigned>(L_Y + 1024)[l & 7] & 4095u); ly = L_Y + ((w8 >> 1) * 32 + (l >> 4) * 8) * 16;
    }
    __device__ __forceinline__ Fin issue(int s, bool on) const { Fin f; int k = on ? 0 : 0x40000000; asm("" : "+s"(k));
#pragma unroll
        for (int i = 0; i < 4; ++i) f.p[i] = __builtin_amdgcn_raw_buffer_load_b128(rp, k + po, s * 524288 + i * 4096, 16);
        f.k = k + xo;
        f.xv = __builtin_amdgcn_raw_buffer_load_b64(rx, f.k, s * 65536, 0);
        f.gt = __uint_as_float(__builtin_amdgcn_raw_buffer_load_b32(rg, k + frow * 4, s * 128, 16));
        return f;
    }
    template <class F> __device__ __forceinline__ void finish(int s, const Fin& f, F side) const {
        const int tok = 32 * s + frow; const float gt = f.gt, omg = rbf(1.0f - gt);
        frag_cd t; v2u o; int k = f.k;
#pragma unroll
        for (int e = 0; e < 4; e += 2) {
            side(e >> 1);
#pragma unroll
            for (int u = e; u < e + 2; ++u) t[u] = fadd(fadd(__uint_as_float(f.p[0][u]), __uint_as_float(f.p[1][u])), fadd(__uint_as_float(f.p[2][u]), __uint_as_float(f.p[3][u])));
            o[e >> 1] = mix(gt, omg, t[e], t[e + 1], lo16(f.xv[e >> 1]), hi16(f.xv[e >> 1])); asm volatile("" : "+v"(o[e >> 1])); }
        for (unsigned mk = (unsigned)__builtin_amdgcn_ballot_w64((seltok >> 4) == (unsigned)(2 * s + (w8 & 1))) & 255u; mk; mk &= mk - 1) {
            const int slot = __builtin_ctz(mk);
            if (__builtin_amdgcn_readlane(seltok, slot) == (unsigned)tok) { *lp<frag_cd>(ly + slot * 16) = t; k = 0x40000000; }
        }
        __builtin_amdgcn_raw_buffer_store_b64(o, ro, k, s * 65536, 0);
    }
};

__device__ __forceinline__ void gwork(const Args& a, const int m, const int h, const unsigned L, const unsigned hb) {
    const int w8 = RFL(threadIdx.x >> 6); int l = threadIdx.x & 63, n = l & 15, q = l >> 4;
    unsigned* const err = a.ctl + C_ERR;
    const unsigned L_EX = L + 131072, L_XS = L_EX, L_HS = L_XS + 16384, L_GU = L_HS + 2048;
    unsigned* const fG = a.ctl + C_G + h * 128 + m * 32;
    const unsigned xo = (l ^ (8 * (w8 & 1))) * 16;
    auto xissue = [&](int s, int i0, int i1) {
        const bf16_t* p = a.x + (size_t)(32 * s + 8 * (w8 & 3)) * DM + (~w8 & 4) * 128; const unsigned d = L + (s & 1) * 65536 + (w8 ^ 4) * 8192;
#pragma unroll
        for (int i = i0; i < i1; ++i) {
            if (i == i0 || i == 4) asm volatile("s_mov_b32 m0, %0" :: "s"(d + (i & 4) * 1024));
            unsigned t; asm volatile("v_xor_b32 %0, %4, %1\n\tglobal_load_lds_dwordx4 %0, %2 offset:%3" : "=&v"(t) : "v"(xo), "s"(p), "n"((i & 3) * 1024), "n"(i * 1040 + (i & 4) * 1024) : "memory"); }
    };
    v4u wf[32];
    { const Tile T{(m == 0 ? a.Wk : (m == 1 ? a.Wv : a.Wq)) + h * DH, HD, 0, 0};
      tile_issue(T, 0, L, w8, l); tile_issue(T, 1, L, w8, l);
      tile_load(T, wf, L, w8, l, [&](int b) { if (b == 0 || w8 >= 4) xissue(b, 0, 8); return true; }); }
    MARK(0);
    const unsigned rb0 = L + (w8 >> 2) * 32768 + n * 1024 + (q ^ n) * 16;
    unsigned pacc[8] = {}, plast = 0; (void)pacc; (void)plast;
    PT(7);
    if (w8 >= 4) {
        const rsrc_t rq = mkrs(a.qkv, NSEG * NH * 24576), rf = mkrs(fG, w8 == 4 ? 4 : 0);
        const int sto = h * 24576 + m * 8192 + (m == 1 ? (w8 & 3) * 64 + l : ((w8 & 2) + (q & 1)) * 64 + (2 * (w8 & 1) + (q >> 1)) * 16 + n) * 16;
        WAITV(8); BAR(); WAITV(0);
        if (w8 == 4) { ctl_wait(a, hb); gadd1(a.ctl + C_READY); }
        for (int sg = 0; sg < NSEG; ++sg) {
            PT(0);
            BAR();
            PT(1);
            PRIO(2);
            if (sg <= GEARLY) __builtin_amdgcn_raw_buffer_store_b32(sg, rf, l * 4, 0, 16);
            frag_cd acc[4]; Half hx;
            seg_gemm<5, true>(wf, rb0 + (sg & 1) * 65536, acc, [&](int i) {
                if (i == BARI) { PRIO(3); PT(5); WAITV(3); BAR(); PT(6); }
                if (i == 26) half_req(L_EX + (w8 & 3) * 4096 + l * 16, L_EX, hx);
                if (i >= 26) xissue((sg + 2) & 63, i - 26, i - 25); });
            PRIO(0);
            PT(2);
            half_get(hx, acc, [&](int i) { if (i & 1) xissue((sg + 2) & 63, 6 + i / 2, 7 + i / 2); });
            v4u r[2];
#pragma unroll
            for (int mt = 0; mt < 2; ++mt) r[mt] = v4u{cvt_pk(acc[mt][0], acc[2 + mt][0]), cvt_pk(acc[mt][1], acc[2 + mt][1]), cvt_pk(acc[mt][2], acc[2 + mt][2]), cvt_pk(acc[mt][3], acc[2 + mt][3])};
            PT(3);
#pragma unroll
            for (int mt = 0; mt < 2; ++mt) STB(r[mt], rq, sto + mt * 4096, sg * (NH * 24576));
            __builtin_amdgcn_raw_buffer_store_b32(sg <= GEARLY ? sg : sg - 1, rf, l * 4, 0, 16);
            FENCE();
            if (sg < GEARLY) WAITV(0);
            PT(4);
        }
        PFLUSH();
        WAITV(0); BAR();
        if (w8 == 4) gst1(fG, NSEG);
    } else {
        for (int sg = 0; sg < NSEG; ++sg) {
            frag_cd acc[4];
            WAITL();
            PT(0);
            BAR();
            PT(1);
            PRIO(2);
            seg_gemm<0, true>(wf, rb0 + (sg & 1) * 65536, acc, [&](int i) { if (i == BARI) { PRIO(3); PT(5); WAITV(0); BAR(); PT(6); } });
            PRIO(1);
            PT(2);
            half_put(L_EX, w8, l, acc);
            xissue((sg + 1) & 63, 0, 8);
            PT(3);
            MARK(sg + 1);
        }
        PFLUSH();
        WAITL(); BAR();
        WAITV(0); BAR();
    }
    asm volatile("v_mbcnt_lo_u32_b32 %0, -1, 0\n\tv_mbcnt_hi_u32_b32 %0, -1, %0" : "=v"(l));
    n = l & 15; q = l >> 4;
    if (m != 2) {
        const int j = TMAP ? h : (h & 23) + 8 * m, mt = TMAP ? m : h >> 3 & 1;
        const unsigned stg = L + w8 * 16384, p = stg + (8 * q + (n >> 2)) * 64 + (n & 3) * 8;
#pragma unroll
        for (int hh = 0; hh < 2; ++hh) {
#pragma unroll 1
            for (int i = 0; i < 16; ++i) {
                (TNT ? gldsw : glds)(a.Wo + (size_t)(512 * w8 + 256 * hh + 32 * (i >> 1) + 8 * (i & 1)) * DM + 32 * j, ((l >> 3) + (l & 4) * 4) * (DM * 2) + (l & 3) * 16, stg + i * 1024); WAITV(TIF); __builtin_amdgcn_s_sleep(TSP); }
            WAITV(0);
#pragma unroll
            for (int i = 0; i < 16; ++i) { const unsigned pp = p + (i >> 1) * 2048 + (i & 1) * 32; wf[hh * 16 + i] = cat2(trd(pp), trd(pp + 256)); }
            WAITL();
        }
        MARK(66);
        const rsrc_t ro = mkrs(a.out, S_TOK * DM * 2), ra = mkrs(a.ox, NSEG * 262144);
        const int xo = (16 * mt + n) * DM + j * 32 + (w8 & 1) * 16 + 4 * q;
        const unsigned L_B = L_EX - SO * 2048;
        if (w8 < 2) {
            const unsigned seltok = 4095u - (topk(a, l) & 4095u);
#pragma unroll 1
            for (int s = SO; s < NSEG; ++s) { const v2u xv = *(const v2u*)(a.x + s * 32768 + xo);
                *lp<v4u>(L_B + s * 2048 + w8 * 1024 + l * 16) = v4u{xv.x, xv.y, gld(a.gate + 32 * s + 16 * mt + n), seltok}; }
        }
        BAR();
        MARK(67);
        { unsigned fv = 0; flags_wait<16, 64>(fv, a.ctl + C_M + (16 * w8 + 2 * (l & 7) + mt) * 32, SO - 1, err); }
        auto peek = [&](int s) { FENCE(); const unsigned v = __builtin_amdgcn_raw_buffer_load_b32(ra, w8 * 32768 + mt * 2048 + (l & 7) * 4096 + 1024, s * 262144, 16); FENCE(); return v; };
        unsigned nx = ~0u;
#define TMK(k)
#pragma unroll 1
        for (int s = SO, it = 0; s < NSEG;) {
            for (int i = 0; __builtin_amdgcn_ballot_w64(nx == ~0u) != 0; ++i) { if (i) __builtin_amdgcn_s_sleep(TS1); nx = peek(s); if (++it > GCAP) fatal(err); }
            TMK(0);
            v4u f[16]; frag_cd acc[4] = {};
            unsigned mx = 0;
#pragma unroll
            for (int i = 0; i < 16 + TD; ++i) { const int ks = i - TD;
                SB();
                if (i < 16) f[i] = __builtin_amdgcn_raw_buffer_load_b128(ra, w8 * 32768 + mt * 2048 + l * 16 + (i & 1) * 1024, s * 262144 + (i >> 1) * 4096, 16);
                if (i == 16) TMK(1);
                if (ks == TFK) nx = peek(min(s + 1, NSEG - 1));
                SB();
                if (ks >= 0) { acc[ks & 1] = MFMA(wf[2 * ks], f[ks], acc[ks & 1]); acc[2 + (ks & 1)] = MFMA(wf[2 * ks + 1], f[ks], acc[2 + (ks & 1)]); mx = umax3(umax3(mx, f[ks].x, f[ks].y), f[ks].z, f[ks].w); } }
            SB();
            TMK(2);
            if (__builtin_amdgcn_ballot_w64(mx == ~0u) != 0) {
                nx = ~0u; continue; }
            const unsigned ex = L + (s - SO) * 16384 + w8 * 2048 + l * 16;
            *lp<frag_cd>(ex) = acc[0] + acc[1]; *lp<frag_cd>(ex + 1024) = acc[2] + acc[3];
            if (s % 3 == 0) {
                WAITL(); BAR();
                if (w8 < 6) { const int u = s - (w8 >> 1), o = (w8 & 1) * 1024 + l * 16;
                    v4u b = *lp<v4u>(L_B + u * 2048 + o);
                    frag_cd t = *lp<frag_cd>(L + (u - SO) * 16384 + o);
#pragma unroll
                    for (int i = 1; i < 8; ++i) t += *lp<frag_cd>(L + (u - SO) * 16384 + o + i * 2048);
                    for (unsigned mk = (unsigned)__builtin_amdgcn_ballot_w64((b.w >> 4) == (unsigned)(2 * u + mt)) & 255u; mk; mk &= mk - 1) {
                        const int slot = __builtin_ctz(mk);
                        flags_wait<4>(a.ctl + C_Y + j * 32 + 1, 1, 1, l, err);
                        const frag_cd y = __builtin_bit_cast(frag_cd, __builtin_amdgcn_raw_buffer_load_b128(mkrs(a.ypart + (32 * 28 + j) * 1024, 1024), ((w8 & 1) * 32 + q * 8 + slot) * 16, 0, 16));
                        if ((__builtin_amdgcn_readlane(b.w, slot) & 15) == (unsigned)n) { b.x = cvt_pk(lo16(b.x) + y[0], hi16(b.x) + y[1]); b.y = cvt_pk(lo16(b.y) + y[2], hi16(b.y) + y[3]); }
                    }
                    const float gt = __uint_as_float(b.z), omg = rbf(1.0f - gt);
                    __builtin_amdgcn_raw_buffer_store_b64(v2u{mix(gt, omg, t[0], t[1], lo16(b.x), hi16(b.x)), mix(gt, omg, t[2], t[3], lo16(b.y), hi16(b.y))}, ro, xo * 2, u * 65536, 0); } }
            TMK(3);
            if (TSPEC && s == NSEG - 2) { __builtin_amdgcn_s_sleep(TS3); nx = 0; }
            ++s;
        }
        MARK(65);
        return;
    }
    const rsrc_t ry = mkrs(a.ypart, 32 * 29 * 1024);
    auto tile = [&](int t) { return t < 2 ? Tile{(t ? a.wu : a.wg) + h * 128, FF, 0, 0} : Tile{a.wd + (size_t)h * 128 * DM, DM, 1, 0}; };
    unsigned mykey = 0;
    if (w8 == 0) {
        mykey = topk(a, l);
#pragma unroll 1
        for (int i = 0; i < 16; ++i) glds(a.x + i * 64, (4095u - (mykey & 4095u)) * (DM * 2) + (l >> 3) * 16, L_XS + i * 1024);
    }
    if (h < FF / 128) {
      v4u wm[32];
      Tile T = tile(0);
      tile_issue(T, 0, L, w8, l); tile_issue(T, 1, L, w8, l);
      frag_cd gv[2] = {};
#pragma unroll 1
      for (int ti = 0; ti < 3; ++ti) {
        const Tile N = tile(ti + 1);
        tile_load(T, wm, L, w8, l, [&](int b) { if (ti < 2) tile_issue(N, b, L, w8, l); return ti < 2; });
        T = N;
        if (ti < 2) {
            frag_cd acc[2] = {};
#pragma unroll
            for (int ks = 0; ks < 16; ++ks) { const v4u xf = *lp<v4u>(L_XS + ((w8 >> 2) * 16 + ks) * 512 + q * 128 + (n & 7) * 16); acc[0] = MFMA(wm[2 * ks], xf, acc[0]); acc[1] = MFMA(wm[2 * ks + 1], xf, acc[1]); }
            if (ti == 1) {
                frag_cd g = n < 8 ? gv[0] : gv[1], u = n < 8 ? acc[0] : acc[1];
                if (w8 >= 4) { *lp<frag_cd>(L_GU + (w8 & 3) * 2048 + l * 16) = g; *lp<frag_cd>(L_GU + (w8 & 3) * 2048 + 1024 + l * 16) = u; WAITL(); }
                BAR();
                if (w8 < 4) { g += *lp<frag_cd>(L_GU + w8 * 2048 + l * 16); u += *lp<frag_cd>(L_GU + w8 * 2048 + 1024 + l * 16);
                    float hv[4];
#pragma unroll
                    for (int e = 0; e < 4; ++e) { const float gg = rbf(g[e]); hv[e] = rbf(gg / (1.0f + __expf(-gg))) * rbf(u[e]); }
                    *lp<v2u>(L_HS + w8 * 512 + (2 * (n >> 3) + (q >> 1)) * 128 + (n & 7) * 16 + (q & 1) * 8) = v2u{cvt_pk(hv[0], hv[1]), cvt_pk(hv[2], hv[3])};
                    WAITL(); } }
            gv[0] = acc[0]; gv[1] = acc[1];
        } else {
#pragma unroll
            for (int cb = 0; cb < 4; ++cb) { frag_cd acc[2] = {};
#pragma unroll
                for (int i = 0; i < 4; ++i) { const v4u hf = *lp<v4u>(L_HS + i * 512 + q * 128 + (n & 7) * 16); acc[0] = MFMA(wm[8 * cb + 2 * i], hf, acc[0]); acc[1] = MFMA(wm[8 * cb + 2 * i + 1], hf, acc[1]); }
                STB(__builtin_bit_cast(v4u, n < 8 ? acc[0] : acc[1]), ry, ((w8 & 3) * 28 + h) * 1024 + ((n >> 3) * 32 + q * 8 + (n & 7)) * 16, ((w8 >> 2) * 4 + cb) * (4 * 28 * 1024)); }
        }
        MARK(67 + ti);
      }
      WAITV(0); BAR();
    }
    if (w8 == 0) {
        gst1(a.ctl + C_Y + h * 32, 1u);
        flags_wait<16>(a.ctl + C_Y, FF / 128, 1, l, err);
        frag_cd y = {};
#pragma unroll 1
        for (int i = 0; i < 28; i += 7) { v4u t[7];
#pragma unroll
            for (int u = 0; u < 7; ++u) t[u] = __builtin_amdgcn_raw_buffer_load_b128(ry, l * 16, (h * 28 + i + u) * 1024, 16);
#pragma unroll
            for (int u = 0; u < 7; ++u) y += __builtin_bit_cast(frag_cd, t[u]); }
        const float r = hi16((mykey & 0x80000000u) ? mykey & 0x7fffffffu : ~mykey), rs = rbf(1.0f / (1.0f + __expf(-r)));
#pragma unroll
        for (int e = 0; e < 4; ++e) y[e] = rbf(rbf(y[e]) * rs);
        STB(__builtin_bit_cast(v4u, y), ry, l * 16, (32 * 28 + h) * 1024);
        WAITV(0);
        gst1(a.ctl + C_Y + h * 32 + 1, 1u);
        MARK(120);
    }
}

__device__ __forceinline__ void owork(const Args& a, const int nt, const int kq, const unsigned L, const unsigned hb) {
    const int tid = threadIdx.x, w8 = RFL(tid >> 6), l = tid & 63, j = nt * 4 + kq;
    unsigned* const err = a.ctl + C_ERR;
    const unsigned L_EX = L + 131072, L_DEC = L_EX + 16384, L_Y = L_DEC + 64, L_KEY = L_Y + 1024;
    if (tid < 4) lp<unsigned>(L_DEC)[tid] = 0;
    {
        const bool lead = j == 0 && w8 == 1;
        if (lead) { const rsrc_t rc = mkrs(a.ctl, C_HB * 4);
#pragma unroll
            for (int i = 0; i < 8; ++i) STB(v4u{}, rc, l * 128, i * 8192);
            STB(v4u{}, rc, l * 16, 0); }
        unsigned char* p = a.ring + ((l >> 3 & 1) * 64 + (j >> 1)) * 32768 + ((l ^ (kq & 1) * 4 ^ kq >> 1) & 7) * 4096 + (w8 * 2 + (nt & 1)) * 256 + (kq & 1) * 128;
        unsigned best = 0;
        if (l < 16) gst((unsigned*)p, ~0u);
        WAITV(0);
#pragma unroll 1
        for (int i = 0; i < 16; ++i) { u64 t1, t2; unsigned v;
            if (lead && !(i & 3)) gadd1(a.ctl + C_HB);
            const u64 pi = ((u64)__builtin_amdgcn_readlane((unsigned)((u64)p >> 32), i) << 32) | (unsigned)__builtin_amdgcn_readlane((unsigned)(u64)p, i);
            asm volatile("s_nop 4\n\ts_memtime %0\n\ts_waitcnt lgkmcnt(0)\n\tglobal_load_dword %2, %3, %4 sc1\n\ts_waitcnt vmcnt(0)\n\ts_memtime %1\n\ts_waitcnt lgkmcnt(0)"
                         : "=&s"(t1), "=&s"(t2), "=&v"(v) : "v"(0u), "s"(pi) : "memory");
            if (l == i) best = ~(unsigned)(t2 - t1); }
        if (l < 16) { gst((unsigned*)p, ~0u); lp<unsigned>(L)[w8 * 16 + l] = best; }
        WAITV(0); WAITL(); BAR();
        if (w8 == 0) {
#pragma unroll
            for (int i = 1; i < 8; ++i) best = max(best, lp<unsigned>(L)[i * 16 + (l & 15)]);
            if (j) ctl_wait(a, hb);
            if (l < 16) __hip_atomic_fetch_max(a.ctl + C_CAL + l, best, __ATOMIC_RELAXED, __HIP_MEMORY_SCOPE_AGENT);
            WAITV(0);
            gadd1(a.ctl + C_READY);
            MARK(66);
        }
        if (lead) for (int i = 0; !RFL(gld(a.ctl + C_ACK)); ++i) { gadd1(a.ctl + C_HB); __builtin_amdgcn_s_sleep(8); if (i > GCAP) fatal(err); }
    }
    BAR();
    {
#pragma unroll 1
        for (int i = 0; i < 16; ++i) glds(a.x + (size_t)(64 * j + 8 * w8) * DM + i * 512, l * 16, L + (w8 * 16 + i) * 1024);
        v4u gw[2], wr[2];
#pragma unroll
        for (int i = 0; i < 2; ++i) { gw[i] = *(const v4u*)(a.gw + 512 * i + 8 * l); wr[i] = *(const v4u*)(a.wr + 512 * i + 8 * l); }
        WAITV(0);
        float res = 0.f;
#pragma unroll 1
        for (int t = 0; t < 8; ++t) { float sg = 0.f, sr = 0.f;
#pragma unroll
            for (int i = 0; i < 2; ++i) { const v4u xv = *lp<v4u>(L + (w8 * 16 + 2 * t + i) * 1024 + l * 16);
#pragma unroll
                for (int e = 0; e < 4; ++e) { const float x0 = lo16(xv[e]), x1 = hi16(xv[e]); const unsigned p = cvt_pk(x0 * lo16(gw[i][e]), x1 * hi16(gw[i][e]));
                    sg += lo16(p) + hi16(p); sr = __builtin_fmaf(x0, lo16(wr[i][e]), __builtin_fmaf(x1, hi16(wr[i][e]), sr)); } }
            float v = tadd32(sg, sr);
            v = tadd16(v, v); v += dppf<ROR8>(v); v += dppf<0x124>(v); v += dppf<0x122>(v); v += dppf<0x121>(v);
            if ((l & 7) == t) res = v; }
        const unsigned rb = cvt_pk(res, 0.f), tok = 64 * j + 8 * w8 + (l & 7);
        const unsigned val = l < 32 ? __float_as_uint(rbf(1.0f / (1.0f + __expf(-lo16(rb))))) : (((rb & 0x8000u) ? ~rb : rb | 0x8000u) << 16) | (4095u - tok);
        if ((l & 31) < 8) gst(a.gate + (l >> 5) * S_TOK + tok, val);
        WAITV(0); BAR();
        if (w8 == 0) gst1(a.ctl + C_R + j * 32, 1u);
        MARK(67);
    }
    v4u wf[32];
    { const Tile T{a.Wo + (size_t)kq * 1024 * DM + nt * 128, DM, 0, 1};
      tile_issue(T, 0, L, w8, l); tile_issue(T, 1, L, w8, l);
      tile_load(T, wf, L, w8, l, [](int) { return false; }); }
    MARK(0);
    if (w8 == 1) { const unsigned mine = topk(a, l); if (l < 8) lp<unsigned>(L_KEY)[l] = mine; WAITL(); }
    BAR();
    MARK(68);
    unsigned* const fP = a.ctl + C_P + nt * 512;
    const unsigned rb = L + (w8 >> 2) * 32768 + l * 16;
    unsigned pacc[8] = {}, plast = 0; (void)pacc; (void)plast;
    PT(7);
    auto oissue = [&](int s, int wv, int i) { glds1<2>(a.ox + (size_t)s * 262144 + kq * 65536 + wv * 8192 + i * 2048, l * 16, L + (s & 1) * 65536 + wv * 8192 + i * 2048); };
    if (w8 >= 4) {
        const rsrc_t rf = mkrs(fP + (kq * 4 + (w8 & 3)) * 32, 4), rp = mkrs(a.part, NSEG * 524288);
        const int stp = ((nt * 4 + (w8 & 3)) * 4 + kq) * 4096 + l * 16;
        const unsigned* fW = l < 32 ? a.ctl + C_M + (kq * 32 + l) * 32 : fP + ((l & 3) * 4 + kq) * 32;
        unsigned fv = 0; int pc = 0;
        auto a_ok = [&](int s) { return s < SO && (unsigned)__builtin_amdgcn_ballot_w64(fv <= (unsigned)s) == 0; };
        auto p_upd = [&]() { if ((__builtin_amdgcn_ballot_w64(fv <= (unsigned)pc) >> 32) == 0) ++pc; if ((__builtin_amdgcn_ballot_w64(fv <= (unsigned)pc) >> 32) == 0) ++pc; lp<unsigned>(L_DEC)[2] = pc; };
        int have = 0, nxt = 0, late = 0;
        for (int s = 0; s <= SO; ++s) {
            if (!have) {
                if (!late) {
                    gld_wait<0>(fv);
                    __builtin_amdgcn_raw_buffer_store_b32(s, rf, l * 4, 0, 16);
                    if (w8 == 4) {
                        for (int i = 0;; ++i) { if (fv <= (unsigned)(l < 32 ? s + 1 : s - 1)) fv = gld(fW); WAITV(0); p_upd(); if (a_ok(s) || pc == SO) break; __builtin_amdgcn_s_sleep(2); if (i > GCAP) fatal(err); }
                        lp<unsigned>(L_DEC)[3] = s + 1; lp<unsigned>(L_DEC)[~s & 1] = a_ok(s + 1); FENCE(); }
                    if (s == SO) break;
                    for (int i = 0; ldsr(L_DEC + 12) <= (unsigned)s; ++i) { __builtin_amdgcn_s_sleep(1); if (i > LCAP) endpgm(); }
                    FENCE();
                    PT(0);
#pragma unroll
                    for (int i = 0; i < 8; ++i) oissue(s, w8 ^ (~i & 4), i & 3);
                    WAITV(8);
                }
                WAITV(12); BAR();
                nxt = RFL(ldsr(L_DEC + 4 * (~s & 1))) & 1;
                if (late) WAITV(4); else WAITV(0);
                if (nxt) {
#pragma unroll
                    for (int i = 0; i < 4; ++i) oissue(s + 1, w8 ^ 4, i); }
                PT(1);
            }
            if (w8 == 4 && !nxt) {
                if (!have) { if (fv <= (unsigned)(s + 1)) gld_req(fv, fW); WAITV(0); }
                gld_wait<4>(fv);
                p_upd(); lp<unsigned>(L_DEC)[~s & 1] = 4 * a_ok(s + 1); WAITL();
                if (fv <= (unsigned)(s + 2)) gld_req(fv, fW); }
            BAR();
            PT(2);
            PRIO(2);
            frag_cd acc[4]; Half hx; unsigned mid;
            seg_gemm<5, false>(wf, rb + (s & 1) * 65536, acc, [&](int i) {
                if (i == BARI - 5) { if (!(have & nxt)) WAITV(0);
                    gld_wait<4>(fv);
                    if (w8 == 4) { p_upd(); const bool k = a_ok(s + 1); if (k) lp<unsigned>(L_DEC)[3] = s + 2; lp<unsigned>(L_DEC)[s & 1] = a_ok(s + 2) + 2 * k; }
                    __builtin_amdgcn_raw_buffer_store_b32(s - (have & nxt), rf, l * 4, 0, 16); FENCE(); }
                if (i == BARI) { PRIO(3); PT(5); BAR(); PT(6); }
                if (i == 3) asm volatile("ds_read_b32 %0, %1" : "=v"(mid) : "v"(L_DEC + 4 * (~s & 1)) : "memory");
                if (i == 26) half_req(L_EX + (w8 & 3) * 4096 + l * 16, L_DEC + 4 * (s & 1), hx);
                if (i == 28) { asm volatile("s_waitcnt lgkmcnt(4)" : "+v"(hx.dec), "+v"(mid)); const int d = RFL(hx.dec); have = nxt | RFL(mid) >> 2; nxt = have & d; late = ~have & d >> 1 & 1; }
                if (i >= 28 && (nxt | late)) oissue(s + 1 + nxt, w8 ^ 4, i - 28); });
            PRIO(0);
            PT(3);
            half_get(hx, acc, [&](int i) { if (late) oissue(s + 1, w8, i); });
            if (w8 == 4 && fv <= (unsigned)(s + 3)) gld_req(fv, fW);
#pragma unroll
            for (int i = 0; i < 4; ++i) STB(__builtin_bit_cast(v4u, acc[i]), rp, stp + i * 1024, s * 524288);
            FENCE();
            PT(4);
        }
        if (w8 == 5) {
            flags_wait<4>(a.ctl + C_Y + j * 32 + 1, 1, 1, l, err);
            const unsigned tok = 4095u - (lp<unsigned>(L_KEY)[l & 7] & 4095u); const int xo = tok * DM + j * 32 + (l >> 3) * 4;
            const frag_cd y = __builtin_bit_cast(frag_cd, __builtin_amdgcn_raw_buffer_load_b128(mkrs(a.ypart + (32 * 28 + j) * 1024, 1024), l * 16, 0, 16));
            const v2u xv = *(const v2u*)(a.x + xo); const float gt = __uint_as_float(gld(a.gate + tok)), omg = rbf(1.0f - gt);
            const float b[4] = {rbf(lo16(xv.x) + y[0]), rbf(hi16(xv.x) + y[1]), rbf(lo16(xv.y) + y[2]), rbf(hi16(xv.y) + y[3])};
            asm volatile("" :: "v"(b[0]), "v"(b[1]), "v"(b[2]), "v"(b[3]), "v"(omg));
            BAR();
            const frag_cd t = *lp<frag_cd>(L_Y + l * 16);
            if (tok < 32 * SO) *(v2u*)(a.out + xo) = v2u{mix(gt, omg, t[0], t[1], b[0], b[1]), mix(gt, omg, t[2], t[3], b[2], b[3])};
        } else BAR();
    } else {
        const Finisher fz(a, j, w8, l, L_Y);
        int fin = 0, have = 0, pc = 0;
        for (int s = 0; s <= SO; ++s) {
            if (!have) {
                for (int i = 0; fin < s; ++i) {
                    if (s < SO && RFL(ldsr(L_DEC + 12)) > s) break;
                    pc = RFL(ldsr(L_DEC + 8));
                    if (fin < pc) { FENCE(); fz.finish(fin, fz.issue(fin, true), [](int) {}); ++fin; }
                    else { __builtin_amdgcn_s_sleep(1); if (i > LCAP) endpgm(); }
                }
                if (s == SO) break;
            }
            FENCE();
            const bool fi = fin < pc;
            const Finisher::Fin f = fz.issue(fin, fi);
            WAITL(); WAITV(15);
            PT(0);
            BAR();
            PT(2);
            PRIO(2);
            frag_cd acc[4];
            v4u dec;
            seg_gemm<1, false>(wf, rb + (s & 1) * 65536, acc, [&](int i) {
                if (i == BARI) { PRIO(3); PT(1); WAITV(6); BAR(); PT(6); }
                if (i == 26) asm volatile("ds_read_b128 %0, %1" : "=v"(dec) : "v"(L_DEC) : "memory"); });
            PRIO(1);
            PT(3);
            asm volatile("s_waitcnt lgkmcnt(0)" : "+v"(dec));
            have = RFL(s & 1 ? dec.x : dec.y) & 5; pc = RFL(dec.z);
            half_put(L_EX, w8, l, acc);
            WAITV(0);
            if (have & 4) {
#pragma unroll
                for (int i = 0; i < 4; ++i) oissue(s + 1, w8, i); }
            if (have) { oissue(s + 1, w8 ^ 4, 0); oissue(s + 1, w8 ^ 4, 1); }
            PT(4);
            if (!have) { WAITL(); BAR(); }
            fz.finish(fin, f, [&](int i) { if (have) oissue(s + 1, w8 ^ 4, 2 + i); });
            fin += fi;
            MARK(1 + s);
            PT(5);
        }
        WAITL(); BAR();
    }
    PFLUSH();
    MARK(65);
}

__global__ void __launch_bounds__(512, 2) k_mega(Args a) {
    __shared__ __attribute__((aligned(1024))) unsigned char smem[163840];
    const unsigned L = (unsigned)(size_t)(LAS unsigned char*)smem;
    const int b = blockIdx.x, u = (b & 3) + 4 * (b >> 3);
    MARK(255); MARKC(254);
    const unsigned hb = __builtin_amdgcn_raw_buffer_load_b32(mkrs(a.ctl + C_HB, 4), threadIdx.x >> 6 == (b & 4 ? (u < 96 ? 4 : 0) : 3) ? 0 : OOB, 0, 16);
    FENCE();
    if (!(b & 4)) chain(a, u, L, hb);
    else if (u < 96) gwork(a, (u >> 2) % 3, (u & 3) * 8 + (u >> 2) / 3, L, hb);
    else owork(a, (u >> 2) - 24, u & 3, L, hb);
}

static constexpr int g_coff = 0, g_force = 0; static constexpr int g_d1 = 620, g_d2 = 1340, g_per = 1800, g_dinit = 250; static constexpr unsigned* g_dbg = nullptr;
extern "C" void kernel_launch(void* const* d_in, const int* in_sizes, int n_in, void* d_out, int out_size, void* d_ws, size_t ws_size, hipStream_t stream) {
    auto in = [&](int i) { return (const bf16_t*)d_in[i]; };
    unsigned char* ws = (unsigned char*)d_ws; size_t off = g_coff;
    auto alloc = [&](size_t bytes) { unsigned char* p = ws + off; off += (bytes + 255) & ~(size_t)255; return p; };
    unsigned* ctl = (unsigned*)alloc(C_WORDS * 4);
    unsigned* gate = (unsigned*)alloc(2 * S_TOK * 4); unsigned char* ypart = alloc(32 * 29 * 1024);
    off += (size_t)(-(uintptr_t)(ws + off)) & ((2u << 20) - 1);
    unsigned char* ring = alloc(RING_BYTES);
    unsigned char* qkv = alloc((size_t)NSEG * NH * 24576);
    unsigned char* ox = alloc((size_t)NSEG * 262144); unsigned char* part = alloc((size_t)NSEG * 524288);
    Args a{in(0), in(1), in(2), in(3), in(4), in(5), (bf16_t*)d_out, ctl, ring, qkv, ox, part, in(8), in(9), in(10), in(11), in(12), gate, ypart, g_dbg,
           g_d1 * 16 / 10, g_d2 * 16 / 10, g_per * 16 / 10, g_dinit * 16 / 10 + (g_force << 16)};
    k_mega<<<256, 512, 0, stream>>>(a);
}
```

```cpp
#include <hip/hip_runtime.h>
#include <stdint.h>
#include <type_traits>

typedef unsigned short bf16_t;
typedef unsigned long long u64;
typedef float frag_cd __attribute__((ext_vector_type(4)));
typedef unsigned v4u __attribute__((ext_vector_type(4)));
typedef unsigned v8u __attribute__((ext_vector_type(8)));
typedef unsigned v2u __attribute__((ext_vector_type(2)));
typedef float v2f __attribute__((ext_vector_type(2)));
typedef __bf16 bf16x2_t __attribute__((ext_vector_type(2)));
typedef __bf16 v8bf __attribute__((ext_vector_type(8)));
typedef __bf16 v16bf __attribute__((ext_vector_type(16)));
typedef short v4s_t __attribute__((ext_vector_type(4)));
typedef __amdgpu_buffer_rsrc_t rsrc_t;
#define LAS __attribute__((address_space(3)))
#define GAS __attribute__((address_space(1)))

static constexpr int S_TOK = 2048, DM = 1024, NH = 32, DH = 128, MM = 8, SEG = 32, NSEG = 64, HD = NH * DH, FF = 3584, TOPC = 8;
static constexpr int C_ERR = 0, C_READY = 32, C_CAL = 64  , C_ACK = 96, C_G = 128  , C_M = C_G + 4096  ,
                     C_P = C_M + 4096  , C_R = C_P + 4096, C_Y = C_R + 1024  , C_HB = C_Y + 1024, C_WORDS = C_HB + 32;
static constexpr int RING_BYTES = 4 << 20, B_H1 = 64, B_H3 = 96, B_TS = 112;
static constexpr unsigned SPIN_CAP = 1u << 18;
static constexpr int OOB = 0x7ffffff0, LCAP = 1 << 22, GCAP = 1 << 20;
static constexpr float SCALE = 0.08838834764831845f, LOG2E = 1.4426950408889634f;

struct Args {
    const bf16_t* x; const bf16_t* mem0; const bf16_t* Wq; const bf16_t* Wk; const bf16_t* Wv; const bf16_t* Wo;
    bf16_t* out; unsigned* ctl; unsigned char* ring; unsigned char* qkv; unsigned char* ox; unsigned char* part;
    const bf16_t* wr; const bf16_t* wg; const bf16_t* wu; const bf16_t* wd; const bf16_t* gw;
    unsigned* gate  ; unsigned char* ypart; unsigned* dbg;
    int d1, d2, per, dinit;
};

__device__ __forceinline__ unsigned cvt_pk(float lo, float hi) { v2f v = {lo, hi}; return __builtin_bit_cast(unsigned, __builtin_convertvector(v, bf16x2_t)); }
__device__ __forceinline__ float lo16(unsigned p) { return __uint_as_float(p << 16); }
__device__ __forceinline__ float hi16(unsigned p) { return __uint_as_float(p & 0xffff0000u); }
__device__ __forceinline__ float rbf(float f) { return lo16(cvt_pk(f, 0.f)); }
template <int C> __device__ __forceinline__ unsigned dppu(unsigned v) { return (unsigned)__builtin_amdgcn_update_dpp(0, (int)v, C, 0xf, 0xf, true); }
template <int C> __device__ __forceinline__ float dppf(float v) { return __uint_as_float(dppu<C>(__float_as_uint(v))); }
#define ROR8 0x128
#define HMIR 0x141
__device__ __forceinline__ v2u sw16(unsigned a, unsigned b) { auto r = __builtin_amdgcn_permlane16_swap(a, b, false, false); return v2u{r[0], r[1]}; }
__device__ __forceinline__ v2u sw32(unsigned a, unsigned b) { auto r = __builtin_amdgcn_permlane32_swap(a, b, false, false); return v2u{r[0], r[1]}; }
__device__ __forceinline__ float tadd16(float a, float b) { v2u r = sw16(__float_as_uint(a), __float_as_uint(b)); return __uint_as_float(r.x) + __uint_as_float(r.y); }
__device__ __forceinline__ float tadd32(float a, float b) { v2u r = sw32(__float_as_uint(a), __float_as_uint(b)); return __uint_as_float(r.x) + __uint_as_float(r.y); }
__device__ __forceinline__ float tmax16(float a) { v2u r = sw16(__float_as_uint(a), __float_as_uint(a)); return fmaxf(__uint_as_float(r.x), __uint_as_float(r.y)); }
__device__ __forceinline__ float tmax32(float a) { v2u r = sw32(__float_as_uint(a), __float_as_uint(a)); return fmaxf(__uint_as_float(r.x), __uint_as_float(r.y)); }
__device__ __forceinline__ unsigned umin2(unsigned a, unsigned b) { return min(a, b); }
__device__ __forceinline__ unsigned umax3(unsigned a, unsigned b, unsigned c) { return max(max(a, b), c); }
__device__ __forceinline__ v4u pack_swap(frag_cd a, frag_cd b) { v2u x0 = sw16(cvt_pk(a[0], a[1]), cvt_pk(b[0], b[1])), x1 = sw16(cvt_pk(a[2], a[3]), cvt_pk(b[2], b[3])); return v4u{x0.x, x1.x, x0.y, x1.y}; }
#define MFMA(a, b, c) __builtin_amdgcn_mfma_f32_16x16x32_bf16(__builtin_bit_cast(v8bf, a), __builtin_bit_cast(v8bf, b), c, 0, 0, 0)
#define SMF(a, b, c, ix) __builtin_amdgcn_smfmac_f32_16x16x64_bf16(__builtin_bit_cast(v8bf, a), __builtin_bit_cast(v16bf, b), c, ix, 0, 0)
#define FENCE() asm volatile("" ::: "memory")
#define SB() __builtin_amdgcn_sched_barrier(0)
#define RFL(x) __builtin_amdgcn_readfirstlane(x)
#define BAR() do { FENCE(); __builtin_amdgcn_s_barrier(); FENCE(); } while (0)
#define WAITV(n) do { FENCE(); __builtin_amdgcn_s_waitcnt(0x0F70 | (n)); FENCE(); } while (0)
#define WAITL() asm volatile("s_waitcnt lgkmcnt(0)" ::: "memory")
__device__ __forceinline__ void STB(v4u v, rsrc_t rs, int off, int soff) { __builtin_amdgcn_raw_buffer_store_b128(v, rs, off, soff, 16); asm volatile("s_nop 1" :: "v"(v) : "memory"); }
template <class T> __device__ __forceinline__ LAS T* lp(unsigned a) { return (LAS T*)(size_t)a; }
__device__ __forceinline__ void glds(const void* g, unsigned voff, unsigned lds) { asm volatile("s_mov_b32 m0, %2\n\ts_nop 0\n\tglobal_load_lds_dwordx4 %0, %1" :: "v"(voff), "s"(g), "s"(lds) : "memory"); }
#define NTC " nt"
#define NTW " nt"
__device__ __forceinline__ void gldsc(const void* g, unsigned voff, unsigned lds) { asm volatile("s_mov_b32 m0, %2\n\ts_nop 0\n\tglobal_load_lds_dwordx4 %0, %1" NTC :: "v"(voff), "s"(g), "s"(lds) : "memory"); }
__device__ __forceinline__ void gldsw(const void* g, unsigned voff, unsigned lds) { asm volatile("s_mov_b32 m0, %2\n\ts_nop 0\n\tglobal_load_lds_dwordx4 %0, %1" NTW :: "v"(voff), "s"(g), "s"(lds) : "memory"); }
template <int N> __device__ __forceinline__ void glds1(const void* g, unsigned voff, unsigned lds) {
#define GL(o) "\n\tglobal_load_lds_dwordx4 %0, %1 offset:" #o " sc1"
    if constexpr (N == 1) asm volatile("s_mov_b32 m0, %2\n\ts_nop 0" GL(0) :: "v"(voff), "s"(g), "s"(lds) : "memory");
    if constexpr (N == 2) asm volatile("s_mov_b32 m0, %2\n\ts_nop 0" GL(0) GL(1024) :: "v"(voff), "s"(g), "s"(lds) : "memory");
    if constexpr (N == 4) asm volatile("s_mov_b32 m0, %2\n\ts_nop 0" GL(0) GL(1024) GL(2048) GL(3072) :: "v"(voff), "s"(g), "s"(lds) : "memory");
}
__device__ __forceinline__ rsrc_t mkrs(const void* p, int bytes) { return __builtin_amdgcn_make_buffer_rsrc((void*)p, 0, bytes, 0x00020000); }
__device__ __forceinline__ unsigned gld(const unsigned* p) { return __hip_atomic_load(p, __ATOMIC_RELAXED, __HIP_MEMORY_SCOPE_AGENT); }
__device__ __forceinline__ void gst(unsigned* p, unsigned v) { __hip_atomic_store(p, v, __ATOMIC_RELAXED, __HIP_MEMORY_SCOPE_AGENT); }
__device__ __forceinline__ void gst1(unsigned* p, unsigned v) { asm volatile("s_mov_b64 exec, 1\n\tglobal_store_dword %0, %1, %2 sc1\n\ts_mov_b64 exec, -1" :: "v"(0u), "v"(v), "s"(p) : "memory"); }
__device__ __forceinline__ unsigned rt16() { return (unsigned)__builtin_amdgcn_s_memrealtime() << 4; }

#define NSLOT 12
#define PT(k) do {} while (0)
#define PFLUSH() do {} while (0)
#define MARK(i) do {} while (0)
#define MARKC(i) do {} while (0)
#define MARKW(i) do {} while (0)

#define OSP 0
#define OD0 0
#define FL4 0
#define FV 0
#define NTV ""
#define GAL 2
#define LF 1
#define GDL 0
#define CDL 0
#define XPF 1
#define GHB 1
#define OPF 0
#define GPF 0
#define CPF 0
#define XA3 1
#define GE2 8
#define VQ 700
#define H3X 0
#define EW 30
#define E0 32
#define E2 160
#define E3 120
#define AT 0
#define XH 0
#define XP 0
#define VR 0
#define DWM 4
#define NQ 1130
#ifndef NV
#define NV 960
#endif
#define LXC 200
#define TSL 1
#define RSL 1
#define SO 58
#define TS1 2
#define TS2 16
#define TMAP 1
#define TIF 0
#define TSP 0
#define OPL 1
#define CPL 0
#define TNT 0
#define EF 0
#define TFK 8
#define TS3 0
#define TSPEC 0
#define TD 10
#define VDEL1 15
#define AEARLY 1
#define XC 4
#define ODEL 1
#define SMQ 0x53b
#define SMV 0x79b
#define AL1 1
#define AL2 0
#define AL3 0
#define AL4 1
#define AL5 7
#define AL6 0
#define HB 0
#define HP 0
#define H3 0
#define HSM 0x2d
#define PF 22
#define US 1
#define ROT 0
#define VL 1
#define VP 0x3911
#define VE 0
#define VN3 36
#define CV 0
#define CD 60
#define XS1 0
#define XS2 6
#define PLO 0
#define MSP 1
#define VD 260
#define LK 300
#define LT 400
#define NS 16
#define GS 6
#define KC 560
#define GM 200
#define SGAP 60
#define HSL 6
#define CSL 3
#define WK 40
#define WKO 100
#define WB 200
#define WC1 0
#define WC2 64
#define WTHR 340
#define SLIP_THR 400
#define CKA 0.75
#define CKF 22.5
#define TG_THR 480
#define SLIP_ADD 640
#define SLIP_INC 10
__device__ __forceinline__ void endpgm() { asm volatile("s_endpgm" ::: "memory"); }
__device__ __forceinline__ void fatal(unsigned* err) { asm volatile("global_store_dword %0, %1, off sc1\n\ts_waitcnt vmcnt(0)\n\ts_endpgm" :: "v"(err), "v"(1u) : "memory"); }
__device__ __forceinline__ unsigned ldsr(unsigned p) { return __hip_atomic_load(lp<unsigned>(p), __ATOMIC_RELAXED, __HIP_MEMORY_SCOPE_WORKGROUP); }
__device__ __forceinline__ v2u trd(unsigned p) { return __builtin_bit_cast(v2u, __builtin_amdgcn_ds_read_tr16_b64_v4i16(lp<v4s_t>(p))); }
__device__ __forceinline__ v8u cat(v2u a, v2u b, v2u c, v2u d) { return v8u{a.x, a.y, b.x, b.y, c.x, c.y, d.x, d.y}; }
__device__ __forceinline__ v4u cat2(v2u a, v2u b) { return v4u{a.x, a.y, b.x, b.y}; }
template <int SL, int FAR = 0> __device__ __forceinline__ void flags_wait(unsigned& v, const unsigned* p, unsigned need, unsigned* err) {
    for (int i = 0; __builtin_amdgcn_ballot_w64(v < need) != 0; ++i) {
        if (i) { if (FAR && __builtin_amdgcn_ballot_w64(v + 1 < need) != 0) __builtin_amdgcn_s_sleep(FAR); __builtin_amdgcn_s_sleep(SL); }
        if (v < need) v = gld(p);
        if (i > GCAP) fatal(err); }
    FENCE();
}
template <int SL> __device__ __forceinline__ void flags_wait(const unsigned* p, int nw, unsigned need, int l, unsigned* err) { unsigned v = need - 1; flags_wait<SL>(v, p + min(l, nw - 1) * 32, need, err); }

__device__ __forceinline__ void gadd1(unsigned* p) { asm volatile("s_mov_b64 exec, 1\n\tglobal_atomic_add %0, %1, off sc1\n\ts_mov_b64 exec, -1" :: "v"(p), "v"(1u) : "memory"); }
__device__ __forceinline__ void gld_req(unsigned& v, const unsigned* p) { asm volatile("global_load_dword %0, %1, off sc1" : "+v"(v) : "v"(p) : "memory"); }
template <int N> __device__ __forceinline__ void gld_wait(unsigned& v) { asm volatile("s_waitcnt vmcnt(%1)" : "+v"(v) : "n"(N) : "memory"); }
__device__ __forceinline__ void ctl_wait(const Args& a, unsigned hb) {
    for (int i = 0; RFL(gld(a.ctl + C_HB)) == RFL(hb); ++i) { __builtin_amdgcn_s_sleep(8); if (i > GCAP) fatal(a.ctl + C_ERR); }
    FENCE();
}

template <class F> __device__ __forceinline__ void ahead(const bf16_t* W, int h, int l, unsigned junk, F row) {
    const unsigned o = (384 - (unsigned)(u64)W) & 896; const int d = (h - (o >> 8)) & 3;
    if (d) {
#pragma unroll 1
        for (int i = 0; i < 4; ++i) gldsw(W, (row(d) + 8 * i + (l >> 3)) * (HD * 2) + (h >> 2) * 1024 + o + (l & 7) * 16, junk); }
}

typedef unsigned v16u __attribute__((ext_vector_type(16)));
typedef unsigned v32u __attribute__((ext_vector_type(32)));
#define STR_(x) #x
#define STR(x) STR_(x)
#define SET(x) "\n.set " #x ", " STR(x)
#define A_STAMPS "0"
#define A_BENCH "0"
static_assert(NH * 24576 == 0xC0000, "");
#define A_CHAIN ".set STAMPS, " A_STAMPS "\n.set BENCH, " A_BENCH "\n.set WB, " STR(WB) "\n.set WC1, " STR(WC1) "\n.set WC2, " STR(WC2) SET(SMQ) SET(SMV) SET(AL1) SET(AL2) SET(AL3) SET(AL4) SET(AL5) SET(AL6) SET(HB) SET(US) SET(PF) SET(HP) SET(H3) "\n.set HS, " STR(HSM) SET(VL) SET(VP) SET(VE) SET(VN3) SET(CV) SET(CD) SET(XS1) SET(XS2) SET(PLO) SET(CSL) SET(MSP) SET(VD) SET(LK) SET(LT) SET(NS) SET(GS) SET(GM) SET(SGAP) SET(EF) SET(NQ) SET(NV) SET(LXC) SET(TSL) SET(RSL) SET(VR) SET(DWM) SET(AT) SET(EW) SET(E0) SET(E2) SET(E3) SET(HSL) SET(XA3) SET(VQ) "\n.set WK, " STR(WK) "\n.set WN, 64\n.set WZ, WN+5\n.set WTHR, " STR(WTHR) "\n" R"(
.macro SLED n
  s_getpc_b64 vcc
4: s_sub_u32 s77, (3f-4b)/4, s77
  s_lshl2_add_u32 vcc_lo, s77, vcc_lo
  s_addc_u32 vcc_hi, vcc_hi, 0
  s_setpc_b64 vcc
.rept \n
  s_nop 1
.endr
3: s_waitcnt lgkmcnt(0)
.endm
.macro WAITC T, thr
  s_movk_i32 s76, 0x1000
1: s_memrealtime vcc
  s_memtime s[6:7]
  s_sub_u32 s76, s76, 1
  s_cbranch_scc1 3f
  s_waitcnt lgkmcnt(0)
  s_lshl_b32 s77, vcc_lo, 4
  s_sub_i32 s77, \T, s77
  s_cmp_ge_i32 s77, \thr
  s_cbranch_scc1 1b
.endm
.macro WAITT T
  WAITC \T, 64
3: s_waitcnt lgkmcnt(0)
.endm
.macro WAITP T
  WAITC \T, WTHR
  s_sub_i32 s77, s77, WC1
  s_cmp_lt_i32 s77, WB
  s_cbranch_scc1 5f
.irp r,30,32,34,36,38,94,96,98
  s_memrealtime s[\r:\r+1]
  s_nop 1
.endr
  s_memtime s[6:7]
  s_sub_i32 s77, \T, WC2
  s_sub_i32 s4, s4, EW
  s_waitcnt lgkmcnt(0)
  s_add_u32 s30, s30, s32
  s_add_u32 s34, s34, s36
  s_add_u32 s38, s38, s94
  s_add_u32 s96, s96, s98
  s_add_u32 s30, s30, s34
  s_add_u32 s38, s38, s96
  s_add_u32 s30, s30, s38
  s_lshl_b32 s30, s30, 1
  s_sub_i32 s77, s77, s30
5: s_max_i32 s77, s77, 0
  s_mulk_i32 s77, 45
  s_lshr_b32 s77, s77, 8
  s_sub_i32 s6, s4, s6
  s_ashr_i32 s6, s6, 3
  s_max_i32 s77, s77, s6
  s_min_u32 s77, s77, WN
  SLED WN
.endm
.macro WAITK x, nom
  s_add_i32 s5, s59, \x-\nom
  s_add_i32 s75, s92, s59
  s_movk_i32 s76, 0x1000
  s_add_i32 s75, s75, \x
1: s_memtime vcc
  s_sub_u32 s76, s76, 1
  s_cbranch_scc1 3f
  s_waitcnt lgkmcnt(0)
  s_sub_i32 s77, s75, vcc_lo
  s_sub_i32 s7, s6, vcc_lo
  s_add_i32 s7, s7, s5
  s_max_i32 s77, s77, s7
  s_cmp_ge_i32 s77, 8*WK
  s_cbranch_scc1 1b
  s_max_i32 s77, s77, 0
  s_lshr_b32 s77, s77, 3
  SLED WK
.endm
.macro H1ST
.if AT == 1
  buffer_atomic_swap v[RES], v[ST1], s[40:43], s66 offen sc1
.elseif AT == 2
  buffer_atomic_and v[RES], v[ST1], s[40:43], s66 offen sc1
.elseif AT == 3
  buffer_store_dword v[RES], v[ST1], s[40:43], s66 offen sc0 sc1
.elseif AT == 4
  buffer_store_dword v[RES], v[ST1], s[40:43], s66 offen sc1 nt
.else
  buffer_store_dword v[RES], v[ST1], s[40:43], s66 offen sc1
.endif
.endm
.macro ALN k
.p2align 6
.rept \k
  s_nop 0
.endr
.endm
.macro HT n, r
.rept \n
  v_mov_b32 v[\r], 0
  s_nop 1
.endr
.endm
.macro TMAX a, t
  v_readlane_b32 \t, v[\a], 0
.irp r,1,2,3
  v_readlane_b32 s77, v[\a], \r
  s_sub_i32 s77, s77, \t
  s_max_i32 s77, s77, 0
  s_add_i32 \t, \t, s77
.endr
.endm
.macro TGET a
  v_mbcnt_lo_u32_b32 v[\a+1], 7, 0
  v_lshlrev_b32 v[\a+1], 7, v[\a+1]
  s_add_i32 s75, s88, s68
8: buffer_load_dword v[\a], v[\a+1], s[40:43], s75 offen sc1
  s_sub_u32 s71, s71, 1
  s_cbranch_scc1 L_fatal
  s_waitcnt vmcnt(0)
  v_cmp_eq_u32 vcc, -1, v[\a]
  s_cbranch_vccz 9f
  s_sleep TSL
  s_branch 8b
9:
  TMAX \a, s75
  s_mov_b32 s4, s92
  WAITP s75
  s_mov_b32 s76, 0x400000
.endm
.macro PLAN
  s_mulk_i32 s74, PF
  s_ashr_i32 s74, s74, 3
  s_add_i32 s73, s73, s58
  s_add_i32 s73, s73, s74
.endm
.macro STAMP k
.if STAMPS && ((SMK >> \k) & 1)
  s_memtime s[8+2*\k:9+2*\k]
.endif
.endm
.macro NEXT t
.if STAMPS
  s_waitcnt lgkmcnt(0)
.irp k,0,1,2,3,4,5,6,7,8,9,10
  v_writelane_b32 v[\t], s[8+2*\k], \k
.endr
  s_lshl_b32 s76, s64, 3
  s_add_i32 s76, s76, s63
  s_mul_i32 s76, s76, 48
  v_mbcnt_lo_u32_b32 v[\t+1], -1, 0
  v_lshl_add_u32 v[\t+1], v[\t+1], 2, s76
  s_mov_b64 exec, 0x7ff
  ds_write_b32 v[\t+1], v[\t]
  s_mov_b64 exec, -1
.endif
  s_lshl_b32 s72, s64, 18
  s_mov_b32 s64, s65
  s_add_i32 s65, s65, 1
  s_xor_b32 s66, s66, 0x8000
  s_xor_b32 s67, s67, 0x8000
  s_mov_b32 s76, s68
  s_mov_b32 s68, s69
  s_mov_b32 s69, s70
  s_mov_b32 s70, s76
.endm
.macro RETRY c
.if BENCH
  s_bitset1_b64 s[80+2*\c:81+2*\c], s64
.endif
  s_sub_u32 s71, s71, 1
  s_cbranch_scc1 L_fatal
  s_sleep RSL
.endm
.macro LD d, a, so, o
  buffer_load_dwordx4 v[\d:\d+3], v[\a], s[40:43], \so offen offset:\o sc1
.endm
.macro SM d, a, b
  v_smfmac_f32_16x16x64_bf16 v[\d:\d+3], v[\a:\a+3], v[\b:\b+7], v[SIX]
.endm
.macro MX0 a
  v_max_u32 v[MX], v[\a], v[\a+1]
  v_max3_u32 v[MX], v[MX], v[\a+2], v[\a+3]
.endm
.macro MX1 a
  v_max3_u32 v[MX], v[MX], v[\a], v[\a+1]
  v_max3_u32 v[MX], v[MX], v[\a+2], v[\a+3]
.endm
.macro AW r, k
  ds_write_addtid_b32 v[\r] offset:63744+256*\k
.endm
.macro HS a
  v_pk_add_f32 v[\a:\a+1], v[\a:\a+1], v[\a+2:\a+3]
.endm
.macro ZR a, n
.set zi, 0
.rept \n
  v_mov_b64 v[\a+zi:\a+zi+1], 0
.set zi, zi+2
.endr
.endm
.macro PRE
  s_mov_b32 s64, 63
  s_mov_b32 s65, 0
  s_mov_b32 s66, 0x8000
  s_mov_b32 s67, 0
  s_mov_b32 s68, 0x8000
  s_mov_b32 s69, 0x10000
  s_mov_b32 s70, 0
.endm
.macro XSL r
  s_and_b32 s75, \r, 3
  s_mulk_i32 s75, 20480
.endm
.macro CAP
  s_sub_u32 s76, s76, 1
  s_cbranch_scc1 L_fatal
.endm
  s_mov_b32 s71, 0x40000
  s_mov_b32 s88, s64
  s_mov_b32 s90, s65
  s_mov_b32 s91, s66
  s_mov_b64 s[80:81], 0
  s_mov_b64 s[82:83], 0
  s_mov_b64 s[84:85], 0
  s_mov_b64 s[86:87], 0
  s_mov_b32 s100, s67
  s_mov_b32 s101, s68
  s_cmp_gt_u32 s63, 3
  s_cbranch_scc1 LV_init
; ======== waves 0..3
.set SMK, SMQ
.set O3,168
.set ST1,169
.set RW1,170
.set RW2,171
.set RW3,172
.set XA,173
.set FW,174
.set FR,175
.set SIX,176
.set STK,177
.set FL,183
.set MB,128
.set AC,144
.set TAG,160
.set MX,161
.set RES,162
.set FA,163
.set FB,164
.set VA,164
.set VB,184
.set WV,188
.set RA,134
.set KX,196
.set XF,177
.set XC,)" STR(XC) R"(
.macro MEMQ
  LD MB, O3, s68, 0
  LD MB+4, O3, s68, 1024
  LD MB+8, O3, s68, 2048
  LD MB+12, O3, s68, 3072
.endm
.macro XCHK R
  s_add_i32 s75, s64, 2
  s_min_u32 s75, s75, 63
  v_cmp_ge_u32 vcc, s75, v[FL]
  s_cbranch_vccnz LX_w\R
LX_g\R:
  s_add_i32 s75, s64, 2
  XSL s75
  s_add_i32 m0, s75, s61
.endm
.macro XL4 a
.irp o,0,1024,2048,3072
  global_load_lds_dwordx4 v[\a], s[52:53] offset:\o sc1
.endr
  s_add_i32 m0, m0, 0x1000
.endm
.macro XREF R
  s_add_i32 s75, s64, 3
  s_min_u32 s75, s75, 63
  v_cmp_ge_u32 vcc, s75, v[FL]
  s_cbranch_vccz LX_n\R
  global_load_dword v[FL], v[XF], s[54:55] sc1
LX_n\R:
.endm
.macro XADV
  s_cmp_lt_u32 s64, 61
  s_cselect_b32 s75, 0xC0000, 0
  s_add_u32 s52, s52, s75
  s_addc_u32 s53, s53, 0
.endm
.macro XWAIT R
LX_w\R:
  s_mov_b32 s76, 0x100000
.if \R == 3
  s_waitcnt vmcnt(0)
  v_cmp_ge_u32 vcc, s75, v[FL]
  s_cbranch_vccz LX_g\R
.endif
1: global_load_dword v[FL], v[XF], s[54:55] sc1
  s_waitcnt vmcnt(0)
  v_cmp_ge_u32 vcc, s75, v[FL]
  s_cbranch_vccz LX_g\R
  s_sleep 4
  CAP
  s_branch 1b
.endm
.macro OXST
  buffer_store_dwordx4 v[KX:KX+3], v[STK], s[44:47], s72 offen sc1
  buffer_store_dwordx4 v[KX+4:KX+7], v[STK], s[44:47], s72 offen offset:1024 sc1
.endm
.macro FLAGQ val
  v_mov_b32 v[MX], 0
  v_mov_b32 v[FA], \val
  s_mov_b64 exec, 1
  global_store_dword v[MX], v[FA], s[50:51] sc1
  s_mov_b64 exec, -1
.endm
.macro SMW i
  SM VA, MB+4*\i, WV+8*\i
  SM VB, MB+4*\i, WV+32+8*\i
.endm
.macro SMN k
.if (VP >> \k) & 1
  SM VA+(VB-VA)*(vn&1), MB+4*(vn>>1), WV+32*(vn&1)+8*(vn>>1)
.set vn, vn+1
.endif
.endm
.macro VOUT
  v_add_f32 v[VA], v[VA], v[VA+2]
  v_add_f32 v[VA+1], v[VA+1], v[VA+3]
  v_add_f32 v[VB], v[VB], v[VB+2]
  v_add_f32 v[VB+1], v[VB+1], v[VB+3]
  AW VA, 0
  AW VA+1, 2
  AW VB, 1
  AW VB+1, 3
  AW TAG, 6
.endm
.macro DM k
.if (HS >> \k) & 1
  v_mfma_f32_16x16x32_bf16 v[VA:VA+3], v[MB:MB+3], v[MB:MB+3], 0
.endif
.endm
.macro ZA
  ZR AC, 8
  ZR VA, 2
  ZR VB, 2
.endm
.macro PHASEA R
LQ_loop\R:
  STAMP 0
  ZA
  v_mov_b32 v[TAG], s65
LQ_bar\R:
  s_barrier
  STAMP 1
.if MSP
  LD MB, O3, s68, 0
  s_memtime s[92:93]
  s_nop MSP-1
  LD MB+4, O3, s68, 1024
  s_mov_b32 m0, s100
  s_nop MSP-1
  LD MB+8, O3, s68, 2048
  s_nop MSP
  LD MB+12, O3, s68, 3072
.else
  s_memtime s[92:93]
  MEMQ
  s_mov_b32 m0, s100
.endif
.set XS, 0
.if \R == 2
.set XS, 2
.irp i,0,1,4,5,6,7,8,9,10,11,12,13,14,15
  ds_read_b128 v[WV+4*\i:WV+4*\i+3], v[XA] offset:4096+1024*\i
.if \i == 9
  s_waitcnt lgkmcnt(6)
.endif
.endr
.rept )" STR(ODEL) R"(
  s_nop 7
.endr
  OXST
  s_nop 1
.irp i,2,3
  ds_read_b128 v[WV+4*\i:WV+4*\i+3], v[XA] offset:4096+1024*\i
.endr
  s_waitcnt lgkmcnt(0)
.endif
  s_mov_b32 s76, 0x400000
  ALN AL1
LQ_a\R:
  s_waitcnt vmcnt(3+XS)
  SM AC+8, MB, 64
  v_max_u32 v[MX], v[MB], v[MB+1]
  SM AC+12, MB, 96
  v_max3_u32 v[MX], v[MX], v[MB+2], v[MB+3]
  SM AC, MB, 0
  SM AC+4, MB, 32
  SMW 0
.irp i,1,2,3
  s_waitcnt vmcnt(3+XS-\i)
  SM AC+8, MB+4*\i, 64+8*\i
  v_max3_u32 v[MX], v[MX], v[MB+4*\i], v[MB+4*\i+1]
  SM AC+12, MB+4*\i, 96+8*\i
  v_max3_u32 v[MX], v[MX], v[MB+4*\i+2], v[MB+4*\i+3]
  SM AC, MB+4*\i, 8*\i
.if \i == 3
  v_cmp_eq_u32 vcc, -1, v[MX]
.endif
  SM AC+4, MB+4*\i, 32+8*\i
.if \i < VL
  SMW \i
.endif
.endr
  STAMP 2
  v_add_f32 v[AC+8], v[AC+8], v[AC+10]
  v_add_f32 v[AC+12], v[AC+12], v[AC+14]
  v_add_f32 v[AC+9], v[AC+9], v[AC+11]
  v_add_f32 v[AC+13], v[AC+13], v[AC+15]
  v_add_f32 v[AC+3], v[AC+1], v[AC+3]
  v_add_f32 v[AC], v[AC], v[AC+2]
  v_add_f32 v[AC+5], v[AC+5], v[AC+7]
  s_cbranch_vccnz LQ_ra\R
.set vn, 2*VL
  AW AC+8, 0
  SMN 0
  AW AC+12, 1
  SMN 1
  AW AC+9, 2
  SMN 2
  AW AC+13, 3
  SMN 3
  AW AC+3, 4
  SMN 4
  AW AC+5, 5
  SMN 5
  AW TAG, 6
  SMN 6
  STAMP 6
  s_barrier
  SMN 7
  ds_read_b32 v[FA], v[FR]
  SMN 8
  ds_read2st64_b32 v[AC+8:AC+9], v[RW1] offset1:1
  SMN 9
  ds_read2st64_b32 v[AC+10:AC+11], v[RW2] offset1:1
  SMN 10
  ds_read2st64_b32 v[AC+12:AC+13], v[RW3] offset1:1
  SMN 11
  s_add_i32 m0, s100, 0x2000
  v_add_f32 v[AC+1], v[AC+4], v[AC+6]
  SMN 12
  SMN 13
  SMN 14
.if vn != 8
.error "v projection incomplete"
.endif
.if VE
  VOUT
.endif
  s_waitcnt lgkmcnt(5*VE)
LQ_p\R:
  v_cmp_gt_u32 vcc, s65, v[FA]
  v_add_f32 v[AC+8], v[AC+8], v[AC]
  v_add_f32 v[AC+9], v[AC+9], v[AC+1]
  v_add_f32 v[AC+8], v[AC+8], v[AC+10]
  v_add_f32 v[AC+9], v[AC+9], v[AC+11]
  v_add_f32 v[AC+8], v[AC+8], v[AC+12]
  v_add_f32 v[AC+9], v[AC+9], v[AC+13]
  v_cvt_pk_bf16_f32 v[RES], v[AC+8], v[AC+9]
  s_cbranch_vccnz LQ_pb\R
  H1ST
  s_memtime s[6:7]
  STAMP 3
.if !VE
  VOUT
.endif
.if \R != 2
  s_cmp_eq_u32 s64, 63
  s_cbranch_scc1 L_last
.elseif EF
  s_cmp_eq_u32 s64, 63
  s_cbranch_scc1 LQ_ef
LQ_eg:
.endif
.if \R != 3
  WAITK 0, NQ
.endif
  STAMP 4
.endm
.macro COLDA R
LQ_pb\R:
.if BENCH && \R != 3
  s_bitset1_b64 s[86:87], s64
.endif
  CAP
  ds_read_b32 v[FA], v[FR]
  ds_read2st64_b32 v[AC+8:AC+9], v[RW1] offset1:1
  ds_read2st64_b32 v[AC+10:AC+11], v[RW2] offset1:1
  ds_read2st64_b32 v[AC+12:AC+13], v[RW3] offset1:1
  s_waitcnt lgkmcnt(0)
  s_branch LQ_p\R
LQ_ra\R:
  RETRY 2
  MEMQ
  s_add_i32 s59, s59, LXC
  TGET RES
  s_cmp_eq_u32 s77, WZ
  s_cbranch_scc1 7f
  MEMQ
7:
  ZA
  s_waitcnt vmcnt(0)
  s_branch LQ_a\R
.endm
  s_mov_b32 s64, 0
  s_mov_b32 s65, 1
  s_mov_b32 s66, 0
  s_mov_b32 s67, 0x8000
  s_mov_b32 s68, 0x10000
  s_mov_b32 s69, 0
  s_mov_b32 s70, 0x8000
  s_mov_b32 s72, 0
  s_setprio 3
  s_cmp_eq_u32 s60, 2
  s_cbranch_scc1 LQ_init2
  s_cmp_eq_u32 s60, 3
  s_cbranch_scc1 LQ_init3
; ---- waves 0,1: scores against the x rows
  PHASEA 0
  LD MB, 178, s66, 0
  LD MB+4, 178, s66, 1024
  XSL s64
  v_add_u32 v[FA], s75, v[179]
  ds_read_b128 v[MB+8:MB+11], v[FA]
  ds_read_b128 v[MB+12:MB+15], v[FA] offset:1024
  ds_read_b128 v[AC+8:AC+11], v[FA] offset:2048
  ds_read_b128 v[AC+12:AC+15], v[FA] offset:3072
  s_waitcnt lgkmcnt(0)
  ALN AL2
LQ_b0:
  s_waitcnt vmcnt(1)
  v_mfma_f32_16x16x32_bf16 v[AC+4:AC+7], v[MB+12:MB+15], v[MB:MB+3], 0
  MX0 MB
  v_mfma_f32_16x16x32_bf16 v[AC:AC+3], v[MB+8:MB+11], v[MB:MB+3], 0
  s_waitcnt vmcnt(0)
  v_mfma_f32_16x16x32_bf16 v[AC+4:AC+7], v[AC+12:AC+15], v[MB+4:MB+7], v[AC+4:AC+7]
  MX1 MB+4
  v_mfma_f32_16x16x32_bf16 v[AC:AC+3], v[AC+8:AC+11], v[MB+4:MB+7], v[AC:AC+3]
  v_cmp_eq_u32 vcc, -1, v[MX]
  s_nop 5
  s_cbranch_vccnz LQ_rb0
.irp e,0,1,2,3
  v_add_f32_dpp v[AC+\e], v[AC+4+\e], v[AC+\e] quad_perm:[2,3,0,1] row_mask:0xf bank_mask:0xf
.endr
  DM 0
  ds_write_b128 v[180], v[AC:AC+3]
  DM 1
  ds_write_b32 v[FW], v[TAG] offset:2048
  DM 2
  s_barrier
  STAMP 5
  s_setprio PLO
  HT HP, VB
.if XS1
  s_sleep XS1
.endif
  XCHK 0
  s_nop 0
  XL4 181
.if XC == 4
  s_nop 0
  XL4 182
.endif
  XREF 0
.if XS2
  s_sleep XS2
.endif
  XADV
  v_mov_b32 v[FA], s62
  ds_read_b32 v[RA], v[FA] offset:8
  s_waitcnt lgkmcnt(0)
  v_readfirstlane_b32 s59, v[RA]
  NEXT MB
  s_setprio 3
  s_branch LQ_loop0
  COLDA 0
LQ_rb0:
  RETRY 0
.if VQ
  v_or_b32 v[TAG], 0x10000, v[TAG]
.endif
  LD MB, 178, s66, 0
  LD MB+4, 178, s66, 1024
  s_branch LQ_b0
  XWAIT 0
; ---- wave 2: scores against the memory rows; attention of 16 x rows (64 columns)
.set AQ,181
.set AVF,182
.set AVM,183
.set RA,184
.set QX,196
.set OA,212
.set MSK,228
.set LX,237
.set PB,238
.set S1,240
.set K7,244
.set P4,244
.macro MF d, a, b, c
  v_mfma_f32_16x16x32_bf16 v[\d:\d+3], v[\a:\a+3], v[\b:\b+3], \c
.endm
.macro XQ op, r
  v_mov_b32 v[FB], v[\r]
  s_nop 1
  v_permlane16_swap_b32 v[\r], v[FB]
  \op v[\r], v[\r], v[FB]
  v_mov_b32 v[FB], v[\r]
  s_nop 1
  v_permlane32_swap_b32 v[\r], v[FB]
  \op v[\r], v[\r], v[FB]
.endm
.macro QKQ
  LD MB, 178, s66, 0
  LD MB+4, 179, s66, 0
  LD MB+8, 178, s66, 1024
  LD MB+12, 179, s66, 1024
.endm
.macro SOFTX
  v_add_f32 v[AC+6], v[AC+4], v[S1+2]
  v_add_f32 v[AC+7], v[AC+5], v[S1+3]
  v_max3_f32 v[MX], v[LX], v[AC+6], v[AC+7]
  XQ v_max_f32, MX
  v_mul_f32 v[MX], s74, v[MX]
.irp i,6,7,8,9,10,11,12,13,14,15
  v_fma_f32 v[AC+\i], v[AC+\i], s73, v[MX]
.endr
.irp i,6,7,8,9,10,11,12,13,14,15
  v_exp_f32 v[AC+\i], v[AC+\i]
.endr
  v_add_f32 v[LX], v[AC+6], v[AC+7]
.irp i,8,9,10,11,12,13,14,15
  v_add_f32 v[LX], v[LX], v[AC+\i]
.endr
  XQ v_add_f32, LX
  v_rcp_f32 v[LX], v[LX]
  s_nop 0
.irp i,6,7,8,9,10,11,12,13,14,15
  v_mul_f32 v[AC+\i], v[AC+\i], v[LX]
.endr
.irp i,0,1,2,3
  v_cvt_pk_bf16_f32 v[P4+\i], v[AC+8+2*\i], v[AC+9+2*\i]
.endr
  v_cvt_pk_bf16_f32 v[PB], v[AC+6], v[AC+7]
.endm
LQ_init2:
  s_mov_b32 s73, 0x3e0293ee
  s_mov_b32 s74, 0xbe0293ee
  PHASEA 2
  QKQ
  XSL s64
  ds_read_b128 v[MSK:MSK+3], v[XA]
  ds_read_b128 v[MSK+4:MSK+7], v[XA] offset:1024
  v_add_u32 v[FB], s75, v[AQ]
  v_add_u32 v[RES], s75, v[AVF]
  v_mov_b32 v[PB+1], 0
.irp i,0,1,2,3
  ds_read_b128 v[QX+4*\i:QX+4*\i+3], v[FB] offset:16384+1024*\i
  ds_read_b128 v[OA+4*\i:OA+4*\i+3], v[FB] offset:1024*\i
.endr
.irp i,0,1,2
  ds_read_b128 v[RA+4*\i:RA+4*\i+3], v[FB] offset:4096+1024*\i
.endr
  ds_read_b128 v[K7:K7+3], v[FB] offset:7168
  s_waitcnt lgkmcnt(10)
  MF AC+8, OA, QX, v[MSK:MSK+3]
.irp i,4,8,12
  s_waitcnt lgkmcnt(10-\i/2)
  MF AC+8, OA+\i, QX+\i, v[AC+8:AC+11]
.endr
  s_waitcnt lgkmcnt(0)
  MF AC+12, RA, QX, v[MSK+4:MSK+7]
  MF AC+12, RA+4, QX+4, v[AC+12:AC+15]
  MF AC+12, RA+8, QX+8, v[AC+12:AC+15]
  MF AC+12, K7, QX+12, v[AC+12:AC+15]
  v_max3_f32 v[LX], v[AC+8], v[AC+9], v[AC+10]
  s_nop 6
  v_max3_f32 v[LX], v[LX], v[AC+11], v[AC+12]
  v_max3_f32 v[LX], v[LX], v[AC+13], v[AC+14]
  v_max_f32 v[LX], v[LX], v[AC+15]
  STAMP 10
  ALN AL3
LQ_b2:
  s_waitcnt vmcnt(3)
  MX0 MB
  s_waitcnt vmcnt(2)
  v_mfma_f32_16x16x32_bf16 v[AC:AC+3], v[MB+4:MB+7], v[MB:MB+3], 0
  MX1 MB+4
  s_waitcnt vmcnt(1)
  MX1 MB+8
  s_waitcnt vmcnt(0)
  v_mfma_f32_16x16x32_bf16 v[AC:AC+3], v[MB+12:MB+15], v[MB+8:MB+11], v[AC:AC+3]
  MX1 MB+12
  v_cmp_eq_u32 vcc, -1, v[MX]
  s_nop 3
  s_cbranch_vccnz LQ_rb2
  v_add_f32_dpp v[AC], v[AC+2], v[AC] quad_perm:[2,3,0,1] row_mask:0xf bank_mask:0xf
  v_add_f32_dpp v[AC+1], v[AC+3], v[AC+1] quad_perm:[2,3,0,1] row_mask:0xf bank_mask:0xf
  DM 3
  ds_write_b64 v[180], v[AC:AC+1]
  DM 4
  ds_write_b32 v[FW], v[TAG] offset:2048
  DM 5
  s_barrier
  STAMP 5
  s_setprio 0
  MF AC+4, MB+4, QX, 0
  MF S1, MB+4, QX+4, 0
  MF AC+4, MB+12, QX+8, v[AC+4:AC+7]
  MF S1, MB+12, QX+12, v[S1:S1+3]
  FLAGQ s64
.irp e,0,1,2,3
  ds_read_b64_tr_b16 v[MB+4*\e:MB+4*\e+1], v[RES] offset:512*\e
  ds_read_b64_tr_b16 v[MB+4*\e+2:MB+4*\e+3], v[RES] offset:512*\e+4096
.endr
.if )" STR(AEARLY) R"(
  SOFTX
.endif
  v_mov_b32 v[FA], s62
  ds_read_b32 v[FB], v[FA] offset:8
  s_mov_b32 s76, 0x400000
LA_w:
  ds_read_b32 v[FA], v[FW] offset:2304
  CAP
  s_waitcnt lgkmcnt(0)
  v_cmp_gt_u32 vcc, s65, v[FA]
  s_cbranch_vccz LA_g
  s_sleep 1
  s_branch LA_w
LA_g:
  STAMP 8
  v_readfirstlane_b32 s59, v[FB]
  s_setprio 2
.irp e,0,1,2,3
  ds_read_b64_tr_b16 v[RA+2*\e:RA+2*\e+1], v[AVM] offset:256*\e
.endr
.if !)" STR(AEARLY) R"(
  SOFTX
  s_nop 0
.endif
.irp e,0,4,8,12
  MF OA+\e, MB+\e, P4, 0
.endr
  s_waitcnt lgkmcnt(0)
.irp e,0,1,2,3
  v_mfma_f32_16x16x16_bf16 v[OA+4*\e:OA+4*\e+3], v[RA+2*\e:RA+2*\e+1], v[PB:PB+1], v[OA+4*\e:OA+4*\e+3]
.endr
  s_nop 1
.irp i,0,1,2,3,4,5,6,7
  v_cvt_pk_bf16_f32 v[KX+\i], v[OA+2*\i], v[OA+2*\i+1]
.endr
.irp i,0,1,4,5
  v_permlane16_swap_b32 v[KX+\i], v[KX+\i+2]
.endr
  STAMP 9
  s_setprio 3
  NEXT MB
  s_cmp_lg_u32 s64, 64
  s_cbranch_scc1 LQ_loop2
  OXST
  s_waitcnt vmcnt(0)
  FLAGQ 64
  s_branch L_end
  COLDA 2
.if EF
LQ_ef:
  s_waitcnt vmcnt(1)
  FLAGQ s64
  s_branch LQ_eg
.endif
LQ_rb2:
  RETRY 0
.if VQ
  v_or_b32 v[TAG], 0x10000, v[TAG]
.endif
  QKQ
  s_branch LQ_b2
; ---- wave 3: pacing, clearing, sum over the heads
.set CK,144
.set RA,162
.set RC,184
.macro PARTQ
  LD MB, 178, s66, 0
  LD MB+4, 178, s66, 1024
  LD MB+8, 178, s66, 2048
  LD MB+12, 178, s66, 3072
.endm
LQ_init3:
  s_memrealtime vcc
  s_mov_b64 s[44:45], 0
  s_mov_b32 s46, s62
  s_mov_b32 s79, NS*GS
  s_mov_b32 s62, s101
  s_waitcnt lgkmcnt(0)
  s_lshl_b32 s73, vcc_lo, 4
  PHASEA 3
  s_setprio PLO
  v_mov_b32 v[MB+4], s46
  s_memtime s[96:97]
  s_memrealtime s[94:95]
  ds_read_b128 v[VA:VA+3], v[XA] offset:8192
  v_mov_b64 v[MB:MB+1], -1
  v_mov_b64 v[MB+2:MB+3], -1
  v_rcp_f32 v[CK+6], s45
  v_mov_b64 v[CK:CK+1], s[44:45]
  v_cvt_f32_u32 v[CK+4], s57
  v_mul_f32 v[CK+6], s44, v[CK+6]
  s_waitcnt lgkmcnt(0)
  s_sub_u32 s76, s96, s90
  s_sub_u32 s77, s94, s91
  s_mov_b32 s90, s96
  s_mov_b32 s91, s94
  v_mul_f32 v[CK+6], 256.0, v[CK+6]
  v_cvt_f32_u32 v[CK+2], s76
  v_cvt_f32_u32 v[CK+3], s77
  v_cvt_i32_f32 v[CK+6], v[CK+6]
  v_fmac_f32 v[CK+2], )" STR(CKA) R"(, v[CK]
  v_fmac_f32 v[CK+3], )" STR(CKA) R"(, v[CK+1]
  v_rcp_f32 v[CK+5], v[CK+2]
  v_readfirstlane_b32 s51, v[CK+6]
  v_readfirstlane_b32 s44, v[CK+2]
  v_readfirstlane_b32 s45, v[CK+3]
  v_mul_f32 v[CK+3], )" STR(CKF) R"(, v[CK+3]
  v_fma_f32 v[CK+3], v[CK+3], v[CK+5], -1.0
  v_med3_f32 v[CK+3], v[CK+3], -0.5, 1.0
  v_mul_f32 v[CK+3], v[CK+3], v[CK+4]
  v_cvt_i32_f32 v[CK+3], v[CK+3]
  STAMP 7
  s_nop 0
  v_readfirstlane_b32 s47, v[CK+3]
  WAITK VD+CD, NQ
  buffer_store_dwordx4 v[MB:MB+3], v[179], s[40:43], s67 offen sc1
  buffer_store_dwordx2 v[MB:MB+1], v[182], s[40:43], s67 offen sc1
  buffer_store_dwordx2 v[MB:MB+1], v[180], s[40:43], s70 offen sc1
  buffer_store_dwordx2 v[MB:MB+1], v[181], s[40:43], s70 offen sc1
  ds_read_b128 v[RC:RC+3], v[MB+4]
  ds_read_b32 v[RA], v[MB+4] offset:24
.if !XA3
  XCHK 3
.endif
  XREF 3
  STAMP 5
  s_barrier
  s_memrealtime s[94:95]
  s_mov_b32 s76, 0x400000
  s_mov_b32 s78, s73
LQ_t3:
  s_waitcnt lgkmcnt(0)
  v_cmp_gt_u32 vcc, s65, v[RC+3]
  s_cbranch_vccnz LQ_t4
  v_readfirstlane_b32 s73, v[RC]
  v_readfirstlane_b32 s74, v[RC+1]
  v_readfirstlane_b32 s59, v[RC+2]
  v_readfirstlane_b32 s92, v[RA]
  s_sub_i32 s78, s73, s78
  s_cmp_gt_i32 s78, 512
  s_cbranch_scc1 LQ_slip
LQ_b5:
  s_lshl1_add_u32 s4, s74, s57
  s_add_i32 s50, s4, s73
  PLAN
  s_sub_i32 s5, s73, s50
  s_mul_i32 s4, s4, s51
  s_mul_i32 s5, s5, s51
  s_lshr_b32 s4, s4, 12
  s_lshr_b32 s5, s5, 12
  s_add_i32 s4, s4, s92
  s_sub_i32 s4, s4, E2
  s_sub_i32 s5, s5, E3
  s_cmp_lt_u32 s64, NS
  s_cselect_b32 s78, GS, 0
  s_sub_u32 s79, s79, s78
  s_ashr_i32 s47, s47, 1
  s_add_i32 s47, s47, s79
.if XA3
  XCHK 3
.endif
.if CSL
  s_sleep CSL
.endif
  s_lshl4_add_u32 s78, s94, s62
  s_sub_i32 s78, s78, s50
  s_cmp_gt_i32 s78, LT
  s_cselect_b32 s78, s78, -GM
  s_add_i32 s78, s78, s50
  s_add_i32 s78, s78, GM
  s_setprio 3
  WAITP s78
  STAMP 8
  PARTQ
  s_memrealtime vcc
  s_memtime s[6:7]
  v_mov_b32 v[RA], s46
  XL4 XA
  XADV
  ZR AC, 8
  v_add_u32 v[TAG], 1, v[TAG]
  s_waitcnt lgkmcnt(0)
  s_add_i32 s6, s6, s5
  v_mov_b32 v[RA+1], s6
  ds_write_b32 v[RA], v[RA+1] offset:20
LQ_b3:
  s_waitcnt lgkmcnt(0)
.if BENCH
  s_mov_b32 m0, s64
  s_mov_b32 s77, vcc_lo
  v_writelane_b32 v[FW], s77, m0
.endif
  s_lshl_b32 s89, vcc_lo, 4
  s_sub_i32 s89, s89, s50
  s_add_i32 s78, s89, )" STR(SLIP_ADD) R"(
  s_sub_i32 s77, s89, E0
  s_max_i32 s77, s77, 0
  s_cmp_gt_i32 s89, )" STR(SLIP_THR) R"(
  s_cselect_b32 s78, s78, s77
  s_cselect_b32 s77, SGAP, 0
  s_add_i32 s78, s78, s73
  s_and_b32 s78, s78, -2
  s_add_i32 s77, s77, s47
  v_mov_b32 v[RA], s78
  v_mov_b32 v[RA+1], s77
  ALN AL4
  s_waitcnt vmcnt(3+XC)
  v_mfma_f32_16x16x32_bf16 v[RC:RC+3], v[VA:VA+3], v[MB:MB+3], 0
  MX0 MB
.irp i,1,2,3
  s_waitcnt vmcnt(3+XC-\i)
  v_mfma_f32_16x16x32_bf16 v[RC:RC+3], v[VA:VA+3], v[MB+4*\i:MB+4*\i+3], v[RC:RC+3]
  MX1 MB+4*\i
.endr
  v_cmp_eq_u32 vcc, -1, v[MX]
  STAMP 9
  s_nop 3
.irp e,0,1,2,3
  v_add_f32_dpp v[RC+\e], v[RC+\e], v[RC+\e] row_ror:8 row_mask:0xf bank_mask:0xf
.endr
  v_cvt_pk_bf16_f32 v[RC], v[RC], v[RC+1]
  v_cvt_pk_bf16_f32 v[RC+1], v[RC+2], v[RC+3]
  s_cbranch_vccnz LQ_rb3
  buffer_store_dwordx2 v[RC:RC+1], v[180], s[40:43], s69 offen sc1
  buffer_store_dwordx2 v[RA:RA+1], v[181], s[40:43], s69 offen sc1
  STAMP 10
  ZR VA, 2
  ZR VB, 2
  NEXT MB
  STAMP 0
  s_cmp_gt_i32 s89, )" STR(TG_THR) R"(
  s_cbranch_scc0 LQ_bar3
  TGET RA
  s_branch LQ_bar3
  COLDA 3
  XWAIT 3
LQ_rb3:
  RETRY 1
  PARTQ
  s_memrealtime vcc
  s_waitcnt vmcnt(0)
  s_branch LQ_b3
LQ_t4:
  ds_read_b128 v[RC:RC+3], v[MB+4]
  CAP
  s_branch LQ_t3
LQ_slip:
.if BENCH
  s_bitset1_b64 s[86:87], s64
.endif
  s_cmp_lt_u32 s64, NS
  s_cbranch_scc1 LQ_b5
  s_add_i32 s79, s79, )" STR(SLIP_INC) R"(
  s_min_u32 s79, s79, 4000
  s_branch LQ_b5
; ======== waves 4..7
.set SMK, SMV
.set UB,128
.set B1,160
.set O3,168
.set O1,169
.set OT,170
.set ST1,171
.set ST2,172
.set AIM,173
.set RR,175
.set FW,176
.set FR,177
.set FS,178
.set ASC,179
.set ASM,180
.set AVX,181
.set SIX,182
.set RES,184
.set TAG,185
.set MX,186
.set TM,187
.set MB,188
.set AC,204
.set TV,212
.set FA,214
.set RA,216
.set DD,188
.set SX,208
.set SMM,212
.set ZZ,224
.set VM,240
.set PA,248
.set VX,188
.set UU,220
.macro SCV
  ds_read_b32 v[FA], v[FS]
  ds_read_b128 v[SX:SX+3], v[ASC]
  ds_read_b64 v[SMM:SMM+1], v[ASM]
.endm
.macro CZV t
  v_cvt_pk_bf16_f32 v[UB+8*\t+2], v[ZZ+4*\t], v[ZZ+4*\t+2]
  v_cvt_pk_bf16_f32 v[UB+8*\t+3], v[ZZ+4*\t+1], v[ZZ+4*\t+3]
.endm
.macro URD h, r
.irp k,0,1,2,3
  ds_read_b128 v[VX+\r+4*\k:VX+\r+4*\k+3], v[TM] offset:1024*(\h+\k)
.endr
.endm
.macro UMM h, r, n
.irp k,0,1,2,3
  s_waitcnt lgkmcnt(\n+3-\k)
.irp t,0,1,2,3
.if \k == 0
  v_mfma_f32_16x16x32_bf16 v[UU+\r+4*\t:UU+\r+4*\t+3], v[VX+\r:VX+\r+3], v[16*\t:16*\t+3], 0
.else
  v_mfma_f32_16x16x32_bf16 v[UU+\r+4*\t:UU+\r+4*\t+3], v[VX+\r+4*\k:VX+\r+4*\k+3], v[16*\t+4*\k:16*\t+4*\k+3], v[UU+\r+4*\t:UU+\r+4*\t+3]
.endif
.endr
.endr
  s_nop 1
.irp t,0,1,2,3
  v_cvt_pk_bf16_f32 v[UB+8*\t+\h], v[UU+\r+4*\t], v[UU+\r+4*\t+1]
  v_cvt_pk_bf16_f32 v[UB+8*\t+\h+1], v[UU+\r+4*\t+2], v[UU+\r+4*\t+3]
.endr
.endm
.macro UREST
.if US
  UMM 4, 0, 0
.endif
  ZR ZZ, 8
  ZR DD, 10
  v_mov_b32 v[TAG], s65
.endm
.macro STEPV R
LV_loop\R:
  STAMP 0
.if US
  URD 4, 0
.if \R == 3
  ds_read_b32 v[MX], v[183] offset:20
.endif
  s_waitcnt lgkmcnt(0)
.endif
.if \R == 3
  v_readfirstlane_b32 s4, v[MX]
  s_setprio 3
  WAITP s73
  s_memtime s[6:7]
.endif
  s_barrier
  s_memtime s[92:93]
  s_setprio 1
  s_nop )" STR(VDEL1) R"(
  STAMP 1
  UREST
.if \R == 3
  v_mov_b32 v[TV+2], s6
  buffer_load_dwordx2 v[TV:TV+1], v[OT], s[40:43], s68 offen sc1
  ds_write_b32 v[183], v[TV+2] offset:24
.endif
.if \R == 2
  v_mov_b64 v[PA:PA+1], -1
  v_mov_b64 v[PA+2:PA+3], -1
.rept CV
  s_nop 3
.endr
.irp o,0,1024,2048,3072
  buffer_store_dwordx4 v[PA:PA+3], v[ST1], s[40:43], s67 offen offset:\o sc1
.endr
.endif
  s_mov_b32 s76, 0x400000
  s_setprio 2
  STAMP 9
  ALN AL6
  s_barrier
  s_memtime s[6:7]
  STAMP 10
.if \R == 3
  s_cmp_eq_u32 s64, 0
  s_cbranch_scc1 LV_t0
LV_t1:
  s_waitcnt vmcnt(0)
  v_cmp_eq_u32 vcc, -1, v[TV]
  v_subrev_u32 v[TV], s73, v[TV]
  v_max_i32_dpp v[TV+1], v[TV+1], v[TV+1] quad_perm:[1,0,3,2] row_mask:0xf bank_mask:0xf
  s_cbranch_vccnz LV_rt
  v_max_i32_dpp v[TV], v[TV], v[TV] quad_perm:[1,0,3,2] row_mask:0xf bank_mask:0xf
  v_max_i32_dpp v[TV+1], v[TV+1], v[TV+1] quad_perm:[2,3,0,1] row_mask:0xf bank_mask:0xf
  s_nop 0
  v_max_i32_dpp v[TV], v[TV], v[TV] quad_perm:[2,3,0,1] row_mask:0xf bank_mask:0xf
  s_nop 0
  v_readfirstlane_b32 s75, v[TV]
  v_add_u32 v[TV], s73, v[TV]
  v_readfirstlane_b32 s74, v[TV+1]
  s_add_i32 s73, s73, s75
  s_cmp_gt_i32 s75, 512
  s_cselect_b32 s75, )" STR(SLIP_INC) R"(*45/32, 0
  s_cmp_lt_u32 s64, NS
  s_cselect_b32 s75, -(GS*45/32), s75
  s_add_i32 s75, s75, s59
  v_mov_b32 v[TV+2], s75
  v_mov_b32 v[TV+3], s65
  ds_write_b128 v[183], v[TV:TV+3]
  PLAN
.endif
.if \R == 2
  HT HB, FA
.endif
.if \R == 0
.rept VN3
  s_nop 1
.endr
LV_p:
  ds_read_b32 v[FA], v[FR]
.irp k,0,1,2,3
  ds_read2st64_b32 v[RA+2*\k:RA+2*\k+1], v[RR-(\k&1)] offset0:8*\k offset1:8*\k+1
.endr
  s_waitcnt lgkmcnt(4)
  v_cmp_gt_u32 vcc, s65, v[FA]
  s_waitcnt lgkmcnt(2)
  v_add_f32 v[RA], v[RA], v[RA+2]
  v_add_f32 v[RA+1], v[RA+1], v[RA+3]
  s_waitcnt lgkmcnt(0)
  v_add_f32 v[RA+4], v[RA+4], v[RA+6]
  v_add_f32 v[RA+5], v[RA+5], v[RA+7]
  v_add_f32 v[RA], v[RA], v[RA+4]
  v_add_f32 v[RA+1], v[RA+1], v[RA+5]
  v_cvt_pk_bf16_f32 v[RES], v[RA], v[RA+1]
  s_cbranch_vccnz LV_pb
  H1ST
.endif
  STAMP 3
.if \R != 2
  s_cmp_eq_u32 s64, 63
  s_cbranch_scc1 L_last
.endif
  s_waitcnt lgkmcnt(0)
.if \R == 2
  s_waitcnt vmcnt(0)
.endif
  s_sub_i32 s77, s6, s92
  s_cmp_gt_i32 s77, NV+200
  s_cselect_b32 s77, LXC, 0
  s_add_i32 s6, s6, s77
  s_setprio 3
  WAITK VD, NV
  STAMP 4
  LD VM, O1, s66, 0
  LD VM+4, O1, s66, 1024
  s_mov_b32 s76, 0x400000
.if \R == 2
  s_cmp_eq_u32 s64, 63
  s_cbranch_scc1 LV_lst
.endif
  ALN AL5
  s_barrier
  SCV
  STAMP 5
.if VR == 0
  s_waitcnt vmcnt(1)
  SM ZZ, VM, 0
  v_max_u32 v[RES], v[VM], v[VM+1]
  v_max3_u32 v[RES], v[RES], v[VM+2], v[VM+3]
  SM ZZ+4, VM, 16
  SM ZZ+8, VM, 32
  SM ZZ+12, VM, 48
LV_s\R:
  s_waitcnt lgkmcnt(0)
  STAMP 6
  v_cmp_ne_u32 vcc, s65, v[FA]
  v_max3_f32 v[MX], v[SX], v[SX+1], v[SX+2]
  v_max3_f32 v[TM], v[SX+3], v[SMM], v[SMM+1]
  v_max_f32 v[MX], v[MX], v[TM]
  s_cbranch_vccnz LV_rs\R
LV_c\R:
  s_nop 0
  v_max_f32_dpp v[MX], v[MX], v[MX] quad_perm:[2,3,0,1] row_mask:0xf bank_mask:0xf
  v_mov_b32 v[TM], v[MX]
  s_nop 1
  v_permlane16_swap_b32 v[MX], v[TM]
  v_max_f32 v[MX], v[MX], v[TM]
  v_mov_b32 v[TM], v[MX]
  s_waitcnt vmcnt(0)
  SM ZZ, VM+4, 8
  v_permlane32_swap_b32 v[MX], v[TM]
  v_max_f32 v[MX], v[MX], v[TM]
  v_mul_f32 v[MX], s79, v[MX]
  SM ZZ+4, VM+4, 24
  v_fma_f32 v[SX], v[SX], s78, v[MX]
  v_fma_f32 v[SX+1], v[SX+1], s78, v[MX]
  v_fma_f32 v[SMM], v[SMM], s78, v[MX]
  SM ZZ+8, VM+4, 40
  v_fma_f32 v[SMM+1], v[SMM+1], s78, v[MX]
  v_fma_f32 v[SX+2], v[SX+2], s78, v[MX]
  v_fma_f32 v[SX+3], v[SX+3], s78, v[MX]
  SM ZZ+12, VM+4, 56
  v_exp_f32 v[SX], v[SX]
  v_exp_f32 v[SX+1], v[SX+1]
  v_exp_f32 v[SMM], v[SMM]
  v_cvt_pk_bf16_f32 v[PA], v[SX], v[SX+1]
  v_exp_f32 v[SMM+1], v[SMM+1]
  v_cvt_pk_bf16_f32 v[PA+1], v[SMM], v[SMM]
  v_exp_f32 v[SX+2], v[SX+2]
  v_exp_f32 v[SX+3], v[SX+3]
  v_cvt_pk_bf16_f32 v[PA+3], v[SMM+1], v[SMM+1]
  v_cvt_pk_bf16_f32 v[PA+2], v[SX+2], v[SX+3]
.else
.rept DWM
  v_mfma_f32_16x16x32_bf16 v[PA:PA+3], v[B1:B1+3], v[B1:B1+3], 0
.endr
LV_s\R:
  s_waitcnt lgkmcnt(0)
  STAMP 6
  v_cmp_gt_u32 vcc, s65, v[FA]
  v_max3_f32 v[MX], v[SX], v[SX+1], v[SX+2]
  v_max3_f32 v[TM], v[SX+3], v[SMM], v[SMM+1]
  v_max_f32 v[MX], v[MX], v[TM]
  s_cbranch_vccnz LV_rs\R
.if VR == 2
  s_waitcnt vmcnt(1)
  SM ZZ, VM, 0
.else
  s_nop 0
.endif
  v_max_f32_dpp v[MX], v[MX], v[MX] quad_perm:[2,3,0,1] row_mask:0xf bank_mask:0xf
  v_mov_b32 v[TM], v[MX]
.if VR == 2
  SM ZZ+4, VM, 16
  s_nop 0
.else
  s_waitcnt vmcnt(1)
  SM ZZ, VM, 0
.endif
  v_permlane16_swap_b32 v[MX], v[TM]
  v_max_f32 v[MX], v[MX], v[TM]
  v_mov_b32 v[TM], v[MX]
  v_max_u32 v[RES], v[VM], v[VM+1]
.if VR == 2
  SM ZZ+8, VM, 32
.else
  SM ZZ+4, VM, 16
.endif
  v_permlane32_swap_b32 v[MX], v[TM]
  v_max_f32 v[MX], v[MX], v[TM]
  v_mul_f32 v[MX], s79, v[MX]
.if VR == 2
  SM ZZ+12, VM, 48
.else
  SM ZZ+8, VM, 32
.endif
  v_fma_f32 v[SX], v[SX], s78, v[MX]
  v_fma_f32 v[SX+1], v[SX+1], s78, v[MX]
  v_fma_f32 v[SMM], v[SMM], s78, v[MX]
.if VR == 2
  s_waitcnt vmcnt(0)
  SM ZZ, VM+4, 8
.else
  SM ZZ+12, VM, 48
.endif
  v_fma_f32 v[SMM+1], v[SMM+1], s78, v[MX]
  v_fma_f32 v[SX+2], v[SX+2], s78, v[MX]
  v_fma_f32 v[SX+3], v[SX+3], s78, v[MX]
.if VR == 2
  SM ZZ+4, VM+4, 24
.else
  s_waitcnt vmcnt(0)
  SM ZZ, VM+4, 8
.endif
  v_exp_f32 v[SX], v[SX]
.if VR == 2
  SM ZZ+8, VM+4, 40
.else
  SM ZZ+4, VM+4, 24
.endif
  v_exp_f32 v[SX+1], v[SX+1]
.if VR == 2
  SM ZZ+12, VM+4, 56
.else
  SM ZZ+8, VM+4, 40
.endif
  v_exp_f32 v[SMM], v[SMM]
.if VR == 1
  SM ZZ+12, VM+4, 56
.endif
  v_cvt_pk_bf16_f32 v[PA], v[SX], v[SX+1]
  v_exp_f32 v[SMM+1], v[SMM+1]
  v_cvt_pk_bf16_f32 v[PA+1], v[SMM], v[SMM]
  v_exp_f32 v[SX+2], v[SX+2]
  v_exp_f32 v[SX+3], v[SX+3]
  v_cvt_pk_bf16_f32 v[PA+3], v[SMM+1], v[SMM+1]
  v_cvt_pk_bf16_f32 v[PA+2], v[SX+2], v[SX+3]
  v_max3_u32 v[RES], v[RES], v[VM+2], v[VM+3]
.endif
LV_z\R:
  CZV 0
  SM DD+16, PA, B1
  CZV 1
  SM DD, PA, UB
  CZV 2
  v_max3_u32 v[RES], v[RES], v[VM+4], v[VM+5]
  SM DD+4, PA, UB+8
  CZV 3
  v_max3_u32 v[RES], v[RES], v[VM+6], v[VM+7]
  SM DD+8, PA, UB+16
  v_add_f32 v[DD+16], v[DD+16], v[DD+18]
  v_add_f32 v[DD+17], v[DD+17], v[DD+19]
  v_cmp_eq_u32 vcc, -1, v[RES]
  SM DD+12, PA, UB+24
  v_rcp_f32 v[DD+16], v[DD+16]
  v_rcp_f32 v[DD+17], v[DD+17]
  s_cbranch_vccnz LV_rb\R
  HS DD
  HS DD+4
  HS DD+8
  v_pk_mul_f32 v[DD:DD+1], v[DD:DD+1], v[DD+16:DD+17]
  v_pk_mul_f32 v[DD+4:DD+5], v[DD+4:DD+5], v[DD+16:DD+17]
  HS DD+12
  v_pk_mul_f32 v[DD+8:DD+9], v[DD+8:DD+9], v[DD+16:DD+17]
  v_cvt_pk_bf16_f32 v[SX], v[DD], v[DD+4]
  v_pk_mul_f32 v[DD+12:DD+13], v[DD+12:DD+13], v[DD+16:DD+17]
  v_cvt_pk_bf16_f32 v[SX+2], v[DD+1], v[DD+5]
  v_cvt_pk_bf16_f32 v[SX+1], v[DD+8], v[DD+12]
  v_cvt_pk_bf16_f32 v[SX+3], v[DD+9], v[DD+13]
  buffer_store_dwordx4 v[SX:SX+3], v[ST2], s[40:43], s66 offen sc1
  STAMP 7
  s_setprio 1
.if \R == 2
LV_img:
  v_add_u32 v[TM], 1024, v[AIM]
  ds_write2_b32 v[AIM], v[VM], v[VM+1] offset1:2
  ds_write2_b32 v[AIM], v[VM+2], v[VM+3] offset0:32 offset1:34
  ds_write2_b32 v[TM], v[VM+4], v[VM+5] offset1:2
  ds_write2_b32 v[TM], v[VM+6], v[VM+7] offset0:32 offset1:34
  ds_write_b32 v[FW], v[TAG] offset:2304
  s_cmp_eq_u32 s64, 63
  s_cbranch_scc1 L_end
.endif
LV_u\R:
  XSL s65
  v_add_u32 v[TM], s75, v[AVX]
  ds_read_b32 v[MX], v[183] offset:8
  URD 0, 0
.if US
  UMM 0, 0, 0
.else
  URD 4, 16
  UMM 0, 0, 4
  UMM 4, 16, 0
.endif
  v_readfirstlane_b32 s59, v[MX]
  STAMP 8
  NEXT MB
  s_branch LV_loop\R
.if \R == 0
LV_pb:
.if BENCH
  s_bitset1_b64 s[86:87], s64
.endif
  CAP
  s_branch LV_p
.endif
.if VQ
LV_q\R:
  s_waitcnt vmcnt(0)
  v_max3_u32 v[FA], v[RES], v[VM+4], v[VM+5]
  v_max3_u32 v[FA], v[FA], v[VM+6], v[VM+7]
  v_cmp_eq_u32 vcc, -1, v[FA]
  s_cbranch_vccz LV_c\R
  s_nop 7
  LD VM, O1, s66, 0
  LD VM+4, O1, s66, 1024
  ZR ZZ, 8
  s_waitcnt vmcnt(1)
  SM ZZ, VM, 0
  v_max_u32 v[RES], v[VM], v[VM+1]
  v_max3_u32 v[RES], v[RES], v[VM+2], v[VM+3]
  SM ZZ+4, VM, 16
  SM ZZ+8, VM, 32
  SM ZZ+12, VM, 48
  s_nop 7
  s_branch LV_c\R
.endif
LV_rb\R:
  RETRY 0
  LD VM, O1, s66, 0
  LD VM+4, O1, s66, 1024
  ZR ZZ, 8
  ZR DD, 10
  s_waitcnt vmcnt(0)
.irp t,0,1,2,3
  SM ZZ+4*\t, VM, 16*\t
  s_nop 7
  SM ZZ+4*\t, VM+4, 16*\t+8
.endr
  v_max_u32 v[RES], v[VM], v[VM+1]
  v_max3_u32 v[RES], v[RES], v[VM+2], v[VM+3]
  s_nop 7
  s_branch LV_z\R
.if \R == 2
LV_lst:
  s_barrier
LV_63:
  s_waitcnt vmcnt(0)
  MX0 VM
  MX1 VM+4
  v_cmp_eq_u32 vcc, -1, v[MX]
  s_cbranch_vccz LV_img
  RETRY 0
  LD VM, O1, s66, 0
  LD VM+4, O1, s66, 1024
  s_branch LV_63
.endif
LV_rs\R:
.if VQ
  v_and_b32 v[FA], 0xffff, v[FA]
  v_cmp_ne_u32 vcc, s65, v[FA]
  s_cbranch_vccz LV_q\R
.endif
.if BENCH
  s_bitset1_b64 s[86:87], s64
.endif
  SCV
  CAP
  s_branch LV_s\R
.if \R == 3
LV_t0:
  s_memrealtime vcc
  s_waitcnt vmcnt(0) lgkmcnt(0)
  s_lshl_b32 s73, vcc_lo, 4
  s_sub_i32 s73, s73, 0x2000
  v_mov_b32 v[TV], s73
  v_mov_b32 v[TV+1], 0
  s_branch LV_t1
LV_rt:
  RETRY 2
  buffer_load_dwordx2 v[TV:TV+1], v[OT], s[40:43], s68 offen sc1
  s_branch LV_t1
.endif
.endm
LV_init:
  s_memrealtime vcc
  PRE
  s_waitcnt lgkmcnt(0)
  s_lshl_b32 s73, vcc_lo, 4
  s_mov_b32 s78, 0x3e0293ee
  s_mov_b32 s79, 0xbe0293ee
  ZR UB, 16
  v_mov_b32 v[B1], 0x3f803f80
  v_mov_b32 v[B1+1], 0x3f803f80
  v_mov_b32 v[B1+2], 0x3f80
  v_mov_b32 v[B1+3], 0x3f80
  v_mov_b32 v[B1+4], 0x3f803f80
  v_mov_b32 v[B1+5], 0x3f803f80
  v_mov_b64 v[B1+6:B1+7], 0
  s_cmp_eq_u32 s60, 2
  s_cbranch_scc1 LV_u2
  s_cmp_eq_u32 s60, 3
  s_cbranch_scc1 LV_u3
  s_branch LV_u0
  STEPV 0
  STEPV 2
  STEPV 3
L_fatal:
  v_mov_b32 v[MX], 0
  v_mov_b32 v[TAG], 1
  global_store_dword v[MX], v[TAG], s[48:49] sc1
  s_waitcnt vmcnt(0)
  s_endpgm
L_last:
  s_barrier
L_end:
  s_waitcnt vmcnt(0) lgkmcnt(0)
)"
static_assert(__builtin_bit_cast(unsigned, SCALE * LOG2E) == 0x3e0293eeu, "");
#define CL10(p) p "0", p "1", p "2", p "3", p "4", p "5", p "6", p "7", p "8", p "9"
#define CL_ST
#define CL_S CL_ST "s4", "s5", "s6", "s7", "s72", "s73", "s74", "s75", "s76", "s77", "s78", "s79", "s88", "s89", "s90", "s91", "s92", "s93", "s94", "s95", "s96", "s97", "s98", "s99", "s100", "s101", CL10("s3"), "vcc", "scc", "memory"

#define WARM() do {} while (0)
__device__ __forceinline__ void chain(const Args& a, const int c, const unsigned L, const unsigned hb) {
    const int tid = threadIdx.x, w8 = RFL(tid >> 6), w = w8 & 3, l = tid & 63;
    const int g = c & 3, aa = c >> 2, h = g * 8 + (aa >> 2), hp = h ^ XH * 8, jj = aa & 3;
    const int n = l & 15, q = l >> 4, hf = (n >> 1) & 1, n2 = (n >> 2) & 1, n3 = n >> 3;
    const unsigned L_EX = L + 114688, L_SC = L + 131072, L_SM = L_SC + 2048, L_FL = L_SM + 512, L_TB = L_FL + 3072, L_VM = L_TB + 256, L_W2 = L + 143360, L_KX = L + 32768, L_VX = L_KX + 8192;
    const rsrc_t rs = mkrs(a.ring, RING_BYTES);
    unsigned* const err = a.ctl + C_ERR;
    const v4u ones = {~0u, ~0u, ~0u, ~0u};
    const unsigned cyc0 = RFL((unsigned)__builtin_amdgcn_s_memtime()), rt0 = RFL((unsigned)__builtin_amdgcn_s_memrealtime());
    if (tid < 384) lp<v2u>(L_FL)[tid] = v2u{0u, 0u};
    const int lg3 = (c & 31) * 128 + n2 * 64 + hf * 32 + (q & 1) * 16 + (n & 1) * 8;
    unsigned m0[4] = {};
    if (w8 == 3) { const bf16_t* p = a.mem0 + ((c & 1) * 4 + n2 * 2 + (q & 1)) * DM + 16 * (c >> 1) + (n & 3);
#pragma unroll
        for (int i = 0; i < 4; ++i) asm volatile("global_load_ushort %0, %1, off offset:%2" : "=v"(m0[i]) : "v"(p), "n"(8 * i) : "memory"); }
    auto marks = [&](auto nl) {
        const int ok = c < B_H3 || (c < B_TS + 2 && !(c & 2)) ? 0 : OOB;
#pragma unroll
        for (int i = 0; i < 2; ++i) STB(ones, rs, (((c & ~1) * 32768 + (c & 1) * 16384 + i * 8192 + tid * 16) | ok), 0);
        if (w8 == 3) asm volatile("s_waitcnt vmcnt(%0)" :: "n"(nl + 2) : "memory");
        asm volatile("" : "+v"(m0[0]), "+v"(m0[1]), "+v"(m0[2]), "+v"(m0[3]) :: "memory");
        const v2u m = {umin2(m0[0] | m0[1] << 16, 0xFFFFFFFEu), umin2(m0[2] | m0[3] << 16, 0xFFFFFFFEu)};
#pragma unroll
        for (int i = 0; i < 2; ++i) __builtin_amdgcn_raw_buffer_store_b64(m, rs, w8 == 3 ? (B_H3 + 4 * (c >> 5) + 2) * 32768 + ((q >> 1) * 2 + n3) * 4096 + lg3 : OOB, i * 16384, 16);
        FENCE();
    };
    if (!LF) marks(std::integral_constant<int, 0>{});
    const unsigned stg = L + w8 * 16384;
    const int rot = 0;
    const int prr = 16 * (q >> 1) + (q & 1) + 4 * n2 + 2 * n3;
    v32u W[6];
    auto trd4 = [&](int k, unsigned p, int o1, int o2) { const v2u t[4] = {trd(p), trd(p + o1), trd(p + o2), trd(p + o2 + o1)};
#pragma unroll
        for (int i = 0; i < 8; ++i) W[k >> 2][(k & 3) * 8 + i] = t[i >> 1][i & 1]; };
    unsigned cal;
    const unsigned d1c = (a.d1 * 45 >> 5) + NS * (GS * 45 / 32) - WKO;
    auto pg = [&](int blk, int x) { return blk * 32768 + (int)((((cal >> (blk >> 6) * 3) ^ ((x ^ blk >> 4) & 1) * 4 ^ ((x >> 1 ^ blk >> 5) & 1)) & 7) << 12); };
    auto h1o = [&](int inner, int x = 0) { return pg(B_H1 + (aa & 28) + (inner >> 12) * 2, g ^ x ^ XP) + (inner & 4095); };
    unsigned mw[8] = {}; (void)mw;
    auto stream = [&](auto issue, auto read, auto adr, bool nt) {
        if (CDL && !(((u64)(a.Wv + hp * DH) >> 8 & 3) == 1 && jj)) __builtin_amdgcn_s_sleep(CDL);
        MARKW(0);
        if (CPF && jj < 3) ahead(jj == 0 ? a.Wq : (jj == 1 ? a.Wk : a.Wv), hp, l, L_W2 + w8 * 1024, [&](int d) { return 256 * w + 64 * d + 32 * (w8 >> 2); });
        issue(0);
        if (LF) marks(std::integral_constant<int, 8>{});
        issue(1);
        MARKW(1);
        WAITV(8); BAR();
        unsigned h2 = hb;
        auto arrive = [&]() { gadd1(a.ctl + C_READY); if (c == 0) gst1(a.ctl + C_ACK, 1u); };
        if (w8 == 3) gld_req(h2, a.ctl + C_HB);
        read(std::integral_constant<int, 0>{}); WAITL(); issue(2);
        gld_wait<8>(h2);
        const bool seen = RFL(h2) != RFL(hb);
        if (seen) arrive();
        read(std::integral_constant<int, 1>{}); WAITL(); issue(3);
        WAITV(8); read(std::integral_constant<int, 2>{});
        WAITV(0); read(std::integral_constant<int, 3>{}); WAITL();
        if (w8 == 3 && !seen) { ctl_wait(a, hb); arrive(); }
    };
    auto start = [&]() {
        if (w8 == 3) {
            for (int i = 0; RFL(gld(a.ctl + C_READY)) < 256; ++i) { if (c == 0) gadd1(a.ctl + C_HB); __builtin_amdgcn_s_sleep(4); if (i > GCAP) fatal(err); }
            FENCE();
            const unsigned v = ~gld(a.ctl + C_CAL + (l & 15)) & 0xffffu;
            unsigned u = (2 * v + dppu<0xB1>(v) + dppu<0x1B>(dppu<HMIR>(v))) * 8 + (l & 7);
            u = min(u, dppu<0xB1>(u)); u = min(u, dppu<0x4E>(u)); u = min(u, dppu<HMIR>(u));
            unsigned t = ((__builtin_amdgcn_readlane(u, 0) & 7) | (__builtin_amdgcn_readlane(u, 8) & 7) << 3) ^ 022;
            *lp<v2u>(L_TB + 16) = v2u{t, cyc0}; *lp<v2u>(L_TB + 8) = v2u{d1c, 0u}; WAITL();
        }
        lp<unsigned>(L_EX + w8 * 2048 + 1536)[l] = 0; WAITL();
        BAR();
        cal = RFL(ldsr(L_TB + 16));
    };
    const unsigned char* xp = a.qkv + h * 24576 + w * 8192 + (w == 2 ? (jj & 1) * 4096 : 0);
    const unsigned char* xq = a.qkv + h * 24576 + 2 * NH * 24576;
    const unsigned xw = w == 3 ? 20480 - XC * 1024 : w * (XC == 4 ? 8192 : 4096);
    const unsigned* fG = a.ctl + C_G + h * 128 + w * 32;
    auto lo = [](const void* p) { return (unsigned)(u64)p; };
    auto hi = [](const void* p) { return (unsigned)((u64)p >> 32); };
    v16u S0 = {lo(a.ring), hi(a.ring), RING_BYTES, 0x20000u, lo(a.ox), hi(a.ox), NSEG * 262144, 0x20000u, lo(err), hi(err), lo(a.ctl + C_M + h * 128 + jj * 32), hi(a.ctl + C_M + h * 128 + jj * 32),
               lo(xq), hi(xq), lo(fG - w * 32), hi(fG - w * 32)};
    v16u S1 = {(unsigned)rot << 10, (unsigned)a.d2, (unsigned)a.per, d1c, (unsigned)w, L_KX + xw, L_TB, (unsigned)w8};
    v8u cnts;
    v16u A;
    const unsigned six = hf ? 0xEEEE : 0x4444;

    if (w8 < 4) {
        { const bf16_t* Wm = (jj >> 1 ? a.Wk : a.Wq) + (size_t)256 * w * HD + hp * DH + 64 * (jj & 1);
          const unsigned p = stg + prr * 128 + (((hf ^ (4 * n2 + 2 * n3)) * 16 + (n & 1) * 8) ^ ((w >> 1) * 64));
          stream([&](int i) { _Pragma("unroll 1") for (int j = 0; j < 8; ++j) gldsc(Wm + (size_t)(64 * (i ^ rot) + 8 * j) * HD, (l >> 3) * (HD * 2) + ((l & 7) ^ (2 * q)) * 16, stg + (i & 1) * 8192 + j * 1024); },
                 [&](auto i) { _Pragma("unroll") for (int t = 0; t < 4; ++t) trd4(4 * t + i, (p ^ (t * 32)) + (i & 1) * 8192, 1024, 4096); },
                 [&](int j, unsigned& vo) { vo = (l >> 3) * (HD * 2) + ((l & 7) ^ (2 * q)) * 16; return Wm + (size_t)(128 + 8 * j) * HD; }, true); }
        MARK(2);
        BAR();
        MARK(100);
        { const unsigned pv = stg + 65536 + prr * 64 + (hf ^ (2 * n2)) * 16 + (n & 1) * 8;
#pragma unroll
          for (int k = 0; k < 8; ++k) trd4(16 + k, (pv ^ ((k >> 2) * 32)) + ((k & 3) ^ rot) * 4096, 512, 2048); }
        WAITL();
        BAR();
        unsigned fv = 0;
        if (w < 3) {
            MARK(101);
            flags_wait<4>(fv, fG, 2, err);
            MARK(102);
#pragma unroll
            for (int i = 0; i < 4; ++i) if (w < 2 || !(i & 1)) glds1<4>(xp + (i >> 1) * (NH * 24576) + (i & 1) * 4096, l * 16, L_KX + (i >> 1) * 20480 + w * 8192 + (i & 1) * 4096);
            WAITV(0);
            MARK(103);
        }
        A[5] = w == 2 ? L_W2 + l * 16 : 16384 + (jj & 1) * 4096 + l * 16;
        A[13] = w < 3 ? xw + l * 16 : 0; A[14] = xw + 4096 + l * 16; A[15] = fv;
        if (w == 2) { const int ck = 8 * (jj >> 1) + hf;
#pragma unroll
            for (int i = 0; i < 8; ++i) lp<unsigned>(A[5] + (i >> 2) * 1024)[i & 3] = 16 * (i >> 2) + 4 * q + (i & 3) > 16 * (jj & 1) + n ? 0xFF800000u : 0u;
#pragma unroll
            for (int i = 0; i < 16; ++i) *lp<v4u>(A[5] + 4096 + i * 1024) = v4u{W[4 + i / 8][i % 8 * 4], W[4 + i / 8][i % 8 * 4 + 1], W[4 + i / 8][i % 8 * 4 + 2], W[4 + i / 8][i % 8 * 4 + 3]};
            A[13] = L_KX + l * 16; A[14] = L_VX + (ck * 16 + 4 * q + (n >> 2)) * 16 + (l & 1) * 8; A[15] = L_VM + ck * 128 + (2 * q + n2) * 16 + (l & 1) * 8; }
        if (w == 3) {
#pragma unroll
            for (int i = 0; i < 4; ++i) lp<unsigned>(A[5] + 8192)[i] = ((n & 7) >> 1) == i ? 0x3F80u << (16 * (n & 1)) : 0u; }
        start();
        MARK(0); MARKC(4);
        const int sl = 2 * (jj & 1) + (w >> 1);
        A[0] = pg(B_H3 + 4 * w, g ^ (H3X)) + (l ^ (w & 1)) * 16;
        A[1] = h1o((jj >> 1) * 2048 + ((sl * 2 + n3) * 2 + (w & 1)) * 128 + (q >> 1) * 64 + n2 * 32 + (q & 1) * 16 + (n & 3) * 4, XH);
        A[2] = L_EX + (w ^ 1) * 2048 + 1024 + l * 4; A[3] = L_EX + (w ^ 2) * 2048 + l * 4; A[4] = L_EX + (w ^ 3) * 2048 + 512 + l * 4;
        A[6] = L_FL + w * 256 + l * 4; A[7] = L_EX + (l & 3) * 2048 + 1536 + l * 4; A[8] = six;
        A[9] = w == 2 ? h * 8192 + jj * 2048 + ((2 * (q & 1) + (q >> 1)) * 16 + n) * 16 : (XC == 4 ? min(w, 2) * 128 : (w == 3 ? 128 + (l & 1) * 128 : 0));
        A[10] = w < 3 ? h1o(l * 16) : pg(2 * aa, g) + l * 16;
        A[11] = w < 2 ? L_KX + w * 4096 + l * 16 : h1o((w == 2 ? 2048 : jj * 1024) + l * 16);
        A[12] = w < 2 ? L_SC + w * 1024 + ((l * 16) ^ ((q & 1) * 32)) : (w == 2 ? L_SM + ((l * 8) ^ ((q & 1) * 16)) : pg(B_H3 + 4 * (c >> 5), (q >> 1) * 2 + n3) + lg3);
        if (w == 3) { A[13] = c % 33 == 0 && l < 4 ? pg(B_TS, l) + (c >> 5) * 128 : OOB; A[14] = h1o(4096 + jj * 512 + l * 8); }
    } else {
        { const bf16_t* Wo = a.Wo + (size_t)h * DH * DM + 256 * jj + 64 * w; const bf16_t* Wv = a.Wv + (size_t)256 * w * HD + hp * DH + 32 * jj;
          const unsigned po = stg + (4 * q + n3 + 16 * n2) * 128 + (((n & 3) * 2) ^ (q & 1)) * 16;
          stream([&](int i) {
                     if (i < 2) { _Pragma("unroll 1") for (int j = 0; j < 8; ++j) (CPL ? glds : gldsc)(Wo + (size_t)(64 * i + 8 * j) * DM, (l >> 3) * (DM * 2) + ((l & 7) ^ (l >> 5)) * 16, stg + i * 8192 + j * 1024); }
                     else { _Pragma("unroll 1") for (int j = 0; j < 8; ++j) glds(Wv + (size_t)(128 * (i - 2) + 16 * j) * HD, (l >> 2) * (HD * 2) + ((l & 3) ^ (2 * (q & 1))) * 16, stg + (i & 1) * 8192 + j * 1024); } },
                 [&](auto i) { _Pragma("unroll") for (int t = 0; t < 4; ++t) if constexpr (i < 2) trd4(2 * t + i, (po ^ ((t >> 1) * 16)) + (t & 1) * 8 + i * 8192, 256, 4096); },
                 [&](int j, unsigned& vo) { vo = (l >> 2) * (HD * 2) + ((l & 3) ^ (2 * (q & 1))) * 16; return Wv + (size_t)(16 * j) * HD; }, false); }
        BAR(); BAR();
        start();
        A[0] = 0; A[1] = h1o(4096 + l * 16); A[2] = pg(B_TS, g) + (l & 3) * 128;
        A[3] = w >= 2 ? pg(2 * aa, g) + l * 16 : h1o(4096 + ((jj * 2 + n3) * 2 + (w & 1)) * 128 + (q & 1) * 64 + (n & 1) * 32 + (q >> 1) * 16 + ((n >> 1) & 3) * 4, XH);
        A[4] = pg(jj * 16 + w * 4 + n3 * 2, n2 * 2 + (q >> 1)) + h * 128 + ((q & 1) * 4 + (n & 3)) * 16;
        A[5] = L_VM + q * 256 + (4 * n3 + 2 * (n & 1) + n2) * 16 + hf * 4;
        A[7] = L_EX + 8192 + (w & 1) * 512 + l * 4; A[6] = A[7] ^ 512;
        A[8] = L_FL + w * 256 + l * 4; A[9] = L_EX + 8192 + (l & 3) * 2048 + 1536 + l * 4; A[10] = L_FL + 2048 + min(l & 3, 2) * 256 + l * 4;
        A[11] = L_SC + q * 512 + hf * 256 + n * 16; A[12] = L_SM + (q & 1) * 256 + hf * 128 + n * 8;
        A[13] = L_VX + q * 256 + (8 * n3 + 4 * hf + 2 * n2 + (n & 1)) * 16; A[14] = six; A[15] = L_TB;
    }
    S1[12] = a.d2 - a.d1 - KC; S1[8] = RFL(pg(B_TS, g)); S1[9] = cyc0; S1[10] = rt0; S1[11] = RFL(L_EX - 63744 + w8 * 2048);
    asm volatile(A_CHAIN : "+{s[40:55]}"(S0), "+{s[56:71]}"(S1), "={s[80:87]}"(cnts), "+{v[168:183]}"(A), "+{v[188:219]}"(W[4]), "+{v[220:251]}"(W[5]) : "{v[0:31]}"(W[0]), "{v[32:63]}"(W[1]), "{v[64:95]}"(W[2]), "{v[96:127]}"(W[3])
                 : "v128", "v129", CL10("v13"), CL10("v14"), CL10("v15"), "v160", "v161", "v162", "v163", "v164", "v165", "v166", "v167", "v184", "v185", "v186", "v187", CL_S);
}

static_assert(NSEG - SO == 6, "");
struct Tile { const bf16_t* W; int ld, wide, perm, keep; };
__device__ __forceinline__ void tile_issue(const Tile& T, int t, unsigned L, int w8, int l) {
    const bf16_t* p = T.W + (T.wide ? (w8 & 3) * 32 * T.ld + (2 * t + (w8 >> 2)) * 128 : (t * 256 + w8 * 32) * T.ld);
    const unsigned vo = (T.perm ? (l >> 5) + (l & 16) : l >> 4) * T.ld * 2 + ((l & 15) ^ (2 * (l >> 4))) * 16;
#pragma unroll 1
    for (int i = 0; i < 8; ++i) { const bf16_t* g = p + i * (T.perm ? 2 : 4) * T.ld; const unsigned v = vo ^ ((i & 2) << 6), d = L + (t & 1) * 65536 + (w8 * 8 + i) * 1024;
        if (T.keep) glds(g, v, d); else gldsw(g, v, d);
        if (OSP && T.perm) __builtin_amdgcn_s_sleep(OSP); }
}
template <class F>
__device__ __forceinline__ void tile_load(const Tile& T, v4u (&wf)[32], unsigned L, int w8, int l, F next) {
    const int n = l & 15, q = l >> 4;
    const unsigned p = L + (8 * q + (n >> 2)) * 256 + ((4 * (w8 & 3) + ((l >> 1) & 1)) ^ (2 * ((n >> 2) | ((q & 1) << 2)))) * 16 + (l & 1) * 8;
    bool more = true;
#pragma unroll 1
    for (int hh = 0; hh < 2; ++hh)
#pragma unroll
    for (int u = 0; u < 2; ++u) {
        if (!more) WAITV(0);
        WAITV(8); BAR();
        if ((w8 >> 2) == hh) {
#pragma unroll
            for (int i = 0; i < 16; ++i) { const unsigned pp = (p ^ ((i & 1) * 32)) + u * 65536 + (i >> 1) * 8192; wf[u * 16 + i] = cat2(trd(pp), trd(pp + 1024)); }
            WAITL(); }
        BAR();
        if (hh == 0) tile_issue(T, u + 2, L, w8, l); else more = next(u);
    }
}
#define NB 8
#define GD 4
template <int XR, bool ROWS, class F>
__device__ __forceinline__ void seg_gemm(const v4u (&wf)[32], unsigned b0, frag_cd (&acc)[4], F side) {
    v4u f[2 * GD];
    const unsigned bb[4] = {b0, ROWS ? b0 ^ 64 : b0, ROWS ? b0 ^ 128 : b0, ROWS ? b0 ^ 192 : b0};
#define RD(ks, mt) asm volatile("ds_read_b128 %0, %1 offset:%2" : "=v"(f[((ks) % GD) * 2 + mt]) : "v"(bb[(ks) & 3]), "n"(ROWS ? ((ks) >> 2) * 256 + mt * 16384 : ((ks) >> 1) * 4096 + ((ks) & 1) * 1024 + mt * 2048) : "memory")
#define MM(ct, mt) do { if (ks == 0) asm volatile("v_mfma_f32_16x16x32_bf16 %0, %1, %2, 0" : "=&v"(acc[2 * ct + mt]) : "v"(wf[ct]), "v"(f[mt])); \
        else asm volatile("v_mfma_f32_16x16x32_bf16 %0, %1, %2, %0" : "+v"(acc[2 * ct + mt]) : "v"(wf[2 * ks + ct]), "v"(f[(ks % GD) * 2 + mt])); } while (0)
#pragma unroll
    for (int ks = 0; ks < GD - 1; ++ks) { RD(ks, 0); RD(ks, 1); }
#pragma unroll
    for (int ks = 0; ks < 16; ++ks) {
        side(2 * ks);
        asm volatile("s_waitcnt lgkmcnt(%0)" :: "n"(2 * (ks + GD - 1 <= 16 ? GD - 2 : 15 - ks) + (ks >= 13 ? XR : 0)));
        MM(0, 0);
        if (ks + GD - 1 < 16) RD(ks + GD - 1, 0);
        MM(0, 1);
        side(2 * ks + 1);
        MM(1, 0);
        if (ks + GD - 1 < 16) RD(ks + GD - 1, 1);
        MM(1, 1);
    }
    asm volatile("s_nop 7" : "+v"(acc[0]), "+v"(acc[1]), "+v"(acc[2]), "+v"(acc[3]));
#undef RD
#undef MM
}
static_assert(GD == 4 && SO == 58, "");
__device__ __forceinline__ float fadd(float a, float b) { float r; asm("v_add_f32 %0, %1, %2" : "=v"(r) : "v"(a), "v"(b)); return r; }
__device__ __forceinline__ float fmul(float a, float b) { float r; asm("v_mul_f32 %0, %1, %2" : "=v"(r) : "v"(a), "v"(b)); return r; }
__device__ __forceinline__ void half_put(unsigned ex, int w8, int l, const frag_cd (&acc)[4]) {
#pragma unroll
    for (int i = 0; i < 4; ++i) *lp<frag_cd>(ex + w8 * 4096 + i * 1024 + l * 16) = acc[i];
    FENCE();
}
struct Half { frag_cd t[4]; unsigned dec; };
__device__ __forceinline__ void half_req(unsigned ex, unsigned dec, Half& x) {
    asm volatile("ds_read_b32 %0, %1" : "=v"(x.dec) : "v"(dec) : "memory");
#pragma unroll
    for (int i = 0; i < 4; ++i) asm volatile("ds_read_b128 %0, %1 offset:%2" : "=v"(x.t[i]) : "v"(ex), "n"(i * 1024) : "memory");
}
template <class F>
__device__ __forceinline__ void half_get(Half& x, frag_cd (&acc)[4], F side) {
    asm volatile("s_waitcnt lgkmcnt(0)" : "+v"(x.t[0]), "+v"(x.t[1]), "+v"(x.t[2]), "+v"(x.t[3]), "+v"(x.dec) :: "memory");
#pragma unroll
    for (int i = 0; i < 4; ++i) { side(i);
#pragma unroll
        for (int e = 0; e < 4; ++e) acc[i][e] = fadd(acc[i][e], x.t[i][e]);
        asm volatile("" : "+v"(acc[i])); }
}
#define GEARLY 2
#define BARI 25
#define PRIO(n) __builtin_amdgcn_s_setprio(n)

__device__ __forceinline__ unsigned topk(const Args& a, int l) {
    const rsrc_t rg = mkrs(a.gate, 2 * S_TOK * 4);
    flags_wait<16>(a.ctl + C_R, 32, 1, l, a.ctl + C_ERR);
    v4u kv[8]; unsigned prev = ~0u, mine = 0;
#pragma unroll
    for (int i = 0; i < 8; ++i) kv[i] = __builtin_amdgcn_raw_buffer_load_b128(rg, S_TOK * 4 + l * 16, i * 1024, 16);
#pragma unroll 1
    for (int c = 0; c < TOPC; ++c) { unsigned mx = 0;
        FENCE();
#pragma unroll
        for (int i = 0; i < 8; ++i)
#pragma unroll
            for (int e = 0; e < 4; ++e) mx = max(mx, kv[i][e] < prev ? kv[i][e] : 0u);
        mx = max(mx, dppu<0x128>(mx)); mx = max(mx, dppu<0x124>(mx)); mx = max(mx, dppu<0x122>(mx)); mx = max(mx, dppu<0x121>(mx));
        { v2u r = sw16(mx, mx); mx = max(r.x, r.y); r = sw32(mx, mx); mx = max(r.x, r.y); }
        prev = RFL(mx); if ((l & 7) == c) mine = prev; }
    return mine;
}
__device__ __forceinline__ unsigned mix(float gt, float omg, float t0, float t1, float b0, float b1) {
    unsigned av = cvt_pk(t0, t1); av = cvt_pk(fmul(gt, lo16(av)), fmul(gt, hi16(av))); const unsigned bv = cvt_pk(fmul(omg, b0), fmul(omg, b1));
    return cvt_pk(fadd(lo16(av), lo16(bv)), fadd(hi16(av), hi16(bv)));
}
struct Finisher {
    rsrc_t rp, rx, rg, ro; int po, frow, xo, w8; unsigned seltok, ly;
    struct Fin { v4u p[4]; v2u xv; float gt; int k; };
    __device__ __forceinline__ Finisher(const Args& a, int j, int w8_, int l, unsigned L_Y) {
        rp = mkrs(a.part, NSEG * 524288); rx = mkrs(a.x, S_TOK * DM * 2); rg = mkrs(a.gate, S_TOK * 4); ro = mkrs(a.out, S_TOK * DM * 2);
        w8 = w8_; po = j * 16384 + w8 * 1024 + l * 16; frow = 16 * (w8 & 1) + (l & 15); xo = (frow * DM + j * 32 + (w8 >> 1) * 16 + 4 * (l >> 4)) * 2;
        seltok = 4095u - (lp<unsigned>(L_Y + 1024)[l & 7] & 4095u); ly = L_Y + ((w8 >> 1) * 32 + (l >> 4) * 8) * 16;
    }
    __device__ __forceinline__ Fin issue(int s, bool on) const { Fin f; int k = on ? 0 : 0x40000000; asm("" : "+s"(k));
#pragma unroll
        for (int i = 0; i < 4; ++i) f.p[i] = __builtin_amdgcn_raw_buffer_load_b128(rp, k + po, s * 524288 + i * 4096, 16);
        f.k = k + xo;
        f.xv = __builtin_amdgcn_raw_buffer_load_b64(rx, f.k, s * 65536, 0);
        f.gt = __uint_as_float(__builtin_amdgcn_raw_buffer_load_b32(rg, k + frow * 4, s * 128, 16));
        return f;
    }
    template <class F> __device__ __forceinline__ void finish(int s, const Fin& f, F side) const {
        const int tok = 32 * s + frow; const float gt = f.gt, omg = rbf(1.0f - gt);
        frag_cd t; v2u o; int k = f.k;
#pragma unroll
        for (int e = 0; e < 4; e += 2) {
            side(e >> 1);
#pragma unroll
            for (int u = e; u < e + 2; ++u) t[u] = fadd(fadd(__uint_as_float(f.p[0][u]), __uint_as_float(f.p[1][u])), fadd(__uint_as_float(f.p[2][u]), __uint_as_float(f.p[3][u])));
            o[e >> 1] = mix(gt, omg, t[e], t[e + 1], lo16(f.xv[e >> 1]), hi16(f.xv[e >> 1])); asm volatile("" : "+v"(o[e >> 1])); }
        for (unsigned mk = (unsigned)__builtin_amdgcn_ballot_w64((seltok >> 4) == (unsigned)(2 * s + (w8 & 1))) & 255u; mk; mk &= mk - 1) {
            const int slot = __builtin_ctz(mk);
            if (__builtin_amdgcn_readlane(seltok, slot) == (unsigned)tok) { *lp<frag_cd>(ly + slot * 16) = t; k = 0x40000000; }
        }
        __builtin_amdgcn_raw_buffer_store_b64(o, ro, k, s * 65536, 0);
    }
};

__device__ __forceinline__ void gwork(const Args& a, const int m, const int h, const unsigned L, const unsigned hb) {
    const int w8 = RFL(threadIdx.x >> 6); int l = threadIdx.x & 63, n = l & 15, q = l >> 4;
    unsigned* const err = a.ctl + C_ERR;
    const unsigned L_EX = L + 131072, L_XS = L_EX, L_HS = L_XS + 16384, L_GU = L_HS + 2048;
    unsigned* const fG = a.ctl + C_G + h * 128 + m * 32;
    const unsigned xo = (l ^ (8 * (w8 & 1))) * 16;
    auto xissue = [&](int s, int i0, int i1) {
        const bf16_t* p = a.x + (size_t)(32 * s + 8 * (w8 & 3)) * DM + (~w8 & 4) * 128; const unsigned d = L + (s & 1) * 65536 + (w8 ^ 4) * 8192;
#pragma unroll
        for (int i = i0; i < i1; ++i) {
            if (i == i0 || i == 4) asm volatile("s_mov_b32 m0, %0" :: "s"(d + (i & 4) * 1024));
            unsigned t; asm volatile("v_xor_b32 %0, %4, %1\n\tglobal_load_lds_dwordx4 %0, %2 offset:%3" : "=&v"(t) : "v"(xo), "s"(p), "n"((i & 3) * 1024), "n"(i * 1040 + (i & 4) * 1024) : "memory"); }
    };
    v4u wf[32];
    unsigned h2 = hb;
    { const Tile T{(m == 0 ? a.Wk : (m == 1 ? a.Wv : a.Wq)) + h * DH, HD, 0, 0, GAL >> m & 1};
      MARK(104);
      if (XPF) glds(a.x, (((h & 7) * 3 + m) * 8 + w8) * 1024 + l * 16, L_HS + w8 * 1024);
      if (GPF) ahead(T.W - h * DH, h, l, L_HS + w8 * 1024, [&](int d) { return 256 * d + 32 * w8; });
      if (GDL && ((u64)T.W >> 8 & 3) != 1) __builtin_amdgcn_s_sleep(GDL);
      tile_issue(T, 0, L, w8, l); tile_issue(T, 1, L, w8, l);
      if (GHB && w8 == 4) gld_req(h2, a.ctl + C_HB);
      tile_load(T, wf, L, w8, l, [&](int b) { if (b == 0 || w8 >= 4) xissue(b, 0, 8); return true; });
    }
    MARK(0);
    const unsigned rb0 = L + (w8 >> 2) * 32768 + n * 1024 + (q ^ n) * 16;
    unsigned pacc[8] = {}, plast = 0; (void)pacc; (void)plast;
    PT(7);
    if (w8 >= 4) {
        const rsrc_t rq = mkrs(a.qkv, NSEG * NH * 24576), rf = mkrs(fG, w8 == 4 ? 4 : 0);
        const int sto = h * 24576 + m * 8192 + (m == 1 ? (w8 & 3) * 64 + l : ((w8 & 2) + (q & 1)) * 64 + (2 * (w8 & 1) + (q >> 1)) * 16 + n) * 16;
        WAITV(8); BAR(); gld_wait<0>(h2);
        if (w8 == 4) { if (RFL(h2) == RFL(hb)) ctl_wait(a, hb); gadd1(a.ctl + C_READY); }
        for (int sg = 0; sg < NSEG; ++sg) {
            PT(0);
            BAR();
            PT(1);
            PRIO(2);
            if (sg <= GEARLY) __builtin_amdgcn_raw_buffer_store_b32(sg, rf, l * 4, 0, 16);
            frag_cd acc[4]; Half hx;
            seg_gemm<5, true>(wf, rb0 + (sg & 1) * 65536, acc, [&](int i) {
                if (i == BARI) { PRIO(3); PT(5); if (sg <= GE2) WAITV(0); WAITV(3); BAR(); PT(6); if (sg <= GE2) __builtin_amdgcn_raw_buffer_store_b32(sg, rf, l * 4, 0, 16); FENCE(); }
                if (i == 26) half_req(L_EX + (w8 & 3) * 4096 + l * 16, L_EX, hx);
                if (i >= 26) xissue((sg + 2) & 63, i - 26, i - 25); });
            PRIO(0);
            PT(2);
            half_get(hx, acc, [&](int i) { if (i & 1) xissue((sg + 2) & 63, 6 + i / 2, 7 + i / 2); });
            v4u r[2];
#pragma unroll
            for (int mt = 0; mt < 2; ++mt) r[mt] = v4u{cvt_pk(acc[mt][0], acc[2 + mt][0]), cvt_pk(acc[mt][1], acc[2 + mt][1]), cvt_pk(acc[mt][2], acc[2 + mt][2]), cvt_pk(acc[mt][3], acc[2 + mt][3])};
            PT(3);
            if (sg == 12) {
#pragma unroll
                for (int i = 0; i < 4; ++i) STB(v4u{~0u, ~0u, ~0u, ~0u}, mkrs(a.ox, NSEG * 262144), h * 8192 + (w8 & 3) * 1024 + l * 16 + (i & 1) * 4096, (SO + 2 * m + (i >> 1)) * 262144); }
#pragma unroll
            for (int mt = 0; mt < 2; ++mt) STB(r[mt], rq, sto + mt * 4096, sg * (NH * 24576));
            __builtin_amdgcn_raw_buffer_store_b32(sg <= GEARLY || sg <= GE2 ? sg : sg - 1, rf, l * 4, 0, 16);
            FENCE();
            if (sg < GEARLY) WAITV(0);
            PT(4);
        }
        PFLUSH();
        WAITV(0); BAR();
        if (w8 == 4) gst1(fG, NSEG);
    } else {
        for (int sg = 0; sg < NSEG; ++sg) {
            frag_cd acc[4];
            WAITL();
            PT(0);
            BAR();
            PT(1);
            PRIO(2);
            seg_gemm<0, true>(wf, rb0 + (sg & 1) * 65536, acc, [&](int i) { if (i == BARI) { PRIO(3); PT(5); WAITV(0); BAR(); PT(6); } });
            PRIO(1);
            PT(2);
            half_put(L_EX, w8, l, acc);
            xissue((sg + 1) & 63, 0, 8);
            PT(3);
            MARK(sg + 1);
        }
        PFLUSH();
        WAITL(); BAR();
        WAITV(0); BAR();
    }
    asm volatile("v_mbcnt_lo_u32_b32 %0, -1, 0\n\tv_mbcnt_hi_u32_b32 %0, -1, %0" : "=v"(l));
    n = l & 15; q = l >> 4;
    if (m != 2) {
        const int j = TMAP ? h : (h & 23) + 8 * m, mt = TMAP ? m : h >> 3 & 1;
        const unsigned stg = L + w8 * 16384, p = stg + (8 * q + (n >> 2)) * 64 + (n & 3) * 8;
#pragma unroll
        for (int hh = 0; hh < 2; ++hh) {
#pragma unroll 1
            for (int i = 0; i < 16; ++i) {
                (TNT ? gldsw : glds)(a.Wo + (size_t)(512 * w8 + 256 * hh + 32 * (i >> 1) + 8 * (i & 1)) * DM + 32 * j, ((l >> 3) + (l & 4) * 4) * (DM * 2) + (l & 3) * 16, stg + i * 1024); WAITV(TIF); __builtin_amdgcn_s_sleep(TSP); }
            WAITV(0);
#pragma unroll
            for (int i = 0; i < 16; ++i) { const unsigned pp = p + (i >> 1) * 2048 + (i & 1) * 32; wf[hh * 16 + i] = cat2(trd(pp), trd(pp + 256)); }
            WAITL();
        }
        MARK(66);
        const rsrc_t ro = mkrs(a.out, S_TOK * DM * 2), ra = mkrs(a.ox, NSEG * 262144);
        const int xo = (16 * mt + n) * DM + j * 32 + (w8 & 1) * 16 + 4 * q;
        const unsigned L_B = L_EX - SO * 2048;
        if (w8 < 2) {
            const unsigned seltok = 4095u - (topk(a, l) & 4095u);
#pragma unroll 1
            for (int s = SO; s < NSEG; ++s) { const v2u xv = *(const v2u*)(a.x + s * 32768 + xo);
                *lp<v4u>(L_B + s * 2048 + w8 * 1024 + l * 16) = v4u{xv.x, xv.y, gld(a.gate + 32 * s + 16 * mt + n), seltok}; }
        }
        BAR();
        MARK(67);
        { unsigned fv = 0; flags_wait<16, 64>(fv, a.ctl + C_M + (16 * w8 + 2 * (l & 7) + mt) * 32, SO - 1, err); }
        auto peek = [&](int s) { FENCE(); const unsigned v = __builtin_amdgcn_raw_buffer_load_b32(ra, w8 * 32768 + mt * 2048 + (l & 7) * 4096 + 1024, s * 262144, 16); FENCE(); return v; };
        unsigned nx = ~0u;
#define TMK(k)
#pragma unroll 1
        for (int s = SO, it = 0; s < NSEG;) {
            for (int i = 0; __builtin_amdgcn_ballot_w64(nx == ~0u) != 0; ++i) { if (i) __builtin_amdgcn_s_sleep(TS1); nx = peek(s); if (++it > GCAP) fatal(err); }
            TMK(0);
            v4u f[16]; frag_cd acc[4] = {};
            unsigned mx = 0;
#pragma unroll
            for (int i = 0; i < 16 + TD; ++i) { const int ks = i - TD;
                SB();
                if (i < 16) f[i] = __builtin_amdgcn_raw_buffer_load_b128(ra, w8 * 32768 + mt * 2048 + l * 16 + (i & 1) * 1024, s * 262144 + (i >> 1) * 4096, 16);
                if (i == 16) TMK(1);
                if (ks == TFK) nx = peek(min(s + 1, NSEG - 1));
                SB();
                if (ks >= 0) { acc[ks & 1] = MFMA(wf[2 * ks], f[ks], acc[ks & 1]); acc[2 + (ks & 1)] = MFMA(wf[2 * ks + 1], f[ks], acc[2 + (ks & 1)]); mx = umax3(umax3(mx, f[ks].x, f[ks].y), f[ks].z, f[ks].w); } }
            SB();
            TMK(2);
            if (__builtin_amdgcn_ballot_w64(mx == ~0u) != 0) {
                nx = ~0u; continue; }
            const unsigned ex = L + (s - SO) * 16384 + w8 * 2048 + l * 16;
            *lp<frag_cd>(ex) = acc[0] + acc[1]; *lp<frag_cd>(ex + 1024) = acc[2] + acc[3];
            if (s % 3 == 0) {
                WAITL(); BAR();
                if (w8 < 6) { const int u = s - (w8 >> 1), o = (w8 & 1) * 1024 + l * 16;
                    v4u b = *lp<v4u>(L_B + u * 2048 + o);
                    frag_cd t = *lp<frag_cd>(L + (u - SO) * 16384 + o);
#pragma unroll
                    for (int i = 1; i < 8; ++i) t += *lp<frag_cd>(L + (u - SO) * 16384 + o + i * 2048);
                    for (unsigned mk = (unsigned)__builtin_amdgcn_ballot_w64((b.w >> 4) == (unsigned)(2 * u + mt)) & 255u; mk; mk &= mk - 1) {
                        const int slot = __builtin_ctz(mk);
                        flags_wait<4>(a.ctl + C_Y + j * 32 + 1, 1, 1, l, err);
                        const frag_cd y = __builtin_bit_cast(frag_cd, __builtin_amdgcn_raw_buffer_load_b128(mkrs(a.ypart + (32 * 28 + j) * 1024, 1024), ((w8 & 1) * 32 + q * 8 + slot) * 16, 0, 16));
                        if ((__builtin_amdgcn_readlane(b.w, slot) & 15) == (unsigned)n) { b.x = cvt_pk(lo16(b.x) + y[0], hi16(b.x) + y[1]); b.y = cvt_pk(lo16(b.y) + y[2], hi16(b.y) + y[3]); }
                    }
                    const float gt = __uint_as_float(b.z), omg = rbf(1.0f - gt);
                    __builtin_amdgcn_raw_buffer_store_b64(v2u{mix(gt, omg, t[0], t[1], lo16(b.x), hi16(b.x)), mix(gt, omg, t[2], t[3], lo16(b.y), hi16(b.y))}, ro, xo * 2, u * 65536, 0); } }
            TMK(3);
            if (TSPEC && s == NSEG - 2) { __builtin_amdgcn_s_sleep(TS3); nx = 0; }
            ++s;
        }
        MARK(65);
        return;
    }
    const rsrc_t ry = mkrs(a.ypart, 32 * 29 * 1024);
    auto tile = [&](int t) { return t < 2 ? Tile{(t ? a.wu : a.wg) + h * 128, FF, 0, 0, 0} : Tile{a.wd + (size_t)h * 128 * DM, DM, 1, 0, 0}; };
    unsigned mykey = 0;
    if (w8 == 0) {
        mykey = topk(a, l);
#pragma unroll 1
        for (int i = 0; i < 16; ++i) glds(a.x + i * 64, (4095u - (mykey & 4095u)) * (DM * 2) + (l >> 3) * 16, L_XS + i * 1024);
    }
    if (h < FF / 128) {
      v4u wm[32];
      Tile T = tile(0);
      tile_issue(T, 0, L, w8, l); tile_issue(T, 1, L, w8, l);
      frag_cd gv[2] = {};
#pragma unroll 1
      for (int ti = 0; ti < 3; ++ti) {
        const Tile N = tile(ti + 1);
        tile_load(T, wm, L, w8, l, [&](int b) { if (ti < 2) tile_issue(N, b, L, w8, l); return ti < 2; });
        T = N;
        if (ti < 2) {
            frag_cd acc[2] = {};
#pragma unroll
            for (int ks = 0; ks < 16; ++ks) { const v4u xf = *lp<v4u>(L_XS + ((w8 >> 2) * 16 + ks) * 512 + q * 128 + (n & 7) * 16); acc[0] = MFMA(wm[2 * ks], xf, acc[0]); acc[1] = MFMA(wm[2 * ks + 1], xf, acc[1]); }
            if (ti == 1) {
                frag_cd g = n < 8 ? gv[0] : gv[1], u = n < 8 ? acc[0] : acc[1];
                if (w8 >= 4) { *lp<frag_cd>(L_GU + (w8 & 3) * 2048 + l * 16) = g; *lp<frag_cd>(L_GU + (w8 & 3) * 2048 + 1024 + l * 16) = u; WAITL(); }
                BAR();
                if (w8 < 4) { g += *lp<frag_cd>(L_GU + w8 * 2048 + l * 16); u += *lp<frag_cd>(L_GU + w8 * 2048 + 1024 + l * 16);
                    float hv[4];
#pragma unroll
                    for (int e = 0; e < 4; ++e) { const float gg = rbf(g[e]); hv[e] = rbf(gg / (1.0f + __expf(-gg))) * rbf(u[e]); }
                    *lp<v2u>(L_HS + w8 * 512 + (2 * (n >> 3) + (q >> 1)) * 128 + (n & 7) * 16 + (q & 1) * 8) = v2u{cvt_pk(hv[0], hv[1]), cvt_pk(hv[2], hv[3])};
                    WAITL(); } }
            gv[0] = acc[0]; gv[1] = acc[1];
        } else {
#pragma unroll
            for (int cb = 0; cb < 4; ++cb) { frag_cd acc[2] = {};
#pragma unroll
                for (int i = 0; i < 4; ++i) { const v4u hf = *lp<v4u>(L_HS + i * 512 + q * 128 + (n & 7) * 16); acc[0] = MFMA(wm[8 * cb + 2 * i], hf, acc[0]); acc[1] = MFMA(wm[8 * cb + 2 * i + 1], hf, acc[1]); }
                STB(__builtin_bit_cast(v4u, n < 8 ? acc[0] : acc[1]), ry, ((w8 & 3) * 28 + h) * 1024 + ((n >> 3) * 32 + q * 8 + (n & 7)) * 16, ((w8 >> 2) * 4 + cb) * (4 * 28 * 1024)); }
        }
        MARK(67 + ti);
      }
      WAITV(0); BAR();
    }
    if (w8 == 0) {
        gst1(a.ctl + C_Y + h * 32, 1u);
        flags_wait<16>(a.ctl + C_Y, FF / 128, 1, l, err);
        frag_cd y = {};
#pragma unroll 1
        for (int i = 0; i < 28; i += 7) { v4u t[7];
#pragma unroll
            for (int u = 0; u < 7; ++u) t[u] = __builtin_amdgcn_raw_buffer_load_b128(ry, l * 16, (h * 28 + i + u) * 1024, 16);
#pragma unroll
            for (int u = 0; u < 7; ++u) y += __builtin_bit_cast(frag_cd, t[u]); }
        const float r = hi16((mykey & 0x80000000u) ? mykey & 0x7fffffffu : ~mykey), rs = rbf(1.0f / (1.0f + __expf(-r)));
#pragma unroll
        for (int e = 0; e < 4; ++e) y[e] = rbf(rbf(y[e]) * rs);
        STB(__builtin_bit_cast(v4u, y), ry, l * 16, (32 * 28 + h) * 1024);
        WAITV(0);
        gst1(a.ctl + C_Y + h * 32 + 1, 1u);
        MARK(120);
    }
}

__device__ __forceinline__ void owork(const Args& a, const int nt, const int kq, const unsigned L, const unsigned hb) {
    const int tid = threadIdx.x, w8 = RFL(tid >> 6), l = tid & 63, j = nt * 4 + kq;
    unsigned* const err = a.ctl + C_ERR;
    const unsigned L_EX = L + 131072, L_DEC = L_EX + 16384, L_Y = L_DEC + 64, L_KEY = L_Y + 1024;
    if (tid < 4) lp<unsigned>(L_DEC)[tid] = 0;
    {
        const bool lead = j == 0 && w8 == 1;
        if (lead) { const rsrc_t rc = mkrs(a.ctl, C_HB * 4);
#pragma unroll
            for (int i = 0; i < 8; ++i) STB(v4u{}, rc, l * 128, i * 8192);
            STB(v4u{}, rc, l * 16, 0); }
        unsigned char* p = a.ring + ((l >> 3 & 1) * 64 + (j >> 1)) * 32768 + ((l ^ (kq & 1) * 4 ^ kq >> 1) & 7) * 4096 + (w8 * 2 + (nt & 1)) * 256 + (kq & 1) * 128;
        unsigned best = 0;
        if (l < 16) gst((unsigned*)p, ~0u);
        if (lead) { WAITV(0); gadd1(a.ctl + C_HB); }
#pragma unroll 1
        for (int i = 0; i < OPF; ++i) {
            const bf16_t* W = i % 3 == 0 ? a.Wk : (i % 3 == 1 ? a.Wv : a.Wq);
            glds(W, ((i < 6 ? 512 : 0) + (nt * 8 + w8) * 8 + (l >> 3)) * (HD * 2) + kq * 2048 + (i / 3 & 1) * 1024 + ((384 - (unsigned)(u64)W) & 896) + (l & 7) * 16, L + 65536 + w8 * 1024); }
        WAITV(0);
#pragma unroll 1
        for (int i = 0; i < 16; ++i) { u64 t1, t2; unsigned v;
            if (lead && !(i & 3)) gadd1(a.ctl + C_HB);
            const u64 pi = ((u64)__builtin_amdgcn_readlane((unsigned)((u64)p >> 32), i) << 32) | (unsigned)__builtin_amdgcn_readlane((unsigned)(u64)p, i);
            asm volatile("s_nop 4\n\ts_memtime %0\n\ts_waitcnt lgkmcnt(0)\n\tglobal_load_dword %2, %3, %4 sc1\n\ts_waitcnt vmcnt(0)\n\ts_memtime %1\n\ts_waitcnt lgkmcnt(0)"
                         : "=&s"(t1), "=&s"(t2), "=&v"(v) : "v"(0u), "s"(pi) : "memory");
            if (l == i) best = ~(unsigned)(t2 - t1); }
        if (l < 16) { gst((unsigned*)p, ~0u); lp<unsigned>(L)[w8 * 16 + l] = best; }
        WAITV(0); WAITL(); BAR();
        if (w8 == 0) {
#pragma unroll
            for (int i = 1; i < 8; ++i) best = max(best, lp<unsigned>(L)[i * 16 + (l & 15)]);
            if (j) ctl_wait(a, hb);
            if (l < 16) __hip_atomic_fetch_max(a.ctl + C_CAL + l, best, __ATOMIC_RELAXED, __HIP_MEMORY_SCOPE_AGENT);
            WAITV(0);
            gadd1(a.ctl + C_READY);
            MARK(66);
        }
        if (lead) for (int i = 0; !RFL(gld(a.ctl + C_ACK)); ++i) { gadd1(a.ctl + C_HB); __builtin_amdgcn_s_sleep(8); if (i > GCAP) fatal(err); }
    }
    BAR();
    {
#pragma unroll 1
        for (int i = 0; i < 16; ++i) glds(a.x + (size_t)(64 * j + 8 * w8) * DM + i * 512, l * 16, L + (w8 * 16 + i) * 1024);
        v4u gw[2], wr[2];
#pragma unroll
        for (int i = 0; i < 2; ++i) { gw[i] = *(const v4u*)(a.gw + 512 * i + 8 * l); wr[i] = *(const v4u*)(a.wr + 512 * i + 8 * l); }
        WAITV(0);
        float res = 0.f;
#pragma unroll 1
        for (int t = 0; t < 8; ++t) { float sg = 0.f, sr = 0.f;
#pragma unroll
            for (int i = 0; i < 2; ++i) { const v4u xv = *lp<v4u>(L + (w8 * 16 + 2 * t + i) * 1024 + l * 16);
#pragma unroll
                for (int e = 0; e < 4; ++e) { const float x0 = lo16(xv[e]), x1 = hi16(xv[e]); const unsigned p = cvt_pk(x0 * lo16(gw[i][e]), x1 * hi16(gw[i][e]));
                    sg += lo16(p) + hi16(p); sr = __builtin_fmaf(x0, lo16(wr[i][e]), __builtin_fmaf(x1, hi16(wr[i][e]), sr)); } }
            float v = tadd32(sg, sr);
            v = tadd16(v, v); v += dppf<ROR8>(v); v += dppf<0x124>(v); v += dppf<0x122>(v); v += dppf<0x121>(v);
            if ((l & 7) == t) res = v; }
        const unsigned rb = cvt_pk(res, 0.f), tok = 64 * j + 8 * w8 + (l & 7);
        const unsigned val = l < 32 ? __float_as_uint(rbf(1.0f / (1.0f + __expf(-lo16(rb))))) : (((rb & 0x8000u) ? ~rb : rb | 0x8000u) << 16) | (4095u - tok);
        if ((l & 31) < 8) gst(a.gate + (l >> 5) * S_TOK + tok, val);
        WAITV(0); BAR();
        if (w8 == 0) gst1(a.ctl + C_R + j * 32, 1u);
        MARK(67);
    }
    v4u wf[32];
    for (int i = 0; i < OD0; ++i) __builtin_amdgcn_s_sleep(32);
    { const Tile T{a.Wo + (size_t)kq * 1024 * DM + nt * 128, DM, 0, 1, OPL};
      tile_issue(T, 0, L, w8, l); tile_issue(T, 1, L, w8, l);
      tile_load(T, wf, L, w8, l, [](int) { return false; }); }
    MARK(0);
    if (w8 == 1) { const unsigned mine = topk(a, l); if (l < 8) lp<unsigned>(L_KEY)[l] = mine; WAITL(); }
    BAR();
    MARK(68);
    unsigned* const fP = a.ctl + C_P + nt * 512;
    const unsigned rb = L + (w8 >> 2) * 32768 + l * 16;
    unsigned pacc[8] = {}, plast = 0; (void)pacc; (void)plast;
    PT(7);
    auto oissue = [&](int s, int wv, int i) { glds1<2>(a.ox + (size_t)s * 262144 + kq * 65536 + wv * 8192 + i * 2048, l * 16, L + (s & 1) * 65536 + wv * 8192 + i * 2048); };
    if (w8 >= 4) {
        const rsrc_t rf = mkrs(fP + (kq * 4 + (w8 & 3)) * 32, 4), rp = mkrs(a.part, NSEG * 524288);
        const int stp = ((nt * 4 + (w8 & 3)) * 4 + kq) * 4096 + l * 16;
        const unsigned* fW = l < 32 ? a.ctl + C_M + (kq * 32 + l) * 32 : fP + ((l & 3) * 4 + kq) * 32;
        unsigned fv = 0; int pc = 0;
        auto a_ok = [&](int s) { return s < SO && (unsigned)__builtin_amdgcn_ballot_w64(fv <= (unsigned)s) == 0; };
        auto p_upd = [&]() { if ((__builtin_amdgcn_ballot_w64(fv <= (unsigned)pc) >> 32) == 0) ++pc; if ((__builtin_amdgcn_ballot_w64(fv <= (unsigned)pc) >> 32) == 0) ++pc; lp<unsigned>(L_DEC)[2] = pc; };
        int have = 0, nxt = 0, late = 0;
        for (int s = 0; s <= SO; ++s) {
            if (!have) {
                if (!late) {
                    gld_wait<0>(fv);
                    __builtin_amdgcn_raw_buffer_store_b32(s, rf, l * 4, 0, 16);
                    if (w8 == 4) {
                        for (int i = 0;; ++i) { if (fv <= (unsigned)(l < 32 ? s + 1 : s - 1)) fv = gld(fW); WAITV(0); p_upd(); if (a_ok(s) || pc == SO) break; __builtin_amdgcn_s_sleep(2); if (i > GCAP) fatal(err); }
                        lp<unsigned>(L_DEC)[3] = s + 1; lp<unsigned>(L_DEC)[~s & 1] = a_ok(s + 1); FENCE(); }
                    if (s == SO) break;
                    for (int i = 0; ldsr(L_DEC + 12) <= (unsigned)s; ++i) { __builtin_amdgcn_s_sleep(1); if (i > LCAP) endpgm(); }
                    FENCE();
                    PT(0);
#pragma unroll
                    for (int i = 0; i < 8; ++i) oissue(s, w8 ^ (~i & 4), i & 3);
                    WAITV(8);
                }
                WAITV(12); BAR();
                nxt = RFL(ldsr(L_DEC + 4 * (~s & 1))) & 1;
                if (late) WAITV(4); else WAITV(0);
                if (nxt) {
#pragma unroll
                    for (int i = 0; i < 4; ++i) oissue(s + 1, w8 ^ 4, i); }
                PT(1);
            }
            if (w8 == 4 && !nxt) {
                if (!have) { if (fv <= (unsigned)(s + 1)) gld_req(fv, fW); WAITV(0); }
                gld_wait<4>(fv);
                p_upd(); lp<unsigned>(L_DEC)[~s & 1] = 4 * a_ok(s + 1); WAITL();
                if (fv <= (unsigned)(s + 2)) gld_req(fv, fW); }
            BAR();
            PT(2);
            PRIO(2);
            frag_cd acc[4]; Half hx; unsigned mid;
            seg_gemm<5, false>(wf, rb + (s & 1) * 65536, acc, [&](int i) {
                if (i == BARI - 5) { if (!(have & nxt)) WAITV(0);
                    gld_wait<4>(fv);
                    if (w8 == 4) { p_upd(); const bool k = a_ok(s + 1); if (k) lp<unsigned>(L_DEC)[3] = s + 2; lp<unsigned>(L_DEC)[s & 1] = a_ok(s + 2) + 2 * k; }
                    __builtin_amdgcn_raw_buffer_store_b32(s - (have & nxt), rf, l * 4, 0, 16); FENCE(); }
                if (i == BARI) { PRIO(3); PT(5); BAR(); PT(6); }
                if (i == 3) asm volatile("ds_read_b32 %0, %1" : "=v"(mid) : "v"(L_DEC + 4 * (~s & 1)) : "memory");
                if (i == 26) half_req(L_EX + (w8 & 3) * 4096 + l * 16, L_DEC + 4 * (s & 1), hx);
                if (i == 28) { asm volatile("s_waitcnt lgkmcnt(4)" : "+v"(hx.dec), "+v"(mid)); const int d = RFL(hx.dec); have = nxt | RFL(mid) >> 2; nxt = have & d; late = ~have & d >> 1 & 1; }
                if (i >= 28 && (nxt | late)) oissue(s + 1 + nxt, w8 ^ 4, i - 28); });
            PRIO(0);
            PT(3);
            half_get(hx, acc, [&](int i) { if (late) oissue(s + 1, w8, i); });
            if (w8 == 4 && fv <= (unsigned)(s + 3)) gld_req(fv, fW);
#pragma unroll
            for (int i = 0; i < 4; ++i) STB(__builtin_bit_cast(v4u, acc[i]), rp, stp + i * 1024, s * 524288);
            FENCE();
            PT(4);
        }
        if (w8 == 5) {
            flags_wait<4>(a.ctl + C_Y + j * 32 + 1, 1, 1, l, err);
            const unsigned tok = 4095u - (lp<unsigned>(L_KEY)[l & 7] & 4095u); const int xo = tok * DM + j * 32 + (l >> 3) * 4;
            const frag_cd y = __builtin_bit_cast(frag_cd, __builtin_amdgcn_raw_buffer_load_b128(mkrs(a.ypart + (32 * 28 + j) * 1024, 1024), l * 16, 0, 16));
            const v2u xv = *(const v2u*)(a.x + xo); const float gt = __uint_as_float(gld(a.gate + tok)), omg = rbf(1.0f - gt);
            const float b[4] = {rbf(lo16(xv.x) + y[0]), rbf(hi16(xv.x) + y[1]), rbf(lo16(xv.y) + y[2]), rbf(hi16(xv.y) + y[3])};
            asm volatile("" :: "v"(b[0]), "v"(b[1]), "v"(b[2]), "v"(b[3]), "v"(omg));
            BAR();
            const frag_cd t = *lp<frag_cd>(L_Y + l * 16);
            if (tok < 32 * SO) *(v2u*)(a.out + xo) = v2u{mix(gt, omg, t[0], t[1], b[0], b[1]), mix(gt, omg, t[2], t[3], b[2], b[3])};
        } else BAR();
    } else {
        const Finisher fz(a, j, w8, l, L_Y);
        int fin = 0, have = 0, pc = 0;
        for (int s = 0; s <= SO; ++s) {
            if (!have) {
                for (int i = 0; fin < s; ++i) {
                    if (s < SO && RFL(ldsr(L_DEC + 12)) > s) break;
                    pc = RFL(ldsr(L_DEC + 8));
                    if (fin < pc) { FENCE(); fz.finish(fin, fz.issue(fin, true), [](int) {}); ++fin; }
                    else { __builtin_amdgcn_s_sleep(1); if (i > LCAP) endpgm(); }
                }
                if (s == SO) break;
            }
            FENCE();
            const bool fi = fin < pc;
            const Finisher::Fin f = fz.issue(fin, fi);
            WAITL(); WAITV(15);
            PT(0);
            BAR();
            PT(2);
            PRIO(2);
            frag_cd acc[4];
            v4u dec;
            seg_gemm<1, false>(wf, rb + (s & 1) * 65536, acc, [&](int i) {
                if (i == BARI) { PRIO(3); PT(1); WAITV(6); BAR(); PT(6); }
                if (i == 26) asm volatile("ds_read_b128 %0, %1" : "=v"(dec) : "v"(L_DEC) : "memory"); });
            PRIO(1);
            PT(3);
            asm volatile("s_waitcnt lgkmcnt(0)" : "+v"(dec));
            have = RFL(s & 1 ? dec.x : dec.y) & 5; pc = RFL(dec.z);
            half_put(L_EX, w8, l, acc);
            WAITV(0);
            if (have & 4) {
#pragma unroll
                for (int i = 0; i < 4; ++i) oissue(s + 1, w8, i); }
            if (have) { oissue(s + 1, w8 ^ 4, 0); oissue(s + 1, w8 ^ 4, 1); }
            PT(4);
            if (!have) { WAITL(); BAR(); }
            fz.finish(fin, f, [&](int i) { if (have) oissue(s + 1, w8 ^ 4, 2 + i); });
            fin += fi;
            MARK(1 + s);
            PT(5);
        }
        WAITL(); BAR();
    }
    PFLUSH();
    MARK(65);
}

__global__ void __launch_bounds__(512, 2) k_mega(Args a) {
    __shared__ __attribute__((aligned(1024))) unsigned char smem[163840];
    const unsigned L = (unsigned)(size_t)(LAS unsigned char*)smem;
    const int b = blockIdx.x, u = (b & 3) + 4 * (b >> 3);
    MARK(255); MARKC(254);
    const unsigned hb = __builtin_amdgcn_raw_buffer_load_b32(mkrs(a.ctl + C_HB, 4), threadIdx.x >> 6 == (b & 4 ? (u < 96 ? 4 : 0) : 3) ? 0 : OOB, 0, 16);
    FENCE();
    if (!(b & 4)) chain(a, u, L, hb);
    else if (u < 96) gwork(a, (u >> 2) % 3, (u & 3) * 8 + (u >> 2) / 3, L, hb);
    else owork(a, (u >> 2) - 24, u & 3, L, hb);
}

static constexpr int g_coff = 0, g_force = 0; static constexpr int g_d1 = 620, g_d2 = 1340, g_per = 1800, g_dinit = 250; static constexpr unsigned* g_dbg = nullptr;
extern "C" void kernel_launch(void* const* d_in, const int* in_sizes, int n_in, void* d_out, int out_size, void* d_ws, size_t ws_size, hipStream_t stream) {
    auto in = [&](int i) { return (const bf16_t*)d_in[i]; };
    unsigned char* ws = (unsigned char*)d_ws; size_t off = g_coff;
    auto alloc = [&](size_t bytes) { unsigned char* p = ws + off; off += (bytes + 255) & ~(size_t)255; return p; };
    unsigned* ctl = (unsigned*)alloc(C_WORDS * 4);
    unsigned* gate = (unsigned*)alloc(2 * S_TOK * 4); unsigned char* ypart = alloc(32 * 29 * 1024);
    off += (size_t)(-(uintptr_t)(ws + off)) & ((2u << 20) - 1);
    unsigned char* ring = alloc(RING_BYTES);
    unsigned char* qkv = alloc((size_t)NSEG * NH * 24576);
    unsigned char* ox = alloc((size_t)NSEG * 262144); unsigned char* part = alloc((size_t)NSEG * 524288);
    Args a{in(0), in(1), in(2), in(3), in(4), in(5), (bf16_t*)d_out, ctl, ring, qkv, ox, part, in(8), in(9), in(10), in(11), in(12), gate, ypart, g_dbg,
           g_d1 * 16 / 10, g_d2 * 16 / 10, g_per * 16 / 10, g_dinit * 16 / 10 + (g_force << 16)};
    k_mega<<<256, 512, 0, stream>>>(a);
}
```

```cpp
#include <hip/hip_runtime.h>
#include <stdint.h>
#include <type_traits>

typedef unsigned short bf16_t;
typedef unsigned long long u64;
typedef float frag_cd __attribute__((ext_vector_type(4)));
typedef unsigned v4u __attribute__((ext_vector_type(4)));
typedef unsigned v8u __attribute__((ext_vector_type(8)));
typedef unsigned v2u __attribute__((ext_vector_type(2)));
typedef float v2f __attribute__((ext_vector_type(2)));
typedef __bf16 bf16x2_t __attribute__((ext_vector_type(2)));
typedef __bf16 v8bf __attribute__((ext_vector_type(8)));
typedef __bf16 v16bf __attribute__((ext_vector_type(16)));
typedef short v4s_t __attribute__((ext_vector_type(4)));
typedef __amdgpu_buffer_rsrc_t rsrc_t;
#define LAS __attribute__((address_space(3)))
#define GAS __attribute__((address_space(1)))

static constexpr int S_TOK = 2048, DM = 1024, NH = 32, DH = 128, MM = 8, SEG = 32, NSEG = 64, HD = NH * DH, FF = 3584, TOPC = 8;
static constexpr int C_ERR = 0, C_READY = 32, C_CAL = 64  , C_ACK = 96, C_G = 128  , C_M = C_G + 4096  ,
                     C_P = C_M + 4096  , C_R = C_P + 4096, C_Y = C_R + 1024  , C_HB = C_Y + 1024, C_WORDS = C_HB + 32;
static constexpr int RING_BYTES = 4 << 20, B_H1 = 64, B_H3 = 96, B_TS = 112;
static constexpr unsigned SPIN_CAP = 1u << 18;
static constexpr int OOB = 0x7ffffff0, LCAP = 1 << 22, GCAP = 1 << 20;
static constexpr float SCALE = 0.08838834764831845f, LOG2E = 1.4426950408889634f;

struct Args {
    const bf16_t* x; const bf16_t* mem0; const bf16_t* Wq; const bf16_t* Wk; const bf16_t* Wv; const bf16_t* Wo;
    bf16_t* out; unsigned* ctl; unsigned char* ring; unsigned char* qkv; unsigned char* ox; unsigned char* part;
    const bf16_t* wr; const bf16_t* wg; const bf16_t* wu; const bf16_t* wd; const bf16_t* gw;
    unsigned* gate  ; unsigned char* ypart; unsigned* dbg;
    int d1, d2, per, dinit;
};

__device__ __forceinline__ unsigned cvt_pk(float lo, float hi) { v2f v = {lo, hi}; return __builtin_bit_cast(unsigned, __builtin_convertvector(v, bf16x2_t)); }
__device__ __forceinline__ float lo16(unsigned p) { return __uint_as_float(p << 16); }
__device__ __forceinline__ float hi16(unsigned p) { return __uint_as_float(p & 0xffff0000u); }
__device__ __forceinline__ float rbf(float f) { return lo16(cvt_pk(f, 0.f)); }
template <int C> __device__ __forceinline__ unsigned dppu(unsigned v) { return (unsigned)__builtin_amdgcn_update_dpp(0, (int)v, C, 0xf, 0xf, true); }
template <int C> __device__ __forceinline__ float dppf(float v) { return __uint_as_float(dppu<C>(__float_as_uint(v))); }
#define ROR8 0x128
#define HMIR 0x141
__device__ __forceinline__ v2u sw16(unsigned a, unsigned b) { auto r = __builtin_amdgcn_permlane16_swap(a, b, false, false); return v2u{r[0], r[1]}; }
__device__ __forceinline__ v2u sw32(unsigned a, unsigned b) { auto r = __builtin_amdgcn_permlane32_swap(a, b, false, false); return v2u{r[0], r[1]}; }
__device__ __forceinline__ float tadd16(float a, float b) { v2u r = sw16(__float_as_uint(a), __float_as_uint(b)); return __uint_as_float(r.x) + __uint_as_float(r.y); }
__device__ __forceinline__ float tadd32(float a, float b) { v2u r = sw32(__float_as_uint(a), __float_as_uint(b)); return __uint_as_float(r.x) + __uint_as_float(r.y); }
__device__ __forceinline__ float tmax16(float a) { v2u r = sw16(__float_as_uint(a), __float_as_uint(a)); return fmaxf(__uint_as_float(r.x), __uint_as_float(r.y)); }
__device__ __forceinline__ float tmax32(float a) { v2u r = sw32(__float_as_uint(a), __float_as_uint(a)); return fmaxf(__uint_as_float(r.x), __uint_as_float(r.y)); }
__device__ __forceinline__ unsigned umin2(unsigned a, unsigned b) { return min(a, b); }
__device__ __forceinline__ unsigned umax3(unsigned a, unsigned b, unsigned c) { return max(max(a, b), c); }
__device__ __forceinline__ v4u pack_swap(frag_cd a, frag_cd b) { v2u x0 = sw16(cvt_pk(a[0], a[1]), cvt_pk(b[0], b[1])), x1 = sw16(cvt_pk(a[2], a[3]), cvt_pk(b[2], b[3])); return v4u{x0.x, x1.x, x0.y, x1.y}; }
#define MFMA(a, b, c) __builtin_amdgcn_mfma_f32_16x16x32_bf16(__builtin_bit_cast(v8bf, a), __builtin_bit_cast(v8bf, b), c, 0, 0, 0)
#define SMF(a, b, c, ix) __builtin_amdgcn_smfmac_f32_16x16x64_bf16(__builtin_bit_cast(v8bf, a), __builtin_bit_cast(v16bf, b), c, ix, 0, 0)
#define FENCE() asm volatile("" ::: "memory")
#define SB() __builtin_amdgcn_sched_barrier(0)
#define RFL(x) __builtin_amdgcn_readfirstlane(x)
#define BAR() do { FENCE(); __builtin_amdgcn_s_barrier(); FENCE(); } while (0)
#define WAITV(n) do { FENCE(); __builtin_amdgcn_s_waitcnt(0x0F70 | (n)); FENCE(); } while (0)
#define WAITL() asm volatile("s_waitcnt lgkmcnt(0)" ::: "memory")
__device__ __forceinline__ void STB(v4u v, rsrc_t rs, int off, int soff) { __builtin_amdgcn_raw_buffer_store_b128(v, rs, off, soff, 16); asm volatile("s_nop 1" :: "v"(v) : "memory"); }
template <class T> __device__ __forceinline__ LAS T* lp(unsigned a) { return (LAS T*)(size_t)a; }
__device__ __forceinline__ void glds(const void* g, unsigned voff, unsigned lds) { asm volatile("s_mov_b32 m0, %2\n\ts_nop 0\n\tglobal_load_lds_dwordx4 %0, %1" :: "v"(voff), "s"(g), "s"(lds) : "memory"); }
#define NTC " nt"
#define NTW " nt"
__device__ __forceinline__ void gldsc(const void* g, unsigned voff, unsigned lds) { asm volatile("s_mov_b32 m0, %2\n\ts_nop 0\n\tglobal_load_lds_dwordx4 %0, %1" NTC :: "v"(voff), "s"(g), "s"(lds) : "memory"); }
__device__ __forceinline__ void gldsw(const void* g, unsigned voff, unsigned lds) { asm volatile("s_mov_b32 m0, %2\n\ts_nop 0\n\tglobal_load_lds_dwordx4 %0, %1" NTW :: "v"(voff), "s"(g), "s"(lds) : "memory"); }
template <int N> __device__ __forceinline__ void glds1(const void* g, unsigned voff, unsigned lds) {
#define GL(o) "\n\tglobal_load_lds_dwordx4 %0, %1 offset:" #o " sc1"
    if constexpr (N == 1) asm volatile("s_mov_b32 m0, %2\n\ts_nop 0" GL(0) :: "v"(voff), "s"(g), "s"(lds) : "memory");
    if constexpr (N == 2) asm volatile("s_mov_b32 m0, %2\n\ts_nop 0" GL(0) GL(1024) :: "v"(voff), "s"(g), "s"(lds) : "memory");
    if constexpr (N == 4) asm volatile("s_mov_b32 m0, %2\n\ts_nop 0" GL(0) GL(1024) GL(2048) GL(3072) :: "v"(voff), "s"(g), "s"(lds) : "memory");
}
__device__ __forceinline__ rsrc_t mkrs(const void* p, int bytes) { return __builtin_amdgcn_make_buffer_rsrc((void*)p, 0, bytes, 0x00020000); }
__device__ __forceinline__ unsigned gld(const unsigned* p) { return __hip_atomic_load(p, __ATOMIC_RELAXED, __HIP_MEMORY_SCOPE_AGENT); }
__device__ __forceinline__ void gst(unsigned* p, unsigned v) { __hip_atomic_store(p, v, __ATOMIC_RELAXED, __HIP_MEMORY_SCOPE_AGENT); }
__device__ __forceinline__ void gst1(unsigned* p, unsigned v) { asm volatile("s_mov_b64 exec, 1\n\tglobal_store_dword %0, %1, %2 sc1\n\ts_mov_b64 exec, -1" :: "v"(0u), "v"(v), "s"(p) : "memory"); }
__device__ __forceinline__ unsigned rt16() { return (unsigned)__builtin_amdgcn_s_memrealtime() << 4; }

#define NSLOT 12
#define PT(k) do {} while (0)
#define PFLUSH() do {} while (0)
#define MARK(i) do {} while (0)
#define MARKC(i) do {} while (0)
#define MARKW(i) do {} while (0)

#define OSP 0
#define OD0 0
#define FL4 0
#define FV 0
#define NTV ""
#define GAL 2
#define LF 1
#define GDL 0
#define CDL 0
#define XPF 1
#define GHB 1
#define OPF 0
#define GPF 0
#define CPF 0
#define DRY 1
#define OWF 1
#define CKL 1
#define SG0 80
#define XA3 1
#define GE2 8
#define VQ 700
#define H3X 0
#define EW 30
#define E0 32
#define E2 160
#define E3 120
#define AT 0
#define XH 0
#define XP 0
#define VR 0
#define DWM 4
#define NQ 1130
#ifndef NV
#define NV 960
#endif
#define LXC 200
#define TSL 1
#define RSL 1
#define SO 58
#define TS1 2
#define TS2 16
#define TMAP 1
#define TIF 0
#define TSP 0
#define OPL 1
#define CPL 0
#define TNT 0
#define EF 0
#define TFK 8
#define TS3 0
#define TSPEC 0
#define TD 10
#define VDEL1 15
#define AEARLY 1
#define XC 4
#define ODEL 1
#define SMQ 0x53b
#define SMV 0x79b
#define AL1 1
#define AL2 0
#define AL3 0
#define AL4 1
#define AL5 7
#define AL6 0
#define HB 0
#define HP 0
#define H3 0
#define HSM 0x2d
#define PF 22
#define US 1
#define ROT 0
#define VL 1
#define VP 0x3911
#define VE 0
#define VN3 36
#define CV 0
#define CD 60
#define XS1 0
#define XS2 6
#define PLO 0
#define MSP 1
#define VD 260
#define LK 300
#define LT 400
#define NS 16
#define GS 6
#define KC 560
#define GM 200
#define SGAP 60
#define HSL 6
#define CSL 3
#define WK 40
#define WKO 120
#define WB 200
#define WC1 0
#define WC2 64
#define WTHR 340
#define SLIP_THR 400
#define CKA 0.75
#define CKF 22.5
#define TG_THR 480
#define SLIP_ADD 640
#define SLIP_INC 10
__device__ __forceinline__ void endpgm() { asm volatile("s_endpgm" ::: "memory"); }
__device__ __forceinline__ void fatal(unsigned* err) { asm volatile("global_store_dword %0, %1, off sc1\n\ts_waitcnt vmcnt(0)\n\ts_endpgm" :: "v"(err), "v"(1u) : "memory"); }
__device__ __forceinline__ unsigned ldsr(unsigned p) { return __hip_atomic_load(lp<unsigned>(p), __ATOMIC_RELAXED, __HIP_MEMORY_SCOPE_WORKGROUP); }
__device__ __forceinline__ v2u trd(unsigned p) { return __builtin_bit_cast(v2u, __builtin_amdgcn_ds_read_tr16_b64_v4i16(lp<v4s_t>(p))); }
__device__ __forceinline__ v8u cat(v2u a, v2u b, v2u c, v2u d) { return v8u{a.x, a.y, b.x, b.y, c.x, c.y, d.x, d.y}; }
__device__ __forceinline__ v4u cat2(v2u a, v2u b) { return v4u{a.x, a.y, b.x, b.y}; }
template <int SL, int FAR = 0> __device__ __forceinline__ void flags_wait(unsigned& v, const unsigned* p, unsigned need, unsigned* err) {
    for (int i = 0; __builtin_amdgcn_ballot_w64(v < need) != 0; ++i) {
        if (i) { if (FAR && __builtin_amdgcn_ballot_w64(v + 1 < need) != 0) __builtin_amdgcn_s_sleep(FAR); __builtin_amdgcn_s_sleep(SL); }
        if (v < need) v = gld(p);
        if (i > GCAP) fatal(err); }
    FENCE();
}
template <int SL> __device__ __forceinline__ void flags_wait(const unsigned* p, int nw, unsigned need, int l, unsigned* err) { unsigned v = need - 1; flags_wait<SL>(v, p + min(l, nw - 1) * 32, need, err); }

__device__ __forceinline__ void gadd1(unsigned* p) { asm volatile("s_mov_b64 exec, 1\n\tglobal_atomic_add %0, %1, off sc1\n\ts_mov_b64 exec, -1" :: "v"(p), "v"(1u) : "memory"); }
__device__ __forceinline__ void gld_req(unsigned& v, const unsigned* p) { asm volatile("global_load_dword %0, %1, off sc1" : "+v"(v) : "v"(p) : "memory"); }
template <int N> __device__ __forceinline__ void gld_wait(unsigned& v) { asm volatile("s_waitcnt vmcnt(%1)" : "+v"(v) : "n"(N) : "memory"); }
__device__ __forceinline__ void ctl_wait(const Args& a, unsigned hb) {
    for (int i = 0; RFL(gld(a.ctl + C_HB)) == RFL(hb); ++i) { __builtin_amdgcn_s_sleep(8); if (i > GCAP) fatal(a.ctl + C_ERR); }
    FENCE();
}

template <class F> __device__ __forceinline__ void ahead(const bf16_t* W, int h, int l, unsigned junk, F row) {
    const unsigned o = (384 - (unsigned)(u64)W) & 896; const int d = (h - (o >> 8)) & 3;
    if (d) {
#pragma unroll 1
        for (int i = 0; i < 4; ++i) gldsw(W, (row(d) + 8 * i + (l >> 3)) * (HD * 2) + (h >> 2) * 1024 + o + (l & 7) * 16, junk); }
}

typedef unsigned v16u __attribute__((ext_vector_type(16)));
typedef unsigned v32u __attribute__((ext_vector_type(32)));
#define STR_(x) #x
#define STR(x) STR_(x)
#define SET(x) "\n.set " #x ", " STR(x)
#define A_STAMPS "0"
#define A_BENCH "0"
static_assert(NH * 24576 == 0xC0000, "");
#define A_CHAIN ".set STAMPS, " A_STAMPS "\n.set BENCH, " A_BENCH "\n.set WB, " STR(WB) "\n.set WC1, " STR(WC1) "\n.set WC2, " STR(WC2) SET(SMQ) SET(SMV) SET(AL1) SET(AL2) SET(AL3) SET(AL4) SET(AL5) SET(AL6) SET(HB) SET(US) SET(PF) SET(HP) SET(H3) "\n.set HS, " STR(HSM) SET(VL) SET(VP) SET(VE) SET(VN3) SET(CV) SET(CD) SET(XS1) SET(XS2) SET(PLO) SET(CSL) SET(MSP) SET(VD) SET(LK) SET(LT) SET(NS) SET(GS) SET(GM) SET(SGAP) SET(EF) SET(NQ) SET(NV) SET(LXC) SET(TSL) SET(RSL) SET(VR) SET(DWM) SET(AT) SET(EW) SET(E0) SET(E2) SET(E3) SET(HSL) SET(XA3) SET(SG0) SET(VQ) "\n.set WK, " STR(WK) "\n.set WN, 64\n.set WZ, WN+5\n.set WTHR, " STR(WTHR) "\n" R"(
.macro SLED n
  s_getpc_b64 vcc
4: s_sub_u32 s77, (3f-4b)/4, s77
  s_lshl2_add_u32 vcc_lo, s77, vcc_lo
  s_addc_u32 vcc_hi, vcc_hi, 0
  s_setpc_b64 vcc
2:
.rept \n
  s_nop 1
.endr
3: s_waitcnt lgkmcnt(0)
.endm
.macro WAITC T, thr
  s_movk_i32 s76, 0x1000
1: s_memrealtime vcc
  s_memtime s[6:7]
  s_sub_u32 s76, s76, 1
  s_cbranch_scc1 3f
  s_waitcnt lgkmcnt(0)
  s_lshl_b32 s77, vcc_lo, 4
  s_sub_i32 s77, \T, s77
  s_cbranch_execz 6f
  s_cmp_ge_i32 s77, \thr
  s_cbranch_scc1 1b
.endm
.macro WAITP T
  WAITC \T, WTHR
  s_sub_i32 s77, s77, WC1
  s_cmp_lt_i32 s77, WB
  s_cbranch_scc1 5f
6:
.irp r,30,32,34,36,38,94,96,98
  s_memrealtime s[\r:\r+1]
  s_nop 1
.endr
  s_memtime s[6:7]
  s_sub_i32 s77, \T, WC2
  s_sub_i32 s4, s4, EW
  s_add_i32 s76, s6, 0x8000
  s_cmp_eq_u64 exec, 0
  s_cselect_b32 s4, s76, s4
  s_waitcnt lgkmcnt(0)
  s_add_u32 s30, s30, s32
  s_add_u32 s34, s34, s36
  s_add_u32 s38, s38, s94
  s_add_u32 s96, s96, s98
  s_add_u32 s30, s30, s34
  s_add_u32 s38, s38, s96
  s_add_u32 s30, s30, s38
  s_lshl_b32 s30, s30, 1
  s_sub_i32 s77, s77, s30
5: s_max_i32 s77, s77, 0
  s_mulk_i32 s77, 45
  s_lshr_b32 s77, s77, 8
  s_sub_i32 s6, s4, s6
  s_ashr_i32 s6, s6, 3
  s_max_i32 s77, s77, s6
  s_min_u32 s77, s77, WN
  SLED WN
.endm
.macro WAITK x, nom
  s_add_i32 s5, s59, \x-\nom
  s_add_i32 s75, s92, s59
  s_movk_i32 s76, 0x1000
  s_add_i32 s75, s75, \x
1: s_memtime vcc
  s_sub_u32 s76, s76, 1
  s_cbranch_scc1 3f
  s_waitcnt lgkmcnt(0)
  s_sub_i32 s77, s75, vcc_lo
  s_sub_i32 s7, s6, vcc_lo
  s_add_i32 s7, s7, s5
  s_max_i32 s77, s77, s7
  s_cbranch_execz 2f
  s_cmp_ge_i32 s77, 8*WK
  s_cbranch_scc1 1b
  s_max_i32 s77, s77, 0
  s_lshr_b32 s77, s77, 3
  SLED WK
.endm
.macro H1ST
.if AT == 1
  buffer_atomic_swap v[RES], v[ST1], s[40:43], s66 offen sc1
.elseif AT == 2
  buffer_atomic_and v[RES], v[ST1], s[40:43], s66 offen sc1
.elseif AT == 3
  buffer_store_dword v[RES], v[ST1], s[40:43], s66 offen sc0 sc1
.elseif AT == 4
  buffer_store_dword v[RES], v[ST1], s[40:43], s66 offen sc1 nt
.else
  buffer_store_dword v[RES], v[ST1], s[40:43], s66 offen sc1
.endif
.endm
.macro ALN k
.p2align 6
.rept \k
  s_nop 0
.endr
.endm
.macro HT n, r
.rept \n
  v_mov_b32 v[\r], 0
  s_nop 1
.endr
.endm
.macro TMAX a, t
  v_readlane_b32 \t, v[\a], 0
.irp r,1,2,3
  v_readlane_b32 s77, v[\a], \r
  s_sub_i32 s77, s77, \t
  s_max_i32 s77, s77, 0
  s_add_i32 \t, \t, s77
.endr
.endm
.macro TGET a
  v_mbcnt_lo_u32_b32 v[\a+1], 7, 0
  v_lshlrev_b32 v[\a+1], 7, v[\a+1]
  s_add_i32 s75, s88, s68
8: buffer_load_dword v[\a], v[\a+1], s[40:43], s75 offen sc1
  s_sub_u32 s71, s71, 1
  s_cbranch_scc1 L_fatal
  s_waitcnt vmcnt(0)
  v_cmp_eq_u32 vcc, -1, v[\a]
  s_cbranch_vccz 9f
  s_sleep TSL
  s_branch 8b
9:
  TMAX \a, s75
  s_mov_b32 s4, s92
  WAITP s75
  s_mov_b32 s76, 0x400000
.endm
.macro PLAN
  s_mulk_i32 s74, PF
  s_ashr_i32 s74, s74, 3
  s_add_i32 s73, s73, s58
  s_add_i32 s73, s73, s74
.endm
.macro STAMP k
.if STAMPS && ((SMK >> \k) & 1)
  s_memtime s[8+2*\k:9+2*\k]
.endif
.endm
.macro NEXT t
.if STAMPS
  s_waitcnt lgkmcnt(0)
.irp k,0,1,2,3,4,5,6,7,8,9,10
  v_writelane_b32 v[\t], s[8+2*\k], \k
.endr
  s_lshl_b32 s76, s64, 3
  s_add_i32 s76, s76, s63
  s_mul_i32 s76, s76, 48
  v_mbcnt_lo_u32_b32 v[\t+1], -1, 0
  v_lshl_add_u32 v[\t+1], v[\t+1], 2, s76
  s_mov_b64 vcc, exec
  s_and_b64 exec, exec, 0x7ff
  ds_write_b32 v[\t+1], v[\t]
  s_mov_b64 exec, vcc
.endif
  s_lshl_b32 s72, s64, 18
  s_mov_b32 s64, s65
  s_add_i32 s65, s65, 1
  s_xor_b32 s66, s66, 0x8000
  s_xor_b32 s67, s67, 0x8000
  s_mov_b32 s76, s68
  s_mov_b32 s68, s69
  s_mov_b32 s69, s70
  s_mov_b32 s70, s76
.endm
.macro RETRY c
.if BENCH
  s_bitset1_b64 s[80+2*\c:81+2*\c], s64
.endif
  s_sub_u32 s71, s71, 1
  s_cbranch_scc1 L_fatal
  s_sleep RSL
.endm
.macro LD d, a, so, o
  buffer_load_dwordx4 v[\d:\d+3], v[\a], s[40:43], \so offen offset:\o sc1
.endm
.macro SM d, a, b
  v_smfmac_f32_16x16x64_bf16 v[\d:\d+3], v[\a:\a+3], v[\b:\b+7], v[SIX]
.endm
.macro MX0 a
  v_max_u32 v[MX], v[\a], v[\a+1]
  v_max3_u32 v[MX], v[MX], v[\a+2], v[\a+3]
.endm
.macro MX1 a
  v_max3_u32 v[MX], v[MX], v[\a], v[\a+1]
  v_max3_u32 v[MX], v[MX], v[\a+2], v[\a+3]
.endm
.macro AW r, k
  ds_write_addtid_b32 v[\r] offset:63744+256*\k
.endm
.macro HS a
  v_pk_add_f32 v[\a:\a+1], v[\a:\a+1], v[\a+2:\a+3]
.endm
.macro ZR a, n
.set zi, 0
.rept \n
  v_mov_b64 v[\a+zi:\a+zi+1], 0
.set zi, zi+2
.endr
.endm
.macro PRE
  s_mov_b32 s64, 63
  s_mov_b32 s65, 0
  s_mov_b32 s66, 0x8000
  s_mov_b32 s67, 0
  s_mov_b32 s68, 0x8000
  s_mov_b32 s69, 0x10000
  s_mov_b32 s70, 0
.endm
.macro XSL r
  s_and_b32 s75, \r, 3
  s_mulk_i32 s75, 20480
.endm
.macro CAP
  s_sub_u32 s76, s76, 1
  s_cbranch_scc1 L_fatal
.endm
LD_go:
  s_mov_b32 s71, 0x40000
  s_mov_b32 s88, s64
  s_mov_b32 s90, s65
  s_mov_b32 s91, s66
  s_mov_b64 s[80:81], 0
  s_mov_b64 s[82:83], 0
  s_mov_b64 s[84:85], 0
  s_mov_b64 s[86:87], 0
  s_mov_b32 s100, s67
  s_mov_b32 s101, s68
  s_cmp_gt_u32 s63, 3
  s_cbranch_scc1 LV_init
; ======== waves 0..3
.set SMK, SMQ
.set O3,168
.set ST1,169
.set RW1,170
.set RW2,171
.set RW3,172
.set XA,173
.set FW,174
.set FR,175
.set SIX,176
.set STK,177
.set FL,183
.set MB,128
.set AC,144
.set TAG,160
.set MX,161
.set RES,162
.set FA,163
.set FB,164
.set VA,164
.set VB,184
.set WV,188
.set RA,134
.set KX,196
.set XF,177
.set XC,)" STR(XC) R"(
.macro MEMQ
  LD MB, O3, s68, 0
  LD MB+4, O3, s68, 1024
  LD MB+8, O3, s68, 2048
  LD MB+12, O3, s68, 3072
.endm
.macro XCHK R
  s_add_i32 s75, s64, 2
  s_min_u32 s75, s75, 63
  v_cmp_ge_u32 vcc, s75, v[FL]
  s_cbranch_vccnz LX_w\R
LX_g\R:
  s_add_i32 s75, s64, 2
  XSL s75
  s_add_i32 m0, s75, s61
.endm
.macro XL4 a
.irp o,0,1024,2048,3072
  global_load_lds_dwordx4 v[\a], s[52:53] offset:\o sc1
.endr
  s_add_i32 m0, m0, 0x1000
.endm
.macro XREF R
  s_add_i32 s75, s64, 3
  s_min_u32 s75, s75, 63
  v_cmp_ge_u32 vcc, s75, v[FL]
  s_cbranch_vccz LX_n\R
  global_load_dword v[FL], v[XF], s[54:55] sc1
LX_n\R:
.endm
.macro XADV
  s_cmp_lt_u32 s64, 61
  s_cselect_b32 s75, 0xC0000, 0
  s_add_u32 s52, s52, s75
  s_addc_u32 s53, s53, 0
.endm
.macro XWAIT R
LX_w\R:
  s_mov_b32 s76, 0x100000
.if \R == 3
  s_waitcnt vmcnt(0)
  v_cmp_ge_u32 vcc, s75, v[FL]
  s_cbranch_execz 1f
  s_cbranch_vccz LX_g\R
.endif
1: global_load_dword v[FL], v[XF], s[54:55] sc1
  s_waitcnt vmcnt(0)
  v_cmp_ge_u32 vcc, s75, v[FL]
  s_cbranch_execz 2f
  s_cbranch_vccz LX_g\R
  s_sleep 4
  CAP
  s_branch 1b
2:
.endm
.macro OXST
  buffer_store_dwordx4 v[KX:KX+3], v[STK], s[44:47], s72 offen sc1
  buffer_store_dwordx4 v[KX+4:KX+7], v[STK], s[44:47], s72 offen offset:1024 sc1
.endm
.macro FLAGQ val
  v_mov_b32 v[MX], 0
  v_mov_b32 v[FA], \val
  s_mov_b64 vcc, exec
  s_and_b64 exec, exec, 1
  global_store_dword v[MX], v[FA], s[50:51] sc1
  s_mov_b64 exec, vcc
.endm
.macro SMW i
  SM VA, MB+4*\i, WV+8*\i
  SM VB, MB+4*\i, WV+32+8*\i
.endm
.macro SMN k
.if (VP >> \k) & 1
  SM VA+(VB-VA)*(vn&1), MB+4*(vn>>1), WV+32*(vn&1)+8*(vn>>1)
.set vn, vn+1
.endif
.endm
.macro VOUT
  v_add_f32 v[VA], v[VA], v[VA+2]
  v_add_f32 v[VA+1], v[VA+1], v[VA+3]
  v_add_f32 v[VB], v[VB], v[VB+2]
  v_add_f32 v[VB+1], v[VB+1], v[VB+3]
  AW VA, 0
  AW VA+1, 2
  AW VB, 1
  AW VB+1, 3
  AW TAG, 6
.endm
.macro DM k
.if (HS >> \k) & 1
  v_mfma_f32_16x16x32_bf16 v[VA:VA+3], v[MB:MB+3], v[MB:MB+3], 0
.endif
.endm
.macro ZA
  ZR AC, 8
  ZR VA, 2
  ZR VB, 2
.endm
.macro PHASEA R
LQ_loop\R:
  STAMP 0
  ZA
  v_mov_b32 v[TAG], s65
LQ_bar\R:
  s_barrier
  STAMP 1
.if MSP
  LD MB, O3, s68, 0
  s_memtime s[92:93]
  s_nop MSP-1
  LD MB+4, O3, s68, 1024
  s_mov_b32 m0, s100
  s_nop MSP-1
  LD MB+8, O3, s68, 2048
  s_nop MSP
  LD MB+12, O3, s68, 3072
.else
  s_memtime s[92:93]
  MEMQ
  s_mov_b32 m0, s100
.endif
.set XS, 0
.if \R == 2
.set XS, 2
.irp i,0,1,4,5,6,7,8,9,10,11,12,13,14,15
  ds_read_b128 v[WV+4*\i:WV+4*\i+3], v[XA] offset:4096+1024*\i
.if \i == 9
  s_waitcnt lgkmcnt(6)
.endif
.endr
.rept )" STR(ODEL) R"(
  s_nop 7
.endr
  OXST
  s_nop 1
.irp i,2,3
  ds_read_b128 v[WV+4*\i:WV+4*\i+3], v[XA] offset:4096+1024*\i
.endr
  s_waitcnt lgkmcnt(0)
.endif
  s_mov_b32 s76, 0x400000
  ALN AL1
LQ_a\R:
  s_waitcnt vmcnt(3+XS)
  SM AC+8, MB, 64
  v_max_u32 v[MX], v[MB], v[MB+1]
  SM AC+12, MB, 96
  v_max3_u32 v[MX], v[MX], v[MB+2], v[MB+3]
  SM AC, MB, 0
  SM AC+4, MB, 32
  SMW 0
.irp i,1,2,3
  s_waitcnt vmcnt(3+XS-\i)
  SM AC+8, MB+4*\i, 64+8*\i
  v_max3_u32 v[MX], v[MX], v[MB+4*\i], v[MB+4*\i+1]
  SM AC+12, MB+4*\i, 96+8*\i
  v_max3_u32 v[MX], v[MX], v[MB+4*\i+2], v[MB+4*\i+3]
  SM AC, MB+4*\i, 8*\i
.if \i == 3
  v_cmp_eq_u32 vcc, -1, v[MX]
.endif
  SM AC+4, MB+4*\i, 32+8*\i
.if \i < VL
  SMW \i
.endif
.endr
  STAMP 2
  v_add_f32 v[AC+8], v[AC+8], v[AC+10]
  v_add_f32 v[AC+12], v[AC+12], v[AC+14]
  v_add_f32 v[AC+9], v[AC+9], v[AC+11]
  v_add_f32 v[AC+13], v[AC+13], v[AC+15]
  v_add_f32 v[AC+3], v[AC+1], v[AC+3]
  v_add_f32 v[AC], v[AC], v[AC+2]
  v_add_f32 v[AC+5], v[AC+5], v[AC+7]
  s_cbranch_vccnz LQ_ra\R
.set vn, 2*VL
  AW AC+8, 0
  SMN 0
  AW AC+12, 1
  SMN 1
  AW AC+9, 2
  SMN 2
  AW AC+13, 3
  SMN 3
  AW AC+3, 4
  SMN 4
  AW AC+5, 5
  SMN 5
  AW TAG, 6
  SMN 6
  STAMP 6
  s_barrier
  SMN 7
  ds_read_b32 v[FA], v[FR]
  SMN 8
  ds_read2st64_b32 v[AC+8:AC+9], v[RW1] offset1:1
  SMN 9
  ds_read2st64_b32 v[AC+10:AC+11], v[RW2] offset1:1
  SMN 10
  ds_read2st64_b32 v[AC+12:AC+13], v[RW3] offset1:1
  SMN 11
  s_add_i32 m0, s100, 0x2000
  v_add_f32 v[AC+1], v[AC+4], v[AC+6]
  SMN 12
  SMN 13
  SMN 14
.if vn != 8
.error "v projection incomplete"
.endif
.if VE
  VOUT
.endif
  s_waitcnt lgkmcnt(5*VE)
LQ_p\R:
  v_cmp_gt_u32 vcc, s65, v[FA]
  v_add_f32 v[AC+8], v[AC+8], v[AC]
  v_add_f32 v[AC+9], v[AC+9], v[AC+1]
  v_add_f32 v[AC+8], v[AC+8], v[AC+10]
  v_add_f32 v[AC+9], v[AC+9], v[AC+11]
  v_add_f32 v[AC+8], v[AC+8], v[AC+12]
  v_add_f32 v[AC+9], v[AC+9], v[AC+13]
  v_cvt_pk_bf16_f32 v[RES], v[AC+8], v[AC+9]
  s_cbranch_vccnz LQ_pb\R
  H1ST
  s_memtime s[6:7]
  STAMP 3
.if !VE
  VOUT
.endif
.if \R != 2
  s_cmp_eq_u32 s64, 63
  s_cbranch_scc1 L_last
.elseif EF
  s_cmp_eq_u32 s64, 63
  s_cbranch_scc1 LQ_ef
LQ_eg:
.endif
.if \R != 3
  WAITK 0, NQ
.endif
  STAMP 4
.endm
.macro COLDA R
LQ_pb\R:
.if BENCH && \R != 3
  s_bitset1_b64 s[86:87], s64
.endif
  CAP
  ds_read_b32 v[FA], v[FR]
  ds_read2st64_b32 v[AC+8:AC+9], v[RW1] offset1:1
  ds_read2st64_b32 v[AC+10:AC+11], v[RW2] offset1:1
  ds_read2st64_b32 v[AC+12:AC+13], v[RW3] offset1:1
  s_waitcnt lgkmcnt(0)
  s_cbranch_execnz LQ_p\R
LQ_ra\R:
  RETRY 2
  MEMQ
  s_add_i32 s59, s59, LXC
  TGET RES
  s_cmp_eq_u32 s77, WZ
  s_cbranch_scc1 7f
  MEMQ
7:
  ZA
  s_waitcnt vmcnt(0)
  s_cbranch_execnz LQ_a\R
.endm
  s_mov_b32 s64, 0
  s_mov_b32 s65, 1
  s_mov_b32 s66, 0
  s_mov_b32 s67, 0x8000
  s_mov_b32 s68, 0x10000
  s_mov_b32 s69, 0
  s_mov_b32 s70, 0x8000
  s_mov_b32 s72, 0
  s_setprio 3
  s_cmp_eq_u32 s60, 2
  s_cbranch_scc1 LQ_init2
  s_cmp_eq_u32 s60, 3
  s_cbranch_scc1 LQ_init3
; ---- waves 0,1: scores against the x rows
  PHASEA 0
  LD MB, 178, s66, 0
  LD MB+4, 178, s66, 1024
  XSL s64
  v_add_u32 v[FA], s75, v[179]
  ds_read_b128 v[MB+8:MB+11], v[FA]
  ds_read_b128 v[MB+12:MB+15], v[FA] offset:1024
  ds_read_b128 v[AC+8:AC+11], v[FA] offset:2048
  ds_read_b128 v[AC+12:AC+15], v[FA] offset:3072
  s_waitcnt lgkmcnt(0)
  ALN AL2
LQ_b0:
  s_waitcnt vmcnt(1)
  v_mfma_f32_16x16x32_bf16 v[AC+4:AC+7], v[MB+12:MB+15], v[MB:MB+3], 0
  MX0 MB
  v_mfma_f32_16x16x32_bf16 v[AC:AC+3], v[MB+8:MB+11], v[MB:MB+3], 0
  s_waitcnt vmcnt(0)
  v_mfma_f32_16x16x32_bf16 v[AC+4:AC+7], v[AC+12:AC+15], v[MB+4:MB+7], v[AC+4:AC+7]
  MX1 MB+4
  v_mfma_f32_16x16x32_bf16 v[AC:AC+3], v[AC+8:AC+11], v[MB+4:MB+7], v[AC:AC+3]
  v_cmp_eq_u32 vcc, -1, v[MX]
  s_nop 5
  s_cbranch_vccnz LQ_rb0
.irp e,0,1,2,3
  v_add_f32_dpp v[AC+\e], v[AC+4+\e], v[AC+\e] quad_perm:[2,3,0,1] row_mask:0xf bank_mask:0xf
.endr
  DM 0
  ds_write_b128 v[180], v[AC:AC+3]
  DM 1
  ds_write_b32 v[FW], v[TAG] offset:2048
  DM 2
  s_barrier
  STAMP 5
  s_setprio PLO
  HT HP, VB
.if XS1
  s_sleep XS1
.endif
  XCHK 0
  s_nop 0
  XL4 181
.if XC == 4
  s_nop 0
  XL4 182
.endif
  XREF 0
.if XS2
  s_sleep XS2
.endif
  XADV
  v_mov_b32 v[FA], s62
  ds_read_b32 v[RA], v[FA] offset:8
  s_waitcnt lgkmcnt(0)
  v_readfirstlane_b32 s59, v[RA]
  NEXT MB
  s_setprio 3
  s_cbranch_execnz LQ_loop0
  s_cmp_eq_u32 s60, 1
  s_cbranch_scc1 LD_c3
  COLDA 0
LQ_rb0:
  RETRY 0
.if VQ
  v_or_b32 v[TAG], 0x10000, v[TAG]
.endif
  LD MB, 178, s66, 0
  LD MB+4, 178, s66, 1024
  s_cbranch_execnz LQ_b0
  XWAIT 0
  s_setpc_b64 s[48:49]
; ---- wave 2: scores against the memory rows; attention of 16 x rows (64 columns)
.set AQ,181
.set AVF,182
.set AVM,183
.set RA,184
.set QX,196
.set OA,212
.set MSK,228
.set LX,237
.set PB,238
.set S1,240
.set K7,244
.set P4,244
.macro MF d, a, b, c
  v_mfma_f32_16x16x32_bf16 v[\d:\d+3], v[\a:\a+3], v[\b:\b+3], \c
.endm
.macro XQ op, r
  v_mov_b32 v[FB], v[\r]
  s_nop 1
  v_permlane16_swap_b32 v[\r], v[FB]
  \op v[\r], v[\r], v[FB]
  v_mov_b32 v[FB], v[\r]
  s_nop 1
  v_permlane32_swap_b32 v[\r], v[FB]
  \op v[\r], v[\r], v[FB]
.endm
.macro QKQ
  LD MB, 178, s66, 0
  LD MB+4, 179, s66, 0
  LD MB+8, 178, s66, 1024
  LD MB+12, 179, s66, 1024
.endm
.macro SOFTX
  v_add_f32 v[AC+6], v[AC+4], v[S1+2]
  v_add_f32 v[AC+7], v[AC+5], v[S1+3]
  v_max3_f32 v[MX], v[LX], v[AC+6], v[AC+7]
  XQ v_max_f32, MX
  v_mul_f32 v[MX], s74, v[MX]
.irp i,6,7,8,9,10,11,12,13,14,15
  v_fma_f32 v[AC+\i], v[AC+\i], s73, v[MX]
.endr
.irp i,6,7,8,9,10,11,12,13,14,15
  v_exp_f32 v[AC+\i], v[AC+\i]
.endr
  v_add_f32 v[LX], v[AC+6], v[AC+7]
.irp i,8,9,10,11,12,13,14,15
  v_add_f32 v[LX], v[LX], v[AC+\i]
.endr
  XQ v_add_f32, LX
  v_rcp_f32 v[LX], v[LX]
  s_nop 0
.irp i,6,7,8,9,10,11,12,13,14,15
  v_mul_f32 v[AC+\i], v[AC+\i], v[LX]
.endr
.irp i,0,1,2,3
  v_cvt_pk_bf16_f32 v[P4+\i], v[AC+8+2*\i], v[AC+9+2*\i]
.endr
  v_cvt_pk_bf16_f32 v[PB], v[AC+6], v[AC+7]
.endm
LQ_init2:
  s_mov_b32 s73, 0x3e0293ee
  s_mov_b32 s74, 0xbe0293ee
  PHASEA 2
  QKQ
  XSL s64
  ds_read_b128 v[MSK:MSK+3], v[XA]
  ds_read_b128 v[MSK+4:MSK+7], v[XA] offset:1024
  v_add_u32 v[FB], s75, v[AQ]
  v_add_u32 v[RES], s75, v[AVF]
  v_mov_b32 v[PB+1], 0
.irp i,0,1,2,3
  ds_read_b128 v[QX+4*\i:QX+4*\i+3], v[FB] offset:16384+1024*\i
  ds_read_b128 v[OA+4*\i:OA+4*\i+3], v[FB] offset:1024*\i
.endr
.irp i,0,1,2
  ds_read_b128 v[RA+4*\i:RA+4*\i+3], v[FB] offset:4096+1024*\i
.endr
  ds_read_b128 v[K7:K7+3], v[FB] offset:7168
  s_waitcnt lgkmcnt(10)
  MF AC+8, OA, QX, v[MSK:MSK+3]
.irp i,4,8,12
  s_waitcnt lgkmcnt(10-\i/2)
  MF AC+8, OA+\i, QX+\i, v[AC+8:AC+11]
.endr
  s_waitcnt lgkmcnt(0)
  MF AC+12, RA, QX, v[MSK+4:MSK+7]
  MF AC+12, RA+4, QX+4, v[AC+12:AC+15]
  MF AC+12, RA+8, QX+8, v[AC+12:AC+15]
  MF AC+12, K7, QX+12, v[AC+12:AC+15]
  v_max3_f32 v[LX], v[AC+8], v[AC+9], v[AC+10]
  s_nop 6
  v_max3_f32 v[LX], v[LX], v[AC+11], v[AC+12]
  v_max3_f32 v[LX], v[LX], v[AC+13], v[AC+14]
  v_max_f32 v[LX], v[LX], v[AC+15]
  STAMP 10
  ALN AL3
LQ_b2:
  s_waitcnt vmcnt(3)
  MX0 MB
  s_waitcnt vmcnt(2)
  v_mfma_f32_16x16x32_bf16 v[AC:AC+3], v[MB+4:MB+7], v[MB:MB+3], 0
  MX1 MB+4
  s_waitcnt vmcnt(1)
  MX1 MB+8
  s_waitcnt vmcnt(0)
  v_mfma_f32_16x16x32_bf16 v[AC:AC+3], v[MB+12:MB+15], v[MB+8:MB+11], v[AC:AC+3]
  MX1 MB+12
  v_cmp_eq_u32 vcc, -1, v[MX]
  s_nop 3
  s_cbranch_vccnz LQ_rb2
  v_add_f32_dpp v[AC], v[AC+2], v[AC] quad_perm:[2,3,0,1] row_mask:0xf bank_mask:0xf
  v_add_f32_dpp v[AC+1], v[AC+3], v[AC+1] quad_perm:[2,3,0,1] row_mask:0xf bank_mask:0xf
  DM 3
  ds_write_b64 v[180], v[AC:AC+1]
  DM 4
  ds_write_b32 v[FW], v[TAG] offset:2048
  DM 5
  s_barrier
  STAMP 5
  s_setprio 0
  MF AC+4, MB+4, QX, 0
  MF S1, MB+4, QX+4, 0
  MF AC+4, MB+12, QX+8, v[AC+4:AC+7]
  MF S1, MB+12, QX+12, v[S1:S1+3]
  FLAGQ s64
.irp e,0,1,2,3
  ds_read_b64_tr_b16 v[MB+4*\e:MB+4*\e+1], v[RES] offset:512*\e
  ds_read_b64_tr_b16 v[MB+4*\e+2:MB+4*\e+3], v[RES] offset:512*\e+4096
.endr
.if )" STR(AEARLY) R"(
  SOFTX
.endif
  v_mov_b32 v[FA], s62
  ds_read_b32 v[FB], v[FA] offset:8
  s_mov_b32 s76, 0x400000
LA_w:
  ds_read_b32 v[FA], v[FW] offset:2304
  CAP
  s_waitcnt lgkmcnt(0)
  v_cmp_gt_u32 vcc, s65, v[FA]
  s_cbranch_vccz LA_g
  s_sleep 1
  s_branch LA_w
LA_g:
  STAMP 8
  v_readfirstlane_b32 s59, v[FB]
  s_setprio 2
.irp e,0,1,2,3
  ds_read_b64_tr_b16 v[RA+2*\e:RA+2*\e+1], v[AVM] offset:256*\e
.endr
.if !)" STR(AEARLY) R"(
  SOFTX
  s_nop 0
.endif
.irp e,0,4,8,12
  MF OA+\e, MB+\e, P4, 0
.endr
  s_waitcnt lgkmcnt(0)
.irp e,0,1,2,3
  v_mfma_f32_16x16x16_bf16 v[OA+4*\e:OA+4*\e+3], v[RA+2*\e:RA+2*\e+1], v[PB:PB+1], v[OA+4*\e:OA+4*\e+3]
.endr
  s_nop 1
.irp i,0,1,2,3,4,5,6,7
  v_cvt_pk_bf16_f32 v[KX+\i], v[OA+2*\i], v[OA+2*\i+1]
.endr
.irp i,0,1,4,5
  v_permlane16_swap_b32 v[KX+\i], v[KX+\i+2]
.endr
  STAMP 9
  s_setprio 3
  NEXT MB
  s_cmp_lg_u32 s64, 64
  s_cbranch_execz 1f
  s_cbranch_scc1 LQ_loop2
1:
  OXST
  s_waitcnt vmcnt(0)
  FLAGQ 64
  s_cbranch_execnz L_end
  COLDA 2
.if EF
LQ_ef:
  s_waitcnt vmcnt(1)
  FLAGQ s64
  s_branch LQ_eg
.endif
LQ_rb2:
  RETRY 0
.if VQ
  v_or_b32 v[TAG], 0x10000, v[TAG]
.endif
  QKQ
  s_cbranch_execnz LQ_b2
  s_setpc_b64 s[48:49]
; ---- wave 3: pacing, clearing, sum over the heads
.set CK,144
.set RA,162
.set RC,184
.macro PARTQ
  LD MB, 178, s66, 0
  LD MB+4, 178, s66, 1024
  LD MB+8, 178, s66, 2048
  LD MB+12, 178, s66, 3072
.endm
LQ_init3:
  s_memrealtime vcc
  s_mov_b64 s[44:45], 0
  s_mov_b32 s46, s62
  s_mov_b32 s79, NS*GS
  s_mov_b32 s62, s101
  s_waitcnt lgkmcnt(0)
  s_lshl_b32 s73, vcc_lo, 4
  PHASEA 3
  s_setprio PLO
  v_mov_b32 v[MB+4], s46
  s_memtime s[96:97]
  s_memrealtime s[94:95]
  ds_read_b128 v[VA:VA+3], v[XA] offset:8192
  v_mov_b64 v[MB:MB+1], -1
  v_mov_b64 v[MB+2:MB+3], -1
  v_rcp_f32 v[CK+6], s45
  v_mov_b64 v[CK:CK+1], s[44:45]
  v_cvt_f32_u32 v[CK+4], s57
  v_mul_f32 v[CK+6], s44, v[CK+6]
  s_waitcnt lgkmcnt(0)
  s_sub_u32 s76, s96, s90
  s_sub_u32 s77, s94, s91
  s_mov_b32 s90, s96
  s_mov_b32 s91, s94
  v_mul_f32 v[CK+6], 256.0, v[CK+6]
  v_cvt_f32_u32 v[CK+2], s76
  v_cvt_f32_u32 v[CK+3], s77
  v_cvt_i32_f32 v[CK+6], v[CK+6]
  v_fmac_f32 v[CK+2], )" STR(CKA) R"(, v[CK]
  v_fmac_f32 v[CK+3], )" STR(CKA) R"(, v[CK+1]
  v_rcp_f32 v[CK+5], v[CK+2]
  v_readfirstlane_b32 s51, v[CK+6]
  v_readfirstlane_b32 s44, v[CK+2]
  v_readfirstlane_b32 s45, v[CK+3]
  v_mul_f32 v[CK+3], )" STR(CKF) R"(, v[CK+3]
  v_fma_f32 v[CK+3], v[CK+3], v[CK+5], -1.0
  v_med3_f32 v[CK+3], v[CK+3], -0.5, 1.0
  v_mul_f32 v[CK+3], v[CK+3], v[CK+4]
  v_cvt_i32_f32 v[CK+3], v[CK+3]
  STAMP 7
  s_nop 0
  v_readfirstlane_b32 s47, v[CK+3]
  WAITK VD+CD, NQ
  buffer_store_dwordx4 v[MB:MB+3], v[179], s[40:43], s67 offen sc1
  buffer_store_dwordx2 v[MB:MB+1], v[182], s[40:43], s67 offen sc1
  buffer_store_dwordx2 v[MB:MB+1], v[180], s[40:43], s70 offen sc1
  buffer_store_dwordx2 v[MB:MB+1], v[181], s[40:43], s70 offen sc1
  ds_read_b128 v[RC:RC+3], v[MB+4]
  ds_read_b32 v[RA], v[MB+4] offset:24
.if !XA3
  XCHK 3
.endif
  XREF 3
  STAMP 5
  s_barrier
  s_memrealtime s[94:95]
  s_mov_b32 s76, 0x400000
  s_mov_b32 s78, s73
LQ_t3:
  s_waitcnt lgkmcnt(0)
  v_cmp_gt_u32 vcc, s65, v[RC+3]
  s_cbranch_vccnz LQ_t4
  v_readfirstlane_b32 s73, v[RC]
  v_readfirstlane_b32 s74, v[RC+1]
  v_readfirstlane_b32 s59, v[RC+2]
  v_readfirstlane_b32 s92, v[RA]
  s_sub_i32 s78, s73, s78
  s_cmp_gt_i32 s78, 512
  s_cbranch_scc1 LQ_slip
LQ_b5:
  s_lshl1_add_u32 s4, s74, s57
  s_add_i32 s50, s4, s73
  PLAN
  s_sub_i32 s5, s73, s50
  s_mul_i32 s4, s4, s51
  s_mul_i32 s5, s5, s51
  s_lshr_b32 s4, s4, 12
  s_lshr_b32 s5, s5, 12
  s_add_i32 s4, s4, s92
  s_sub_i32 s4, s4, E2
  s_sub_i32 s5, s5, E3
  s_cmp_lt_u32 s64, NS
  s_cselect_b32 s78, GS, 0
  s_sub_u32 s79, s79, s78
  s_ashr_i32 s47, s47, 1
  s_add_i32 s47, s47, s79
.if XA3
  XCHK 3
.endif
.if CSL
  s_sleep CSL
.endif
  s_lshl4_add_u32 s78, s94, s62
  s_sub_i32 s78, s78, s50
  s_cmp_gt_i32 s78, LT
  s_cselect_b32 s78, s78, -GM
  s_add_i32 s78, s78, s50
  s_add_i32 s78, s78, GM
  s_setprio 3
  WAITP s78
  STAMP 8
  PARTQ
  s_memrealtime vcc
  s_memtime s[6:7]
  v_mov_b32 v[RA], s46
  XL4 XA
  XADV
  ZR AC, 8
  v_add_u32 v[TAG], 1, v[TAG]
  s_waitcnt lgkmcnt(0)
  s_add_i32 s6, s6, s5
  v_mov_b32 v[RA+1], s6
  ds_write_b32 v[RA], v[RA+1] offset:20
LQ_b3:
  s_waitcnt lgkmcnt(0)
.if BENCH
  s_mov_b32 m0, s64
  s_mov_b32 s77, vcc_lo
  v_writelane_b32 v[FW], s77, m0
.endif
  s_lshl_b32 s89, vcc_lo, 4
  s_sub_i32 s89, s89, s50
  s_add_i32 s78, s89, )" STR(SLIP_ADD) R"(
  s_sub_i32 s77, s89, E0
  s_max_i32 s77, s77, 0
  s_cmp_gt_i32 s89, )" STR(SLIP_THR) R"(
  s_cselect_b32 s78, s78, s77
  s_cselect_b32 s77, SGAP, 0
  s_add_i32 s78, s78, s73
  s_and_b32 s78, s78, -2
  s_add_i32 s77, s77, s47
  s_lshr_b32 s76, SG0, s64
  s_cmp_lt_u32 s64, 8
  s_cselect_b32 s76, s76, 0
  s_add_i32 s77, s77, s76
  s_cmp_eq_u64 exec, 0
  s_cselect_b32 s89, 0x100000, s89
  v_mov_b32 v[RA], s78
  v_mov_b32 v[RA+1], s77
  ALN AL4
  s_waitcnt vmcnt(3+XC)
  v_mfma_f32_16x16x32_bf16 v[RC:RC+3], v[VA:VA+3], v[MB:MB+3], 0
  MX0 MB
.irp i,1,2,3
  s_waitcnt vmcnt(3+XC-\i)
  v_mfma_f32_16x16x32_bf16 v[RC:RC+3], v[VA:VA+3], v[MB+4*\i:MB+4*\i+3], v[RC:RC+3]
  MX1 MB+4*\i
.endr
  v_cmp_eq_u32 vcc, -1, v[MX]
  STAMP 9
  s_nop 3
.irp e,0,1,2,3
  v_add_f32_dpp v[RC+\e], v[RC+\e], v[RC+\e] row_ror:8 row_mask:0xf bank_mask:0xf
.endr
  v_cvt_pk_bf16_f32 v[RC], v[RC], v[RC+1]
  v_cvt_pk_bf16_f32 v[RC+1], v[RC+2], v[RC+3]
  s_cbranch_vccnz LQ_rb3
  buffer_store_dwordx2 v[RC:RC+1], v[180], s[40:43], s69 offen sc1
  buffer_store_dwordx2 v[RA:RA+1], v[181], s[40:43], s69 offen sc1
  STAMP 10
  ZR VA, 2
  ZR VB, 2
  NEXT MB
  STAMP 0
  s_cmp_gt_i32 s89, )" STR(TG_THR) R"(
  s_cbranch_scc0 LQ_bar3
  s_cbranch_execz LD_r3
LD_t3:
  TGET RA
  s_cbranch_execnz LQ_bar3
LD_r3:
  s_setpc_b64 s[48:49]
LD_c3:
  COLDA 3
  XWAIT 3
LQ_rb3:
  RETRY 1
  PARTQ
  s_memrealtime vcc
  s_waitcnt vmcnt(0)
  s_cbranch_execnz LQ_b3
LQ_t4:
  ds_read_b128 v[RC:RC+3], v[MB+4]
  CAP
  s_cbranch_execnz LQ_t3
  s_setpc_b64 s[48:49]
LQ_slip:
.if BENCH
  s_bitset1_b64 s[86:87], s64
.endif
  s_cmp_lt_u32 s64, NS
  s_cbranch_scc1 LQ_b5
  s_add_i32 s79, s79, )" STR(SLIP_INC) R"(
  s_min_u32 s79, s79, 4000
  s_branch LQ_b5
; ======== waves 4..7
.set SMK, SMV
.set UB,128
.set B1,160
.set O3,168
.set O1,169
.set OT,170
.set ST1,171
.set ST2,172
.set AIM,173
.set RR,175
.set FW,176
.set FR,177
.set FS,178
.set ASC,179
.set ASM,180
.set AVX,181
.set SIX,182
.set RES,184
.set TAG,185
.set MX,186
.set TM,187
.set MB,188
.set AC,204
.set TV,212
.set FA,214
.set RA,216
.set DD,188
.set SX,208
.set SMM,212
.set ZZ,224
.set VM,240
.set PA,248
.set VX,188
.set UU,220
.macro SCV
  ds_read_b32 v[FA], v[FS]
  ds_read_b128 v[SX:SX+3], v[ASC]
  ds_read_b64 v[SMM:SMM+1], v[ASM]
.endm
.macro CZV t
  v_cvt_pk_bf16_f32 v[UB+8*\t+2], v[ZZ+4*\t], v[ZZ+4*\t+2]
  v_cvt_pk_bf16_f32 v[UB+8*\t+3], v[ZZ+4*\t+1], v[ZZ+4*\t+3]
.endm
.macro URD h, r
.irp k,0,1,2,3
  ds_read_b128 v[VX+\r+4*\k:VX+\r+4*\k+3], v[TM] offset:1024*(\h+\k)
.endr
.endm
.macro UMM h, r, n
.irp k,0,1,2,3
  s_waitcnt lgkmcnt(\n+3-\k)
.irp t,0,1,2,3
.if \k == 0
  v_mfma_f32_16x16x32_bf16 v[UU+\r+4*\t:UU+\r+4*\t+3], v[VX+\r:VX+\r+3], v[16*\t:16*\t+3], 0
.else
  v_mfma_f32_16x16x32_bf16 v[UU+\r+4*\t:UU+\r+4*\t+3], v[VX+\r+4*\k:VX+\r+4*\k+3], v[16*\t+4*\k:16*\t+4*\k+3], v[UU+\r+4*\t:UU+\r+4*\t+3]
.endif
.endr
.endr
  s_nop 1
.irp t,0,1,2,3
  v_cvt_pk_bf16_f32 v[UB+8*\t+\h], v[UU+\r+4*\t], v[UU+\r+4*\t+1]
  v_cvt_pk_bf16_f32 v[UB+8*\t+\h+1], v[UU+\r+4*\t+2], v[UU+\r+4*\t+3]
.endr
.endm
.macro UREST
.if US
  UMM 4, 0, 0
.endif
  ZR ZZ, 8
  ZR DD, 10
  v_mov_b32 v[TAG], s65
.endm
.macro STEPV R
LV_loop\R:
  STAMP 0
.if US
  URD 4, 0
.if \R == 3
  ds_read_b32 v[MX], v[183] offset:20
.endif
  s_waitcnt lgkmcnt(0)
.endif
.if \R == 3
  v_readfirstlane_b32 s4, v[MX]
  s_setprio 3
  WAITP s73
  s_memtime s[6:7]
.endif
  s_barrier
  s_memtime s[92:93]
  s_setprio 1
  s_nop )" STR(VDEL1) R"(
  STAMP 1
  UREST
.if \R == 3
  v_mov_b32 v[TV+2], s6
  buffer_load_dwordx2 v[TV:TV+1], v[OT], s[40:43], s68 offen sc1
  ds_write_b32 v[183], v[TV+2] offset:24
.endif
.if \R == 2
  v_mov_b64 v[PA:PA+1], -1
  v_mov_b64 v[PA+2:PA+3], -1
.rept CV
  s_nop 3
.endr
.irp o,0,1024,2048,3072
  buffer_store_dwordx4 v[PA:PA+3], v[ST1], s[40:43], s67 offen offset:\o sc1
.endr
.endif
  s_mov_b32 s76, 0x400000
  s_setprio 2
  STAMP 9
  ALN AL6
  s_barrier
  s_memtime s[6:7]
  STAMP 10
.if \R == 3
  s_cmp_eq_u32 s64, 0
  s_cbranch_scc1 LV_t0
LV_t1:
  s_waitcnt vmcnt(0)
  v_cmp_eq_u32 vcc, -1, v[TV]
  v_subrev_u32 v[TV], s73, v[TV]
  v_max_i32_dpp v[TV+1], v[TV+1], v[TV+1] quad_perm:[1,0,3,2] row_mask:0xf bank_mask:0xf
  s_cbranch_vccnz LV_rt
  v_max_i32_dpp v[TV], v[TV], v[TV] quad_perm:[1,0,3,2] row_mask:0xf bank_mask:0xf
  v_max_i32_dpp v[TV+1], v[TV+1], v[TV+1] quad_perm:[2,3,0,1] row_mask:0xf bank_mask:0xf
  s_nop 0
  v_max_i32_dpp v[TV], v[TV], v[TV] quad_perm:[2,3,0,1] row_mask:0xf bank_mask:0xf
  s_nop 0
  v_readfirstlane_b32 s75, v[TV]
  v_add_u32 v[TV], s73, v[TV]
  v_readfirstlane_b32 s74, v[TV+1]
  s_add_i32 s73, s73, s75
  s_cmp_gt_i32 s75, 512
  s_cselect_b32 s75, )" STR(SLIP_INC) R"(*45/32, 0
  s_cmp_lt_u32 s64, NS
  s_cselect_b32 s75, -(GS*45/32), s75
  s_add_i32 s75, s75, s59
  v_mov_b32 v[TV+2], s75
  v_mov_b32 v[TV+3], s65
  ds_write_b128 v[183], v[TV:TV+3]
  PLAN
.endif
.if \R == 2
  HT HB, FA
.endif
.if \R == 0
.rept VN3
  s_nop 1
.endr
LV_p:
  ds_read_b32 v[FA], v[FR]
.irp k,0,1,2,3
  ds_read2st64_b32 v[RA+2*\k:RA+2*\k+1], v[RR-(\k&1)] offset0:8*\k offset1:8*\k+1
.endr
  s_waitcnt lgkmcnt(4)
  v_cmp_gt_u32 vcc, s65, v[FA]
  s_waitcnt lgkmcnt(2)
  v_add_f32 v[RA], v[RA], v[RA+2]
  v_add_f32 v[RA+1], v[RA+1], v[RA+3]
  s_waitcnt lgkmcnt(0)
  v_add_f32 v[RA+4], v[RA+4], v[RA+6]
  v_add_f32 v[RA+5], v[RA+5], v[RA+7]
  v_add_f32 v[RA], v[RA], v[RA+4]
  v_add_f32 v[RA+1], v[RA+1], v[RA+5]
  v_cvt_pk_bf16_f32 v[RES], v[RA], v[RA+1]
  s_cbranch_vccnz LV_pb
  H1ST
.endif
  STAMP 3
.if \R != 2
  s_cmp_eq_u32 s64, 63
  s_cbranch_scc1 L_last
.endif
  s_waitcnt lgkmcnt(0)
.if \R == 2
  s_waitcnt vmcnt(0)
.endif
  s_sub_i32 s77, s6, s92
  s_cmp_gt_i32 s77, NV+200
  s_cselect_b32 s77, LXC, 0
  s_add_i32 s6, s6, s77
  s_setprio 3
  WAITK VD, NV
  STAMP 4
  LD VM, O1, s66, 0
  LD VM+4, O1, s66, 1024
  s_mov_b32 s76, 0x400000
.if \R == 2
  s_cmp_eq_u32 s64, 63
  s_cbranch_scc1 LV_lst
.endif
  ALN AL5
  s_barrier
  SCV
  STAMP 5
.if VR == 0
  s_waitcnt vmcnt(1)
  SM ZZ, VM, 0
  v_max_u32 v[RES], v[VM], v[VM+1]
  v_max3_u32 v[RES], v[RES], v[VM+2], v[VM+3]
  SM ZZ+4, VM, 16
  SM ZZ+8, VM, 32
  SM ZZ+12, VM, 48
LV_s\R:
  s_waitcnt lgkmcnt(0)
  STAMP 6
  v_cmp_ne_u32 vcc, s65, v[FA]
  v_max3_f32 v[MX], v[SX], v[SX+1], v[SX+2]
  v_max3_f32 v[TM], v[SX+3], v[SMM], v[SMM+1]
  v_max_f32 v[MX], v[MX], v[TM]
  s_cbranch_vccnz LV_rs\R
LV_c\R:
  s_nop 0
  v_max_f32_dpp v[MX], v[MX], v[MX] quad_perm:[2,3,0,1] row_mask:0xf bank_mask:0xf
  v_mov_b32 v[TM], v[MX]
  s_nop 1
  v_permlane16_swap_b32 v[MX], v[TM]
  v_max_f32 v[MX], v[MX], v[TM]
  v_mov_b32 v[TM], v[MX]
  s_waitcnt vmcnt(0)
  SM ZZ, VM+4, 8
  v_permlane32_swap_b32 v[MX], v[TM]
  v_max_f32 v[MX], v[MX], v[TM]
  v_mul_f32 v[MX], s79, v[MX]
  SM ZZ+4, VM+4, 24
  v_fma_f32 v[SX], v[SX], s78, v[MX]
  v_fma_f32 v[SX+1], v[SX+1], s78, v[MX]
  v_fma_f32 v[SMM], v[SMM], s78, v[MX]
  SM ZZ+8, VM+4, 40
  v_fma_f32 v[SMM+1], v[SMM+1], s78, v[MX]
  v_fma_f32 v[SX+2], v[SX+2], s78, v[MX]
  v_fma_f32 v[SX+3], v[SX+3], s78, v[MX]
  SM ZZ+12, VM+4, 56
  v_exp_f32 v[SX], v[SX]
  v_exp_f32 v[SX+1], v[SX+1]
  v_exp_f32 v[SMM], v[SMM]
  v_cvt_pk_bf16_f32 v[PA], v[SX], v[SX+1]
  v_exp_f32 v[SMM+1], v[SMM+1]
  v_cvt_pk_bf16_f32 v[PA+1], v[SMM], v[SMM]
  v_exp_f32 v[SX+2], v[SX+2]
  v_exp_f32 v[SX+3], v[SX+3]
  v_cvt_pk_bf16_f32 v[PA+3], v[SMM+1], v[SMM+1]
  v_cvt_pk_bf16_f32 v[PA+2], v[SX+2], v[SX+3]
.else
.rept DWM
  v_mfma_f32_16x16x32_bf16 v[PA:PA+3], v[B1:B1+3], v[B1:B1+3], 0
.endr
LV_s\R:
  s_waitcnt lgkmcnt(0)
  STAMP 6
  v_cmp_gt_u32 vcc, s65, v[FA]
  v_max3_f32 v[MX], v[SX], v[SX+1], v[SX+2]
  v_max3_f32 v[TM], v[SX+3], v[SMM], v[SMM+1]
  v_max_f32 v[MX], v[MX], v[TM]
  s_cbranch_vccnz LV_rs\R
.if VR == 2
  s_waitcnt vmcnt(1)
  SM ZZ, VM, 0
.else
  s_nop 0
.endif
  v_max_f32_dpp v[MX], v[MX], v[MX] quad_perm:[2,3,0,1] row_mask:0xf bank_mask:0xf
  v_mov_b32 v[TM], v[MX]
.if VR == 2
  SM ZZ+4, VM, 16
  s_nop 0
.else
  s_waitcnt vmcnt(1)
  SM ZZ, VM, 0
.endif
  v_permlane16_swap_b32 v[MX], v[TM]
  v_max_f32 v[MX], v[MX], v[TM]
  v_mov_b32 v[TM], v[MX]
  v_max_u32 v[RES], v[VM], v[VM+1]
.if VR == 2
  SM ZZ+8, VM, 32
.else
  SM ZZ+4, VM, 16
.endif
  v_permlane32_swap_b32 v[MX], v[TM]
  v_max_f32 v[MX], v[MX], v[TM]
  v_mul_f32 v[MX], s79, v[MX]
.if VR == 2
  SM ZZ+12, VM, 48
.else
  SM ZZ+8, VM, 32
.endif
  v_fma_f32 v[SX], v[SX], s78, v[MX]
  v_fma_f32 v[SX+1], v[SX+1], s78, v[MX]
  v_fma_f32 v[SMM], v[SMM], s78, v[MX]
.if VR == 2
  s_waitcnt vmcnt(0)
  SM ZZ, VM+4, 8
.else
  SM ZZ+12, VM, 48
.endif
  v_fma_f32 v[SMM+1], v[SMM+1], s78, v[MX]
  v_fma_f32 v[SX+2], v[SX+2], s78, v[MX]
  v_fma_f32 v[SX+3], v[SX+3], s78, v[MX]
.if VR == 2
  SM ZZ+4, VM+4, 24
.else
  s_waitcnt vmcnt(0)
  SM ZZ, VM+4, 8
.endif
  v_exp_f32 v[SX], v[SX]
.if VR == 2
  SM ZZ+8, VM+4, 40
.else
  SM ZZ+4, VM+4, 24
.endif
  v_exp_f32 v[SX+1], v[SX+1]
.if VR == 2
  SM ZZ+12, VM+4, 56
.else
  SM ZZ+8, VM+4, 40
.endif
  v_exp_f32 v[SMM], v[SMM]
.if VR == 1
  SM ZZ+12, VM+4, 56
.endif
  v_cvt_pk_bf16_f32 v[PA], v[SX], v[SX+1]
  v_exp_f32 v[SMM+1], v[SMM+1]
  v_cvt_pk_bf16_f32 v[PA+1], v[SMM], v[SMM]
  v_exp_f32 v[SX+2], v[SX+2]
  v_exp_f32 v[SX+3], v[SX+3]
  v_cvt_pk_bf16_f32 v[PA+3], v[SMM+1], v[SMM+1]
  v_cvt_pk_bf16_f32 v[PA+2], v[SX+2], v[SX+3]
  v_max3_u32 v[RES], v[RES], v[VM+2], v[VM+3]
.endif
LV_z\R:
  CZV 0
  SM DD+16, PA, B1
  CZV 1
  SM DD, PA, UB
  CZV 2
  v_max3_u32 v[RES], v[RES], v[VM+4], v[VM+5]
  SM DD+4, PA, UB+8
  CZV 3
  v_max3_u32 v[RES], v[RES], v[VM+6], v[VM+7]
  SM DD+8, PA, UB+16
  v_add_f32 v[DD+16], v[DD+16], v[DD+18]
  v_add_f32 v[DD+17], v[DD+17], v[DD+19]
  v_cmp_eq_u32 vcc, -1, v[RES]
  SM DD+12, PA, UB+24
  v_rcp_f32 v[DD+16], v[DD+16]
  v_rcp_f32 v[DD+17], v[DD+17]
  s_cbranch_vccnz LV_rb\R
  HS DD
  HS DD+4
  HS DD+8
  v_pk_mul_f32 v[DD:DD+1], v[DD:DD+1], v[DD+16:DD+17]
  v_pk_mul_f32 v[DD+4:DD+5], v[DD+4:DD+5], v[DD+16:DD+17]
  HS DD+12
  v_pk_mul_f32 v[DD+8:DD+9], v[DD+8:DD+9], v[DD+16:DD+17]
  v_cvt_pk_bf16_f32 v[SX], v[DD], v[DD+4]
  v_pk_mul_f32 v[DD+12:DD+13], v[DD+12:DD+13], v[DD+16:DD+17]
  v_cvt_pk_bf16_f32 v[SX+2], v[DD+1], v[DD+5]
  v_cvt_pk_bf16_f32 v[SX+1], v[DD+8], v[DD+12]
  v_cvt_pk_bf16_f32 v[SX+3], v[DD+9], v[DD+13]
  buffer_store_dwordx4 v[SX:SX+3], v[ST2], s[40:43], s66 offen sc1
  STAMP 7
  s_setprio 1
.if \R == 2
LV_img:
  v_add_u32 v[TM], 1024, v[AIM]
  ds_write2_b32 v[AIM], v[VM], v[VM+1] offset1:2
  ds_write2_b32 v[AIM], v[VM+2], v[VM+3] offset0:32 offset1:34
  ds_write2_b32 v[TM], v[VM+4], v[VM+5] offset1:2
  ds_write2_b32 v[TM], v[VM+6], v[VM+7] offset0:32 offset1:34
  ds_write_b32 v[FW], v[TAG] offset:2304
  s_cmp_eq_u32 s64, 63
  s_cbranch_scc1 L_end
.endif
LV_u\R:
  XSL s65
  v_add_u32 v[TM], s75, v[AVX]
  ds_read_b32 v[MX], v[183] offset:8
  URD 0, 0
.if US
  UMM 0, 0, 0
.else
  URD 4, 16
  UMM 0, 0, 4
  UMM 4, 16, 0
.endif
  v_readfirstlane_b32 s59, v[MX]
  STAMP 8
  NEXT MB
  s_cbranch_execnz LV_loop\R
.if \R == 0
  s_cmp_eq_u32 s60, 1
  s_cbranch_scc1 LD_t3
LV_pb:
.if BENCH
  s_bitset1_b64 s[86:87], s64
.endif
  CAP
  s_cbranch_execnz LV_p
.endif
.if VQ
LV_q\R:
  s_waitcnt vmcnt(0)
  v_max3_u32 v[FA], v[RES], v[VM+4], v[VM+5]
  v_max3_u32 v[FA], v[FA], v[VM+6], v[VM+7]
  v_cmp_eq_u32 vcc, -1, v[FA]
  s_cbranch_execz 1f
  s_cbranch_vccz LV_c\R
1:
  s_nop 7
  LD VM, O1, s66, 0
  LD VM+4, O1, s66, 1024
  ZR ZZ, 8
  s_waitcnt vmcnt(1)
  SM ZZ, VM, 0
  v_max_u32 v[RES], v[VM], v[VM+1]
  v_max3_u32 v[RES], v[RES], v[VM+2], v[VM+3]
  SM ZZ+4, VM, 16
  SM ZZ+8, VM, 32
  SM ZZ+12, VM, 48
  s_nop 7
  s_cbranch_execnz LV_c\R
.endif
LV_rb\R:
  RETRY 0
  LD VM, O1, s66, 0
  LD VM+4, O1, s66, 1024
  ZR ZZ, 8
  ZR DD, 10
  s_waitcnt vmcnt(0)
.irp t,0,1,2,3
  SM ZZ+4*\t, VM, 16*\t
  s_nop 7
  SM ZZ+4*\t, VM+4, 16*\t+8
.endr
  v_max_u32 v[RES], v[VM], v[VM+1]
  v_max3_u32 v[RES], v[RES], v[VM+2], v[VM+3]
  s_nop 7
  s_cbranch_execnz LV_z\R
.if \R == 2
LV_lst:
  s_cbranch_execz LV_63
  s_barrier
LV_63:
  s_waitcnt vmcnt(0)
  MX0 VM
  MX1 VM+4
  v_cmp_eq_u32 vcc, -1, v[MX]
  s_cbranch_execz 1f
  s_cbranch_vccz LV_img
1:
  RETRY 0
  LD VM, O1, s66, 0
  LD VM+4, O1, s66, 1024
  s_cbranch_execnz LV_63
.endif
LV_rs\R:
.if VQ
  v_and_b32 v[FA], 0xffff, v[FA]
  v_cmp_ne_u32 vcc, s65, v[FA]
  s_cbranch_execz 1f
  s_cbranch_vccz LV_q\R
1:
.endif
.if BENCH
  s_bitset1_b64 s[86:87], s64
.endif
  SCV
  CAP
  s_cbranch_execnz LV_s\R
.if \R == 3
LV_t0:
  s_memrealtime vcc
  s_waitcnt vmcnt(0) lgkmcnt(0)
  s_lshl_b32 s73, vcc_lo, 4
  s_sub_i32 s73, s73, 0x2000
  v_mov_b32 v[TV], s73
  v_mov_b32 v[TV+1], 0
  s_cbranch_execnz LV_t1
LV_rt:
  RETRY 2
  buffer_load_dwordx2 v[TV:TV+1], v[OT], s[40:43], s68 offen sc1
  s_cbranch_execnz LV_t1
.endif
  s_setpc_b64 s[48:49]
.endm
LV_init:
  s_memrealtime vcc
  PRE
  s_waitcnt lgkmcnt(0)
  s_lshl_b32 s73, vcc_lo, 4
  s_mov_b32 s78, 0x3e0293ee
  s_mov_b32 s79, 0xbe0293ee
  ZR UB, 16
  v_mov_b32 v[B1], 0x3f803f80
  v_mov_b32 v[B1+1], 0x3f803f80
  v_mov_b32 v[B1+2], 0x3f80
  v_mov_b32 v[B1+3], 0x3f80
  v_mov_b32 v[B1+4], 0x3f803f80
  v_mov_b32 v[B1+5], 0x3f803f80
  v_mov_b64 v[B1+6:B1+7], 0
  s_cbranch_execz LD_v
  s_cmp_eq_u32 s60, 2
  s_cbranch_scc1 LV_u2
  s_cmp_eq_u32 s60, 3
  s_cbranch_scc1 LV_u3
  s_branch LV_u0
LD_v:
  s_mov_b32 s64, 1
  s_cmp_eq_u32 s60, 2
  s_cbranch_scc1 LV_loop2
  s_cmp_eq_u32 s60, 3
  s_cbranch_scc1 LV_loop3
  STEPV 0
  STEPV 2
  STEPV 3
L_fatal:
  v_mov_b32 v[MX], 0
  v_mov_b32 v[TAG], 1
  global_store_dword v[MX], v[TAG], s[48:49] sc1
  s_waitcnt vmcnt(0)
  s_endpgm
L_last:
  s_barrier
L_end:
  s_waitcnt vmcnt(0) lgkmcnt(0)
)"
static_assert(__builtin_bit_cast(unsigned, SCALE * LOG2E) == 0x3e0293eeu, "");
#define CL10(p) p "0", p "1", p "2", p "3", p "4", p "5", p "6", p "7", p "8", p "9"
#define CL_ST
#define CL_S CL_ST "s4", "s5", "s6", "s7", "s72", "s73", "s74", "s75", "s76", "s77", "s78", "s79", "s88", "s89", "s90", "s91", "s92", "s93", "s94", "s95", "s96", "s97", "s98", "s99", "s100", "s101", CL10("s3"), "vcc", "scc", "memory"

#define WARM() do {} while (0)
#define CL_DRY
__device__ __forceinline__ void chain(const Args& a, const int c, const unsigned L, const unsigned hb) {
    const int tid = threadIdx.x, w8 = RFL(tid >> 6), w = w8 & 3; int l = tid & 63;
    const int g = c & 3, aa = c >> 2, h = g * 8 + (aa >> 2), hp = h ^ XH * 8, jj = aa & 3;
    int n = l & 15, q = l >> 4, hf = (n >> 1) & 1, n2 = (n >> 2) & 1, n3 = n >> 3;
    const unsigned L_EX = L + 114688, L_SC = L + 131072, L_SM = L_SC + 2048, L_FL = L_SM + 512, L_TB = L_FL + 3072, L_VM = L_TB + 256, L_W2 = L + 143360, L_KX = L + 32768, L_VX = L_KX + 8192;
    const rsrc_t rs = mkrs(a.ring, RING_BYTES);
    unsigned* const err = a.ctl + C_ERR;
    const v4u ones = {~0u, ~0u, ~0u, ~0u};
    unsigned cyc0 = RFL((unsigned)__builtin_amdgcn_s_memtime()), rt0 = RFL((unsigned)__builtin_amdgcn_s_memrealtime());
    if (tid < 384) lp<v2u>(L_FL)[tid] = v2u{0u, 0u};
    int lg3 = (c & 31) * 128 + n2 * 64 + hf * 32 + (q & 1) * 16 + (n & 1) * 8;
    unsigned m0[4] = {};
    if (w8 == 3) { const bf16_t* p = a.mem0 + ((c & 1) * 4 + n2 * 2 + (q & 1)) * DM + 16 * (c >> 1) + (n & 3);
#pragma unroll
        for (int i = 0; i < 4; ++i) asm volatile("global_load_ushort %0, %1, off offset:%2" : "=v"(m0[i]) : "v"(p), "n"(8 * i) : "memory"); }
    auto marks = [&](auto nl) {
        const int ok = c < B_H3 || (c < B_TS + 2 && !(c & 2)) ? 0 : OOB;
#pragma unroll
        for (int i = 0; i < 2; ++i) STB(ones, rs, (((c & ~1) * 32768 + (c & 1) * 16384 + i * 8192 + tid * 16) | ok), 0);
        if (w8 == 3) asm volatile("s_waitcnt vmcnt(%0)" :: "n"(nl + 2) : "memory");
        asm volatile("" : "+v"(m0[0]), "+v"(m0[1]), "+v"(m0[2]), "+v"(m0[3]) :: "memory");
        const v2u m = {umin2(m0[0] | m0[1] << 16, 0xFFFFFFFEu), umin2(m0[2] | m0[3] << 16, 0xFFFFFFFEu)};
#pragma unroll
        for (int i = 0; i < 2; ++i) __builtin_amdgcn_raw_buffer_store_b64(m, rs, w8 == 3 ? (B_H3 + 4 * (c >> 5) + 2) * 32768 + ((q >> 1) * 2 + n3) * 4096 + lg3 : OOB, i * 16384, 16);
        FENCE();
    };
    if (!LF) marks(std::integral_constant<int, 0>{});
    const unsigned stg = L + w8 * 16384;
    const int rot = 0;
    const int prr = 16 * (q >> 1) + (q & 1) + 4 * n2 + 2 * n3;
    v32u W[6];
    auto trd4 = [&](int k, unsigned p, int o1, int o2) { const v2u t[4] = {trd(p), trd(p + o1), trd(p + o2), trd(p + o2 + o1)};
#pragma unroll
        for (int i = 0; i < 8; ++i) W[k >> 2][(k & 3) * 8 + i] = t[i >> 1][i & 1]; };
    unsigned cal;
    const unsigned d1c = (a.d1 * 45 >> 5) + NS * (GS * 45 / 32) - WKO;
    auto pg = [&](int blk, int x) { return blk * 32768 + (int)((((cal >> (blk >> 6) * 3) ^ ((x ^ blk >> 4) & 1) * 4 ^ ((x >> 1 ^ blk >> 5) & 1)) & 7) << 12); };
    auto h1o = [&](int inner, int x = 0) { return pg(B_H1 + (aa & 28) + (inner >> 12) * 2, g ^ x ^ XP) + (inner & 4095); };
    unsigned mw[8] = {}; (void)mw;
    auto stream = [&](auto issue, auto read, auto adr, bool nt) {
        if (CDL && !(((u64)(a.Wv + hp * DH) >> 8 & 3) == 1 && jj)) __builtin_amdgcn_s_sleep(CDL);
        MARKW(0);
        if (CPF && jj < 3) ahead(jj == 0 ? a.Wq : (jj == 1 ? a.Wk : a.Wv), hp, l, L_W2 + w8 * 1024, [&](int d) { return 256 * w + 64 * d + 32 * (w8 >> 2); });
        issue(0);
        if (LF) marks(std::integral_constant<int, 8>{});
        issue(1);
        MARKW(1);
        WAITV(8); BAR();
        unsigned h2 = hb;
        auto arrive = [&]() { gadd1(a.ctl + C_READY); if (c == 0) gst1(a.ctl + C_ACK, 1u); };
        if (w8 == 3) gld_req(h2, a.ctl + C_HB);
        read(std::integral_constant<int, 0>{}); WAITL(); issue(2);
        gld_wait<8>(h2);
        const bool seen = RFL(h2) != RFL(hb);
        if (seen) arrive();
        read(std::integral_constant<int, 1>{}); WAITL(); issue(3);
        WAITV(8); read(std::integral_constant<int, 2>{});
        WAITV(0); read(std::integral_constant<int, 3>{}); WAITL();
        if (w8 == 3 && !seen) { ctl_wait(a, hb); arrive(); }
    };
    auto start = [&]() {
        if (w8 == 6) {
            for (int i = 0; RFL(gld(a.ctl + C_READY)) < 256; ++i) { if (c == 0) gadd1(a.ctl + C_HB); __builtin_amdgcn_s_sleep(4); if (i > GCAP) fatal(err); }
            FENCE();
            const unsigned v = ~gld(a.ctl + C_CAL + (l & 15)) & 0xffffu;
            unsigned u = (2 * v + dppu<0xB1>(v) + dppu<0x1B>(dppu<HMIR>(v))) * 8 + (l & 7);
            u = min(u, dppu<0xB1>(u)); u = min(u, dppu<0x4E>(u)); u = min(u, dppu<HMIR>(u));
            unsigned t = ((__builtin_amdgcn_readlane(u, 0) & 7) | (__builtin_amdgcn_readlane(u, 8) & 7) << 3) ^ 022;
            *lp<v2u>(L_TB + 16) = v2u{t, cyc0}; *lp<v2u>(L_TB + 8) = v2u{d1c, 0u}; WAITL();
        }
        lp<unsigned>(L_EX + w8 * 2048 + 1536)[l] = 0; WAITL();
        BAR();
        cal = RFL(ldsr(L_TB + 16));
    };
    const unsigned char* xp = a.qkv + h * 24576 + w * 8192 + (w == 2 ? (jj & 1) * 4096 : 0);
    const unsigned char* xq = a.qkv + h * 24576 + 2 * NH * 24576;
    const unsigned xw = w == 3 ? 20480 - XC * 1024 : w * (XC == 4 ? 8192 : 4096);
    const unsigned* fG = a.ctl + C_G + h * 128 + w * 32;
    auto lo = [](const void* p) { return (unsigned)(u64)p; };
    auto hi = [](const void* p) { return (unsigned)((u64)p >> 32); };
    v16u S0 = {lo(a.ring), hi(a.ring), RING_BYTES, 0x20000u, lo(a.ox), hi(a.ox), NSEG * 262144, 0x20000u, lo(err), hi(err), lo(a.ctl + C_M + h * 128 + jj * 32), hi(a.ctl + C_M + h * 128 + jj * 32),
               lo(xq), hi(xq), lo(fG - w * 32), hi(fG - w * 32)};
    v16u S1 = {(unsigned)rot << 10, (unsigned)a.d2, (unsigned)a.per, d1c, (unsigned)w, L_KX + xw, L_TB, (unsigned)w8, 0u, cyc0, rt0, (unsigned)RFL(L_EX - 63744 + w8 * 2048), (unsigned)(a.d2 - a.d1 - KC), 0u, 0u, 0u};
    v8u cnts;
    v16u A;
    unsigned six;
    auto dry = [&]() {
        v16u T0 = S0, T1 = S1;
        asm volatile("s_mov_b64 exec, 0\n\ts_getpc_b64 s[48:49]\n\ts_add_u32 s48, s48, 12\n\ts_addc_u32 s49, s49, 0\n\ts_branch LD_go\n\ts_mov_b64 exec, -1\n\ts_setprio 0\n\ts_waitcnt lgkmcnt(0)\n\ts_nop 7\n\ts_nop 7"
                     : "+{s[40:55]}"(T0), "+{s[56:71]}"(T1), "+{v[188:219]}"(W[4]), "+{v[220:251]}"(W[5]) : "{v[0:31]}"(W[0]), "{v[32:63]}"(W[1]), "{v[64:95]}"(W[2]), "{v[96:127]}"(W[3])
                     : "v128", "v129", CL10("v13"), CL10("v14"), CL10("v15"), CL10("v16"), CL10("v17"), "v180", "v181", "v182", "v183", "v184", "v185", "v186", "v187",
                       "s80", "s81", "s82", "s83", "s84", "s85", "s86", "s87", CL_S);
        asm volatile("v_mbcnt_lo_u32_b32 %0, -1, 0\n\tv_mbcnt_hi_u32_b32 %0, -1, %0" : "=v"(l));
        n = l & 15; q = l >> 4; hf = (n >> 1) & 1; n2 = (n >> 2) & 1; n3 = n >> 3; six = hf ? 0xEEEE : 0x4444;
        lg3 = (c & 31) * 128 + n2 * 64 + hf * 32 + (q & 1) * 16 + (n & 1) * 8;
        if (CKL) { cyc0 = RFL((unsigned)__builtin_amdgcn_s_memtime()); rt0 = RFL((unsigned)__builtin_amdgcn_s_memrealtime()); S1[9] = cyc0; S1[10] = rt0; }
    };

    if (w8 < 4) {
        { const bf16_t* Wm = (jj >> 1 ? a.Wk : a.Wq) + (size_t)256 * w * HD + hp * DH + 64 * (jj & 1);
          const unsigned p = stg + prr * 128 + (((hf ^ (4 * n2 + 2 * n3)) * 16 + (n & 1) * 8) ^ ((w >> 1) * 64));
          stream([&](int i) { _Pragma("unroll 1") for (int j = 0; j < 8; ++j) gldsc(Wm + (size_t)(64 * (i ^ rot) + 8 * j) * HD, (l >> 3) * (HD * 2) + ((l & 7) ^ (2 * q)) * 16, stg + (i & 1) * 8192 + j * 1024); },
                 [&](auto i) { _Pragma("unroll") for (int t = 0; t < 4; ++t) trd4(4 * t + i, (p ^ (t * 32)) + (i & 1) * 8192, 1024, 4096); },
                 [&](int j, unsigned& vo) { vo = (l >> 3) * (HD * 2) + ((l & 7) ^ (2 * q)) * 16; return Wm + (size_t)(128 + 8 * j) * HD; }, true); }
        MARK(2);
        BAR();
        MARK(100);
        { const unsigned pv = stg + 65536 + prr * 64 + (hf ^ (2 * n2)) * 16 + (n & 1) * 8;
#pragma unroll
          for (int k = 0; k < 8; ++k) trd4(16 + k, (pv ^ ((k >> 2) * 32)) + ((k & 3) ^ rot) * 4096, 512, 2048); }
        if (w == 2) {
#pragma unroll
            for (int i = 0; i < 16; ++i) *lp<v4u>(L_W2 + l * 16 + 4096 + i * 1024) = v4u{W[4 + i / 8][i % 8 * 4], W[4 + i / 8][i % 8 * 4 + 1], W[4 + i / 8][i % 8 * 4 + 2], W[4 + i / 8][i % 8 * 4 + 3]}; }
        WAITL();
        dry();
        unsigned fv = 0;
        auto rows = [&]() {
#pragma unroll
            for (int i = 0; i < 4; ++i) if (w < 2 || !(i & 1)) glds1<4>(xp + (i >> 1) * (NH * 24576) + (i & 1) * 4096, l * 16, L_KX + (i >> 1) * 20480 + w * 8192 + (i & 1) * 4096); };
        MARK(101);
        if (w < 3) { fv = RFL(gld(fG)); FENCE(); }
        A[5] = w == 2 ? L_W2 + l * 16 : 16384 + (jj & 1) * 4096 + l * 16;
        A[13] = w < 3 ? xw + l * 16 : 0; A[14] = xw + 4096 + l * 16; A[15] = 0;
        if (w == 2) { const int ck = 8 * (jj >> 1) + hf;
#pragma unroll
            for (int i = 0; i < 8; ++i) lp<unsigned>(A[5] + (i >> 2) * 1024)[i & 3] = 16 * (i >> 2) + 4 * q + (i & 3) > 16 * (jj & 1) + n ? 0xFF800000u : 0u;
            A[13] = L_KX + l * 16; A[14] = L_VX + (ck * 16 + 4 * q + (n >> 2)) * 16 + (l & 1) * 8; A[15] = L_VM + ck * 128 + (2 * q + n2) * 16 + (l & 1) * 8; }
        if (w == 3) {
#pragma unroll
            for (int i = 0; i < 4; ++i) lp<unsigned>(A[5] + 8192)[i] = ((n & 7) >> 1) == i ? 0x3F80u << (16 * (n & 1)) : 0u; }
        const bool early = fv >= 2;
        if (w < 3 && early) rows();
        start();
        const int sl = 2 * (jj & 1) + (w >> 1);
        A[0] = pg(B_H3 + 4 * w, g ^ (H3X)) + (l ^ (w & 1)) * 16;
        A[1] = h1o((jj >> 1) * 2048 + ((sl * 2 + n3) * 2 + (w & 1)) * 128 + (q >> 1) * 64 + n2 * 32 + (q & 1) * 16 + (n & 3) * 4, XH);
        A[2] = L_EX + (w ^ 1) * 2048 + 1024 + l * 4; A[3] = L_EX + (w ^ 2) * 2048 + l * 4; A[4] = L_EX + (w ^ 3) * 2048 + 512 + l * 4;
        A[6] = L_FL + w * 256 + l * 4; A[7] = L_EX + (l & 3) * 2048 + 1536 + l * 4; A[8] = six;
        A[9] = w == 2 ? h * 8192 + jj * 2048 + ((2 * (q & 1) + (q >> 1)) * 16 + n) * 16 : (XC == 4 ? min(w, 2) * 128 : (w == 3 ? 128 + (l & 1) * 128 : 0));
        A[10] = w < 3 ? h1o(l * 16) : pg(2 * aa, g) + l * 16;
        A[11] = w < 2 ? L_KX + w * 4096 + l * 16 : h1o((w == 2 ? 2048 : jj * 1024) + l * 16);
        A[12] = w < 2 ? L_SC + w * 1024 + ((l * 16) ^ ((q & 1) * 32)) : (w == 2 ? L_SM + ((l * 8) ^ ((q & 1) * 16)) : pg(B_H3 + 4 * (c >> 5), (q >> 1) * 2 + n3) + lg3);
        if (w == 3) { A[13] = c % 33 == 0 && l < 4 ? pg(B_TS, l) + (c >> 5) * 128 : OOB; A[14] = h1o(4096 + jj * 512 + l * 8); }
        asm volatile("" : "+v"(A));
        if (w < 3 && !early) { fv = 0; flags_wait<4>(fv, fG, 2, err); rows(); }
        if (w < 2) A[15] = fv;
        MARK(102);
    } else {
        { const bf16_t* Wo = a.Wo + (size_t)h * DH * DM + 256 * jj + 64 * w; const bf16_t* Wv = a.Wv + (size_t)256 * w * HD + hp * DH + 32 * jj;
          const unsigned po = stg + (4 * q + n3 + 16 * n2) * 128 + (((n & 3) * 2) ^ (q & 1)) * 16;
          stream([&](int i) {
                     if (i < 2) { _Pragma("unroll 1") for (int j = 0; j < 8; ++j) (CPL ? glds : gldsc)(Wo + (size_t)(64 * i + 8 * j) * DM, (l >> 3) * (DM * 2) + ((l & 7) ^ (l >> 5)) * 16, stg + i * 8192 + j * 1024); }
                     else { _Pragma("unroll 1") for (int j = 0; j < 8; ++j) glds(Wv + (size_t)(128 * (i - 2) + 16 * j) * HD, (l >> 2) * (HD * 2) + ((l & 3) ^ (2 * (q & 1))) * 16, stg + (i & 1) * 8192 + j * 1024); } },
                 [&](auto i) { _Pragma("unroll") for (int t = 0; t < 4; ++t) if constexpr (i < 2) trd4(2 * t + i, (po ^ ((t >> 1) * 16)) + (t & 1) * 8 + i * 8192, 256, 4096); },
                 [&](int j, unsigned& vo) { vo = (l >> 2) * (HD * 2) + ((l & 3) ^ (2 * (q & 1))) * 16; return Wv + (size_t)(16 * j) * HD; }, false); }
        BAR(); dry();
        start();
        A[0] = 0; A[1] = h1o(4096 + l * 16); A[2] = pg(B_TS, g) + (l & 3) * 128;
        A[3] = w >= 2 ? pg(2 * aa, g) + l * 16 : h1o(4096 + ((jj * 2 + n3) * 2 + (w & 1)) * 128 + (q & 1) * 64 + (n & 1) * 32 + (q >> 1) * 16 + ((n >> 1) & 3) * 4, XH);
        A[4] = pg(jj * 16 + w * 4 + n3 * 2, n2 * 2 + (q >> 1)) + h * 128 + ((q & 1) * 4 + (n & 3)) * 16;
        A[5] = L_VM + q * 256 + (4 * n3 + 2 * (n & 1) + n2) * 16 + hf * 4;
        A[7] = L_EX + 8192 + (w & 1) * 512 + l * 4; A[6] = A[7] ^ 512;
        A[8] = L_FL + w * 256 + l * 4; A[9] = L_EX + 8192 + (l & 3) * 2048 + 1536 + l * 4; A[10] = L_FL + 2048 + min(l & 3, 2) * 256 + l * 4;
        A[11] = L_SC + q * 512 + hf * 256 + n * 16; A[12] = L_SM + (q & 1) * 256 + hf * 128 + n * 8;
        A[13] = L_VX + q * 256 + (8 * n3 + 4 * hf + 2 * n2 + (n & 1)) * 16; A[14] = six; A[15] = L_TB;
    }
    S1[8] = RFL(pg(B_TS, g));
    asm volatile("" : "+{v[168:183]}"(A), "+{s[40:55]}"(S0), "+{s[56:71]}"(S1));
    WAITV(0); BAR();
    MARK(0); MARKC(4);
    asm volatile(A_CHAIN : "+{s[40:55]}"(S0), "+{s[56:71]}"(S1), "={s[80:87]}"(cnts), "+{v[168:183]}"(A), "+{v[188:219]}"(W[4]), "+{v[220:251]}"(W[5]) : "{v[0:31]}"(W[0]), "{v[32:63]}"(W[1]), "{v[64:95]}"(W[2]), "{v[96:127]}"(W[3])
                 : "v128", "v129", CL10("v13"), CL10("v14"), CL10("v15"), "v160", "v161", "v162", "v163", "v164", "v165", "v166", "v167", "v184", "v185", "v186", "v187", CL_S);
}

static_assert(NSEG - SO == 6, "");
struct Tile { const bf16_t* W; int ld, wide, perm, keep; };
__device__ __forceinline__ void tile_issue(const Tile& T, int t, unsigned L, int w8, int l) {
    const bf16_t* p = T.W + (T.wide ? (w8 & 3) * 32 * T.ld + (2 * t + (w8 >> 2)) * 128 : (t * 256 + w8 * 32) * T.ld);
    const unsigned vo = (T.perm ? (l >> 5) + (l & 16) : l >> 4) * T.ld * 2 + ((l & 15) ^ (2 * (l >> 4))) * 16;
#pragma unroll 1
    for (int i = 0; i < 8; ++i) { const bf16_t* g = p + i * (T.perm ? 2 : 4) * T.ld; const unsigned v = vo ^ ((i & 2) << 6), d = L + (t & 1) * 65536 + (w8 * 8 + i) * 1024;
        if (T.keep) glds(g, v, d); else gldsw(g, v, d);
        if (OSP && T.perm) __builtin_amdgcn_s_sleep(OSP); }
}
template <class F>
__device__ __forceinline__ void tile_load(const Tile& T, v4u (&wf)[32], unsigned L, int w8, int l, F next) {
    const int n = l & 15, q = l >> 4;
    const unsigned p = L + (8 * q + (n >> 2)) * 256 + ((4 * (w8 & 3) + ((l >> 1) & 1)) ^ (2 * ((n >> 2) | ((q & 1) << 2)))) * 16 + (l & 1) * 8;
    bool more = true;
#pragma unroll 1
    for (int hh = 0; hh < 2; ++hh)
#pragma unroll
    for (int u = 0; u < 2; ++u) {
        if (!more) WAITV(0);
        WAITV(8); BAR();
        if ((w8 >> 2) == hh) {
#pragma unroll
            for (int i = 0; i < 16; ++i) { const unsigned pp = (p ^ ((i & 1) * 32)) + u * 65536 + (i >> 1) * 8192; wf[u * 16 + i] = cat2(trd(pp), trd(pp + 1024)); }
            WAITL(); }
        BAR();
        if (hh == 0) tile_issue(T, u + 2, L, w8, l); else more = next(u);
    }
}
#define NB 8
#define GD 4
template <int XR, bool ROWS, class F>
__device__ __forceinline__ void seg_gemm(const v4u (&wf)[32], unsigned b0, frag_cd (&acc)[4], F side) {
    v4u f[2 * GD];
    const unsigned bb[4] = {b0, ROWS ? b0 ^ 64 : b0, ROWS ? b0 ^ 128 : b0, ROWS ? b0 ^ 192 : b0};
#define RD(ks, mt) asm volatile("ds_read_b128 %0, %1 offset:%2" : "=v"(f[((ks) % GD) * 2 + mt]) : "v"(bb[(ks) & 3]), "n"(ROWS ? ((ks) >> 2) * 256 + mt * 16384 : ((ks) >> 1) * 4096 + ((ks) & 1) * 1024 + mt * 2048) : "memory")
#define MM(ct, mt) do { if (ks == 0) asm volatile("v_mfma_f32_16x16x32_bf16 %0, %1, %2, 0" : "=&v"(acc[2 * ct + mt]) : "v"(wf[ct]), "v"(f[mt])); \
        else asm volatile("v_mfma_f32_16x16x32_bf16 %0, %1, %2, %0" : "+v"(acc[2 * ct + mt]) : "v"(wf[2 * ks + ct]), "v"(f[(ks % GD) * 2 + mt])); } while (0)
#pragma unroll
    for (int ks = 0; ks < GD - 1; ++ks) { RD(ks, 0); RD(ks, 1); }
#pragma unroll
    for (int ks = 0; ks < 16; ++ks) {
        side(2 * ks);
        asm volatile("s_waitcnt lgkmcnt(%0)" :: "n"(2 * (ks + GD - 1 <= 16 ? GD - 2 : 15 - ks) + (ks >= 13 ? XR : 0)));
        MM(0, 0);
        if (ks + GD - 1 < 16) RD(ks + GD - 1, 0);
        MM(0, 1);
        side(2 * ks + 1);
        MM(1, 0);
        if (ks + GD - 1 < 16) RD(ks + GD - 1, 1);
        MM(1, 1);
    }
    asm volatile("s_nop 7" : "+v"(acc[0]), "+v"(acc[1]), "+v"(acc[2]), "+v"(acc[3]));
#undef RD
#undef MM
}
static_assert(GD == 4 && SO == 58, "");
__device__ __forceinline__ float fadd(float a, float b) { float r; asm("v_add_f32 %0, %1, %2" : "=v"(r) : "v"(a), "v"(b)); return r; }
__device__ __forceinline__ float fmul(float a, float b) { float r; asm("v_mul_f32 %0, %1, %2" : "=v"(r) : "v"(a), "v"(b)); return r; }
__device__ __forceinline__ void half_put(unsigned ex, int w8, int l, const frag_cd (&acc)[4]) {
#pragma unroll
    for (int i = 0; i < 4; ++i) *lp<frag_cd>(ex + w8 * 4096 + i * 1024 + l * 16) = acc[i];
    FENCE();
}
struct Half { frag_cd t[4]; unsigned dec; };
__device__ __forceinline__ void half_req(unsigned ex, unsigned dec, Half& x) {
    asm volatile("ds_read_b32 %0, %1" : "=v"(x.dec) : "v"(dec) : "memory");
#pragma unroll
    for (int i = 0; i < 4; ++i) asm volatile("ds_read_b128 %0, %1 offset:%2" : "=v"(x.t[i]) : "v"(ex), "n"(i * 1024) : "memory");
}
template <class F>
__device__ __forceinline__ void half_get(Half& x, frag_cd (&acc)[4], F side) {
    asm volatile("s_waitcnt lgkmcnt(0)" : "+v"(x.t[0]), "+v"(x.t[1]), "+v"(x.t[2]), "+v"(x.t[3]), "+v"(x.dec) :: "memory");
#pragma unroll
    for (int i = 0; i < 4; ++i) { side(i);
#pragma unroll
        for (int e = 0; e < 4; ++e) acc[i][e] = fadd(acc[i][e], x.t[i][e]);
        asm volatile("" : "+v"(acc[i])); }
}
#define GEARLY 2
#define BARI 25
#define PRIO(n) __builtin_amdgcn_s_setprio(n)

__device__ __forceinline__ unsigned topk(const Args& a, int l) {
    const rsrc_t rg = mkrs(a.gate, 2 * S_TOK * 4);
    flags_wait<16>(a.ctl + C_R, 32, 1, l, a.ctl + C_ERR);
    v4u kv[8]; unsigned prev = ~0u, mine = 0;
#pragma unroll
    for (int i = 0; i < 8; ++i) kv[i] = __builtin_amdgcn_raw_buffer_load_b128(rg, S_TOK * 4 + l * 16, i * 1024, 16);
#pragma unroll 1
    for (int c = 0; c < TOPC; ++c) { unsigned mx = 0;
        FENCE();
#pragma unroll
        for (int i = 0; i < 8; ++i)
#pragma unroll
            for (int e = 0; e < 4; ++e) mx = max(mx, kv[i][e] < prev ? kv[i][e] : 0u);
        mx = max(mx, dppu<0x128>(mx)); mx = max(mx, dppu<0x124>(mx)); mx = max(mx, dppu<0x122>(mx)); mx = max(mx, dppu<0x121>(mx));
        { v2u r = sw16(mx, mx); mx = max(r.x, r.y); r = sw32(mx, mx); mx = max(r.x, r.y); }
        prev = RFL(mx); if ((l & 7) == c) mine = prev; }
    return mine;
}
__device__ __forceinline__ unsigned mix(float gt, float omg, float t0, float t1, float b0, float b1) {
    unsigned av = cvt_pk(t0, t1); av = cvt_pk(fmul(gt, lo16(av)), fmul(gt, hi16(av))); const unsigned bv = cvt_pk(fmul(omg, b0), fmul(omg, b1));
    return cvt_pk(fadd(lo16(av), lo16(bv)), fadd(hi16(av), hi16(bv)));
}
struct Finisher {
    rsrc_t rp, rx, rg, ro; int po, frow, xo, w8; unsigned seltok, ly;
    struct Fin { v4u p[4]; v2u xv; float gt; int k; };
    __device__ __forceinline__ Finisher(const Args& a, int j, int w8_, int l, unsigned L_Y) {
        rp = mkrs(a.part, NSEG * 524288); rx = mkrs(a.x, S_TOK * DM * 2); rg = mkrs(a.gate, S_TOK * 4); ro = mkrs(a.out, S_TOK * DM * 2);
        w8 = w8_; po = j * 16384 + w8 * 1024 + l * 16; frow = 16 * (w8 & 1) + (l & 15); xo = (frow * DM + j * 32 + (w8 >> 1) * 16 + 4 * (l >> 4)) * 2;
        seltok = 4095u - (lp<unsigned>(L_Y + 1024)[l & 7] & 4095u); ly = L_Y + ((w8 >> 1) * 32 + (l >> 4) * 8) * 16;
    }
    __device__ __forceinline__ Fin issue(int s, bool on) const { Fin f; int k = on ? 0 : 0x40000000; asm("" : "+s"(k));
#pragma unroll
        for (int i = 0; i < 4; ++i) f.p[i] = __builtin_amdgcn_raw_buffer_load_b128(rp, k + po, s * 524288 + i * 4096, 16);
        f.k = k + xo;
        f.xv = __builtin_amdgcn_raw_buffer_load_b64(rx, f.k, s * 65536, 0);
        f.gt = __uint_as_float(__builtin_amdgcn_raw_buffer_load_b32(rg, k + frow * 4, s * 128, 16));
        return f;
    }
    template <class F> __device__ __forceinline__ void finish(int s, const Fin& f, F side) const {
        const int tok = 32 * s + frow; const float gt = f.gt, omg = rbf(1.0f - gt);
        frag_cd t; v2u o; int k = f.k;
#pragma unroll
        for (int e = 0; e < 4; e += 2) {
            side(e >> 1);
#pragma unroll
            for (int u = e; u < e + 2; ++u) t[u] = fadd(fadd(__uint_as_float(f.p[0][u]), __uint_as_float(f.p[1][u])), fadd(__uint_as_float(f.p[2][u]), __uint_as_float(f.p[3][u])));
            o[e >> 1] = mix(gt, omg, t[e], t[e + 1], lo16(f.xv[e >> 1]), hi16(f.xv[e >> 1])); asm volatile("" : "+v"(o[e >> 1])); }
        for (unsigned mk = (unsigned)__builtin_amdgcn_ballot_w64((seltok >> 4) == (unsigned)(2 * s + (w8 & 1))) & 255u; mk; mk &= mk - 1) {
            const int slot = __builtin_ctz(mk);
            if (__builtin_amdgcn_readlane(seltok, slot) == (unsigned)tok) { *lp<frag_cd>(ly + slot * 16) = t; k = 0x40000000; }
        }
        __builtin_amdgcn_raw_buffer_store_b64(o, ro, k, s * 65536, 0);
    }
};

__device__ __forceinline__ void gwork(const Args& a, const int m, const int h, const unsigned L, const unsigned hb) {
    const int w8 = RFL(threadIdx.x >> 6); int l = threadIdx.x & 63, n = l & 15, q = l >> 4;
    unsigned* const err = a.ctl + C_ERR;
    const unsigned L_EX = L + 131072, L_XS = L_EX, L_HS = L_XS + 16384, L_GU = L_HS + 2048;
    unsigned* const fG = a.ctl + C_G + h * 128 + m * 32;
    const unsigned xo = (l ^ (8 * (w8 & 1))) * 16;
    auto xissue = [&](int s, int i0, int i1) {
        const bf16_t* p = a.x + (size_t)(32 * s + 8 * (w8 & 3)) * DM + (~w8 & 4) * 128; const unsigned d = L + (s & 1) * 65536 + (w8 ^ 4) * 8192;
#pragma unroll
        for (int i = i0; i < i1; ++i) {
            if (i == i0 || i == 4) asm volatile("s_mov_b32 m0, %0" :: "s"(d + (i & 4) * 1024));
            unsigned t; asm volatile("v_xor_b32 %0, %4, %1\n\tglobal_load_lds_dwordx4 %0, %2 offset:%3" : "=&v"(t) : "v"(xo), "s"(p), "n"((i & 3) * 1024), "n"(i * 1040 + (i & 4) * 1024) : "memory"); }
    };
    v4u wf[32];
    unsigned h2 = hb;
    { const Tile T{(m == 0 ? a.Wk : (m == 1 ? a.Wv : a.Wq)) + h * DH, HD, 0, 0, GAL >> m & 1};
      MARK(104);
      if (XPF) glds(a.x, (((h & 7) * 3 + m) * 8 + w8) * 1024 + l * 16, L_HS + w8 * 1024);
      if (GPF) ahead(T.W - h * DH, h, l, L_HS + w8 * 1024, [&](int d) { return 256 * d + 32 * w8; });
      if (GDL && ((u64)T.W >> 8 & 3) != 1) __builtin_amdgcn_s_sleep(GDL);
      tile_issue(T, 0, L, w8, l); tile_issue(T, 1, L, w8, l);
      if (GHB && w8 == 4) gld_req(h2, a.ctl + C_HB);
      tile_load(T, wf, L, w8, l, [&](int b) { if (b == 0 || w8 >= 4) xissue(b, 0, 8); return true; });
    }
    MARK(0);
    const unsigned rb0 = L + (w8 >> 2) * 32768 + n * 1024 + (q ^ n) * 16;
    unsigned pacc[8] = {}, plast = 0; (void)pacc; (void)plast;
    PT(7);
    if (w8 >= 4) {
        const rsrc_t rq = mkrs(a.qkv, NSEG * NH * 24576), rf = mkrs(fG, w8 == 4 ? 4 : 0);
        const int sto = h * 24576 + m * 8192 + (m == 1 ? (w8 & 3) * 64 + l : ((w8 & 2) + (q & 1)) * 64 + (2 * (w8 & 1) + (q >> 1)) * 16 + n) * 16;
        WAITV(8); BAR(); gld_wait<0>(h2);
        if (w8 == 4) { if (RFL(h2) == RFL(hb)) ctl_wait(a, hb); gadd1(a.ctl + C_READY); }
        for (int sg = 0; sg < NSEG; ++sg) {
            PT(0);
            BAR();
            PT(1);
            PRIO(2);
            if (sg <= GEARLY) __builtin_amdgcn_raw_buffer_store_b32(sg, rf, l * 4, 0, 16);
            frag_cd acc[4]; Half hx;
            seg_gemm<5, true>(wf, rb0 + (sg & 1) * 65536, acc, [&](int i) {
                if (i == BARI) { PRIO(3); PT(5); if (sg <= GE2) WAITV(0); WAITV(3); BAR(); PT(6); if (sg <= GE2) __builtin_amdgcn_raw_buffer_store_b32(sg, rf, l * 4, 0, 16); FENCE(); }
                if (i == 26) half_req(L_EX + (w8 & 3) * 4096 + l * 16, L_EX, hx);
                if (i >= 26) xissue((sg + 2) & 63, i - 26, i - 25); });
            PRIO(0);
            PT(2);
            half_get(hx, acc, [&](int i) { if (i & 1) xissue((sg + 2) & 63, 6 + i / 2, 7 + i / 2); });
            v4u r[2];
#pragma unroll
            for (int mt = 0; mt < 2; ++mt) r[mt] = v4u{cvt_pk(acc[mt][0], acc[2 + mt][0]), cvt_pk(acc[mt][1], acc[2 + mt][1]), cvt_pk(acc[mt][2], acc[2 + mt][2]), cvt_pk(acc[mt][3], acc[2 + mt][3])};
            PT(3);
            if (sg == 12) {
#pragma unroll
                for (int i = 0; i < 4; ++i) STB(v4u{~0u, ~0u, ~0u, ~0u}, mkrs(a.ox, NSEG * 262144), h * 8192 + (w8 & 3) * 1024 + l * 16 + (i & 1) * 4096, (SO + 2 * m + (i >> 1)) * 262144); }
#pragma unroll
            for (int mt = 0; mt < 2; ++mt) STB(r[mt], rq, sto + mt * 4096, sg * (NH * 24576));
            __builtin_amdgcn_raw_buffer_store_b32(sg <= GEARLY || sg <= GE2 ? sg : sg - 1, rf, l * 4, 0, 16);
            FENCE();
            if (sg < GEARLY) WAITV(0);
            PT(4);
        }
        PFLUSH();
        WAITV(0); BAR();
        if (w8 == 4) gst1(fG, NSEG);
    } else {
        for (int sg = 0; sg < NSEG; ++sg) {
            frag_cd acc[4];
            WAITL();
            PT(0);
            BAR();
            PT(1);
            PRIO(2);
            seg_gemm<0, true>(wf, rb0 + (sg & 1) * 65536, acc, [&](int i) { if (i == BARI) { PRIO(3); PT(5); WAITV(0); BAR(); PT(6); } });
            PRIO(1);
            PT(2);
            half_put(L_EX, w8, l, acc);
            xissue((sg + 1) & 63, 0, 8);
            PT(3);
            MARK(sg + 1);
        }
        PFLUSH();
        WAITL(); BAR();
        WAITV(0); BAR();
    }
    asm volatile("v_mbcnt_lo_u32_b32 %0, -1, 0\n\tv_mbcnt_hi_u32_b32 %0, -1, %0" : "=v"(l));
    n = l & 15; q = l >> 4;
    if (m != 2) {
        const int j = TMAP ? h : (h & 23) + 8 * m, mt = TMAP ? m : h >> 3 & 1;
        const unsigned stg = L + w8 * 16384, p = stg + (8 * q + (n >> 2)) * 64 + (n & 3) * 8;
#pragma unroll
        for (int hh = 0; hh < 2; ++hh) {
#pragma unroll 1
            for (int i = 0; i < 16; ++i) {
                (TNT ? gldsw : glds)(a.Wo + (size_t)(512 * w8 + 256 * hh + 32 * (i >> 1) + 8 * (i & 1)) * DM + 32 * j, ((l >> 3) + (l & 4) * 4) * (DM * 2) + (l & 3) * 16, stg + i * 1024); WAITV(TIF); __builtin_amdgcn_s_sleep(TSP); }
            WAITV(0);
#pragma unroll
            for (int i = 0; i < 16; ++i) { const unsigned pp = p + (i >> 1) * 2048 + (i & 1) * 32; wf[hh * 16 + i] = cat2(trd(pp), trd(pp + 256)); }
            WAITL();
        }
        MARK(66);
        const rsrc_t ro = mkrs(a.out, S_TOK * DM * 2), ra = mkrs(a.ox, NSEG * 262144);
        const int xo = (16 * mt + n) * DM + j * 32 + (w8 & 1) * 16 + 4 * q;
        const unsigned L_B = L_EX - SO * 2048;
        if (w8 < 2) {
            const unsigned seltok = 4095u - (topk(a, l) & 4095u);
#pragma unroll 1
            for (int s = SO; s < NSEG; ++s) { const v2u xv = *(const v2u*)(a.x + s * 32768 + xo);
                *lp<v4u>(L_B + s * 2048 + w8 * 1024 + l * 16) = v4u{xv.x, xv.y, gld(a.gate + 32 * s + 16 * mt + n), seltok}; }
        }
        BAR();
        MARK(67);
        { unsigned fv = 0; flags_wait<16, 64>(fv, a.ctl + C_M + (16 * w8 + 2 * (l & 7) + mt) * 32, SO - 1, err); }
        auto peek = [&](int s) { FENCE(); const unsigned v = __builtin_amdgcn_raw_buffer_load_b32(ra, w8 * 32768 + mt * 2048 + (l & 7) * 4096 + 1024, s * 262144, 16); FENCE(); return v; };
        unsigned nx = ~0u;
#define TMK(k)
#pragma unroll 1
        for (int s = SO, it = 0; s < NSEG;) {
            for (int i = 0; __builtin_amdgcn_ballot_w64(nx == ~0u) != 0; ++i) { if (i) __builtin_amdgcn_s_sleep(TS1); nx = peek(s); if (++it > GCAP) fatal(err); }
            TMK(0);
            v4u f[16]; frag_cd acc[4] = {};
            unsigned mx = 0;
#pragma unroll
            for (int i = 0; i < 16 + TD; ++i) { const int ks = i - TD;
                SB();
                if (i < 16) f[i] = __builtin_amdgcn_raw_buffer_load_b128(ra, w8 * 32768 + mt * 2048 + l * 16 + (i & 1) * 1024, s * 262144 + (i >> 1) * 4096, 16);
                if (i == 16) TMK(1);
                if (ks == TFK) nx = peek(min(s + 1, NSEG - 1));
                SB();
                if (ks >= 0) { acc[ks & 1] = MFMA(wf[2 * ks], f[ks], acc[ks & 1]); acc[2 + (ks & 1)] = MFMA(wf[2 * ks + 1], f[ks], acc[2 + (ks & 1)]); mx = umax3(umax3(mx, f[ks].x, f[ks].y), f[ks].z, f[ks].w); } }
            SB();
            TMK(2);
            if (__builtin_amdgcn_ballot_w64(mx == ~0u) != 0) {
                nx = ~0u; continue; }
            const unsigned ex = L + (s - SO) * 16384 + w8 * 2048 + l * 16;
            *lp<frag_cd>(ex) = acc[0] + acc[1]; *lp<frag_cd>(ex + 1024) = acc[2] + acc[3];
            if (s % 3 == 0) {
                WAITL(); BAR();
                if (w8 < 6) { const int u = s - (w8 >> 1), o = (w8 & 1) * 1024 + l * 16;
                    v4u b = *lp<v4u>(L_B + u * 2048 + o);
                    frag_cd t = *lp<frag_cd>(L + (u - SO) * 16384 + o);
#pragma unroll
                    for (int i = 1; i < 8; ++i) t += *lp<frag_cd>(L + (u - SO) * 16384 + o + i * 2048);
                    for (unsigned mk = (unsigned)__builtin_amdgcn_ballot_w64((b.w >> 4) == (unsigned)(2 * u + mt)) & 255u; mk; mk &= mk - 1) {
                        const int slot = __builtin_ctz(mk);
                        flags_wait<4>(a.ctl + C_Y + j * 32 + 1, 1, 1, l, err);
                        const frag_cd y = __builtin_bit_cast(frag_cd, __builtin_amdgcn_raw_buffer_load_b128(mkrs(a.ypart + (32 * 28 + j) * 1024, 1024), ((w8 & 1) * 32 + q * 8 + slot) * 16, 0, 16));
                        if ((__builtin_amdgcn_readlane(b.w, slot) & 15) == (unsigned)n) { b.x = cvt_pk(lo16(b.x) + y[0], hi16(b.x) + y[1]); b.y = cvt_pk(lo16(b.y) + y[2], hi16(b.y) + y[3]); }
                    }
                    const float gt = __uint_as_float(b.z), omg = rbf(1.0f - gt);
                    __builtin_amdgcn_raw_buffer_store_b64(v2u{mix(gt, omg, t[0], t[1], lo16(b.x), hi16(b.x)), mix(gt, omg, t[2], t[3], lo16(b.y), hi16(b.y))}, ro, xo * 2, u * 65536, 0); } }
            TMK(3);
            if (TSPEC && s == NSEG - 2) { __builtin_amdgcn_s_sleep(TS3); nx = 0; }
            ++s;
        }
        MARK(65);
        return;
    }
    const rsrc_t ry = mkrs(a.ypart, 32 * 29 * 1024);
    auto tile = [&](int t) { return t < 2 ? Tile{(t ? a.wu : a.wg) + h * 128, FF, 0, 0, 0} : Tile{a.wd + (size_t)h * 128 * DM, DM, 1, 0, 0}; };
    unsigned mykey = 0;
    if (w8 == 0) {
        mykey = topk(a, l);
#pragma unroll 1
        for (int i = 0; i < 16; ++i) glds(a.x + i * 64, (4095u - (mykey & 4095u)) * (DM * 2) + (l >> 3) * 16, L_XS + i * 1024);
    }
    if (h < FF / 128) {
      v4u wm[32];
      Tile T = tile(0);
      tile_issue(T, 0, L, w8, l); tile_issue(T, 1, L, w8, l);
      frag_cd gv[2] = {};
#pragma unroll 1
      for (int ti = 0; ti < 3; ++ti) {
        const Tile N = tile(ti + 1);
        tile_load(T, wm, L, w8, l, [&](int b) { if (ti < 2) tile_issue(N, b, L, w8, l); return ti < 2; });
        T = N;
        if (ti < 2) {
            frag_cd acc[2] = {};
#pragma unroll
            for (int ks = 0; ks < 16; ++ks) { const v4u xf = *lp<v4u>(L_XS + ((w8 >> 2) * 16 + ks) * 512 + q * 128 + (n & 7) * 16); acc[0] = MFMA(wm[2 * ks], xf, acc[0]); acc[1] = MFMA(wm[2 * ks + 1], xf, acc[1]); }
            if (ti == 1) {
                frag_cd g = n < 8 ? gv[0] : gv[1], u = n < 8 ? acc[0] : acc[1];
                if (w8 >= 4) { *lp<frag_cd>(L_GU + (w8 & 3) * 2048 + l * 16) = g; *lp<frag_cd>(L_GU + (w8 & 3) * 2048 + 1024 + l * 16) = u; WAITL(); }
                BAR();
                if (w8 < 4) { g += *lp<frag_cd>(L_GU + w8 * 2048 + l * 16); u += *lp<frag_cd>(L_GU + w8 * 2048 + 1024 + l * 16);
                    float hv[4];
#pragma unroll
                    for (int e = 0; e < 4; ++e) { const float gg = rbf(g[e]); hv[e] = rbf(gg / (1.0f + __expf(-gg))) * rbf(u[e]); }
                    *lp<v2u>(L_HS + w8 * 512 + (2 * (n >> 3) + (q >> 1)) * 128 + (n & 7) * 16 + (q & 1) * 8) = v2u{cvt_pk(hv[0], hv[1]), cvt_pk(hv[2], hv[3])};
                    WAITL(); } }
            gv[0] = acc[0]; gv[1] = acc[1];
        } else {
#pragma unroll
            for (int cb = 0; cb < 4; ++cb) { frag_cd acc[2] = {};
#pragma unroll
                for (int i = 0; i < 4; ++i) { const v4u hf = *lp<v4u>(L_HS + i * 512 + q * 128 + (n & 7) * 16); acc[0] = MFMA(wm[8 * cb + 2 * i], hf, acc[0]); acc[1] = MFMA(wm[8 * cb + 2 * i + 1], hf, acc[1]); }
                STB(__builtin_bit_cast(v4u, n < 8 ? acc[0] : acc[1]), ry, ((w8 & 3) * 28 + h) * 1024 + ((n >> 3) * 32 + q * 8 + (n & 7)) * 16, ((w8 >> 2) * 4 + cb) * (4 * 28 * 1024)); }
        }
        MARK(67 + ti);
      }
      WAITV(0); BAR();
    }
    if (w8 == 0) {
        gst1(a.ctl + C_Y + h * 32, 1u);
        flags_wait<16>(a.ctl + C_Y, FF / 128, 1, l, err);
        frag_cd y = {};
#pragma unroll 1
        for (int i = 0; i < 28; i += 7) { v4u t[7];
#pragma unroll
            for (int u = 0; u < 7; ++u) t[u] = __builtin_amdgcn_raw_buffer_load_b128(ry, l * 16, (h * 28 + i + u) * 1024, 16);
#pragma unroll
            for (int u = 0; u < 7; ++u) y += __builtin_bit_cast(frag_cd, t[u]); }
        const float r = hi16((mykey & 0x80000000u) ? mykey & 0x7fffffffu : ~mykey), rs = rbf(1.0f / (1.0f + __expf(-r)));
#pragma unroll
        for (int e = 0; e < 4; ++e) y[e] = rbf(rbf(y[e]) * rs);
        STB(__builtin_bit_cast(v4u, y), ry, l * 16, (32 * 28 + h) * 1024);
        WAITV(0);
        gst1(a.ctl + C_Y + h * 32 + 1, 1u);
        MARK(120);
    }
}

__device__ __forceinline__ void owork(const Args& a, const int nt, const int kq, const unsigned L, const unsigned hb) {
    const int tid = threadIdx.x, w8 = RFL(tid >> 6), l = tid & 63, j = nt * 4 + kq;
    unsigned* const err = a.ctl + C_ERR;
    const unsigned L_EX = L + 131072, L_DEC = L_EX + 16384, L_Y = L_DEC + 64, L_KEY = L_Y + 1024;
    if (tid < 4) lp<unsigned>(L_DEC)[tid] = 0;
    {
        const bool lead = j == 0 && w8 == 1;
        if (lead) { const rsrc_t rc = mkrs(a.ctl, C_HB * 4);
#pragma unroll
            for (int i = 0; i < 8; ++i) STB(v4u{}, rc, l * 128, i * 8192);
            STB(v4u{}, rc, l * 16, 0); }
        unsigned char* p = a.ring + ((l >> 3 & 1) * 64 + (j >> 1)) * 32768 + ((l ^ (kq & 1) * 4 ^ kq >> 1) & 7) * 4096 + (w8 * 2 + (nt & 1)) * 256 + (kq & 1) * 128;
        unsigned best = 0;
        if (l < 16) gst((unsigned*)p, ~0u);
        if (lead) { WAITV(0); gadd1(a.ctl + C_HB); }
#pragma unroll 1
        for (int i = 0; i < OPF; ++i) {
            const bf16_t* W = i % 3 == 0 ? a.Wk : (i % 3 == 1 ? a.Wv : a.Wq);
            glds(W, ((i < 6 ? 512 : 0) + (nt * 8 + w8) * 8 + (l >> 3)) * (HD * 2) + kq * 2048 + (i / 3 & 1) * 1024 + ((384 - (unsigned)(u64)W) & 896) + (l & 7) * 16, L + 65536 + w8 * 1024); }
        WAITV(0);
#pragma unroll 1
        for (int i = 0; i < 16; ++i) { u64 t1, t2; unsigned v;
            if (lead && !(i & 3)) gadd1(a.ctl + C_HB);
            const u64 pi = ((u64)__builtin_amdgcn_readlane((unsigned)((u64)p >> 32), i) << 32) | (unsigned)__builtin_amdgcn_readlane((unsigned)(u64)p, i);
            asm volatile("s_nop 4\n\ts_memtime %0\n\ts_waitcnt lgkmcnt(0)\n\tglobal_load_dword %2, %3, %4 sc1\n\ts_waitcnt vmcnt(0)\n\ts_memtime %1\n\ts_waitcnt lgkmcnt(0)"
                         : "=&s"(t1), "=&s"(t2), "=&v"(v) : "v"(0u), "s"(pi) : "memory");
            if (l == i) best = ~(unsigned)(t2 - t1); }
        if (l < 16) { gst((unsigned*)p, ~0u); lp<unsigned>(L)[w8 * 16 + l] = best; }
        WAITV(0); WAITL(); BAR();
        if (w8 == 0) {
#pragma unroll
            for (int i = 1; i < 8; ++i) best = max(best, lp<unsigned>(L)[i * 16 + (l & 15)]);
            if (j) ctl_wait(a, hb);
            if (l < 16) __hip_atomic_fetch_max(a.ctl + C_CAL + l, best, __ATOMIC_RELAXED, __HIP_MEMORY_SCOPE_AGENT);
            WAITV(0);
            gadd1(a.ctl + C_READY);
            MARK(66);
        }
        if (lead) for (int i = 0; !RFL(gld(a.ctl + C_ACK)); ++i) { gadd1(a.ctl + C_HB); __builtin_amdgcn_s_sleep(8); if (i > GCAP) fatal(err); }
    }
    BAR();
    v4u wf[32];
    const Tile T{a.Wo + (size_t)kq * 1024 * DM + nt * 128, DM, 0, 1, OPL};
    if (OWF) { v4u r[16];
#pragma unroll
        for (int i = 0; i < 16; ++i) asm volatile("global_load_dwordx4 %0, %1, %2" : "=v"(r[i]) : "v"(l * 16), "s"(a.x + (size_t)(64 * j + 8 * w8) * DM + i * 512) : "memory");
        tile_issue(T, 0, L, w8, l); tile_issue(T, 1, L, w8, l);
        tile_load(T, wf, L, w8, l, [](int) { return false; });
        asm volatile("" : "+v"(r[0]), "+v"(r[1]), "+v"(r[2]), "+v"(r[3]), "+v"(r[4]), "+v"(r[5]), "+v"(r[6]), "+v"(r[7]), "+v"(r[8]), "+v"(r[9]), "+v"(r[10]), "+v"(r[11]), "+v"(r[12]), "+v"(r[13]), "+v"(r[14]), "+v"(r[15]) :: "memory");
#pragma unroll
        for (int i = 0; i < 16; ++i) *lp<v4u>(L + (w8 * 16 + i) * 1024 + l * 16) = r[i];
        MARK(0); }
    {
#pragma unroll 1
        for (int i = 0; i < 16; ++i) if (!OWF) glds(a.x + (size_t)(64 * j + 8 * w8) * DM + i * 512, l * 16, L + (w8 * 16 + i) * 1024);
        v4u gw[2], wr[2];
#pragma unroll
        for (int i = 0; i < 2; ++i) { gw[i] = *(const v4u*)(a.gw + 512 * i + 8 * l); wr[i] = *(const v4u*)(a.wr + 512 * i + 8 * l); }
        WAITV(0);
        float res = 0.f;
#pragma unroll 1
        for (int t = 0; t < 8; ++t) { float sg = 0.f, sr = 0.f;
#pragma unroll
            for (int i = 0; i < 2; ++i) { const v4u xv = *lp<v4u>(L + (w8 * 16 + 2 * t + i) * 1024 + l * 16);
#pragma unroll
                for (int e = 0; e < 4; ++e) { const float x0 = lo16(xv[e]), x1 = hi16(xv[e]); const unsigned p = cvt_pk(x0 * lo16(gw[i][e]), x1 * hi16(gw[i][e]));
                    sg += lo16(p) + hi16(p); sr = __builtin_fmaf(x0, lo16(wr[i][e]), __builtin_fmaf(x1, hi16(wr[i][e]), sr)); } }
            float v = tadd32(sg, sr);
            v = tadd16(v, v); v += dppf<ROR8>(v); v += dppf<0x124>(v); v += dppf<0x122>(v); v += dppf<0x121>(v);
            if ((l & 7) == t) res = v; }
        const unsigned rb = cvt_pk(res, 0.f), tok = 64 * j + 8 * w8 + (l & 7);
        const unsigned val = l < 32 ? __float_as_uint(rbf(1.0f / (1.0f + __expf(-lo16(rb))))) : (((rb & 0x8000u) ? ~rb : rb | 0x8000u) << 16) | (4095u - tok);
        if ((l & 31) < 8) gst(a.gate + (l >> 5) * S_TOK + tok, val);
        WAITV(0); BAR();
        if (w8 == 0) gst1(a.ctl + C_R + j * 32, 1u);
        MARK(67);
    }
    if (!OWF) { tile_issue(T, 0, L, w8, l); tile_issue(T, 1, L, w8, l);
      tile_load(T, wf, L, w8, l, [](int) { return false; });
      MARK(0); }
    if (w8 == 1) { const unsigned mine = topk(a, l); if (l < 8) lp<unsigned>(L_KEY)[l] = mine; WAITL(); }
    BAR();
    MARK(68);
    unsigned* const fP = a.ctl + C_P + nt * 512;
    const unsigned rb = L + (w8 >> 2) * 32768 + l * 16;
    unsigned pacc[8] = {}, plast = 0; (void)pacc; (void)plast;
    PT(7);
    auto oissue = [&](int s, int wv, int i) { glds1<2>(a.ox + (size_t)s * 262144 + kq * 65536 + wv * 8192 + i * 2048, l * 16, L + (s & 1) * 65536 + wv * 8192 + i * 2048); };
    if (w8 >= 4) {
        const rsrc_t rf = mkrs(fP + (kq * 4 + (w8 & 3)) * 32, 4), rp = mkrs(a.part, NSEG * 524288);
        const int stp = ((nt * 4 + (w8 & 3)) * 4 + kq) * 4096 + l * 16;
        const unsigned* fW = l < 32 ? a.ctl + C_M + (kq * 32 + l) * 32 : fP + ((l & 3) * 4 + kq) * 32;
        unsigned fv = 0; int pc = 0;
        auto a_ok = [&](int s) { return s < SO && (unsigned)__builtin_amdgcn_ballot_w64(fv <= (unsigned)s) == 0; };
        auto p_upd = [&]() { if ((__builtin_amdgcn_ballot_w64(fv <= (unsigned)pc) >> 32) == 0) ++pc; if ((__builtin_amdgcn_ballot_w64(fv <= (unsigned)pc) >> 32) == 0) ++pc; lp<unsigned>(L_DEC)[2] = pc; };
        int have = 0, nxt = 0, late = 0;
        for (int s = 0; s <= SO; ++s) {
            if (!have) {
                if (!late) {
                    gld_wait<0>(fv);
                    __builtin_amdgcn_raw_buffer_store_b32(s, rf, l * 4, 0, 16);
                    if (w8 == 4) {
                        for (int i = 0;; ++i) { if (fv <= (unsigned)(l < 32 ? s + 1 : s - 1)) fv = gld(fW); WAITV(0); p_upd(); if (a_ok(s) || pc == SO) break; __builtin_amdgcn_s_sleep(2); if (i > GCAP) fatal(err); }
                        lp<unsigned>(L_DEC)[3] = s + 1; lp<unsigned>(L_DEC)[~s & 1] = a_ok(s + 1); FENCE(); }
                    if (s == SO) break;
                    for (int i = 0; ldsr(L_DEC + 12) <= (unsigned)s; ++i) { __builtin_amdgcn_s_sleep(1); if (i > LCAP) endpgm(); }
                    FENCE();
                    PT(0);
#pragma unroll
                    for (int i = 0; i < 8; ++i) oissue(s, w8 ^ (~i & 4), i & 3);
                    WAITV(8);
                }
                WAITV(12); BAR();
                nxt = RFL(ldsr(L_DEC + 4 * (~s & 1))) & 1;
                if (late) WAITV(4); else WAITV(0);
                if (nxt) {
#pragma unroll
                    for (int i = 0; i < 4; ++i) oissue(s + 1, w8 ^ 4, i); }
                PT(1);
            }
            if (w8 == 4 && !nxt) {
                if (!have) { if (fv <= (unsigned)(s + 1)) gld_req(fv, fW); WAITV(0); }
                gld_wait<4>(fv);
                p_upd(); lp<unsigned>(L_DEC)[~s & 1] = 4 * a_ok(s + 1); WAITL();
                if (fv <= (unsigned)(s + 2)) gld_req(fv, fW); }
            BAR();
            PT(2);
            PRIO(2);
            frag_cd acc[4]; Half hx; unsigned mid;
            seg_gemm<5, false>(wf, rb + (s & 1) * 65536, acc, [&](int i) {
                if (i == BARI - 5) { if (!(have & nxt)) WAITV(0);
                    gld_wait<4>(fv);
                    if (w8 == 4) { p_upd(); const bool k = a_ok(s + 1); if (k) lp<unsigned>(L_DEC)[3] = s + 2; lp<unsigned>(L_DEC)[s & 1] = a_ok(s + 2) + 2 * k; }
                    __builtin_amdgcn_raw_buffer_store_b32(s - (have & nxt), rf, l * 4, 0, 16); FENCE(); }
                if (i == BARI) { PRIO(3); PT(5); BAR(); PT(6); }
                if (i == 3) asm volatile("ds_read_b32 %0, %1" : "=v"(mid) : "v"(L_DEC + 4 * (~s & 1)) : "memory");
                if (i == 26) half_req(L_EX + (w8 & 3) * 4096 + l * 16, L_DEC + 4 * (s & 1), hx);
                if (i == 28) { asm volatile("s_waitcnt lgkmcnt(4)" : "+v"(hx.dec), "+v"(mid)); const int d = RFL(hx.dec); have = nxt | RFL(mid) >> 2; nxt = have & d; late = ~have & d >> 1 & 1; }
                if (i >= 28 && (nxt | late)) oissue(s + 1 + nxt, w8 ^ 4, i - 28); });
            PRIO(0);
            PT(3);
            half_get(hx, acc, [&](int i) { if (late) oissue(s + 1, w8, i); });
            if (w8 == 4 && fv <= (unsigned)(s + 3)) gld_req(fv, fW);
#pragma unroll
            for (int i = 0; i < 4; ++i) STB(__builtin_bit_cast(v4u, acc[i]), rp, stp + i * 1024, s * 524288);
            FENCE();
            PT(4);
        }
        if (w8 == 5) {
            flags_wait<4>(a.ctl + C_Y + j * 32 + 1, 1, 1, l, err);
            const unsigned tok = 4095u - (lp<unsigned>(L_KEY)[l & 7] & 4095u); const int xo = tok * DM + j * 32 + (l >> 3) * 4;
            const frag_cd y = __builtin_bit_cast(frag_cd, __builtin_amdgcn_raw_buffer_load_b128(mkrs(a.ypart + (32 * 28 + j) * 1024, 1024), l * 16, 0, 16));
            const v2u xv = *(const v2u*)(a.x + xo); const float gt = __uint_as_float(gld(a.gate + tok)), omg = rbf(1.0f - gt);
            const float b[4] = {rbf(lo16(xv.x) + y[0]), rbf(hi16(xv.x) + y[1]), rbf(lo16(xv.y) + y[2]), rbf(hi16(xv.y) + y[3])};
            asm volatile("" :: "v"(b[0]), "v"(b[1]), "v"(b[2]), "v"(b[3]), "v"(omg));
            BAR();
            const frag_cd t = *lp<frag_cd>(L_Y + l * 16);
            if (tok < 32 * SO) *(v2u*)(a.out + xo) = v2u{mix(gt, omg, t[0], t[1], b[0], b[1]), mix(gt, omg, t[2], t[3], b[2], b[3])};
        } else BAR();
    } else {
        const Finisher fz(a, j, w8, l, L_Y);
        int fin = 0, have = 0, pc = 0;
        for (int s = 0; s <= SO; ++s) {
            if (!have) {
                for (int i = 0; fin < s; ++i) {
                    if (s < SO && RFL(ldsr(L_DEC + 12)) > s) break;
                    pc = RFL(ldsr(L_DEC + 8));
                    if (fin < pc) { FENCE(); fz.finish(fin, fz.issue(fin, true), [](int) {}); ++fin; }
                    else { __builtin_amdgcn_s_sleep(1); if (i > LCAP) endpgm(); }
                }
                if (s == SO) break;
            }
            FENCE();
            const bool fi = fin < pc;
            const Finisher::Fin f = fz.issue(fin, fi);
            WAITL(); WAITV(15);
            PT(0);
            BAR();
            PT(2);
            PRIO(2);
            frag_cd acc[4];
            v4u dec;
            seg_gemm<1, false>(wf, rb + (s & 1) * 65536, acc, [&](int i) {
                if (i == BARI) { PRIO(3); PT(1); WAITV(6); BAR(); PT(6); }
                if (i == 26) asm volatile("ds_read_b128 %0, %1" : "=v"(dec) : "v"(L_DEC) : "memory"); });
            PRIO(1);
            PT(3);
            asm volatile("s_waitcnt lgkmcnt(0)" : "+v"(dec));
            have = RFL(s & 1 ? dec.x : dec.y) & 5; pc = RFL(dec.z);
            half_put(L_EX, w8, l, acc);
            WAITV(0);
            if (have & 4) {
#pragma unroll
                for (int i = 0; i < 4; ++i) oissue(s + 1, w8, i); }
            if (have) { oissue(s + 1, w8 ^ 4, 0); oissue(s + 1, w8 ^ 4, 1); }
            PT(4);
            if (!have) { WAITL(); BAR(); }
            fz.finish(fin, f, [&](int i) { if (have) oissue(s + 1, w8 ^ 4, 2 + i); });
            fin += fi;
            MARK(1 + s);
            PT(5);
        }
        WAITL(); BAR();
    }
    PFLUSH();
    MARK(65);
}

__global__ void __launch_bounds__(512, 2) k_mega(Args a) {
    __shared__ __attribute__((aligned(1024))) unsigned char smem[163840];
    const unsigned L = (unsigned)(size_t)(LAS unsigned char*)smem;
    const int b = blockIdx.x, u = (b & 3) + 4 * (b >> 3);
    MARK(255); MARKC(254);
    const unsigned hb = __builtin_amdgcn_raw_buffer_load_b32(mkrs(a.ctl + C_HB, 4), threadIdx.x >> 6 == (b & 4 ? (u < 96 ? 4 : 0) : 3) ? 0 : OOB, 0, 16);
    FENCE();
    if (!(b & 4)) chain(a, u, L, hb);
    else if (u < 96) gwork(a, (u >> 2) % 3, (u & 3) * 8 + (u >> 2) / 3, L, hb);
    else owork(a, (u >> 2) - 24, u & 3, L, hb);
}

static constexpr int g_coff = 0, g_force = 0; static constexpr int g_d1 = 620, g_d2 = 1340, g_per = 1800, g_dinit = 250; static constexpr unsigned* g_dbg = nullptr;
extern "C" void kernel_launch(void* const* d_in, const int* in_sizes, int n_in, void* d_out, int out_size, void* d_ws, size_t ws_size, hipStream_t stream) {
    auto in = [&](int i) { return (const bf16_t*)d_in[i]; };
    unsigned char* ws = (unsigned char*)d_ws; size_t off = g_coff;
    auto alloc = [&](size_t bytes) { unsigned char* p = ws + off; off += (bytes + 255) & ~(size_t)255; return p; };
    unsigned* ctl = (unsigned*)alloc(C_WORDS * 4);
    unsigned* gate = (unsigned*)alloc(2 * S_TOK * 4); unsigned char* ypart = alloc(32 * 29 * 1024);
    off += (size_t)(-(uintptr_t)(ws + off)) & ((2u << 20) - 1);
    unsigned char* ring = alloc(RING_BYTES);
    unsigned char* qkv = alloc((size_t)NSEG * NH * 24576);
    unsigned char* ox = alloc((size_t)NSEG * 262144); unsigned char* part = alloc((size_t)NSEG * 524288);
    Args a{in(0), in(1), in(2), in(3), in(4), in(5), (bf16_t*)d_out, ctl, ring, qkv, ox, part, in(8), in(9), in(10), in(11), in(12), gate, ypart, g_dbg,
           g_d1 * 16 / 10, g_d2 * 16 / 10, g_per * 16 / 10, g_dinit * 16 / 10 + (g_force << 16)};
    k_mega<<<256, 512, 0, stream>>>(a);
}
```

```cpp
#include <hip/hip_runtime.h>
#include <stdint.h>
#include <type_traits>

typedef unsigned short bf16_t;
typedef unsigned long long u64;
typedef float frag_cd __attribute__((ext_vector_type(4)));
typedef unsigned v4u __attribute__((ext_vector_type(4)));
typedef unsigned v8u __attribute__((ext_vector_type(8)));
typedef unsigned v2u __attribute__((ext_vector_type(2)));
typedef float v2f __attribute__((ext_vector_type(2)));
typedef __bf16 bf16x2_t __attribute__((ext_vector_type(2)));
typedef __bf16 v8bf __attribute__((ext_vector_type(8)));
typedef __bf16 v16bf __attribute__((ext_vector_type(16)));
typedef short v4s_t __attribute__((ext_vector_type(4)));
typedef __amdgpu_buffer_rsrc_t rsrc_t;
#define LAS __attribute__((address_space(3)))
#define GAS __attribute__((address_space(1)))

static constexpr int S_TOK = 2048, DM = 1024, NH = 32, DH = 128, MM = 8, SEG = 32, NSEG = 64, HD = NH * DH, FF = 3584, TOPC = 8;
static constexpr int C_ERR = 0, C_READY = 32, C_CAL = 64  , C_ACK = 96, C_G = 128  , C_M = C_G + 4096  ,
                     C_P = C_M + 4096  , C_R = C_P + 4096, C_Y = C_R + 1024  , C_HB = C_Y + 1024, C_WORDS = C_HB + 32;
static constexpr int RING_BYTES = 4 << 20, B_H1 = 64, B_H3 = 96, B_TS = 112;
static constexpr unsigned SPIN_CAP = 1u << 18;
static constexpr int OOB = 0x7ffffff0, LCAP = 1 << 22, GCAP = 1 << 20;
static constexpr float SCALE = 0.08838834764831845f, LOG2E = 1.4426950408889634f;

struct Args {
    const bf16_t* x; const bf16_t* mem0; const bf16_t* Wq; const bf16_t* Wk; const bf16_t* Wv; const bf16_t* Wo;
    bf16_t* out; unsigned* ctl; unsigned char* ring; unsigned char* qkv; unsigned char* ox; unsigned char* part;
    const bf16_t* wr; const bf16_t* wg; const bf16_t* wu; const bf16_t* wd; const bf16_t* gw;
    unsigned* gate  ; unsigned char* ypart; unsigned* dbg;
    int d1, d2, per, dinit;
};

__device__ __forceinline__ unsigned cvt_pk(float lo, float hi) { v2f v = {lo, hi}; return __builtin_bit_cast(unsigned, __builtin_convertvector(v, bf16x2_t)); }
__device__ __forceinline__ float lo16(unsigned p) { return __uint_as_float(p << 16); }
__device__ __forceinline__ float hi16(unsigned p) { return __uint_as_float(p & 0xffff0000u); }
__device__ __forceinline__ float rbf(float f) { return lo16(cvt_pk(f, 0.f)); }
template <int C> __device__ __forceinline__ unsigned dppu(unsigned v) { return (unsigned)__builtin_amdgcn_update_dpp(0, (int)v, C, 0xf, 0xf, true); }
template <int C> __device__ __forceinline__ float dppf(float v) { return __uint_as_float(dppu<C>(__float_as_uint(v))); }
#define ROR8 0x128
#define HMIR 0x141
__device__ __forceinline__ v2u sw16(unsigned a, unsigned b) { auto r = __builtin_amdgcn_permlane16_swap(a, b, false, false); return v2u{r[0], r[1]}; }
__device__ __forceinline__ v2u sw32(unsigned a, unsigned b) { auto r = __builtin_amdgcn_permlane32_swap(a, b, false, false); return v2u{r[0], r[1]}; }
__device__ __forceinline__ float tadd16(float a, float b) { v2u r = sw16(__float_as_uint(a), __float_as_uint(b)); return __uint_as_float(r.x) + __uint_as_float(r.y); }
__device__ __forceinline__ float tadd32(float a, float b) { v2u r = sw32(__float_as_uint(a), __float_as_uint(b)); return __uint_as_float(r.x) + __uint_as_float(r.y); }
__device__ __forceinline__ float tmax16(float a) { v2u r = sw16(__float_as_uint(a), __float_as_uint(a)); return fmaxf(__uint_as_float(r.x), __uint_as_float(r.y)); }
__device__ __forceinline__ float tmax32(float a) { v2u r = sw32(__float_as_uint(a), __float_as_uint(a)); return fmaxf(__uint_as_float(r.x), __uint_as_float(r.y)); }
__device__ __forceinline__ unsigned umin2(unsigned a, unsigned b) { return min(a, b); }
__device__ __forceinline__ unsigned umax3(unsigned a, unsigned b, unsigned c) { return max(max(a, b), c); }
__device__ __forceinline__ v4u pack_swap(frag_cd a, frag_cd b) { v2u x0 = sw16(cvt_pk(a[0], a[1]), cvt_pk(b[0], b[1])), x1 = sw16(cvt_pk(a[2], a[3]), cvt_pk(b[2], b[3])); return v4u{x0.x, x1.x, x0.y, x1.y}; }
#define MFMA(a, b, c) __builtin_amdgcn_mfma_f32_16x16x32_bf16(__builtin_bit_cast(v8bf, a), __builtin_bit_cast(v8bf, b), c, 0, 0, 0)
#define SMF(a, b, c, ix) __builtin_amdgcn_smfmac_f32_16x16x64_bf16(__builtin_bit_cast(v8bf, a), __builtin_bit_cast(v16bf, b), c, ix, 0, 0)
#define FENCE() asm volatile("" ::: "memory")
#define SB() __builtin_amdgcn_sched_barrier(0)
#define RFL(x) __builtin_amdgcn_readfirstlane(x)
#define BAR() do { FENCE(); __builtin_amdgcn_s_barrier(); FENCE(); } while (0)
#define WAITV(n) do { FENCE(); __builtin_amdgcn_s_waitcnt(0x0F70 | (n)); FENCE(); } while (0)
#define WAITL() asm volatile("s_waitcnt lgkmcnt(0)" ::: "memory")
__device__ __forceinline__ void STB(v4u v, rsrc_t rs, int off, int soff) { __builtin_amdgcn_raw_buffer_store_b128(v, rs, off, soff, 16); asm volatile("s_nop 1" :: "v"(v) : "memory"); }
template <class T> __device__ __forceinline__ LAS T* lp(unsigned a) { return (LAS T*)(size_t)a; }
__device__ __forceinline__ void glds(const void* g, unsigned voff, unsigned lds) { asm volatile("s_mov_b32 m0, %2\n\ts_nop 0\n\tglobal_load_lds_dwordx4 %0, %1" :: "v"(voff), "s"(g), "s"(lds) : "memory"); }
#define NTC " nt"
#define NTW " nt"
__device__ __forceinline__ void gldsc(const void* g, unsigned voff, unsigned lds) { asm volatile("s_mov_b32 m0, %2\n\ts_nop 0\n\tglobal_load_lds_dwordx4 %0, %1" NTC :: "v"(voff), "s"(g), "s"(lds) : "memory"); }
__device__ __forceinline__ void gldsw(const void* g, unsigned voff, unsigned lds) { asm volatile("s_mov_b32 m0, %2\n\ts_nop 0\n\tglobal_load_lds_dwordx4 %0, %1" NTW :: "v"(voff), "s"(g), "s"(lds) : "memory"); }
template <int N> __device__ __forceinline__ void glds1(const void* g, unsigned voff, unsigned lds) {
#define GL(o) "\n\tglobal_load_lds_dwordx4 %0, %1 offset:" #o " sc1"
    if constexpr (N == 1) asm volatile("s_mov_b32 m0, %2\n\ts_nop 0" GL(0) :: "v"(voff), "s"(g), "s"(lds) : "memory");
    if constexpr (N == 2) asm volatile("s_mov_b32 m0, %2\n\ts_nop 0" GL(0) GL(1024) :: "v"(voff), "s"(g), "s"(lds) : "memory");
    if constexpr (N == 4) asm volatile("s_mov_b32 m0, %2\n\ts_nop 0" GL(0) GL(1024) GL(2048) GL(3072) :: "v"(voff), "s"(g), "s"(lds) : "memory");
}
__device__ __forceinline__ rsrc_t mkrs(const void* p, int bytes) { return __builtin_amdgcn_make_buffer_rsrc((void*)p, 0, bytes, 0x00020000); }
__device__ __forceinline__ unsigned gld(const unsigned* p) { return __hip_atomic_load(p, __ATOMIC_RELAXED, __HIP_MEMORY_SCOPE_AGENT); }
__device__ __forceinline__ void gst(unsigned* p, unsigned v) { __hip_atomic_store(p, v, __ATOMIC_RELAXED, __HIP_MEMORY_SCOPE_AGENT); }
__device__ __forceinline__ void gst1(unsigned* p, unsigned v) { asm volatile("s_mov_b64 exec, 1\n\tglobal_store_dword %0, %1, %2 sc1\n\ts_mov_b64 exec, -1" :: "v"(0u), "v"(v), "s"(p) : "memory"); }
__device__ __forceinline__ unsigned rt16() { return (unsigned)__builtin_amdgcn_s_memrealtime() << 4; }

#define NSLOT 12
#define PT(k) do {} while (0)
#define PFLUSH() do {} while (0)
#define MARK(i) do {} while (0)
#define MARKC(i) do {} while (0)
#define MARKW(i) do {} while (0)

#define OSP 0
#define OD0 0
#define FL4 0
#define FV 0
#define NTV ""
#define GAL 2
#define LF 1
#define GDL 0
#define CDL 0
#define XPF 1
#define GHB 1
#define OPF 0
#define GPF 0
#define CPF 0
#define DRY 1
#define OWF 0
#define CKL 1
#define SG0 140
#define TLW 0
#define RGAP 0
#define XA3 1
#define GE2 8
#define VQ 700
#define H3X 0
#define EW 30
#define E0 32
#define E2 160
#define E3 120
#define AT 0
#define XH 0
#define XP 0
#define VR 0
#define DWM 4
#define NQ 1130
#ifndef NV
#define NV 960
#endif
#define LXC 200
#define TSL 1
#define RSL 1
#define SO 58
#define TS1 2
#define TS2 16
#define TMAP 1
#define TIF 0
#define TSP 0
#define OPL 1
#define CPL 0
#define TNT 0
#define EF 0
#define TFK 8
#define TS3 0
#define TSPEC 0
#define TD 10
#define VDEL1 15
#define AEARLY 1
#define XC 4
#define ODEL 1
#define SMQ 0x53b
#define SMV 0x79b
#define AL1 1
#define AL2 0
#define AL3 0
#define AL4 1
#define AL5 7
#define AL6 0
#define HB 0
#define HP 0
#define H3 0
#define HSM 0x2d
#define PF 22
#define US 1
#define ROT 0
#define VL 1
#define VP 0x3911
#define VE 0
#define VN3 36
#define CV 0
#define CD 60
#define XS1 0
#define XS2 6
#define PLO 0
#define MSP 1
#define VD 260
#define LK 300
#define LT 400
#define NS 16
#define GS 6
#define KC 560
#define GM 200
#define SGAP 60
#define HSL 6
#define CSL 3
#define WK 40
#define WKO 106
#define WB 200
#define WC1 0
#define WC2 64
#define WTHR 340
#define SLIP_THR 400
#define CKA 0.75
#define CKF 22.5
#define TG_THR 480
#define SLIP_ADD 640
#define SLIP_INC 10
__device__ __forceinline__ void endpgm() { asm volatile("s_endpgm" ::: "memory"); }
__device__ __forceinline__ void fatal(unsigned* err) { asm volatile("global_store_dword %0, %1, off sc1\n\ts_waitcnt vmcnt(0)\n\ts_endpgm" :: "v"(err), "v"(1u) : "memory"); }
__device__ __forceinline__ unsigned ldsr(unsigned p) { return __hip_atomic_load(lp<unsigned>(p), __ATOMIC_RELAXED, __HIP_MEMORY_SCOPE_WORKGROUP); }
__device__ __forceinline__ v2u trd(unsigned p) { return __builtin_bit_cast(v2u, __builtin_amdgcn_ds_read_tr16_b64_v4i16(lp<v4s_t>(p))); }
__device__ __forceinline__ v8u cat(v2u a, v2u b, v2u c, v2u d) { return v8u{a.x, a.y, b.x, b.y, c.x, c.y, d.x, d.y}; }
__device__ __forceinline__ v4u cat2(v2u a, v2u b) { return v4u{a.x, a.y, b.x, b.y}; }
template <int SL, int FAR = 0> __device__ __forceinline__ void flags_wait(unsigned& v, const unsigned* p, unsigned need, unsigned* err) {
    for (int i = 0; __builtin_amdgcn_ballot_w64(v < need) != 0; ++i) {
        if (i) { if (FAR && __builtin_amdgcn_ballot_w64(v + 1 < need) != 0) __builtin_amdgcn_s_sleep(FAR); __builtin_amdgcn_s_sleep(SL); }
        if (v < need) v = gld(p);
        if (i > GCAP) fatal(err); }
    FENCE();
}
template <int SL> __device__ __forceinline__ void flags_wait(const unsigned* p, int nw, unsigned need, int l, unsigned* err) { unsigned v = need - 1; flags_wait<SL>(v, p + min(l, nw - 1) * 32, need, err); }

__device__ __forceinline__ void gadd1(unsigned* p) { asm volatile("s_mov_b64 exec, 1\n\tglobal_atomic_add %0, %1, off sc1\n\ts_mov_b64 exec, -1" :: "v"(p), "v"(1u) : "memory"); }
__device__ __forceinline__ void gld_req(unsigned& v, const unsigned* p) { asm volatile("global_load_dword %0, %1, off sc1" : "+v"(v) : "v"(p) : "memory"); }
template <int N> __device__ __forceinline__ void gld_wait(unsigned& v) { asm volatile("s_waitcnt vmcnt(%1)" : "+v"(v) : "n"(N) : "memory"); }
__device__ __forceinline__ void ctl_wait(const Args& a, unsigned hb) {
    for (int i = 0; RFL(gld(a.ctl + C_HB)) == RFL(hb); ++i) { __builtin_amdgcn_s_sleep(8); if (i > GCAP) fatal(a.ctl + C_ERR); }
    FENCE();
}

template <class F> __device__ __forceinline__ void ahead(const bf16_t* W, int h, int l, unsigned junk, F row) {
    const unsigned o = (384 - (unsigned)(u64)W) & 896; const int d = (h - (o >> 8)) & 3;
    if (d) {
#pragma unroll 1
        for (int i = 0; i < 4; ++i) gldsw(W, (row(d) + 8 * i + (l >> 3)) * (HD * 2) + (h >> 2) * 1024 + o + (l & 7) * 16, junk); }
}

typedef unsigned v16u __attribute__((ext_vector_type(16)));
typedef unsigned v32u __attribute__((ext_vector_type(32)));
#define STR_(x) #x
#define STR(x) STR_(x)
#define SET(x) "\n.set " #x ", " STR(x)
#define A_STAMPS "0"
#define A_BENCH "0"
static_assert(NH * 24576 == 0xC0000, "");
#define A_CHAIN ".set STAMPS, " A_STAMPS "\n.set BENCH, " A_BENCH "\n.set WB, " STR(WB) "\n.set WC1, " STR(WC1) "\n.set WC2, " STR(WC2) SET(SMQ) SET(SMV) SET(AL1) SET(AL2) SET(AL3) SET(AL4) SET(AL5) SET(AL6) SET(HB) SET(US) SET(PF) SET(HP) SET(H3) "\n.set HS, " STR(HSM) SET(VL) SET(VP) SET(VE) SET(VN3) SET(CV) SET(CD) SET(XS1) SET(XS2) SET(PLO) SET(CSL) SET(MSP) SET(VD) SET(LK) SET(LT) SET(NS) SET(GS) SET(GM) SET(SGAP) SET(EF) SET(NQ) SET(NV) SET(LXC) SET(TSL) SET(RSL) SET(VR) SET(DWM) SET(AT) SET(EW) SET(E0) SET(E2) SET(E3) SET(HSL) SET(XA3) SET(SG0) SET(VQ) "\n.set WK, " STR(WK) "\n.set WN, 64\n.set WZ, WN+5\n.set WTHR, " STR(WTHR) "\n" R"(
.macro SLED n
  s_getpc_b64 vcc
4: s_sub_u32 s77, (3f-4b)/4, s77
  s_lshl2_add_u32 vcc_lo, s77, vcc_lo
  s_addc_u32 vcc_hi, vcc_hi, 0
  s_setpc_b64 vcc
2:
.rept \n
  s_nop 1
.endr
3: s_waitcnt lgkmcnt(0)
.endm
.macro WAITC T, thr
  s_movk_i32 s76, 0x1000
1: s_memrealtime vcc
  s_memtime s[6:7]
  s_sub_u32 s76, s76, 1
  s_cbranch_scc1 3f
  s_waitcnt lgkmcnt(0)
  s_lshl_b32 s77, vcc_lo, 4
  s_sub_i32 s77, \T, s77
  s_cbranch_execz 6f
  s_cmp_ge_i32 s77, \thr
  s_cbranch_scc1 1b
.endm
.macro WAITP T
  WAITC \T, WTHR
  s_sub_i32 s77, s77, WC1
  s_cmp_lt_i32 s77, WB
  s_cbranch_scc1 5f
6:
.irp r,30,32,34,36,38,94,96,98
  s_memrealtime s[\r:\r+1]
  s_nop 1
.endr
  s_memtime s[6:7]
  s_sub_i32 s77, \T, WC2
  s_sub_i32 s4, s4, EW
  s_add_i32 s76, s6, 0x8000
  s_cmp_eq_u64 exec, 0
  s_cselect_b32 s4, s76, s4
  s_waitcnt lgkmcnt(0)
  s_add_u32 s30, s30, s32
  s_add_u32 s34, s34, s36
  s_add_u32 s38, s38, s94
  s_add_u32 s96, s96, s98
  s_add_u32 s30, s30, s34
  s_add_u32 s38, s38, s96
  s_add_u32 s30, s30, s38
  s_lshl_b32 s30, s30, 1
  s_sub_i32 s77, s77, s30
5: s_max_i32 s77, s77, 0
  s_mulk_i32 s77, 45
  s_lshr_b32 s77, s77, 8
  s_sub_i32 s6, s4, s6
  s_ashr_i32 s6, s6, 3
  s_max_i32 s77, s77, s6
  s_min_u32 s77, s77, WN
  SLED WN
.endm
.macro WAITK x, nom
  s_add_i32 s5, s59, \x-\nom
  s_add_i32 s75, s92, s59
  s_movk_i32 s76, 0x1000
  s_add_i32 s75, s75, \x
1: s_memtime vcc
  s_sub_u32 s76, s76, 1
  s_cbranch_scc1 3f
  s_waitcnt lgkmcnt(0)
  s_sub_i32 s77, s75, vcc_lo
  s_sub_i32 s7, s6, vcc_lo
  s_add_i32 s7, s7, s5
  s_max_i32 s77, s77, s7
  s_cbranch_execz 2f
  s_cmp_ge_i32 s77, 8*WK
  s_cbranch_scc1 1b
  s_max_i32 s77, s77, 0
  s_lshr_b32 s77, s77, 3
  SLED WK
.endm
.macro H1ST
.if AT == 1
  buffer_atomic_swap v[RES], v[ST1], s[40:43], s66 offen sc1
.elseif AT == 2
  buffer_atomic_and v[RES], v[ST1], s[40:43], s66 offen sc1
.elseif AT == 3
  buffer_store_dword v[RES], v[ST1], s[40:43], s66 offen sc0 sc1
.elseif AT == 4
  buffer_store_dword v[RES], v[ST1], s[40:43], s66 offen sc1 nt
.else
  buffer_store_dword v[RES], v[ST1], s[40:43], s66 offen sc1
.endif
.endm
.macro ALN k
.p2align 6
.rept \k
  s_nop 0
.endr
.endm
.macro HT n, r
.rept \n
  v_mov_b32 v[\r], 0
  s_nop 1
.endr
.endm
.macro TMAX a, t
  v_readlane_b32 \t, v[\a], 0
.irp r,1,2,3
  v_readlane_b32 s77, v[\a], \r
  s_sub_i32 s77, s77, \t
  s_max_i32 s77, s77, 0
  s_add_i32 \t, \t, s77
.endr
.endm
.macro TGET a
  v_mbcnt_lo_u32_b32 v[\a+1], 7, 0
  v_lshlrev_b32 v[\a+1], 7, v[\a+1]
  s_add_i32 s75, s88, s68
8: buffer_load_dword v[\a], v[\a+1], s[40:43], s75 offen sc1
  s_sub_u32 s71, s71, 1
  s_cbranch_scc1 L_fatal
  s_waitcnt vmcnt(0)
  v_cmp_eq_u32 vcc, -1, v[\a]
  s_cbranch_vccz 9f
  s_sleep TSL
  s_branch 8b
9:
  TMAX \a, s75
  s_mov_b32 s4, s92
  WAITP s75
  s_mov_b32 s76, 0x400000
.endm
.macro PLAN
  s_mulk_i32 s74, PF
  s_ashr_i32 s74, s74, 3
  s_add_i32 s73, s73, s58
  s_add_i32 s73, s73, s74
.endm
.macro STAMP k
.if STAMPS && ((SMK >> \k) & 1)
  s_memtime s[8+2*\k:9+2*\k]
.endif
.endm
.macro NEXT t
.if STAMPS
  s_waitcnt lgkmcnt(0)
.irp k,0,1,2,3,4,5,6,7,8,9,10
  v_writelane_b32 v[\t], s[8+2*\k], \k
.endr
  s_lshl_b32 s76, s64, 3
  s_add_i32 s76, s76, s63
  s_mul_i32 s76, s76, 48
  v_mbcnt_lo_u32_b32 v[\t+1], -1, 0
  v_lshl_add_u32 v[\t+1], v[\t+1], 2, s76
  s_mov_b64 vcc, exec
  s_and_b64 exec, exec, 0x7ff
  ds_write_b32 v[\t+1], v[\t]
  s_mov_b64 exec, vcc
.endif
  s_lshl_b32 s72, s64, 18
  s_mov_b32 s64, s65
  s_add_i32 s65, s65, 1
  s_xor_b32 s66, s66, 0x8000
  s_xor_b32 s67, s67, 0x8000
  s_mov_b32 s76, s68
  s_mov_b32 s68, s69
  s_mov_b32 s69, s70
  s_mov_b32 s70, s76
.endm
.macro RETRY c
.if BENCH
  s_bitset1_b64 s[80+2*\c:81+2*\c], s64
.endif
  s_sub_u32 s71, s71, 1
  s_cbranch_scc1 L_fatal
  s_sleep RSL
.endm
.macro LD d, a, so, o
  buffer_load_dwordx4 v[\d:\d+3], v[\a], s[40:43], \so offen offset:\o sc1
.endm
.macro SM d, a, b
  v_smfmac_f32_16x16x64_bf16 v[\d:\d+3], v[\a:\a+3], v[\b:\b+7], v[SIX]
.endm
.macro MX0 a
  v_max_u32 v[MX], v[\a], v[\a+1]
  v_max3_u32 v[MX], v[MX], v[\a+2], v[\a+3]
.endm
.macro MX1 a
  v_max3_u32 v[MX], v[MX], v[\a], v[\a+1]
  v_max3_u32 v[MX], v[MX], v[\a+2], v[\a+3]
.endm
.macro AW r, k
  ds_write_addtid_b32 v[\r] offset:63744+256*\k
.endm
.macro HS a
  v_pk_add_f32 v[\a:\a+1], v[\a:\a+1], v[\a+2:\a+3]
.endm
.macro ZR a, n
.set zi, 0
.rept \n
  v_mov_b64 v[\a+zi:\a+zi+1], 0
.set zi, zi+2
.endr
.endm
.macro PRE
  s_mov_b32 s64, 63
  s_mov_b32 s65, 0
  s_mov_b32 s66, 0x8000
  s_mov_b32 s67, 0
  s_mov_b32 s68, 0x8000
  s_mov_b32 s69, 0x10000
  s_mov_b32 s70, 0
.endm
.macro XSL r
  s_and_b32 s75, \r, 3
  s_mulk_i32 s75, 20480
.endm
.macro CAP
  s_sub_u32 s76, s76, 1
  s_cbranch_scc1 L_fatal
.endm
LD_go:
  s_mov_b32 s71, 0x40000
  s_mov_b32 s88, s64
  s_mov_b32 s90, s65
  s_mov_b32 s91, s66
  s_mov_b64 s[80:81], 0
  s_mov_b64 s[82:83], 0
  s_mov_b64 s[84:85], 0
  s_mov_b64 s[86:87], 0
  s_mov_b32 s100, s67
  s_mov_b32 s101, s68
  s_cmp_gt_u32 s63, 3
  s_cbranch_scc1 LV_init
; ======== waves 0..3
.set SMK, SMQ
.set O3,168
.set ST1,169
.set RW1,170
.set RW2,171
.set RW3,172
.set XA,173
.set FW,174
.set FR,175
.set SIX,176
.set STK,177
.set FL,183
.set MB,128
.set AC,144
.set TAG,160
.set MX,161
.set RES,162
.set FA,163
.set FB,164
.set VA,164
.set VB,184
.set WV,188
.set RA,134
.set KX,196
.set XF,177
.set XC,)" STR(XC) R"(
.macro MEMQ
  LD MB, O3, s68, 0
  LD MB+4, O3, s68, 1024
  LD MB+8, O3, s68, 2048
  LD MB+12, O3, s68, 3072
.endm
.macro XCHK R
  s_add_i32 s75, s64, 2
  s_min_u32 s75, s75, 63
  v_cmp_ge_u32 vcc, s75, v[FL]
  s_cbranch_vccnz LX_w\R
LX_g\R:
  s_add_i32 s75, s64, 2
  XSL s75
  s_add_i32 m0, s75, s61
.endm
.macro XL4 a
.irp o,0,1024,2048,3072
  global_load_lds_dwordx4 v[\a], s[52:53] offset:\o sc1
.endr
  s_add_i32 m0, m0, 0x1000
.endm
.macro XREF R
  s_add_i32 s75, s64, 3
  s_min_u32 s75, s75, 63
  v_cmp_ge_u32 vcc, s75, v[FL]
  s_cbranch_vccz LX_n\R
  global_load_dword v[FL], v[XF], s[54:55] sc1
LX_n\R:
.endm
.macro XADV
  s_cmp_lt_u32 s64, 61
  s_cselect_b32 s75, 0xC0000, 0
  s_add_u32 s52, s52, s75
  s_addc_u32 s53, s53, 0
.endm
.macro XWAIT R
LX_w\R:
  s_mov_b32 s76, 0x100000
.if \R == 3
  s_waitcnt vmcnt(0)
  v_cmp_ge_u32 vcc, s75, v[FL]
  s_cbranch_execz 1f
  s_cbranch_vccz LX_g\R
.endif
1: global_load_dword v[FL], v[XF], s[54:55] sc1
  s_waitcnt vmcnt(0)
  v_cmp_ge_u32 vcc, s75, v[FL]
  s_cbranch_execz 2f
  s_cbranch_vccz LX_g\R
  s_sleep 4
  CAP
  s_branch 1b
2:
.endm
.macro OXST
  buffer_store_dwordx4 v[KX:KX+3], v[STK], s[44:47], s72 offen sc1
  buffer_store_dwordx4 v[KX+4:KX+7], v[STK], s[44:47], s72 offen offset:1024 sc1
.endm
.macro FLAGQ val
  v_mov_b32 v[MX], 0
  v_mov_b32 v[FA], \val
  s_mov_b64 vcc, exec
  s_and_b64 exec, exec, 1
  global_store_dword v[MX], v[FA], s[50:51] sc1
  s_mov_b64 exec, vcc
.endm
.macro SMW i
  SM VA, MB+4*\i, WV+8*\i
  SM VB, MB+4*\i, WV+32+8*\i
.endm
.macro SMN k
.if (VP >> \k) & 1
  SM VA+(VB-VA)*(vn&1), MB+4*(vn>>1), WV+32*(vn&1)+8*(vn>>1)
.set vn, vn+1
.endif
.endm
.macro VOUT
  v_add_f32 v[VA], v[VA], v[VA+2]
  v_add_f32 v[VA+1], v[VA+1], v[VA+3]
  v_add_f32 v[VB], v[VB], v[VB+2]
  v_add_f32 v[VB+1], v[VB+1], v[VB+3]
  AW VA, 0
  AW VA+1, 2
  AW VB, 1
  AW VB+1, 3
  AW TAG, 6
.endm
.macro DM k
.if (HS >> \k) & 1
  v_mfma_f32_16x16x32_bf16 v[VA:VA+3], v[MB:MB+3], v[MB:MB+3], 0
.endif
.endm
.macro ZA
  ZR AC, 8
  ZR VA, 2
  ZR VB, 2
.endm
.macro PHASEA R
LQ_loop\R:
  STAMP 0
  ZA
  v_mov_b32 v[TAG], s65
LQ_bar\R:
  s_barrier
  STAMP 1
.if MSP
  LD MB, O3, s68, 0
  s_memtime s[92:93]
  s_nop MSP-1
  LD MB+4, O3, s68, 1024
  s_mov_b32 m0, s100
  s_nop MSP-1
  LD MB+8, O3, s68, 2048
  s_nop MSP
  LD MB+12, O3, s68, 3072
.else
  s_memtime s[92:93]
  MEMQ
  s_mov_b32 m0, s100
.endif
.set XS, 0
.if \R == 2
.set XS, 2
.irp i,0,1,4,5,6,7,8,9,10,11,12,13,14,15
  ds_read_b128 v[WV+4*\i:WV+4*\i+3], v[XA] offset:4096+1024*\i
.if \i == 9
  s_waitcnt lgkmcnt(6)
.endif
.endr
.rept )" STR(ODEL) R"(
  s_nop 7
.endr
  OXST
  s_nop 1
.irp i,2,3
  ds_read_b128 v[WV+4*\i:WV+4*\i+3], v[XA] offset:4096+1024*\i
.endr
  s_waitcnt lgkmcnt(0)
.endif
  s_mov_b32 s76, 0x400000
  ALN AL1
LQ_a\R:
  s_waitcnt vmcnt(3+XS)
  SM AC+8, MB, 64
  v_max_u32 v[MX], v[MB], v[MB+1]
  SM AC+12, MB, 96
  v_max3_u32 v[MX], v[MX], v[MB+2], v[MB+3]
  SM AC, MB, 0
  SM AC+4, MB, 32
  SMW 0
.irp i,1,2,3
  s_waitcnt vmcnt(3+XS-\i)
  SM AC+8, MB+4*\i, 64+8*\i
  v_max3_u32 v[MX], v[MX], v[MB+4*\i], v[MB+4*\i+1]
  SM AC+12, MB+4*\i, 96+8*\i
  v_max3_u32 v[MX], v[MX], v[MB+4*\i+2], v[MB+4*\i+3]
  SM AC, MB+4*\i, 8*\i
.if \i == 3
  v_cmp_eq_u32 vcc, -1, v[MX]
.endif
  SM AC+4, MB+4*\i, 32+8*\i
.if \i < VL
  SMW \i
.endif
.endr
  STAMP 2
  v_add_f32 v[AC+8], v[AC+8], v[AC+10]
  v_add_f32 v[AC+12], v[AC+12], v[AC+14]
  v_add_f32 v[AC+9], v[AC+9], v[AC+11]
  v_add_f32 v[AC+13], v[AC+13], v[AC+15]
  v_add_f32 v[AC+3], v[AC+1], v[AC+3]
  v_add_f32 v[AC], v[AC], v[AC+2]
  v_add_f32 v[AC+5], v[AC+5], v[AC+7]
  s_cbranch_vccnz LQ_ra\R
.set vn, 2*VL
  AW AC+8, 0
  SMN 0
  AW AC+12, 1
  SMN 1
  AW AC+9, 2
  SMN 2
  AW AC+13, 3
  SMN 3
  AW AC+3, 4
  SMN 4
  AW AC+5, 5
  SMN 5
  AW TAG, 6
  SMN 6
  STAMP 6
  s_barrier
  SMN 7
  ds_read_b32 v[FA], v[FR]
  SMN 8
  ds_read2st64_b32 v[AC+8:AC+9], v[RW1] offset1:1
  SMN 9
  ds_read2st64_b32 v[AC+10:AC+11], v[RW2] offset1:1
  SMN 10
  ds_read2st64_b32 v[AC+12:AC+13], v[RW3] offset1:1
  SMN 11
  s_add_i32 m0, s100, 0x2000
  v_add_f32 v[AC+1], v[AC+4], v[AC+6]
  SMN 12
  SMN 13
  SMN 14
.if vn != 8
.error "v projection incomplete"
.endif
.if VE
  VOUT
.endif
  s_waitcnt lgkmcnt(5*VE)
LQ_p\R:
  v_cmp_gt_u32 vcc, s65, v[FA]
  v_add_f32 v[AC+8], v[AC+8], v[AC]
  v_add_f32 v[AC+9], v[AC+9], v[AC+1]
  v_add_f32 v[AC+8], v[AC+8], v[AC+10]
  v_add_f32 v[AC+9], v[AC+9], v[AC+11]
  v_add_f32 v[AC+8], v[AC+8], v[AC+12]
  v_add_f32 v[AC+9], v[AC+9], v[AC+13]
  v_cvt_pk_bf16_f32 v[RES], v[AC+8], v[AC+9]
  s_cbranch_vccnz LQ_pb\R
  H1ST
  s_memtime s[6:7]
  STAMP 3
.if !VE
  VOUT
.endif
.if \R != 2
  s_cmp_eq_u32 s64, 63
  s_cbranch_scc1 L_last
.elseif EF
  s_cmp_eq_u32 s64, 63
  s_cbranch_scc1 LQ_ef
LQ_eg:
.endif
.if \R != 3
  WAITK 0, NQ
.endif
  STAMP 4
.endm
.macro COLDA R
LQ_pb\R:
.if BENCH && \R != 3
  s_bitset1_b64 s[86:87], s64
.endif
  CAP
  ds_read_b32 v[FA], v[FR]
  ds_read2st64_b32 v[AC+8:AC+9], v[RW1] offset1:1
  ds_read2st64_b32 v[AC+10:AC+11], v[RW2] offset1:1
  ds_read2st64_b32 v[AC+12:AC+13], v[RW3] offset1:1
  s_waitcnt lgkmcnt(0)
  s_cbranch_execnz LQ_p\R
LQ_ra\R:
  RETRY 2
  MEMQ
  s_add_i32 s59, s59, LXC
  TGET RES
  s_cmp_eq_u32 s77, WZ
  s_cbranch_scc1 7f
  MEMQ
7:
  ZA
  s_waitcnt vmcnt(0)
  s_cbranch_execnz LQ_a\R
.endm
  s_mov_b32 s64, 0
  s_mov_b32 s65, 1
  s_mov_b32 s66, 0
  s_mov_b32 s67, 0x8000
  s_mov_b32 s68, 0x10000
  s_mov_b32 s69, 0
  s_mov_b32 s70, 0x8000
  s_mov_b32 s72, 0
  s_setprio 3
  s_cmp_eq_u32 s60, 2
  s_cbranch_scc1 LQ_init2
  s_cmp_eq_u32 s60, 3
  s_cbranch_scc1 LQ_init3
; ---- waves 0,1: scores against the x rows
  PHASEA 0
  LD MB, 178, s66, 0
  LD MB+4, 178, s66, 1024
  XSL s64
  v_add_u32 v[FA], s75, v[179]
  ds_read_b128 v[MB+8:MB+11], v[FA]
  ds_read_b128 v[MB+12:MB+15], v[FA] offset:1024
  ds_read_b128 v[AC+8:AC+11], v[FA] offset:2048
  ds_read_b128 v[AC+12:AC+15], v[FA] offset:3072
  s_waitcnt lgkmcnt(0)
  ALN AL2
LQ_b0:
  s_waitcnt vmcnt(1)
  v_mfma_f32_16x16x32_bf16 v[AC+4:AC+7], v[MB+12:MB+15], v[MB:MB+3], 0
  MX0 MB
  v_mfma_f32_16x16x32_bf16 v[AC:AC+3], v[MB+8:MB+11], v[MB:MB+3], 0
  s_waitcnt vmcnt(0)
  v_mfma_f32_16x16x32_bf16 v[AC+4:AC+7], v[AC+12:AC+15], v[MB+4:MB+7], v[AC+4:AC+7]
  MX1 MB+4
  v_mfma_f32_16x16x32_bf16 v[AC:AC+3], v[AC+8:AC+11], v[MB+4:MB+7], v[AC:AC+3]
  v_cmp_eq_u32 vcc, -1, v[MX]
  s_nop 5
  s_cbranch_vccnz LQ_rb0
.irp e,0,1,2,3
  v_add_f32_dpp v[AC+\e], v[AC+4+\e], v[AC+\e] quad_perm:[2,3,0,1] row_mask:0xf bank_mask:0xf
.endr
  DM 0
  ds_write_b128 v[180], v[AC:AC+3]
  DM 1
  ds_write_b32 v[FW], v[TAG] offset:2048
  DM 2
  s_barrier
  STAMP 5
  s_setprio PLO
  HT HP, VB
.if XS1
  s_sleep XS1
.endif
  XCHK 0
  s_nop 0
  XL4 181
.if XC == 4
  s_nop 0
  XL4 182
.endif
  XREF 0
.if XS2
  s_sleep XS2
.endif
  XADV
  v_mov_b32 v[FA], s62
  ds_read_b32 v[RA], v[FA] offset:8
  s_waitcnt lgkmcnt(0)
  v_readfirstlane_b32 s59, v[RA]
  NEXT MB
  s_setprio 3
  s_cbranch_execnz LQ_loop0
  s_cmp_eq_u32 s60, 1
  s_cbranch_scc1 LD_c3
  COLDA 0
LQ_rb0:
  RETRY 0
.if VQ
  v_or_b32 v[TAG], 0x10000, v[TAG]
.endif
  LD MB, 178, s66, 0
  LD MB+4, 178, s66, 1024
  s_cbranch_execnz LQ_b0
  XWAIT 0
  s_setpc_b64 s[48:49]
; ---- wave 2: scores against the memory rows; attention of 16 x rows (64 columns)
.set AQ,181
.set AVF,182
.set AVM,183
.set RA,184
.set QX,196
.set OA,212
.set MSK,228
.set LX,237
.set PB,238
.set S1,240
.set K7,244
.set P4,244
.macro MF d, a, b, c
  v_mfma_f32_16x16x32_bf16 v[\d:\d+3], v[\a:\a+3], v[\b:\b+3], \c
.endm
.macro XQ op, r
  v_mov_b32 v[FB], v[\r]
  s_nop 1
  v_permlane16_swap_b32 v[\r], v[FB]
  \op v[\r], v[\r], v[FB]
  v_mov_b32 v[FB], v[\r]
  s_nop 1
  v_permlane32_swap_b32 v[\r], v[FB]
  \op v[\r], v[\r], v[FB]
.endm
.macro QKQ
  LD MB, 178, s66, 0
  LD MB+4, 179, s66, 0
  LD MB+8, 178, s66, 1024
  LD MB+12, 179, s66, 1024
.endm
.macro SOFTX
  v_add_f32 v[AC+6], v[AC+4], v[S1+2]
  v_add_f32 v[AC+7], v[AC+5], v[S1+3]
  v_max3_f32 v[MX], v[LX], v[AC+6], v[AC+7]
  XQ v_max_f32, MX
  v_mul_f32 v[MX], s74, v[MX]
.irp i,6,7,8,9,10,11,12,13,14,15
  v_fma_f32 v[AC+\i], v[AC+\i], s73, v[MX]
.endr
.irp i,6,7,8,9,10,11,12,13,14,15
  v_exp_f32 v[AC+\i], v[AC+\i]
.endr
  v_add_f32 v[LX], v[AC+6], v[AC+7]
.irp i,8,9,10,11,12,13,14,15
  v_add_f32 v[LX], v[LX], v[AC+\i]
.endr
  XQ v_add_f32, LX
  v_rcp_f32 v[LX], v[LX]
  s_nop 0
.irp i,6,7,8,9,10,11,12,13,14,15
  v_mul_f32 v[AC+\i], v[AC+\i], v[LX]
.endr
.irp i,0,1,2,3
  v_cvt_pk_bf16_f32 v[P4+\i], v[AC+8+2*\i], v[AC+9+2*\i]
.endr
  v_cvt_pk_bf16_f32 v[PB], v[AC+6], v[AC+7]
.endm
LQ_init2:
  s_mov_b32 s73, 0x3e0293ee
  s_mov_b32 s74, 0xbe0293ee
  PHASEA 2
  QKQ
  XSL s64
  ds_read_b128 v[MSK:MSK+3], v[XA]
  ds_read_b128 v[MSK+4:MSK+7], v[XA] offset:1024
  v_add_u32 v[FB], s75, v[AQ]
  v_add_u32 v[RES], s75, v[AVF]
  v_mov_b32 v[PB+1], 0
.irp i,0,1,2,3
  ds_read_b128 v[QX+4*\i:QX+4*\i+3], v[FB] offset:16384+1024*\i
  ds_read_b128 v[OA+4*\i:OA+4*\i+3], v[FB] offset:1024*\i
.endr
.irp i,0,1,2
  ds_read_b128 v[RA+4*\i:RA+4*\i+3], v[FB] offset:4096+1024*\i
.endr
  ds_read_b128 v[K7:K7+3], v[FB] offset:7168
  s_waitcnt lgkmcnt(10)
  MF AC+8, OA, QX, v[MSK:MSK+3]
.irp i,4,8,12
  s_waitcnt lgkmcnt(10-\i/2)
  MF AC+8, OA+\i, QX+\i, v[AC+8:AC+11]
.endr
  s_waitcnt lgkmcnt(0)
  MF AC+12, RA, QX, v[MSK+4:MSK+7]
  MF AC+12, RA+4, QX+4, v[AC+12:AC+15]
  MF AC+12, RA+8, QX+8, v[AC+12:AC+15]
  MF AC+12, K7, QX+12, v[AC+12:AC+15]
  v_max3_f32 v[LX], v[AC+8], v[AC+9], v[AC+10]
  s_nop 6
  v_max3_f32 v[LX], v[LX], v[AC+11], v[AC+12]
  v_max3_f32 v[LX], v[LX], v[AC+13], v[AC+14]
  v_max_f32 v[LX], v[LX], v[AC+15]
  STAMP 10
  ALN AL3
LQ_b2:
  s_waitcnt vmcnt(3)
  MX0 MB
  s_waitcnt vmcnt(2)
  v_mfma_f32_16x16x32_bf16 v[AC:AC+3], v[MB+4:MB+7], v[MB:MB+3], 0
  MX1 MB+4
  s_waitcnt vmcnt(1)
  MX1 MB+8
  s_waitcnt vmcnt(0)
  v_mfma_f32_16x16x32_bf16 v[AC:AC+3], v[MB+12:MB+15], v[MB+8:MB+11], v[AC:AC+3]
  MX1 MB+12
  v_cmp_eq_u32 vcc, -1, v[MX]
  s_nop 3
  s_cbranch_vccnz LQ_rb2
  v_add_f32_dpp v[AC], v[AC+2], v[AC] quad_perm:[2,3,0,1] row_mask:0xf bank_mask:0xf
  v_add_f32_dpp v[AC+1], v[AC+3], v[AC+1] quad_perm:[2,3,0,1] row_mask:0xf bank_mask:0xf
  DM 3
  ds_write_b64 v[180], v[AC:AC+1]
  DM 4
  ds_write_b32 v[FW], v[TAG] offset:2048
  DM 5
  s_barrier
  STAMP 5
  s_setprio 0
  MF AC+4, MB+4, QX, 0
  MF S1, MB+4, QX+4, 0
  MF AC+4, MB+12, QX+8, v[AC+4:AC+7]
  MF S1, MB+12, QX+12, v[S1:S1+3]
  FLAGQ s64
.irp e,0,1,2,3
  ds_read_b64_tr_b16 v[MB+4*\e:MB+4*\e+1], v[RES] offset:512*\e
  ds_read_b64_tr_b16 v[MB+4*\e+2:MB+4*\e+3], v[RES] offset:512*\e+4096
.endr
.if )" STR(AEARLY) R"(
  SOFTX
.endif
  v_mov_b32 v[FA], s62
  ds_read_b32 v[FB], v[FA] offset:8
  s_mov_b32 s76, 0x400000
LA_w:
  ds_read_b32 v[FA], v[FW] offset:2304
  CAP
  s_waitcnt lgkmcnt(0)
  v_cmp_gt_u32 vcc, s65, v[FA]
  s_cbranch_vccz LA_g
  s_sleep 1
  s_branch LA_w
LA_g:
  STAMP 8
  v_readfirstlane_b32 s59, v[FB]
  s_setprio 2
.irp e,0,1,2,3
  ds_read_b64_tr_b16 v[RA+2*\e:RA+2*\e+1], v[AVM] offset:256*\e
.endr
.if !)" STR(AEARLY) R"(
  SOFTX
  s_nop 0
.endif
.irp e,0,4,8,12
  MF OA+\e, MB+\e, P4, 0
.endr
  s_waitcnt lgkmcnt(0)
.irp e,0,1,2,3
  v_mfma_f32_16x16x16_bf16 v[OA+4*\e:OA+4*\e+3], v[RA+2*\e:RA+2*\e+1], v[PB:PB+1], v[OA+4*\e:OA+4*\e+3]
.endr
  s_nop 1
.irp i,0,1,2,3,4,5,6,7
  v_cvt_pk_bf16_f32 v[KX+\i], v[OA+2*\i], v[OA+2*\i+1]
.endr
.irp i,0,1,4,5
  v_permlane16_swap_b32 v[KX+\i], v[KX+\i+2]
.endr
  STAMP 9
  s_setprio 3
  NEXT MB
  s_cmp_lg_u32 s64, 64
  s_cbranch_execz 1f
  s_cbranch_scc1 LQ_loop2
1:
  OXST
  s_waitcnt vmcnt(0)
  FLAGQ 64
  s_cbranch_execnz L_end
  COLDA 2
.if EF
LQ_ef:
  s_waitcnt vmcnt(1)
  FLAGQ s64
  s_branch LQ_eg
.endif
LQ_rb2:
  RETRY 0
.if VQ
  v_or_b32 v[TAG], 0x10000, v[TAG]
.endif
  QKQ
  s_cbranch_execnz LQ_b2
  s_setpc_b64 s[48:49]
; ---- wave 3: pacing, clearing, sum over the heads
.set CK,144
.set RA,162
.set RC,184
.macro PARTQ
  LD MB, 178, s66, 0
  LD MB+4, 178, s66, 1024
  LD MB+8, 178, s66, 2048
  LD MB+12, 178, s66, 3072
.endm
LQ_init3:
  s_memrealtime vcc
  s_mov_b64 s[44:45], 0
  s_mov_b32 s46, s62
  s_mov_b32 s79, NS*GS
  s_mov_b32 s62, s101
  s_waitcnt lgkmcnt(0)
  s_lshl_b32 s73, vcc_lo, 4
  PHASEA 3
  s_setprio PLO
  v_mov_b32 v[MB+4], s46
  s_memtime s[96:97]
  s_memrealtime s[94:95]
  ds_read_b128 v[VA:VA+3], v[XA] offset:8192
  v_mov_b64 v[MB:MB+1], -1
  v_mov_b64 v[MB+2:MB+3], -1
  v_rcp_f32 v[CK+6], s45
  v_mov_b64 v[CK:CK+1], s[44:45]
  v_cvt_f32_u32 v[CK+4], s57
  v_mul_f32 v[CK+6], s44, v[CK+6]
  s_waitcnt lgkmcnt(0)
  s_sub_u32 s76, s96, s90
  s_sub_u32 s77, s94, s91
  s_mov_b32 s90, s96
  s_mov_b32 s91, s94
  v_mul_f32 v[CK+6], 256.0, v[CK+6]
  v_cvt_f32_u32 v[CK+2], s76
  v_cvt_f32_u32 v[CK+3], s77
  v_cvt_i32_f32 v[CK+6], v[CK+6]
  v_fmac_f32 v[CK+2], )" STR(CKA) R"(, v[CK]
  v_fmac_f32 v[CK+3], )" STR(CKA) R"(, v[CK+1]
  v_rcp_f32 v[CK+5], v[CK+2]
  v_readfirstlane_b32 s51, v[CK+6]
  v_readfirstlane_b32 s44, v[CK+2]
  v_readfirstlane_b32 s45, v[CK+3]
  v_mul_f32 v[CK+3], )" STR(CKF) R"(, v[CK+3]
  v_fma_f32 v[CK+3], v[CK+3], v[CK+5], -1.0
  v_med3_f32 v[CK+3], v[CK+3], -0.5, 1.0
  v_mul_f32 v[CK+3], v[CK+3], v[CK+4]
  v_cvt_i32_f32 v[CK+3], v[CK+3]
  STAMP 7
  s_nop 0
  v_readfirstlane_b32 s47, v[CK+3]
  WAITK VD+CD, NQ
  buffer_store_dwordx4 v[MB:MB+3], v[179], s[40:43], s67 offen sc1
  buffer_store_dwordx2 v[MB:MB+1], v[182], s[40:43], s67 offen sc1
  buffer_store_dwordx2 v[MB:MB+1], v[180], s[40:43], s70 offen sc1
  buffer_store_dwordx2 v[MB:MB+1], v[181], s[40:43], s70 offen sc1
  ds_read_b128 v[RC:RC+3], v[MB+4]
  ds_read_b32 v[RA], v[MB+4] offset:24
.if !XA3
  XCHK 3
.endif
  XREF 3
  STAMP 5
  s_barrier
  s_memrealtime s[94:95]
  s_mov_b32 s76, 0x400000
  s_mov_b32 s78, s73
LQ_t3:
  s_waitcnt lgkmcnt(0)
  v_cmp_gt_u32 vcc, s65, v[RC+3]
  s_cbranch_vccnz LQ_t4
  v_readfirstlane_b32 s73, v[RC]
  v_readfirstlane_b32 s74, v[RC+1]
  v_readfirstlane_b32 s59, v[RC+2]
  v_readfirstlane_b32 s92, v[RA]
  s_sub_i32 s78, s73, s78
  s_cmp_gt_i32 s78, 512
  s_cbranch_scc1 LQ_slip
LQ_b5:
  s_lshl1_add_u32 s4, s74, s57
  s_add_i32 s50, s4, s73
  PLAN
  s_sub_i32 s5, s73, s50
  s_mul_i32 s4, s4, s51
  s_mul_i32 s5, s5, s51
  s_lshr_b32 s4, s4, 12
  s_lshr_b32 s5, s5, 12
  s_add_i32 s4, s4, s92
  s_sub_i32 s4, s4, E2
  s_sub_i32 s5, s5, E3
  s_cmp_lt_u32 s64, NS
  s_cselect_b32 s78, GS, 0
  s_sub_u32 s79, s79, s78
  s_ashr_i32 s47, s47, 1
  s_add_i32 s47, s47, s79
.if XA3
  XCHK 3
.endif
.if CSL
  s_sleep CSL
.endif
  s_lshl4_add_u32 s78, s94, s62
  s_sub_i32 s78, s78, s50
  s_cmp_gt_i32 s78, LT
  s_cselect_b32 s78, s78, -GM
  s_add_i32 s78, s78, s50
  s_add_i32 s78, s78, GM
  s_setprio 3
  WAITP s78
  STAMP 8
  PARTQ
  s_memrealtime vcc
  s_memtime s[6:7]
  v_mov_b32 v[RA], s46
  XL4 XA
  XADV
  ZR AC, 8
  v_add_u32 v[TAG], 1, v[TAG]
  s_waitcnt lgkmcnt(0)
  s_add_i32 s6, s6, s5
  v_mov_b32 v[RA+1], s6
  ds_write_b32 v[RA], v[RA+1] offset:20
LQ_b3:
  s_waitcnt lgkmcnt(0)
.if BENCH
  s_mov_b32 m0, s64
  s_mov_b32 s77, vcc_lo
  v_writelane_b32 v[FW], s77, m0
.endif
  s_lshl_b32 s89, vcc_lo, 4
  s_sub_i32 s89, s89, s50
  s_add_i32 s78, s89, )" STR(SLIP_ADD) R"(
  s_sub_i32 s77, s89, E0
  s_max_i32 s77, s77, 0
  s_cmp_gt_i32 s89, )" STR(SLIP_THR) R"(
  s_cselect_b32 s78, s78, s77
  s_cselect_b32 s77, SGAP, 0
  s_add_i32 s78, s78, s73
  s_and_b32 s78, s78, -2
  s_add_i32 s77, s77, s47
  s_lshr_b32 s76, SG0, s64
  s_cmp_lt_u32 s64, 8
  s_cselect_b32 s76, s76, 0
  s_add_i32 s77, s77, s76
  s_cmp_eq_u64 exec, 0
  s_cselect_b32 s89, 0x100000, s89
  v_mov_b32 v[RA], s78
  v_mov_b32 v[RA+1], s77
  ALN AL4
  s_waitcnt vmcnt(3+XC)
  v_mfma_f32_16x16x32_bf16 v[RC:RC+3], v[VA:VA+3], v[MB:MB+3], 0
  MX0 MB
.irp i,1,2,3
  s_waitcnt vmcnt(3+XC-\i)
  v_mfma_f32_16x16x32_bf16 v[RC:RC+3], v[VA:VA+3], v[MB+4*\i:MB+4*\i+3], v[RC:RC+3]
  MX1 MB+4*\i
.endr
  v_cmp_eq_u32 vcc, -1, v[MX]
  STAMP 9
  s_nop 3
.irp e,0,1,2,3
  v_add_f32_dpp v[RC+\e], v[RC+\e], v[RC+\e] row_ror:8 row_mask:0xf bank_mask:0xf
.endr
  v_cvt_pk_bf16_f32 v[RC], v[RC], v[RC+1]
  v_cvt_pk_bf16_f32 v[RC+1], v[RC+2], v[RC+3]
  s_cbranch_vccnz LQ_rb3
  buffer_store_dwordx2 v[RC:RC+1], v[180], s[40:43], s69 offen sc1
  buffer_store_dwordx2 v[RA:RA+1], v[181], s[40:43], s69 offen sc1
  STAMP 10
  ZR VA, 2
  ZR VB, 2
  NEXT MB
  STAMP 0
  s_cmp_gt_i32 s89, )" STR(TG_THR) R"(
  s_cbranch_scc0 LQ_bar3
  s_cbranch_execz LD_r3
LD_t3:
  TGET RA
  s_cbranch_execnz LQ_bar3
LD_r3:
  s_setpc_b64 s[48:49]
LD_c3:
  COLDA 3
  XWAIT 3
LQ_rb3:
  RETRY 1
  PARTQ
  s_memrealtime vcc
  s_waitcnt vmcnt(0)
  s_cbranch_execnz LQ_b3
LQ_t4:
  ds_read_b128 v[RC:RC+3], v[MB+4]
  CAP
  s_cbranch_execnz LQ_t3
  s_setpc_b64 s[48:49]
LQ_slip:
.if BENCH
  s_bitset1_b64 s[86:87], s64
.endif
  s_cmp_lt_u32 s64, NS
  s_cbranch_scc1 LQ_b5
  s_add_i32 s79, s79, )" STR(SLIP_INC) R"(
  s_min_u32 s79, s79, 4000
  s_branch LQ_b5
; ======== waves 4..7
.set SMK, SMV
.set UB,128
.set B1,160
.set O3,168
.set O1,169
.set OT,170
.set ST1,171
.set ST2,172
.set AIM,173
.set RR,175
.set FW,176
.set FR,177
.set FS,178
.set ASC,179
.set ASM,180
.set AVX,181
.set SIX,182
.set RES,184
.set TAG,185
.set MX,186
.set TM,187
.set MB,188
.set AC,204
.set TV,212
.set FA,214
.set RA,216
.set DD,188
.set SX,208
.set SMM,212
.set ZZ,224
.set VM,240
.set PA,248
.set VX,188
.set UU,220
.macro SCV
  ds_read_b32 v[FA], v[FS]
  ds_read_b128 v[SX:SX+3], v[ASC]
  ds_read_b64 v[SMM:SMM+1], v[ASM]
.endm
.macro CZV t
  v_cvt_pk_bf16_f32 v[UB+8*\t+2], v[ZZ+4*\t], v[ZZ+4*\t+2]
  v_cvt_pk_bf16_f32 v[UB+8*\t+3], v[ZZ+4*\t+1], v[ZZ+4*\t+3]
.endm
.macro URD h, r
.irp k,0,1,2,3
  ds_read_b128 v[VX+\r+4*\k:VX+\r+4*\k+3], v[TM] offset:1024*(\h+\k)
.endr
.endm
.macro UMM h, r, n
.irp k,0,1,2,3
  s_waitcnt lgkmcnt(\n+3-\k)
.irp t,0,1,2,3
.if \k == 0
  v_mfma_f32_16x16x32_bf16 v[UU+\r+4*\t:UU+\r+4*\t+3], v[VX+\r:VX+\r+3], v[16*\t:16*\t+3], 0
.else
  v_mfma_f32_16x16x32_bf16 v[UU+\r+4*\t:UU+\r+4*\t+3], v[VX+\r+4*\k:VX+\r+4*\k+3], v[16*\t+4*\k:16*\t+4*\k+3], v[UU+\r+4*\t:UU+\r+4*\t+3]
.endif
.endr
.endr
  s_nop 1
.irp t,0,1,2,3
  v_cvt_pk_bf16_f32 v[UB+8*\t+\h], v[UU+\r+4*\t], v[UU+\r+4*\t+1]
  v_cvt_pk_bf16_f32 v[UB+8*\t+\h+1], v[UU+\r+4*\t+2], v[UU+\r+4*\t+3]
.endr
.endm
.macro UREST
.if US
  UMM 4, 0, 0
.endif
  ZR ZZ, 8
  ZR DD, 10
  v_mov_b32 v[TAG], s65
.endm
.macro STEPV R
LV_loop\R:
  STAMP 0
.if US
  URD 4, 0
.if \R == 3
  ds_read_b32 v[MX], v[183] offset:20
.endif
  s_waitcnt lgkmcnt(0)
.endif
.if \R == 3
  v_readfirstlane_b32 s4, v[MX]
  s_setprio 3
  WAITP s73
  s_memtime s[6:7]
.endif
  s_barrier
  s_memtime s[92:93]
  s_setprio 1
  s_nop )" STR(VDEL1) R"(
  STAMP 1
  UREST
.if \R == 3
  v_mov_b32 v[TV+2], s6
  buffer_load_dwordx2 v[TV:TV+1], v[OT], s[40:43], s68 offen sc1
  ds_write_b32 v[183], v[TV+2] offset:24
.endif
.if \R == 2
  v_mov_b64 v[PA:PA+1], -1
  v_mov_b64 v[PA+2:PA+3], -1
.rept CV
  s_nop 3
.endr
.irp o,0,1024,2048,3072
  buffer_store_dwordx4 v[PA:PA+3], v[ST1], s[40:43], s67 offen offset:\o sc1
.endr
.endif
  s_mov_b32 s76, 0x400000
  s_setprio 2
  STAMP 9
  ALN AL6
  s_barrier
  s_memtime s[6:7]
  STAMP 10
.if \R == 3
  s_cmp_eq_u32 s64, 0
  s_cbranch_scc1 LV_t0
LV_t1:
  s_waitcnt vmcnt(0)
  v_cmp_eq_u32 vcc, -1, v[TV]
  v_subrev_u32 v[TV], s73, v[TV]
  v_max_i32_dpp v[TV+1], v[TV+1], v[TV+1] quad_perm:[1,0,3,2] row_mask:0xf bank_mask:0xf
  s_cbranch_vccnz LV_rt
  v_max_i32_dpp v[TV], v[TV], v[TV] quad_perm:[1,0,3,2] row_mask:0xf bank_mask:0xf
  v_max_i32_dpp v[TV+1], v[TV+1], v[TV+1] quad_perm:[2,3,0,1] row_mask:0xf bank_mask:0xf
  s_nop 0
  v_max_i32_dpp v[TV], v[TV], v[TV] quad_perm:[2,3,0,1] row_mask:0xf bank_mask:0xf
  s_nop 0
  v_readfirstlane_b32 s75, v[TV]
  v_add_u32 v[TV], s73, v[TV]
  v_readfirstlane_b32 s74, v[TV+1]
  s_add_i32 s73, s73, s75
  s_cmp_gt_i32 s75, 512
  s_cselect_b32 s75, )" STR(SLIP_INC) R"(*45/32, 0
  s_cmp_lt_u32 s64, NS
  s_cselect_b32 s75, -(GS*45/32), s75
  s_add_i32 s75, s75, s59
  v_mov_b32 v[TV+2], s75
  v_mov_b32 v[TV+3], s65
  ds_write_b128 v[183], v[TV:TV+3]
  PLAN
.endif
.if \R == 2
  HT HB, FA
.endif
.if \R == 0
.rept VN3
  s_nop 1
.endr
LV_p:
  ds_read_b32 v[FA], v[FR]
.irp k,0,1,2,3
  ds_read2st64_b32 v[RA+2*\k:RA+2*\k+1], v[RR-(\k&1)] offset0:8*\k offset1:8*\k+1
.endr
  s_waitcnt lgkmcnt(4)
  v_cmp_gt_u32 vcc, s65, v[FA]
  s_waitcnt lgkmcnt(2)
  v_add_f32 v[RA], v[RA], v[RA+2]
  v_add_f32 v[RA+1], v[RA+1], v[RA+3]
  s_waitcnt lgkmcnt(0)
  v_add_f32 v[RA+4], v[RA+4], v[RA+6]
  v_add_f32 v[RA+5], v[RA+5], v[RA+7]
  v_add_f32 v[RA], v[RA], v[RA+4]
  v_add_f32 v[RA+1], v[RA+1], v[RA+5]
  v_cvt_pk_bf16_f32 v[RES], v[RA], v[RA+1]
  s_cbranch_vccnz LV_pb
  H1ST
.endif
  STAMP 3
.if \R != 2
  s_cmp_eq_u32 s64, 63
  s_cbranch_scc1 L_last
.endif
  s_waitcnt lgkmcnt(0)
.if \R == 2
  s_waitcnt vmcnt(0)
.endif
  s_sub_i32 s77, s6, s92
  s_cmp_gt_i32 s77, NV+200
  s_cselect_b32 s77, LXC, 0
  s_add_i32 s6, s6, s77
  s_setprio 3
  WAITK VD, NV
  STAMP 4
  LD VM, O1, s66, 0
  LD VM+4, O1, s66, 1024
  s_mov_b32 s76, 0x400000
.if \R == 2
  s_cmp_eq_u32 s64, 63
  s_cbranch_scc1 LV_lst
.endif
  ALN AL5
  s_barrier
  SCV
  STAMP 5
.if VR == 0
  s_waitcnt vmcnt(1)
  SM ZZ, VM, 0
  v_max_u32 v[RES], v[VM], v[VM+1]
  v_max3_u32 v[RES], v[RES], v[VM+2], v[VM+3]
  SM ZZ+4, VM, 16
  SM ZZ+8, VM, 32
  SM ZZ+12, VM, 48
LV_s\R:
  s_waitcnt lgkmcnt(0)
  STAMP 6
  v_cmp_ne_u32 vcc, s65, v[FA]
  v_max3_f32 v[MX], v[SX], v[SX+1], v[SX+2]
  v_max3_f32 v[TM], v[SX+3], v[SMM], v[SMM+1]
  v_max_f32 v[MX], v[MX], v[TM]
  s_cbranch_vccnz LV_rs\R
LV_c\R:
  s_nop 0
  v_max_f32_dpp v[MX], v[MX], v[MX] quad_perm:[2,3,0,1] row_mask:0xf bank_mask:0xf
  v_mov_b32 v[TM], v[MX]
  s_nop 1
  v_permlane16_swap_b32 v[MX], v[TM]
  v_max_f32 v[MX], v[MX], v[TM]
  v_mov_b32 v[TM], v[MX]
  s_waitcnt vmcnt(0)
  SM ZZ, VM+4, 8
  v_permlane32_swap_b32 v[MX], v[TM]
  v_max_f32 v[MX], v[MX], v[TM]
  v_mul_f32 v[MX], s79, v[MX]
  SM ZZ+4, VM+4, 24
  v_fma_f32 v[SX], v[SX], s78, v[MX]
  v_fma_f32 v[SX+1], v[SX+1], s78, v[MX]
  v_fma_f32 v[SMM], v[SMM], s78, v[MX]
  SM ZZ+8, VM+4, 40
  v_fma_f32 v[SMM+1], v[SMM+1], s78, v[MX]
  v_fma_f32 v[SX+2], v[SX+2], s78, v[MX]
  v_fma_f32 v[SX+3], v[SX+3], s78, v[MX]
  SM ZZ+12, VM+4, 56
  v_exp_f32 v[SX], v[SX]
  v_exp_f32 v[SX+1], v[SX+1]
  v_exp_f32 v[SMM], v[SMM]
  v_cvt_pk_bf16_f32 v[PA], v[SX], v[SX+1]
  v_exp_f32 v[SMM+1], v[SMM+1]
  v_cvt_pk_bf16_f32 v[PA+1], v[SMM], v[SMM]
  v_exp_f32 v[SX+2], v[SX+2]
  v_exp_f32 v[SX+3], v[SX+3]
  v_cvt_pk_bf16_f32 v[PA+3], v[SMM+1], v[SMM+1]
  v_cvt_pk_bf16_f32 v[PA+2], v[SX+2], v[SX+3]
.else
.rept DWM
  v_mfma_f32_16x16x32_bf16 v[PA:PA+3], v[B1:B1+3], v[B1:B1+3], 0
.endr
LV_s\R:
  s_waitcnt lgkmcnt(0)
  STAMP 6
  v_cmp_gt_u32 vcc, s65, v[FA]
  v_max3_f32 v[MX], v[SX], v[SX+1], v[SX+2]
  v_max3_f32 v[TM], v[SX+3], v[SMM], v[SMM+1]
  v_max_f32 v[MX], v[MX], v[TM]
  s_cbranch_vccnz LV_rs\R
.if VR == 2
  s_waitcnt vmcnt(1)
  SM ZZ, VM, 0
.else
  s_nop 0
.endif
  v_max_f32_dpp v[MX], v[MX], v[MX] quad_perm:[2,3,0,1] row_mask:0xf bank_mask:0xf
  v_mov_b32 v[TM], v[MX]
.if VR == 2
  SM ZZ+4, VM, 16
  s_nop 0
.else
  s_waitcnt vmcnt(1)
  SM ZZ, VM, 0
.endif
  v_permlane16_swap_b32 v[MX], v[TM]
  v_max_f32 v[MX], v[MX], v[TM]
  v_mov_b32 v[TM], v[MX]
  v_max_u32 v[RES], v[VM], v[VM+1]
.if VR == 2
  SM ZZ+8, VM, 32
.else
  SM ZZ+4, VM, 16
.endif
  v_permlane32_swap_b32 v[MX], v[TM]
  v_max_f32 v[MX], v[MX], v[TM]
  v_mul_f32 v[MX], s79, v[MX]
.if VR == 2
  SM ZZ+12, VM, 48
.else
  SM ZZ+8, VM, 32
.endif
  v_fma_f32 v[SX], v[SX], s78, v[MX]
  v_fma_f32 v[SX+1], v[SX+1], s78, v[MX]
  v_fma_f32 v[SMM], v[SMM], s78, v[MX]
.if VR == 2
  s_waitcnt vmcnt(0)
  SM ZZ, VM+4, 8
.else
  SM ZZ+12, VM, 48
.endif
  v_fma_f32 v[SMM+1], v[SMM+1], s78, v[MX]
  v_fma_f32 v[SX+2], v[SX+2], s78, v[MX]
  v_fma_f32 v[SX+3], v[SX+3], s78, v[MX]
.if VR == 2
  SM ZZ+4, VM+4, 24
.else
  s_waitcnt vmcnt(0)
  SM ZZ, VM+4, 8
.endif
  v_exp_f32 v[SX], v[SX]
.if VR == 2
  SM ZZ+8, VM+4, 40
.else
  SM ZZ+4, VM+4, 24
.endif
  v_exp_f32 v[SX+1], v[SX+1]
.if VR == 2
  SM ZZ+12, VM+4, 56
.else
  SM ZZ+8, VM+4, 40
.endif
  v_exp_f32 v[SMM], v[SMM]
.if VR == 1
  SM ZZ+12, VM+4, 56
.endif
  v_cvt_pk_bf16_f32 v[PA], v[SX], v[SX+1]
  v_exp_f32 v[SMM+1], v[SMM+1]
  v_cvt_pk_bf16_f32 v[PA+1], v[SMM], v[SMM]
  v_exp_f32 v[SX+2], v[SX+2]
  v_exp_f32 v[SX+3], v[SX+3]
  v_cvt_pk_bf16_f32 v[PA+3], v[SMM+1], v[SMM+1]
  v_cvt_pk_bf16_f32 v[PA+2], v[SX+2], v[SX+3]
  v_max3_u32 v[RES], v[RES], v[VM+2], v[VM+3]
.endif
LV_z\R:
  CZV 0
  SM DD+16, PA, B1
  CZV 1
  SM DD, PA, UB
  CZV 2
  v_max3_u32 v[RES], v[RES], v[VM+4], v[VM+5]
  SM DD+4, PA, UB+8
  CZV 3
  v_max3_u32 v[RES], v[RES], v[VM+6], v[VM+7]
  SM DD+8, PA, UB+16
  v_add_f32 v[DD+16], v[DD+16], v[DD+18]
  v_add_f32 v[DD+17], v[DD+17], v[DD+19]
  v_cmp_eq_u32 vcc, -1, v[RES]
  SM DD+12, PA, UB+24
  v_rcp_f32 v[DD+16], v[DD+16]
  v_rcp_f32 v[DD+17], v[DD+17]
  s_cbranch_vccnz LV_rb\R
  HS DD
  HS DD+4
  HS DD+8
  v_pk_mul_f32 v[DD:DD+1], v[DD:DD+1], v[DD+16:DD+17]
  v_pk_mul_f32 v[DD+4:DD+5], v[DD+4:DD+5], v[DD+16:DD+17]
  HS DD+12
  v_pk_mul_f32 v[DD+8:DD+9], v[DD+8:DD+9], v[DD+16:DD+17]
  v_cvt_pk_bf16_f32 v[SX], v[DD], v[DD+4]
  v_pk_mul_f32 v[DD+12:DD+13], v[DD+12:DD+13], v[DD+16:DD+17]
  v_cvt_pk_bf16_f32 v[SX+2], v[DD+1], v[DD+5]
  v_cvt_pk_bf16_f32 v[SX+1], v[DD+8], v[DD+12]
  v_cvt_pk_bf16_f32 v[SX+3], v[DD+9], v[DD+13]
  buffer_store_dwordx4 v[SX:SX+3], v[ST2], s[40:43], s66 offen sc1
  STAMP 7
  s_setprio 1
.if \R == 2
LV_img:
  v_add_u32 v[TM], 1024, v[AIM]
  ds_write2_b32 v[AIM], v[VM], v[VM+1] offset1:2
  ds_write2_b32 v[AIM], v[VM+2], v[VM+3] offset0:32 offset1:34
  ds_write2_b32 v[TM], v[VM+4], v[VM+5] offset1:2
  ds_write2_b32 v[TM], v[VM+6], v[VM+7] offset0:32 offset1:34
  ds_write_b32 v[FW], v[TAG] offset:2304
  s_cmp_eq_u32 s64, 63
  s_cbranch_scc1 L_end
.endif
LV_u\R:
  XSL s65
  v_add_u32 v[TM], s75, v[AVX]
  ds_read_b32 v[MX], v[183] offset:8
  URD 0, 0
.if US
  UMM 0, 0, 0
.else
  URD 4, 16
  UMM 0, 0, 4
  UMM 4, 16, 0
.endif
  v_readfirstlane_b32 s59, v[MX]
  STAMP 8
  NEXT MB
  s_cbranch_execnz LV_loop\R
.if \R == 0
  s_cmp_eq_u32 s60, 1
  s_cbranch_scc1 LD_t3
LV_pb:
.if BENCH
  s_bitset1_b64 s[86:87], s64
.endif
  CAP
  s_cbranch_execnz LV_p
.endif
.if VQ
LV_q\R:
  s_waitcnt vmcnt(0)
  v_max3_u32 v[FA], v[RES], v[VM+4], v[VM+5]
  v_max3_u32 v[FA], v[FA], v[VM+6], v[VM+7]
  v_cmp_eq_u32 vcc, -1, v[FA]
  s_cbranch_execz 1f
  s_cbranch_vccz LV_c\R
1:
  s_nop 7
  LD VM, O1, s66, 0
  LD VM+4, O1, s66, 1024
  ZR ZZ, 8
  s_waitcnt vmcnt(1)
  SM ZZ, VM, 0
  v_max_u32 v[RES], v[VM], v[VM+1]
  v_max3_u32 v[RES], v[RES], v[VM+2], v[VM+3]
  SM ZZ+4, VM, 16
  SM ZZ+8, VM, 32
  SM ZZ+12, VM, 48
  s_nop 7
  s_cbranch_execnz LV_c\R
.endif
LV_rb\R:
  RETRY 0
  LD VM, O1, s66, 0
  LD VM+4, O1, s66, 1024
  ZR ZZ, 8
  ZR DD, 10
  s_waitcnt vmcnt(0)
.irp t,0,1,2,3
  SM ZZ+4*\t, VM, 16*\t
  s_nop 7
  SM ZZ+4*\t, VM+4, 16*\t+8
.endr
  v_max_u32 v[RES], v[VM], v[VM+1]
  v_max3_u32 v[RES], v[RES], v[VM+2], v[VM+3]
  s_nop 7
  s_cbranch_execnz LV_z\R
.if \R == 2
LV_lst:
  s_cbranch_execz LV_63
  s_barrier
LV_63:
  s_waitcnt vmcnt(0)
  MX0 VM
  MX1 VM+4
  v_cmp_eq_u32 vcc, -1, v[MX]
  s_cbranch_execz 1f
  s_cbranch_vccz LV_img
1:
  RETRY 0
  LD VM, O1, s66, 0
  LD VM+4, O1, s66, 1024
  s_cbranch_execnz LV_63
.endif
LV_rs\R:
.if VQ
  v_and_b32 v[FA], 0xffff, v[FA]
  v_cmp_ne_u32 vcc, s65, v[FA]
  s_cbranch_execz 1f
  s_cbranch_vccz LV_q\R
1:
.endif
.if BENCH
  s_bitset1_b64 s[86:87], s64
.endif
  SCV
  CAP
  s_cbranch_execnz LV_s\R
.if \R == 3
LV_t0:
  s_memrealtime vcc
  s_waitcnt vmcnt(0) lgkmcnt(0)
  s_lshl_b32 s73, vcc_lo, 4
  s_sub_i32 s73, s73, 0x2000
  v_mov_b32 v[TV], s73
  v_mov_b32 v[TV+1], 0
  s_cbranch_execnz LV_t1
LV_rt:
  RETRY 2
  buffer_load_dwordx2 v[TV:TV+1], v[OT], s[40:43], s68 offen sc1
  s_cbranch_execnz LV_t1
.endif
  s_setpc_b64 s[48:49]
.endm
LV_init:
  s_memrealtime vcc
  PRE
  s_waitcnt lgkmcnt(0)
  s_lshl_b32 s73, vcc_lo, 4
  s_mov_b32 s78, 0x3e0293ee
  s_mov_b32 s79, 0xbe0293ee
  ZR UB, 16
  v_mov_b32 v[B1], 0x3f803f80
  v_mov_b32 v[B1+1], 0x3f803f80
  v_mov_b32 v[B1+2], 0x3f80
  v_mov_b32 v[B1+3], 0x3f80
  v_mov_b32 v[B1+4], 0x3f803f80
  v_mov_b32 v[B1+5], 0x3f803f80
  v_mov_b64 v[B1+6:B1+7], 0
  s_cbranch_execz LD_v
  s_cmp_eq_u32 s60, 2
  s_cbranch_scc1 LV_u2
  s_cmp_eq_u32 s60, 3
  s_cbranch_scc1 LV_u3
  s_branch LV_u0
LD_v:
  s_mov_b32 s64, 1
  s_cmp_eq_u32 s60, 2
  s_cbranch_scc1 LV_loop2
  s_cmp_eq_u32 s60, 3
  s_cbranch_scc1 LV_loop3
  STEPV 0
  STEPV 2
  STEPV 3
L_fatal:
  v_mov_b32 v[MX], 0
  v_mov_b32 v[TAG], 1
  global_store_dword v[MX], v[TAG], s[48:49] sc1
  s_waitcnt vmcnt(0)
  s_endpgm
L_last:
  s_barrier
L_end:
  s_waitcnt vmcnt(0) lgkmcnt(0)
)"
static_assert(__builtin_bit_cast(unsigned, SCALE * LOG2E) == 0x3e0293eeu, "");
#define CL10(p) p "0", p "1", p "2", p "3", p "4", p "5", p "6", p "7", p "8", p "9"
#define CL_ST
#define CL_S CL_ST "s4", "s5", "s6", "s7", "s72", "s73", "s74", "s75", "s76", "s77", "s78", "s79", "s88", "s89", "s90", "s91", "s92", "s93", "s94", "s95", "s96", "s97", "s98", "s99", "s100", "s101", CL10("s3"), "vcc", "scc", "memory"

#define WARM() do {} while (0)
#define CL_DRY
__device__ __forceinline__ void chain(const Args& a, const int c, const unsigned L, const unsigned hb) {
    const int tid = threadIdx.x, w8 = RFL(tid >> 6), w = w8 & 3; int l = tid & 63;
    const int g = c & 3, aa = c >> 2, h = g * 8 + (aa >> 2), hp = h ^ XH * 8, jj = aa & 3;
    int n = l & 15, q = l >> 4, hf = (n >> 1) & 1, n2 = (n >> 2) & 1, n3 = n >> 3;
    const unsigned L_EX = L + 114688, L_SC = L + 131072, L_SM = L_SC + 2048, L_FL = L_SM + 512, L_TB = L_FL + 3072, L_VM = L_TB + 256, L_W2 = L + 143360, L_KX = L + 32768, L_VX = L_KX + 8192;
    const rsrc_t rs = mkrs(a.ring, RING_BYTES);
    unsigned* const err = a.ctl + C_ERR;
    const v4u ones = {~0u, ~0u, ~0u, ~0u};
    unsigned cyc0 = RFL((unsigned)__builtin_amdgcn_s_memtime()), rt0 = RFL((unsigned)__builtin_amdgcn_s_memrealtime());
    if (tid < 384) lp<v2u>(L_FL)[tid] = v2u{0u, 0u};
    int lg3 = (c & 31) * 128 + n2 * 64 + hf * 32 + (q & 1) * 16 + (n & 1) * 8;
    unsigned m0[4] = {};
    if (w8 == 3) { const bf16_t* p = a.mem0 + ((c & 1) * 4 + n2 * 2 + (q & 1)) * DM + 16 * (c >> 1) + (n & 3);
#pragma unroll
        for (int i = 0; i < 4; ++i) asm volatile("global_load_ushort %0, %1, off offset:%2" : "=v"(m0[i]) : "v"(p), "n"(8 * i) : "memory"); }
    auto marks = [&](auto nl) {
        const int ok = c < B_H3 || (c < B_TS + 2 && !(c & 2)) ? 0 : OOB;
#pragma unroll
        for (int i = 0; i < 2; ++i) STB(ones, rs, (((c & ~1) * 32768 + (c & 1) * 16384 + i * 8192 + tid * 16) | ok), 0);
        if (w8 == 3) asm volatile("s_waitcnt vmcnt(%0)" :: "n"(nl + 2) : "memory");
        asm volatile("" : "+v"(m0[0]), "+v"(m0[1]), "+v"(m0[2]), "+v"(m0[3]) :: "memory");
        const v2u m = {umin2(m0[0] | m0[1] << 16, 0xFFFFFFFEu), umin2(m0[2] | m0[3] << 16, 0xFFFFFFFEu)};
#pragma unroll
        for (int i = 0; i < 2; ++i) __builtin_amdgcn_raw_buffer_store_b64(m, rs, w8 == 3 ? (B_H3 + 4 * (c >> 5) + 2) * 32768 + ((q >> 1) * 2 + n3) * 4096 + lg3 : OOB, i * 16384, 16);
        FENCE();
    };
    if (!LF) marks(std::integral_constant<int, 0>{});
    const unsigned stg = L + w8 * 16384;
    const int rot = 0;
    const int prr = 16 * (q >> 1) + (q & 1) + 4 * n2 + 2 * n3;
    v32u W[6];
    auto trd4 = [&](int k, unsigned p, int o1, int o2) { const v2u t[4] = {trd(p), trd(p + o1), trd(p + o2), trd(p + o2 + o1)};
#pragma unroll
        for (int i = 0; i < 8; ++i) W[k >> 2][(k & 3) * 8 + i] = t[i >> 1][i & 1]; };
    unsigned cal;
    const unsigned d1c = (a.d1 * 45 >> 5) + NS * (GS * 45 / 32) - WKO;
    auto pg = [&](int blk, int x) { return blk * 32768 + (int)((((cal >> (blk >> 6) * 3) ^ ((x ^ blk >> 4) & 1) * 4 ^ ((x >> 1 ^ blk >> 5) & 1)) & 7) << 12); };
    auto h1o = [&](int inner, int x = 0) { return pg(B_H1 + (aa & 28) + (inner >> 12) * 2, g ^ x ^ XP) + (inner & 4095); };
    unsigned mw[8] = {}; (void)mw;
    auto stream = [&](auto issue, auto read, auto adr, bool nt) {
        if (CDL && !(((u64)(a.Wv + hp * DH) >> 8 & 3) == 1 && jj)) __builtin_amdgcn_s_sleep(CDL);
        MARKW(0);
        if (TLW) asm volatile("s_mov_b32 m0, %2\n\ts_nop 0\n\tglobal_load_lds_dword %0, %1 nt"
                              :: "v"(w8 < 6 ? l * (NH * 24576) + h * 24576 + w8 * 4096 : l * 262144 + (h * 4 + jj) * 2048), "s"(w8 < 6 ? a.qkv : a.ox), "s"(L_W2 + 8192 + w8 * 256) : "memory");
        if (CPF && jj < 3) ahead(jj == 0 ? a.Wq : (jj == 1 ? a.Wk : a.Wv), hp, l, L_W2 + w8 * 1024, [&](int d) { return 256 * w + 64 * d + 32 * (w8 >> 2); });
        issue(0);
        if (LF) marks(std::integral_constant<int, 8>{});
        issue(1);
        MARKW(1);
        WAITV(8); BAR();
        unsigned h2 = hb;
        auto arrive = [&]() { gadd1(a.ctl + C_READY); if (c == 0) gst1(a.ctl + C_ACK, 1u); };
        if (w8 == 3) gld_req(h2, a.ctl + C_HB);
        read(std::integral_constant<int, 0>{}); WAITL(); issue(2);
        gld_wait<8>(h2);
        const bool seen = RFL(h2) != RFL(hb);
        if (seen) arrive();
        read(std::integral_constant<int, 1>{}); WAITL(); issue(3);
        WAITV(8); read(std::integral_constant<int, 2>{});
        WAITV(0); read(std::integral_constant<int, 3>{}); WAITL();
        if (w8 == 3 && !seen) { ctl_wait(a, hb); arrive(); }
    };
    auto start = [&]() {
        if (w8 == 6) {
            for (int i = 0; RFL(gld(a.ctl + C_READY)) < 256; ++i) { if (c == 0) gadd1(a.ctl + C_HB); __builtin_amdgcn_s_sleep(4); if (i > GCAP) fatal(err); }
            FENCE();
            const unsigned v = ~gld(a.ctl + C_CAL + (l & 15)) & 0xffffu;
            unsigned u = (2 * v + dppu<0xB1>(v) + dppu<0x1B>(dppu<HMIR>(v))) * 8 + (l & 7);
            u = min(u, dppu<0xB1>(u)); u = min(u, dppu<0x4E>(u)); u = min(u, dppu<HMIR>(u));
            unsigned t = ((__builtin_amdgcn_readlane(u, 0) & 7) | (__builtin_amdgcn_readlane(u, 8) & 7) << 3) ^ 022;
            *lp<v2u>(L_TB + 16) = v2u{t, cyc0}; *lp<v2u>(L_TB + 8) = v2u{d1c, 0u}; WAITL();
        }
        lp<unsigned>(L_EX + w8 * 2048 + 1536)[l] = 0; WAITL();
        BAR();
        cal = RFL(ldsr(L_TB + 16));
    };
    const unsigned char* xp = a.qkv + h * 24576 + w * 8192 + (w == 2 ? (jj & 1) * 4096 : 0);
    const unsigned char* xq = a.qkv + h * 24576 + 2 * NH * 24576;
    const unsigned xw = w == 3 ? 20480 - XC * 1024 : w * (XC == 4 ? 8192 : 4096);
    const unsigned* fG = a.ctl + C_G + h * 128 + w * 32;
    auto lo = [](const void* p) { return (unsigned)(u64)p; };
    auto hi = [](const void* p) { return (unsigned)((u64)p >> 32); };
    v16u S0 = {lo(a.ring), hi(a.ring), RING_BYTES, 0x20000u, lo(a.ox), hi(a.ox), NSEG * 262144, 0x20000u, lo(err), hi(err), lo(a.ctl + C_M + h * 128 + jj * 32), hi(a.ctl + C_M + h * 128 + jj * 32),
               lo(xq), hi(xq), lo(fG - w * 32), hi(fG - w * 32)};
    v16u S1 = {(unsigned)rot << 10, (unsigned)a.d2, (unsigned)a.per, d1c, (unsigned)w, L_KX + xw, L_TB, (unsigned)w8, 0u, cyc0, rt0, (unsigned)RFL(L_EX - 63744 + w8 * 2048), (unsigned)(a.d2 - a.d1 - KC), 0u, 0u, 0u};
    v8u cnts;
    v16u A;
    unsigned six;
    auto dry = [&]() {
        v16u T0 = S0, T1 = S1;
        asm volatile("s_mov_b64 exec, 0\n\ts_getpc_b64 s[48:49]\n\ts_add_u32 s48, s48, 12\n\ts_addc_u32 s49, s49, 0\n\ts_branch LD_go\n\ts_mov_b64 exec, -1\n\ts_setprio 0\n\ts_waitcnt lgkmcnt(0)\n\ts_nop 7\n\ts_nop 7"
                     : "+{s[40:55]}"(T0), "+{s[56:71]}"(T1), "+{v[188:219]}"(W[4]), "+{v[220:251]}"(W[5]) : "{v[0:31]}"(W[0]), "{v[32:63]}"(W[1]), "{v[64:95]}"(W[2]), "{v[96:127]}"(W[3])
                     : "v128", "v129", CL10("v13"), CL10("v14"), CL10("v15"), CL10("v16"), CL10("v17"), "v180", "v181", "v182", "v183", "v184", "v185", "v186", "v187",
                       "s80", "s81", "s82", "s83", "s84", "s85", "s86", "s87", CL_S);
        asm volatile("v_mbcnt_lo_u32_b32 %0, -1, 0\n\tv_mbcnt_hi_u32_b32 %0, -1, %0" : "=v"(l));
        n = l & 15; q = l >> 4; hf = (n >> 1) & 1; n2 = (n >> 2) & 1; n3 = n >> 3; six = hf ? 0xEEEE : 0x4444;
        lg3 = (c & 31) * 128 + n2 * 64 + hf * 32 + (q & 1) * 16 + (n & 1) * 8;
        if (CKL) { cyc0 = RFL((unsigned)__builtin_amdgcn_s_memtime()); rt0 = RFL((unsigned)__builtin_amdgcn_s_memrealtime()); S1[9] = cyc0; S1[10] = rt0; }
    };

    if (w8 < 4) {
        { const bf16_t* Wm = (jj >> 1 ? a.Wk : a.Wq) + (size_t)256 * w * HD + hp * DH + 64 * (jj & 1);
          const unsigned p = stg + prr * 128 + (((hf ^ (4 * n2 + 2 * n3)) * 16 + (n & 1) * 8) ^ ((w >> 1) * 64));
          stream([&](int i) { _Pragma("unroll 1") for (int j = 0; j < 8; ++j) gldsc(Wm + (size_t)(64 * (i ^ rot) + 8 * j) * HD, (l >> 3) * (HD * 2) + ((l & 7) ^ (2 * q)) * 16, stg + (i & 1) * 8192 + j * 1024); },
                 [&](auto i) { _Pragma("unroll") for (int t = 0; t < 4; ++t) trd4(4 * t + i, (p ^ (t * 32)) + (i & 1) * 8192, 1024, 4096); },
                 [&](int j, unsigned& vo) { vo = (l >> 3) * (HD * 2) + ((l & 7) ^ (2 * q)) * 16; return Wm + (size_t)(128 + 8 * j) * HD; }, true); }
        MARK(2);
        BAR();
        MARK(100);
        { const unsigned pv = stg + 65536 + prr * 64 + (hf ^ (2 * n2)) * 16 + (n & 1) * 8;
#pragma unroll
          for (int k = 0; k < 8; ++k) trd4(16 + k, (pv ^ ((k >> 2) * 32)) + ((k & 3) ^ rot) * 4096, 512, 2048); }
        if (w == 2) {
#pragma unroll
            for (int i = 0; i < 16; ++i) *lp<v4u>(L_W2 + l * 16 + 4096 + i * 1024) = v4u{W[4 + i / 8][i % 8 * 4], W[4 + i / 8][i % 8 * 4 + 1], W[4 + i / 8][i % 8 * 4 + 2], W[4 + i / 8][i % 8 * 4 + 3]}; }
        WAITL();
        dry();
        unsigned fv = 0;
        auto rows = [&]() {
#pragma unroll
            for (int i = 0; i < 4; ++i) if (w < 2 || !(i & 1)) glds1<4>(xp + (i >> 1) * (NH * 24576) + (i & 1) * 4096, l * 16, L_KX + (i >> 1) * 20480 + w * 8192 + (i & 1) * 4096); };
        MARK(101);
        if (w < 3) { fv = RFL(gld(fG)); FENCE(); }
        A[5] = w == 2 ? L_W2 + l * 16 : 16384 + (jj & 1) * 4096 + l * 16;
        A[13] = w < 3 ? xw + l * 16 : 0; A[14] = xw + 4096 + l * 16; A[15] = 0;
        if (w == 2) { const int ck = 8 * (jj >> 1) + hf;
#pragma unroll
            for (int i = 0; i < 8; ++i) lp<unsigned>(A[5] + (i >> 2) * 1024)[i & 3] = 16 * (i >> 2) + 4 * q + (i & 3) > 16 * (jj & 1) + n ? 0xFF800000u : 0u;
            A[13] = L_KX + l * 16; A[14] = L_VX + (ck * 16 + 4 * q + (n >> 2)) * 16 + (l & 1) * 8; A[15] = L_VM + ck * 128 + (2 * q + n2) * 16 + (l & 1) * 8; }
        if (w == 3) {
#pragma unroll
            for (int i = 0; i < 4; ++i) lp<unsigned>(A[5] + 8192)[i] = ((n & 7) >> 1) == i ? 0x3F80u << (16 * (n & 1)) : 0u; }
        const bool early = fv >= 2;
        if (w < 3 && early) rows();
        start();
        const int sl = 2 * (jj & 1) + (w >> 1);
        A[0] = pg(B_H3 + 4 * w, g ^ (H3X)) + (l ^ (w & 1)) * 16;
        A[1] = h1o((jj >> 1) * 2048 + ((sl * 2 + n3) * 2 + (w & 1)) * 128 + (q >> 1) * 64 + n2 * 32 + (q & 1) * 16 + (n & 3) * 4, XH);
        A[2] = L_EX + (w ^ 1) * 2048 + 1024 + l * 4; A[3] = L_EX + (w ^ 2) * 2048 + l * 4; A[4] = L_EX + (w ^ 3) * 2048 + 512 + l * 4;
        A[6] = L_FL + w * 256 + l * 4; A[7] = L_EX + (l & 3) * 2048 + 1536 + l * 4; A[8] = six;
        A[9] = w == 2 ? h * 8192 + jj * 2048 + ((2 * (q & 1) + (q >> 1)) * 16 + n) * 16 : (XC == 4 ? min(w, 2) * 128 : (w == 3 ? 128 + (l & 1) * 128 : 0));
        A[10] = w < 3 ? h1o(l * 16) : pg(2 * aa, g) + l * 16;
        A[11] = w < 2 ? L_KX + w * 4096 + l * 16 : h1o((w == 2 ? 2048 : jj * 1024) + l * 16);
        A[12] = w < 2 ? L_SC + w * 1024 + ((l * 16) ^ ((q & 1) * 32)) : (w == 2 ? L_SM + ((l * 8) ^ ((q & 1) * 16)) : pg(B_H3 + 4 * (c >> 5), (q >> 1) * 2 + n3) + lg3);
        if (w == 3) { A[13] = c % 33 == 0 && l < 4 ? pg(B_TS, l) + (c >> 5) * 128 : OOB; A[14] = h1o(4096 + jj * 512 + l * 8); }
        asm volatile("" : "+v"(A));
        if (w < 3 && !early) { fv = 0; flags_wait<4>(fv, fG, 2, err); rows(); }
        if (w < 2) A[15] = fv;
        MARK(102);
    } else {
        { const bf16_t* Wo = a.Wo + (size_t)h * DH * DM + 256 * jj + 64 * w; const bf16_t* Wv = a.Wv + (size_t)256 * w * HD + hp * DH + 32 * jj;
          const unsigned po = stg + (4 * q + n3 + 16 * n2) * 128 + (((n & 3) * 2) ^ (q & 1)) * 16;
          stream([&](int i) {
                     if (i < 2) { _Pragma("unroll 1") for (int j = 0; j < 8; ++j) (CPL ? glds : gldsc)(Wo + (size_t)(64 * i + 8 * j) * DM, (l >> 3) * (DM * 2) + ((l & 7) ^ (l >> 5)) * 16, stg + i * 8192 + j * 1024); }
                     else { _Pragma("unroll 1") for (int j = 0; j < 8; ++j) glds(Wv + (size_t)(128 * (i - 2) + 16 * j) * HD, (l >> 2) * (HD * 2) + ((l & 3) ^ (2 * (q & 1))) * 16, stg + (i & 1) * 8192 + j * 1024); } },
                 [&](auto i) { _Pragma("unroll") for (int t = 0; t < 4; ++t) if constexpr (i < 2) trd4(2 * t + i, (po ^ ((t >> 1) * 16)) + (t & 1) * 8 + i * 8192, 256, 4096); },
                 [&](int j, unsigned& vo) { vo = (l >> 2) * (HD * 2) + ((l & 3) ^ (2 * (q & 1))) * 16; return Wv + (size_t)(16 * j) * HD; }, false); }
        BAR(); dry();
        start();
        A[0] = 0; A[1] = h1o(4096 + l * 16); A[2] = pg(B_TS, g) + (l & 3) * 128;
        A[3] = w >= 2 ? pg(2 * aa, g) + l * 16 : h1o(4096 + ((jj * 2 + n3) * 2 + (w & 1)) * 128 + (q & 1) * 64 + (n & 1) * 32 + (q >> 1) * 16 + ((n >> 1) & 3) * 4, XH);
        A[4] = pg(jj * 16 + w * 4 + n3 * 2, n2 * 2 + (q >> 1)) + h * 128 + ((q & 1) * 4 + (n & 3)) * 16;
        A[5] = L_VM + q * 256 + (4 * n3 + 2 * (n & 1) + n2) * 16 + hf * 4;
        A[7] = L_EX + 8192 + (w & 1) * 512 + l * 4; A[6] = A[7] ^ 512;
        A[8] = L_FL + w * 256 + l * 4; A[9] = L_EX + 8192 + (l & 3) * 2048 + 1536 + l * 4; A[10] = L_FL + 2048 + min(l & 3, 2) * 256 + l * 4;
        A[11] = L_SC + q * 512 + hf * 256 + n * 16; A[12] = L_SM + (q & 1) * 256 + hf * 128 + n * 8;
        A[13] = L_VX + q * 256 + (8 * n3 + 4 * hf + 2 * n2 + (n & 1)) * 16; A[14] = six; A[15] = L_TB;
    }
    S1[8] = RFL(pg(B_TS, g));
    asm volatile("" : "+{v[168:183]}"(A), "+{s[40:55]}"(S0), "+{s[56:71]}"(S1));
    WAITV(0); BAR();
    MARK(0); MARKC(4);
    asm volatile(A_CHAIN : "+{s[40:55]}"(S0), "+{s[56:71]}"(S1), "={s[80:87]}"(cnts), "+{v[168:183]}"(A), "+{v[188:219]}"(W[4]), "+{v[220:251]}"(W[5]) : "{v[0:31]}"(W[0]), "{v[32:63]}"(W[1]), "{v[64:95]}"(W[2]), "{v[96:127]}"(W[3])
                 : "v128", "v129", CL10("v13"), CL10("v14"), CL10("v15"), "v160", "v161", "v162", "v163", "v164", "v165", "v166", "v167", "v184", "v185", "v186", "v187", CL_S);
}

static_assert(NSEG - SO == 6, "");
struct Tile { const bf16_t* W; int ld, wide, perm, keep; };
__device__ __forceinline__ void tile_issue(const Tile& T, int t, unsigned L, int w8, int l) {
    const bf16_t* p = T.W + (T.wide ? (w8 & 3) * 32 * T.ld + (2 * t + (w8 >> 2)) * 128 : (t * 256 + w8 * 32) * T.ld);
    const unsigned vo = (T.perm ? (l >> 5) + (l & 16) : l >> 4) * T.ld * 2 + ((l & 15) ^ (2 * (l >> 4))) * 16;
#pragma unroll 1
    for (int i = 0; i < 8; ++i) { const bf16_t* g = p + i * (T.perm ? 2 : 4) * T.ld; const unsigned v = vo ^ ((i & 2) << 6), d = L + (t & 1) * 65536 + (w8 * 8 + i) * 1024;
        if (T.keep) glds(g, v, d); else gldsw(g, v, d);
        if (OSP && T.perm) __builtin_amdgcn_s_sleep(OSP); }
}
template <class F>
__device__ __forceinline__ void tile_load(const Tile& T, v4u (&wf)[32], unsigned L, int w8, int l, F next) {
    const int n = l & 15, q = l >> 4;
    const unsigned p = L + (8 * q + (n >> 2)) * 256 + ((4 * (w8 & 3) + ((l >> 1) & 1)) ^ (2 * ((n >> 2) | ((q & 1) << 2)))) * 16 + (l & 1) * 8;
    bool more = true;
#pragma unroll 1
    for (int hh = 0; hh < 2; ++hh)
#pragma unroll
    for (int u = 0; u < 2; ++u) {
        if (!more) WAITV(0);
        WAITV(8); BAR();
        if ((w8 >> 2) == hh) {
#pragma unroll
            for (int i = 0; i < 16; ++i) { const unsigned pp = (p ^ ((i & 1) * 32)) + u * 65536 + (i >> 1) * 8192; wf[u * 16 + i] = cat2(trd(pp), trd(pp + 1024)); }
            WAITL(); }
        BAR();
        if (hh == 0) tile_issue(T, u + 2, L, w8, l); else more = next(u);
    }
}
#define NB 8
#define GD 4
template <int XR, bool ROWS, class F>
__device__ __forceinline__ void seg_gemm(const v4u (&wf)[32], unsigned b0, frag_cd (&acc)[4], F side) {
    v4u f[2 * GD];
    const unsigned bb[4] = {b0, ROWS ? b0 ^ 64 : b0, ROWS ? b0 ^ 128 : b0, ROWS ? b0 ^ 192 : b0};
#define RD(ks, mt) asm volatile("ds_read_b128 %0, %1 offset:%2" : "=v"(f[((ks) % GD) * 2 + mt]) : "v"(bb[(ks) & 3]), "n"(ROWS ? ((ks) >> 2) * 256 + mt * 16384 : ((ks) >> 1) * 4096 + ((ks) & 1) * 1024 + mt * 2048) : "memory")
#define MM(ct, mt) do { if (ks == 0) asm volatile("v_mfma_f32_16x16x32_bf16 %0, %1, %2, 0" : "=&v"(acc[2 * ct + mt]) : "v"(wf[ct]), "v"(f[mt])); \
        else asm volatile("v_mfma_f32_16x16x32_bf16 %0, %1, %2, %0" : "+v"(acc[2 * ct + mt]) : "v"(wf[2 * ks + ct]), "v"(f[(ks % GD) * 2 + mt])); } while (0)
#pragma unroll
    for (int ks = 0; ks < GD - 1; ++ks) { RD(ks, 0); RD(ks, 1); }
#pragma unroll
    for (int ks = 0; ks < 16; ++ks) {
        side(2 * ks);
        asm volatile("s_waitcnt lgkmcnt(%0)" :: "n"(2 * (ks + GD - 1 <= 16 ? GD - 2 : 15 - ks) + (ks >= 13 ? XR : 0)));
        MM(0, 0);
        if (ks + GD - 1 < 16) RD(ks + GD - 1, 0);
        MM(0, 1);
        side(2 * ks + 1);
        MM(1, 0);
        if (ks + GD - 1 < 16) RD(ks + GD - 1, 1);
        MM(1, 1);
    }
    asm volatile("s_nop 7" : "+v"(acc[0]), "+v"(acc[1]), "+v"(acc[2]), "+v"(acc[3]));
#undef RD
#undef MM
}
static_assert(GD == 4 && SO == 58, "");
__device__ __forceinline__ float fadd(float a, float b) { float r; asm("v_add_f32 %0, %1, %2" : "=v"(r) : "v"(a), "v"(b)); return r; }
__device__ __forceinline__ float fmul(float a, float b) { float r; asm("v_mul_f32 %0, %1, %2" : "=v"(r) : "v"(a), "v"(b)); return r; }
__device__ __forceinline__ void half_put(unsigned ex, int w8, int l, const frag_cd (&acc)[4]) {
#pragma unroll
    for (int i = 0; i < 4; ++i) *lp<frag_cd>(ex + w8 * 4096 + i * 1024 + l * 16) = acc[i];
    FENCE();
}
struct Half { frag_cd t[4]; unsigned dec; };
__device__ __forceinline__ void half_req(unsigned ex, unsigned dec, Half& x) {
    asm volatile("ds_read_b32 %0, %1" : "=v"(x.dec) : "v"(dec) : "memory");
#pragma unroll
    for (int i = 0; i < 4; ++i) asm volatile("ds_read_b128 %0, %1 offset:%2" : "=v"(x.t[i]) : "v"(ex), "n"(i * 1024) : "memory");
}
template <class F>
__device__ __forceinline__ void half_get(Half& x, frag_cd (&acc)[4], F side) {
    asm volatile("s_waitcnt lgkmcnt(0)" : "+v"(x.t[0]), "+v"(x.t[1]), "+v"(x.t[2]), "+v"(x.t[3]), "+v"(x.dec) :: "memory");
#pragma unroll
    for (int i = 0; i < 4; ++i) { side(i);
#pragma unroll
        for (int e = 0; e < 4; ++e) acc[i][e] = fadd(acc[i][e], x.t[i][e]);
        asm volatile("" : "+v"(acc[i])); }
}
#define GEARLY 2
#define BARI 25
#define PRIO(n) __builtin_amdgcn_s_setprio(n)

__device__ __forceinline__ unsigned topk(const Args& a, int l) {
    const rsrc_t rg = mkrs(a.gate, 2 * S_TOK * 4);
    flags_wait<16>(a.ctl + C_R, 32, 1, l, a.ctl + C_ERR);
    v4u kv[8]; unsigned prev = ~0u, mine = 0;
#pragma unroll
    for (int i = 0; i < 8; ++i) kv[i] = __builtin_amdgcn_raw_buffer_load_b128(rg, S_TOK * 4 + l * 16, i * 1024, 16);
#pragma unroll 1
    for (int c = 0; c < TOPC; ++c) { unsigned mx = 0;
        FENCE();
#pragma unroll
        for (int i = 0; i < 8; ++i)
#pragma unroll
            for (int e = 0; e < 4; ++e) mx = max(mx, kv[i][e] < prev ? kv[i][e] : 0u);
        mx = max(mx, dppu<0x128>(mx)); mx = max(mx, dppu<0x124>(mx)); mx = max(mx, dppu<0x122>(mx)); mx = max(mx, dppu<0x121>(mx));
        { v2u r = sw16(mx, mx); mx = max(r.x, r.y); r = sw32(mx, mx); mx = max(r.x, r.y); }
        prev = RFL(mx); if ((l & 7) == c) mine = prev; }
    return mine;
}
__device__ __forceinline__ unsigned mix(float gt, float omg, float t0, float t1, float b0, float b1) {
    unsigned av = cvt_pk(t0, t1); av = cvt_pk(fmul(gt, lo16(av)), fmul(gt, hi16(av))); const unsigned bv = cvt_pk(fmul(omg, b0), fmul(omg, b1));
    return cvt_pk(fadd(lo16(av), lo16(bv)), fadd(hi16(av), hi16(bv)));
}
struct Finisher {
    rsrc_t rp, rx, rg, ro; int po, frow, xo, w8; unsigned seltok, ly;
    struct Fin { v4u p[4]; v2u xv; float gt; int k; };
    __device__ __forceinline__ Finisher(const Args& a, int j, int w8_, int l, unsigned L_Y) {
        rp = mkrs(a.part, NSEG * 524288); rx = mkrs(a.x, S_TOK * DM * 2); rg = mkrs(a.gate, S_TOK * 4); ro = mkrs(a.out, S_TOK * DM * 2);
        w8 = w8_; po = j * 16384 + w8 * 1024 + l * 16; frow = 16 * (w8 & 1) + (l & 15); xo = (frow * DM + j * 32 + (w8 >> 1) * 16 + 4 * (l >> 4)) * 2;
        seltok = 4095u - (lp<unsigned>(L_Y + 1024)[l & 7] & 4095u); ly = L_Y + ((w8 >> 1) * 32 + (l >> 4) * 8) * 16;
    }
    __device__ __forceinline__ Fin issue(int s, bool on) const { Fin f; int k = on ? 0 : 0x40000000; asm("" : "+s"(k));
#pragma unroll
        for (int i = 0; i < 4; ++i) f.p[i] = __builtin_amdgcn_raw_buffer_load_b128(rp, k + po, s * 524288 + i * 4096, 16);
        f.k = k + xo;
        f.xv = __builtin_amdgcn_raw_buffer_load_b64(rx, f.k, s * 65536, 0);
        f.gt = __uint_as_float(__builtin_amdgcn_raw_buffer_load_b32(rg, k + frow * 4, s * 128, 16));
        return f;
    }
    template <class F> __device__ __forceinline__ void finish(int s, const Fin& f, F side) const {
        const int tok = 32 * s + frow; const float gt = f.gt, omg = rbf(1.0f - gt);
        frag_cd t; v2u o; int k = f.k;
#pragma unroll
        for (int e = 0; e < 4; e += 2) {
            side(e >> 1);
#pragma unroll
            for (int u = e; u < e + 2; ++u) t[u] = fadd(fadd(__uint_as_float(f.p[0][u]), __uint_as_float(f.p[1][u])), fadd(__uint_as_float(f.p[2][u]), __uint_as_float(f.p[3][u])));
            o[e >> 1] = mix(gt, omg, t[e], t[e + 1], lo16(f.xv[e >> 1]), hi16(f.xv[e >> 1])); asm volatile("" : "+v"(o[e >> 1])); }
        for (unsigned mk = (unsigned)__builtin_amdgcn_ballot_w64((seltok >> 4) == (unsigned)(2 * s + (w8 & 1))) & 255u; mk; mk &= mk - 1) {
            const int slot = __builtin_ctz(mk);
            if (__builtin_amdgcn_readlane(seltok, slot) == (unsigned)tok) { *lp<frag_cd>(ly + slot * 16) = t; k = 0x40000000; }
        }
        __builtin_amdgcn_raw_buffer_store_b64(o, ro, k, s * 65536, 0);
    }
};

__device__ __forceinline__ void gwork(const Args& a, const int m, const int h, const unsigned L, const unsigned hb) {
    const int w8 = RFL(threadIdx.x >> 6); int l = threadIdx.x & 63, n = l & 15, q = l >> 4;
    unsigned* const err = a.ctl + C_ERR;
    const unsigned L_EX = L + 131072, L_XS = L_EX, L_HS = L_XS + 16384, L_GU = L_HS + 2048;
    unsigned* const fG = a.ctl + C_G + h * 128 + m * 32;
    const unsigned xo = (l ^ (8 * (w8 & 1))) * 16;
    auto xissue = [&](int s, int i0, int i1) {
        const bf16_t* p = a.x + (size_t)(32 * s + 8 * (w8 & 3)) * DM + (~w8 & 4) * 128; const unsigned d = L + (s & 1) * 65536 + (w8 ^ 4) * 8192;
#pragma unroll
        for (int i = i0; i < i1; ++i) {
            if (i == i0 || i == 4) asm volatile("s_mov_b32 m0, %0" :: "s"(d + (i & 4) * 1024));
            unsigned t; asm volatile("v_xor_b32 %0, %4, %1\n\tglobal_load_lds_dwordx4 %0, %2 offset:%3" : "=&v"(t) : "v"(xo), "s"(p), "n"((i & 3) * 1024), "n"(i * 1040 + (i & 4) * 1024) : "memory"); }
    };
    v4u wf[32];
    unsigned h2 = hb;
    { const Tile T{(m == 0 ? a.Wk : (m == 1 ? a.Wv : a.Wq)) + h * DH, HD, 0, 0, GAL >> m & 1};
      MARK(104);
      if (XPF) glds(a.x, (((h & 7) * 3 + m) * 8 + w8) * 1024 + l * 16, L_HS + w8 * 1024);
      if (GPF) ahead(T.W - h * DH, h, l, L_HS + w8 * 1024, [&](int d) { return 256 * d + 32 * w8; });
      if (GDL && ((u64)T.W >> 8 & 3) != 1) __builtin_amdgcn_s_sleep(GDL);
      tile_issue(T, 0, L, w8, l); tile_issue(T, 1, L, w8, l);
      if (GHB && w8 == 4) gld_req(h2, a.ctl + C_HB);
      tile_load(T, wf, L, w8, l, [&](int b) { if (b == 0 || w8 >= 4) xissue(b, 0, 8); return true; });
    }
    MARK(0);
    const unsigned rb0 = L + (w8 >> 2) * 32768 + n * 1024 + (q ^ n) * 16;
    unsigned pacc[8] = {}, plast = 0; (void)pacc; (void)plast;
    PT(7);
    if (w8 >= 4) {
        const rsrc_t rq = mkrs(a.qkv, NSEG * NH * 24576), rf = mkrs(fG, w8 == 4 ? 4 : 0);
        const int sto = h * 24576 + m * 8192 + (m == 1 ? (w8 & 3) * 64 + l : ((w8 & 2) + (q & 1)) * 64 + (2 * (w8 & 1) + (q >> 1)) * 16 + n) * 16;
        WAITV(8); BAR(); gld_wait<0>(h2);
        if (w8 == 4) { if (RFL(h2) == RFL(hb)) ctl_wait(a, hb); gadd1(a.ctl + C_READY); }
        for (int sg = 0; sg < NSEG; ++sg) {
            PT(0);
            BAR();
            PT(1);
            PRIO(2);
            if (sg <= GEARLY) __builtin_amdgcn_raw_buffer_store_b32(sg, rf, l * 4, 0, 16);
            frag_cd acc[4]; Half hx;
            seg_gemm<5, true>(wf, rb0 + (sg & 1) * 65536, acc, [&](int i) {
                if (i == BARI) { PRIO(3); PT(5); if (sg <= GE2) WAITV(0); WAITV(3); BAR(); PT(6); if (sg <= GE2) __builtin_amdgcn_raw_buffer_store_b32(sg, rf, l * 4, 0, 16); FENCE(); }
                if (i == 26) half_req(L_EX + (w8 & 3) * 4096 + l * 16, L_EX, hx);
                if (i >= 26) xissue((sg + 2) & 63, i - 26, i - 25); });
            PRIO(0);
            PT(2);
            half_get(hx, acc, [&](int i) { if (i & 1) xissue((sg + 2) & 63, 6 + i / 2, 7 + i / 2); });
            v4u r[2];
#pragma unroll
            for (int mt = 0; mt < 2; ++mt) r[mt] = v4u{cvt_pk(acc[mt][0], acc[2 + mt][0]), cvt_pk(acc[mt][1], acc[2 + mt][1]), cvt_pk(acc[mt][2], acc[2 + mt][2]), cvt_pk(acc[mt][3], acc[2 + mt][3])};
            PT(3);
            if (sg == 12) {
#pragma unroll
                for (int i = 0; i < 4; ++i) STB(v4u{~0u, ~0u, ~0u, ~0u}, mkrs(a.ox, NSEG * 262144), h * 8192 + (w8 & 3) * 1024 + l * 16 + (i & 1) * 4096, (SO + 2 * m + (i >> 1)) * 262144); }
#pragma unroll
            for (int mt = 0; mt < 2; ++mt) STB(r[mt], rq, sto + mt * 4096, sg * (NH * 24576));
            __builtin_amdgcn_raw_buffer_store_b32(sg <= GEARLY || sg <= GE2 ? sg : sg - 1, rf, l * 4, 0, 16);
            FENCE();
            if (sg < GEARLY) WAITV(0);
            PT(4);
        }
        PFLUSH();
        WAITV(0); BAR();
        if (w8 == 4) gst1(fG, NSEG);
    } else {
        for (int sg = 0; sg < NSEG; ++sg) {
            frag_cd acc[4];
            WAITL();
            PT(0);
            BAR();
            PT(1);
            PRIO(2);
            seg_gemm<0, true>(wf, rb0 + (sg & 1) * 65536, acc, [&](int i) { if (i == BARI) { PRIO(3); PT(5); WAITV(0); BAR(); PT(6); } });
            PRIO(1);
            PT(2);
            half_put(L_EX, w8, l, acc);
            xissue((sg + 1) & 63, 0, 8);
            PT(3);
            MARK(sg + 1);
        }
        PFLUSH();
        WAITL(); BAR();
        WAITV(0); BAR();
    }
    asm volatile("v_mbcnt_lo_u32_b32 %0, -1, 0\n\tv_mbcnt_hi_u32_b32 %0, -1, %0" : "=v"(l));
    n = l & 15; q = l >> 4;
    if (m != 2) {
        const int j = TMAP ? h : (h & 23) + 8 * m, mt = TMAP ? m : h >> 3 & 1;
        const unsigned stg = L + w8 * 16384, p = stg + (8 * q + (n >> 2)) * 64 + (n & 3) * 8;
#pragma unroll
        for (int hh = 0; hh < 2; ++hh) {
#pragma unroll 1
            for (int i = 0; i < 16; ++i) {
                (TNT ? gldsw : glds)(a.Wo + (size_t)(512 * w8 + 256 * hh + 32 * (i >> 1) + 8 * (i & 1)) * DM + 32 * j, ((l >> 3) + (l & 4) * 4) * (DM * 2) + (l & 3) * 16, stg + i * 1024); WAITV(TIF); __builtin_amdgcn_s_sleep(TSP); }
            WAITV(0);
#pragma unroll
            for (int i = 0; i < 16; ++i) { const unsigned pp = p + (i >> 1) * 2048 + (i & 1) * 32; wf[hh * 16 + i] = cat2(trd(pp), trd(pp + 256)); }
            WAITL();
        }
        MARK(66);
        const rsrc_t ro = mkrs(a.out, S_TOK * DM * 2), ra = mkrs(a.ox, NSEG * 262144);
        const int xo = (16 * mt + n) * DM + j * 32 + (w8 & 1) * 16 + 4 * q;
        const unsigned L_B = L_EX - SO * 2048;
        if (w8 < 2) {
            const unsigned seltok = 4095u - (topk(a, l) & 4095u);
#pragma unroll 1
            for (int s = SO; s < NSEG; ++s) { const v2u xv = *(const v2u*)(a.x + s * 32768 + xo);
                *lp<v4u>(L_B + s * 2048 + w8 * 1024 + l * 16) = v4u{xv.x, xv.y, gld(a.gate + 32 * s + 16 * mt + n), seltok}; }
        }
        BAR();
        MARK(67);
        { unsigned fv = 0; flags_wait<16, 64>(fv, a.ctl + C_M + (16 * w8 + 2 * (l & 7) + mt) * 32, SO - 1, err); }
        auto peek = [&](int s) { FENCE(); const unsigned v = __builtin_amdgcn_raw_buffer_load_b32(ra, w8 * 32768 + mt * 2048 + (l & 7) * 4096 + 1024, s * 262144, 16); FENCE(); return v; };
        unsigned nx = ~0u;
#define TMK(k)
#pragma unroll 1
        for (int s = SO, it = 0; s < NSEG;) {
            for (int i = 0; __builtin_amdgcn_ballot_w64(nx == ~0u) != 0; ++i) { if (i) __builtin_amdgcn_s_sleep(TS1); nx = peek(s); if (++it > GCAP) fatal(err); }
            TMK(0);
            v4u f[16]; frag_cd acc[4] = {};
            unsigned mx = 0;
#pragma unroll
            for (int i = 0; i < 16 + TD; ++i) { const int ks = i - TD;
                SB();
                if (i < 16) f[i] = __builtin_amdgcn_raw_buffer_load_b128(ra, w8 * 32768 + mt * 2048 + l * 16 + (i & 1) * 1024, s * 262144 + (i >> 1) * 4096, 16);
                if (i == 16) TMK(1);
                if (ks == TFK) nx = peek(min(s + 1, NSEG - 1));
                SB();
                if (ks >= 0) { acc[ks & 1] = MFMA(wf[2 * ks], f[ks], acc[ks & 1]); acc[2 + (ks & 1)] = MFMA(wf[2 * ks + 1], f[ks], acc[2 + (ks & 1)]); mx = umax3(umax3(mx, f[ks].x, f[ks].y), f[ks].z, f[ks].w); } }
            SB();
            TMK(2);
            if (__builtin_amdgcn_ballot_w64(mx == ~0u) != 0) {
                nx = ~0u; continue; }
            const unsigned ex = L + (s - SO) * 16384 + w8 * 2048 + l * 16;
            *lp<frag_cd>(ex) = acc[0] + acc[1]; *lp<frag_cd>(ex + 1024) = acc[2] + acc[3];
            if (s % 3 == 0) {
                WAITL(); BAR();
                if (w8 < 6) { const int u = s - (w8 >> 1), o = (w8 & 1) * 1024 + l * 16;
                    v4u b = *lp<v4u>(L_B + u * 2048 + o);
                    frag_cd t = *lp<frag_cd>(L + (u - SO) * 16384 + o);
#pragma unroll
                    for (int i = 1; i < 8; ++i) t += *lp<frag_cd>(L + (u - SO) * 16384 + o + i * 2048);
                    for (unsigned mk = (unsigned)__builtin_amdgcn_ballot_w64((b.w >> 4) == (unsigned)(2 * u + mt)) & 255u; mk; mk &= mk - 1) {
                        const int slot = __builtin_ctz(mk);
                        flags_wait<4>(a.ctl + C_Y + j * 32 + 1, 1, 1, l, err);
                        const frag_cd y = __builtin_bit_cast(frag_cd, __builtin_amdgcn_raw_buffer_load_b128(mkrs(a.ypart + (32 * 28 + j) * 1024, 1024), ((w8 & 1) * 32 + q * 8 + slot) * 16, 0, 16));
                        if ((__builtin_amdgcn_readlane(b.w, slot) & 15) == (unsigned)n) { b.x = cvt_pk(lo16(b.x) + y[0], hi16(b.x) + y[1]); b.y = cvt_pk(lo16(b.y) + y[2], hi16(b.y) + y[3]); }
                    }
                    const float gt = __uint_as_float(b.z), omg = rbf(1.0f - gt);
                    __builtin_amdgcn_raw_buffer_store_b64(v2u{mix(gt, omg, t[0], t[1], lo16(b.x), hi16(b.x)), mix(gt, omg, t[2], t[3], lo16(b.y), hi16(b.y))}, ro, xo * 2, u * 65536, 0); } }
            TMK(3);
            if (TSPEC && s == NSEG - 2) { __builtin_amdgcn_s_sleep(TS3); nx = 0; }
            ++s;
        }
        MARK(65);
        return;
    }
    const rsrc_t ry = mkrs(a.ypart, 32 * 29 * 1024);
    auto tile = [&](int t) { return t < 2 ? Tile{(t ? a.wu : a.wg) + h * 128, FF, 0, 0, 0} : Tile{a.wd + (size_t)h * 128 * DM, DM, 1, 0, 0}; };
    unsigned mykey = 0;
    if (w8 == 0) {
        mykey = topk(a, l);
#pragma unroll 1
        for (int i = 0; i < 16; ++i) glds(a.x + i * 64, (4095u - (mykey & 4095u)) * (DM * 2) + (l >> 3) * 16, L_XS + i * 1024);
    }
    if (h < FF / 128) {
      v4u wm[32];
      Tile T = tile(0);
      tile_issue(T, 0, L, w8, l); tile_issue(T, 1, L, w8, l);
      frag_cd gv[2] = {};
#pragma unroll 1
      for (int ti = 0; ti < 3; ++ti) {
        const Tile N = tile(ti + 1);
        tile_load(T, wm, L, w8, l, [&](int b) { if (ti < 2) tile_issue(N, b, L, w8, l); return ti < 2; });
        T = N;
        if (ti < 2) {
            frag_cd acc[2] = {};
#pragma unroll
            for (int ks = 0; ks < 16; ++ks) { const v4u xf = *lp<v4u>(L_XS + ((w8 >> 2) * 16 + ks) * 512 + q * 128 + (n & 7) * 16); acc[0] = MFMA(wm[2 * ks], xf, acc[0]); acc[1] = MFMA(wm[2 * ks + 1], xf, acc[1]); }
            if (ti == 1) {
                frag_cd g = n < 8 ? gv[0] : gv[1], u = n < 8 ? acc[0] : acc[1];
                if (w8 >= 4) { *lp<frag_cd>(L_GU + (w8 & 3) * 2048 + l * 16) = g; *lp<frag_cd>(L_GU + (w8 & 3) * 2048 + 1024 + l * 16) = u; WAITL(); }
                BAR();
                if (w8 < 4) { g += *lp<frag_cd>(L_GU + w8 * 2048 + l * 16); u += *lp<frag_cd>(L_GU + w8 * 2048 + 1024 + l * 16);
                    float hv[4];
#pragma unroll
                    for (int e = 0; e < 4; ++e) { const float gg = rbf(g[e]); hv[e] = rbf(gg / (1.0f + __expf(-gg))) * rbf(u[e]); }
                    *lp<v2u>(L_HS + w8 * 512 + (2 * (n >> 3) + (q >> 1)) * 128 + (n & 7) * 16 + (q & 1) * 8) = v2u{cvt_pk(hv[0], hv[1]), cvt_pk(hv[2], hv[3])};
                    WAITL(); } }
            gv[0] = acc[0]; gv[1] = acc[1];
        } else {
#pragma unroll
            for (int cb = 0; cb < 4; ++cb) { frag_cd acc[2] = {};
#pragma unroll
                for (int i = 0; i < 4; ++i) { const v4u hf = *lp<v4u>(L_HS + i * 512 + q * 128 + (n & 7) * 16); acc[0] = MFMA(wm[8 * cb + 2 * i], hf, acc[0]); acc[1] = MFMA(wm[8 * cb + 2 * i + 1], hf, acc[1]); }
                STB(__builtin_bit_cast(v4u, n < 8 ? acc[0] : acc[1]), ry, ((w8 & 3) * 28 + h) * 1024 + ((n >> 3) * 32 + q * 8 + (n & 7)) * 16, ((w8 >> 2) * 4 + cb) * (4 * 28 * 1024)); }
        }
        MARK(67 + ti);
      }
      WAITV(0); BAR();
    }
    if (w8 == 0) {
        gst1(a.ctl + C_Y + h * 32, 1u);
        flags_wait<16>(a.ctl + C_Y, FF / 128, 1, l, err);
        frag_cd y = {};
#pragma unroll 1
        for (int i = 0; i < 28; i += 7) { v4u t[7];
#pragma unroll
            for (int u = 0; u < 7; ++u) t[u] = __builtin_amdgcn_raw_buffer_load_b128(ry, l * 16, (h * 28 + i + u) * 1024, 16);
#pragma unroll
            for (int u = 0; u < 7; ++u) y += __builtin_bit_cast(frag_cd, t[u]); }
        const float r = hi16((mykey & 0x80000000u) ? mykey & 0x7fffffffu : ~mykey), rs = rbf(1.0f / (1.0f + __expf(-r)));
#pragma unroll
        for (int e = 0; e < 4; ++e) y[e] = rbf(rbf(y[e]) * rs);
        STB(__builtin_bit_cast(v4u, y), ry, l * 16, (32 * 28 + h) * 1024);
        WAITV(0);
        gst1(a.ctl + C_Y + h * 32 + 1, 1u);
        MARK(120);
    }
}

__device__ __forceinline__ void owork(const Args& a, const int nt, const int kq, const unsigned L, const unsigned hb) {
    const int tid = threadIdx.x, w8 = RFL(tid >> 6), l = tid & 63, j = nt * 4 + kq;
    unsigned* const err = a.ctl + C_ERR;
    const unsigned L_EX = L + 131072, L_DEC = L_EX + 16384, L_Y = L_DEC + 64, L_KEY = L_Y + 1024;
    if (tid < 4) lp<unsigned>(L_DEC)[tid] = 0;
    {
        const bool lead = j == 0 && w8 == 1;
        if (lead) { const rsrc_t rc = mkrs(a.ctl, C_HB * 4);
#pragma unroll
            for (int i = 0; i < 8; ++i) STB(v4u{}, rc, l * 128, i * 8192);
            STB(v4u{}, rc, l * 16, 0); }
        unsigned char* p = a.ring + ((l >> 3 & 1) * 64 + (j >> 1)) * 32768 + ((l ^ (kq & 1) * 4 ^ kq >> 1) & 7) * 4096 + (w8 * 2 + (nt & 1)) * 256 + (kq & 1) * 128;
        unsigned best = 0;
        if (l < 16) gst((unsigned*)p, ~0u);
        if (lead) { WAITV(0); gadd1(a.ctl + C_HB); }
#pragma unroll 1
        for (int i = 0; i < OPF; ++i) {
            const bf16_t* W = i % 3 == 0 ? a.Wk : (i % 3 == 1 ? a.Wv : a.Wq);
            glds(W, ((i < 6 ? 512 : 0) + (nt * 8 + w8) * 8 + (l >> 3)) * (HD * 2) + kq * 2048 + (i / 3 & 1) * 1024 + ((384 - (unsigned)(u64)W) & 896) + (l & 7) * 16, L + 65536 + w8 * 1024); }
        WAITV(0);
#pragma unroll 1
        for (int i = 0; i < 16; ++i) { u64 t1, t2; unsigned v;
            if (lead && !(i & 3)) gadd1(a.ctl + C_HB);
            const u64 pi = ((u64)__builtin_amdgcn_readlane((unsigned)((u64)p >> 32), i) << 32) | (unsigned)__builtin_amdgcn_readlane((unsigned)(u64)p, i);
            asm volatile("s_nop 4\n\ts_memtime %0\n\ts_waitcnt lgkmcnt(0)\n\tglobal_load_dword %2, %3, %4 sc1\n\ts_waitcnt vmcnt(0)\n\ts_memtime %1\n\ts_waitcnt lgkmcnt(0)"
                         : "=&s"(t1), "=&s"(t2), "=&v"(v) : "v"(0u), "s"(pi) : "memory");
            if (l == i) best = ~(unsigned)(t2 - t1); }
        if (l < 16) { gst((unsigned*)p, ~0u); lp<unsigned>(L)[w8 * 16 + l] = best; }
        WAITV(0); WAITL(); BAR();
        if (w8 == 0) {
#pragma unroll
            for (int i = 1; i < 8; ++i) best = max(best, lp<unsigned>(L)[i * 16 + (l & 15)]);
            if (j) ctl_wait(a, hb);
            if (l < 16) __hip_atomic_fetch_max(a.ctl + C_CAL + l, best, __ATOMIC_RELAXED, __HIP_MEMORY_SCOPE_AGENT);
            WAITV(0);
            gadd1(a.ctl + C_READY);
            MARK(66);
        }
        if (lead) for (int i = 0; !RFL(gld(a.ctl + C_ACK)); ++i) { gadd1(a.ctl + C_HB); __builtin_amdgcn_s_sleep(8); if (i > GCAP) fatal(err); }
    }
    BAR();
    v4u wf[32];
    const Tile T{a.Wo + (size_t)kq * 1024 * DM + nt * 128, DM, 0, 1, OPL};
    if (OWF) { v4u r[16];
#pragma unroll
        for (int i = 0; i < 16; ++i) asm volatile("global_load_dwordx4 %0, %1, %2" : "=v"(r[i]) : "v"(l * 16), "s"(a.x + (size_t)(64 * j + 8 * w8) * DM + i * 512) : "memory");
        tile_issue(T, 0, L, w8, l); tile_issue(T, 1, L, w8, l);
        tile_load(T, wf, L, w8, l, [](int) { return false; });
        asm volatile("" : "+v"(r[0]), "+v"(r[1]), "+v"(r[2]), "+v"(r[3]), "+v"(r[4]), "+v"(r[5]), "+v"(r[6]), "+v"(r[7]), "+v"(r[8]), "+v"(r[9]), "+v"(r[10]), "+v"(r[11]), "+v"(r[12]), "+v"(r[13]), "+v"(r[14]), "+v"(r[15]) :: "memory");
#pragma unroll
        for (int i = 0; i < 16; ++i) *lp<v4u>(L + (w8 * 16 + i) * 1024 + l * 16) = r[i];
        MARK(0); }
    {
#pragma unroll 1
        for (int i = 0; i < 16; ++i) if (!OWF) glds(a.x + (size_t)(64 * j + 8 * w8) * DM + i * 512, l * 16, L + (w8 * 16 + i) * 1024);
        v4u gw[2], wr[2];
#pragma unroll
        for (int i = 0; i < 2; ++i) { gw[i] = *(const v4u*)(a.gw + 512 * i + 8 * l); wr[i] = *(const v4u*)(a.wr + 512 * i + 8 * l); }
        WAITV(0);
        float res = 0.f;
#pragma unroll 1
        for (int t = 0; t < 8; ++t) { float sg = 0.f, sr = 0.f;
#pragma unroll
            for (int i = 0; i < 2; ++i) { const v4u xv = *lp<v4u>(L + (w8 * 16 + 2 * t + i) * 1024 + l * 16);
#pragma unroll
                for (int e = 0; e < 4; ++e) { const float x0 = lo16(xv[e]), x1 = hi16(xv[e]); const unsigned p = cvt_pk(x0 * lo16(gw[i][e]), x1 * hi16(gw[i][e]));
                    sg += lo16(p) + hi16(p); sr = __builtin_fmaf(x0, lo16(wr[i][e]), __builtin_fmaf(x1, hi16(wr[i][e]), sr)); } }
            float v = tadd32(sg, sr);
            v = tadd16(v, v); v += dppf<ROR8>(v); v += dppf<0x124>(v); v += dppf<0x122>(v); v += dppf<0x121>(v);
            if ((l & 7) == t) res = v; }
        const unsigned rb = cvt_pk(res, 0.f), tok = 64 * j + 8 * w8 + (l & 7);
        const unsigned val = l < 32 ? __float_as_uint(rbf(1.0f / (1.0f + __expf(-lo16(rb))))) : (((rb & 0x8000u) ? ~rb : rb | 0x8000u) << 16) | (4095u - tok);
        if ((l & 31) < 8) gst(a.gate + (l >> 5) * S_TOK + tok, val);
        WAITV(0); BAR();
        if (w8 == 0) gst1(a.ctl + C_R + j * 32, 1u);
        MARK(67);
    }
    if (!OWF) { tile_issue(T, 0, L, w8, l); tile_issue(T, 1, L, w8, l);
      tile_load(T, wf, L, w8, l, [](int) { return false; });
      MARK(0); }
    if (w8 == 1) { const unsigned mine = topk(a, l); if (l < 8) lp<unsigned>(L_KEY)[l] = mine; WAITL(); }
    BAR();
    MARK(68);
    unsigned* const fP = a.ctl + C_P + nt * 512;
    const unsigned rb = L + (w8 >> 2) * 32768 + l * 16;
    unsigned pacc[8] = {}, plast = 0; (void)pacc; (void)plast;
    PT(7);
    auto oissue = [&](int s, int wv, int i) { glds1<2>(a.ox + (size_t)s * 262144 + kq * 65536 + wv * 8192 + i * 2048, l * 16, L + (s & 1) * 65536 + wv * 8192 + i * 2048); };
    if (w8 >= 4) {
        const rsrc_t rf = mkrs(fP + (kq * 4 + (w8 & 3)) * 32, 4), rp = mkrs(a.part, NSEG * 524288);
        const int stp = ((nt * 4 + (w8 & 3)) * 4 + kq) * 4096 + l * 16;
        const unsigned* fW = l < 32 ? a.ctl + C_M + (kq * 32 + l) * 32 : fP + ((l & 3) * 4 + kq) * 32;
        unsigned fv = 0; int pc = 0;
        auto a_ok = [&](int s) { return s < SO && (unsigned)__builtin_amdgcn_ballot_w64(fv <= (unsigned)s) == 0; };
        auto p_upd = [&]() { if ((__builtin_amdgcn_ballot_w64(fv <= (unsigned)pc) >> 32) == 0) ++pc; if ((__builtin_amdgcn_ballot_w64(fv <= (unsigned)pc) >> 32) == 0) ++pc; lp<unsigned>(L_DEC)[2] = pc; };
        int have = 0, nxt = 0, late = 0;
        for (int s = 0; s <= SO; ++s) {
            if (!have) {
                if (!late) {
                    gld_wait<0>(fv);
                    __builtin_amdgcn_raw_buffer_store_b32(s, rf, l * 4, 0, 16);
                    if (w8 == 4) {
                        for (int i = 0;; ++i) { if (fv <= (unsigned)(l < 32 ? s + 1 : s - 1)) fv = gld(fW); WAITV(0); p_upd(); if (a_ok(s) || pc == SO) break; __builtin_amdgcn_s_sleep(2); if (i > GCAP) fatal(err); }
                        lp<unsigned>(L_DEC)[3] = s + 1; lp<unsigned>(L_DEC)[~s & 1] = a_ok(s + 1); FENCE(); }
                    if (s == SO) break;
                    for (int i = 0; ldsr(L_DEC + 12) <= (unsigned)s; ++i) { __builtin_amdgcn_s_sleep(1); if (i > LCAP) endpgm(); }
                    FENCE();
                    PT(0);
#pragma unroll
                    for (int i = 0; i < 8; ++i) oissue(s, w8 ^ (~i & 4), i & 3);
                    WAITV(8);
                }
                WAITV(12); BAR();
                nxt = RFL(ldsr(L_DEC + 4 * (~s & 1))) & 1;
                if (late) WAITV(4); else WAITV(0);
                if (nxt) {
#pragma unroll
                    for (int i = 0; i < 4; ++i) oissue(s + 1, w8 ^ 4, i); }
                PT(1);
            }
            if (w8 == 4 && !nxt) {
                if (!have) { if (fv <= (unsigned)(s + 1)) gld_req(fv, fW); WAITV(0); }
                gld_wait<4>(fv);
                p_upd(); lp<unsigned>(L_DEC)[~s & 1] = 4 * a_ok(s + 1); WAITL();
                if (fv <= (unsigned)(s + 2)) gld_req(fv, fW); }
            BAR();
            PT(2);
            PRIO(2);
            frag_cd acc[4]; Half hx; unsigned mid;
            seg_gemm<5, false>(wf, rb + (s & 1) * 65536, acc, [&](int i) {
                if (i == BARI - 5) { if (!(have & nxt)) WAITV(0);
                    gld_wait<4>(fv);
                    if (w8 == 4) { p_upd(); const bool k = a_ok(s + 1); if (k) lp<unsigned>(L_DEC)[3] = s + 2; lp<unsigned>(L_DEC)[s & 1] = a_ok(s + 2) + 2 * k; }
                    __builtin_amdgcn_raw_buffer_store_b32(s - (have & nxt), rf, l * 4, 0, 16); FENCE(); }
                if (i == BARI) { PRIO(3); PT(5); BAR(); PT(6); }
                if (i == 3) asm volatile("ds_read_b32 %0, %1" : "=v"(mid) : "v"(L_DEC + 4 * (~s & 1)) : "memory");
                if (i == 26) half_req(L_EX + (w8 & 3) * 4096 + l * 16, L_DEC + 4 * (s & 1), hx);
                if (i == 28) { asm volatile("s_waitcnt lgkmcnt(4)" : "+v"(hx.dec), "+v"(mid)); const int d = RFL(hx.dec); have = nxt | RFL(mid) >> 2; nxt = have & d; late = ~have & d >> 1 & 1; }
                if (i >= 28 && (nxt | late)) oissue(s + 1 + nxt, w8 ^ 4, i - 28); });
            PRIO(0);
            PT(3);
            half_get(hx, acc, [&](int i) { if (late) oissue(s + 1, w8, i); });
            if (w8 == 4 && fv <= (unsigned)(s + 3)) gld_req(fv, fW);
#pragma unroll
            for (int i = 0; i < 4; ++i) STB(__builtin_bit_cast(v4u, acc[i]), rp, stp + i * 1024, s * 524288);
            FENCE();
            PT(4);
        }
        if (w8 == 5) {
            flags_wait<4>(a.ctl + C_Y + j * 32 + 1, 1, 1, l, err);
            const unsigned tok = 4095u - (lp<unsigned>(L_KEY)[l & 7] & 4095u); const int xo = tok * DM + j * 32 + (l >> 3) * 4;
            const frag_cd y = __builtin_bit_cast(frag_cd, __builtin_amdgcn_raw_buffer_load_b128(mkrs(a.ypart + (32 * 28 + j) * 1024, 1024), l * 16, 0, 16));
            const v2u xv = *(const v2u*)(a.x + xo); const float gt = __uint_as_float(gld(a.gate + tok)), omg = rbf(1.0f - gt);
            const float b[4] = {rbf(lo16(xv.x) + y[0]), rbf(hi16(xv.x) + y[1]), rbf(lo16(xv.y) + y[2]), rbf(hi16(xv.y) + y[3])};
            asm volatile("" :: "v"(b[0]), "v"(b[1]), "v"(b[2]), "v"(b[3]), "v"(omg));
            BAR();
            const frag_cd t = *lp<frag_cd>(L_Y + l * 16);
            if (tok < 32 * SO) *(v2u*)(a.out + xo) = v2u{mix(gt, omg, t[0], t[1], b[0], b[1]), mix(gt, omg, t[2], t[3], b[2], b[3])};
        } else BAR();
    } else {
        const Finisher fz(a, j, w8, l, L_Y);
        int fin = 0, have = 0, pc = 0;
        for (int s = 0; s <= SO; ++s) {
            if (!have) {
                for (int i = 0; fin < s; ++i) {
                    if (s < SO && RFL(ldsr(L_DEC + 12)) > s) break;
                    pc = RFL(ldsr(L_DEC + 8));
                    if (fin < pc) { FENCE(); fz.finish(fin, fz.issue(fin, true), [](int) {}); ++fin; }
                    else { __builtin_amdgcn_s_sleep(1); if (i > LCAP) endpgm(); }
                }
                if (s == SO) break;
            }
            FENCE();
            const bool fi = fin < pc;
            const Finisher::Fin f = fz.issue(fin, fi);
            WAITL(); WAITV(15);
            PT(0);
            BAR();
            PT(2);
            PRIO(2);
            frag_cd acc[4];
            v4u dec;
            seg_gemm<1, false>(wf, rb + (s & 1) * 65536, acc, [&](int i) {
                if (i == BARI) { PRIO(3); PT(1); WAITV(6); BAR(); PT(6); }
                if (i == 26) asm volatile("ds_read_b128 %0, %1" : "=v"(dec) : "v"(L_DEC) : "memory"); });
            PRIO(1);
            PT(3);
            asm volatile("s_waitcnt lgkmcnt(0)" : "+v"(dec));
            have = RFL(s & 1 ? dec.x : dec.y) & 5; pc = RFL(dec.z);
            half_put(L_EX, w8, l, acc);
            WAITV(0);
            if (have & 4) {
#pragma unroll
                for (int i = 0; i < 4; ++i) oissue(s + 1, w8, i); }
            if (have) { oissue(s + 1, w8 ^ 4, 0); oissue(s + 1, w8 ^ 4, 1); }
            PT(4);
            if (!have) { WAITL(); BAR(); }
            fz.finish(fin, f, [&](int i) { if (have) oissue(s + 1, w8 ^ 4, 2 + i); });
            fin += fi;
            MARK(1 + s);
            PT(5);
        }
        WAITL(); BAR();
    }
    PFLUSH();
    MARK(65);
}

__global__ void __launch_bounds__(512, 2) k_mega(Args a) {
    __shared__ __attribute__((aligned(1024))) unsigned char smem[163840];
    const unsigned L = (unsigned)(size_t)(LAS unsigned char*)smem;
    const int b = blockIdx.x, u = (b & 3) + 4 * (b >> 3);
    MARK(255); MARKC(254);
    const unsigned hb = __builtin_amdgcn_raw_buffer_load_b32(mkrs(a.ctl + C_HB, 4), threadIdx.x >> 6 == (b & 4 ? (u < 96 ? 4 : 0) : 3) ? 0 : OOB, 0, 16);
    FENCE();
    if (!(b & 4)) chain(a, u, L, hb);
    else if (u < 96) gwork(a, (u >> 2) % 3, (u & 3) * 8 + (u >> 2) / 3, L, hb);
    else owork(a, (u >> 2) - 24, u & 3, L, hb);
}

static constexpr int g_coff = 0, g_force = 0; static constexpr int g_d1 = 620, g_d2 = 1340, g_per = 1800, g_dinit = 250; static constexpr unsigned* g_dbg = nullptr;
extern "C" void kernel_launch(void* const* d_in, const int* in_sizes, int n_in, void* d_out, int out_size, void* d_ws, size_t ws_size, hipStream_t stream) {
    auto in = [&](int i) { return (const bf16_t*)d_in[i]; };
    unsigned char* ws = (unsigned char*)d_ws; size_t off = g_coff;
    auto alloc = [&](size_t bytes) { unsigned char* p = ws + off; off += (bytes + 255) & ~(size_t)255; return p; };
    unsigned* ctl = (unsigned*)alloc(C_WORDS * 4);
    unsigned* gate = (unsigned*)alloc(2 * S_TOK * 4); unsigned char* ypart = alloc(32 * 29 * 1024);
    off += (size_t)(-(uintptr_t)(ws + off)) & ((2u << 20) - 1);
    unsigned char* ring = alloc(RING_BYTES + ((size_t)RGAP << 20));
    unsigned char* qkv = alloc((size_t)NSEG * NH * 24576);
    unsigned char* ox = alloc((size_t)NSEG * 262144); unsigned char* part = alloc((size_t)NSEG * 524288);
    Args a{in(0), in(1), in(2), in(3), in(4), in(5), (bf16_t*)d_out, ctl, ring, qkv, ox, part, in(8), in(9), in(10), in(11), in(12), gate, ypart, g_dbg,
           g_d1 * 16 / 10, g_d2 * 16 / 10, g_per * 16 / 10, g_dinit * 16 / 10 + (g_force << 16)};
    k_mega<<<256, 512, 0, stream>>>(a);
}
```
